```python
import torch

def _softmax(x, axis=-1):
    return torch.softmax(x, dim=axis)

def _gelu(x):
    return torch.nn.functional.gelu(x, approximate='tanh')

def _silu(x):
    return torch.nn.functional.silu(x)

def _sigmoid(x):
    return torch.sigmoid(x)

def _layernorm(x, axis=-1, eps=1e-05):
    m = x.mean(dim=axis, keepdim=True)
    v = x.var(dim=axis, keepdim=True, unbiased=False)
    return (x - m) / torch.sqrt(v + eps)

def _scatter_add(tgt, idx, src, axis=0):
    return tgt.clone().index_add_(axis, idx, src)

def _segment_sum(src, seg, n):
    out = torch.zeros((n,) + src.shape[1:], dtype=src.dtype)
    return out.index_add_(0, seg, src)

def _pad(x, pw):
    flat = tuple((v for pair in reversed(pw) for v in pair))
    return torch.nn.functional.pad(x, flat)
kernel_name = 'parallel_sum__memory_aug_recurrent_mem_tr__swiglu_ffn__prompt'

def setup_inputs():

    _w = lambda a: (lambda t: t.to(torch.bfloat16) if t.is_floating_point() else t)(torch.as_tensor(a))

    def _A_setup():
        B, S, Dm = (1, 2048, 640)
        H, Dh = (32, 192)
        M, seg = (8, 32)
        seg = min(seg, S)
        S = S // seg * seg
        return {'x': _w((lambda *s: torch.randn(*s, dtype=torch.bfloat16) * 0.05)(B, S, Dm)), 'mem0': _w((lambda *s: torch.randn(*s, dtype=torch.bfloat16) * 1.6)(B, M, Dm)), 'Wq': _w((lambda *s: torch.randn(*s, dtype=torch.bfloat16) * 0.8)(Dm, H, Dh) * Dm ** (-0.5)), 'Wk': _w((lambda *s: torch.randn(*s, dtype=torch.bfloat16) * 0.8)(Dm, H, Dh) * Dm ** (-0.5)), 'Wv': _w((lambda *s: torch.randn(*s, dtype=torch.bfloat16) * 1.6)(Dm, H, Dh) * Dm ** (-0.5)), 'Wo': _w((lambda *s: torch.randn(*s, dtype=torch.bfloat16) * 1.6)(H, Dh, Dm) * (H * Dh) ** (-0.5)), 'seg_len': seg}
    _A_in = _A_setup()

    def _B_setup():
        B, S, D, F = (1, 2048, 640, 1536)
        return {'x': _w((lambda *s: torch.randn(*s, dtype=torch.bfloat16) * 0.05)(B, S, D)), 'w_gate': _w((lambda *s: torch.randn(*s, dtype=torch.bfloat16) * 0.05)(D, F)), 'w_up': _w((lambda *s: torch.randn(*s, dtype=torch.bfloat16) * 0.05)(D, F)), 'w_down': _w((lambda *s: torch.randn(*s, dtype=torch.bfloat16) * 0.05)(F, D))}
    _B_in = _B_setup()
    return {'A_x': _A_in['x'], 'A_mem0': _A_in['mem0'], 'A_Wq': _A_in['Wq'], 'A_Wk': _A_in['Wk'], 'A_Wv': _A_in['Wv'], 'A_Wo': _A_in['Wo'], 'A_seg_len': _A_in['seg_len'], 'B_x': _B_in['x'], 'B_w_gate': _B_in['w_gate'], 'B_w_up': _B_in['w_up'], 'B_w_down': _B_in['w_down']}

def reference(A_x, A_mem0, A_Wq, A_Wk, A_Wv, A_Wo, A_seg_len, B_x, B_w_gate, B_w_up, B_w_down):

    def _A_ref(x, mem0, Wq, Wk, Wv, Wo, seg_len):
        B, S, Dm = x.shape
        H, Dh = (Wq.shape[1], Wq.shape[2])
        M = mem0.shape[1]
        scale = Dh ** (-0.5)
        n_seg = S // seg_len
        L = M + seg_len
        idx = torch.arange(L)
        mask = (idx[:, None] < M) | (idx[:, None] >= idx[None, :])
        mem = mem0
        outs = []
        for s in range(n_seg):
            xs = x[:, s * seg_len:(s + 1) * seg_len, :].clone()
            h = torch.concatenate([mem, xs], axis=1)
            q = torch.einsum('btd,dhe->bhte', h, Wq)
            k = torch.einsum('btd,dhe->bhte', h, Wk)
            v = torch.einsum('btd,dhe->bhte', h, Wv)
            scores = torch.einsum('bhqe,bhke->bhqk', q, k) * scale
            scores = torch.where(mask[None, None], scores, -1000000000.0)
            p = _softmax(scores, axis=-1)
            o = torch.einsum('bhqk,bhke->bhqe', p, v)
            o = torch.einsum('bhte,hed->btd', o, Wo)
            mem = o[:, :M, :].clone()
            outs.append(o[:, M:, :])
        return torch.concatenate(outs, axis=1)

    def _B_ref(x, w_gate, w_up, w_down):
        g = _silu(x @ w_gate)
        u = x @ w_up
        return g * u @ w_down
    x = A_x
    xn = _layernorm(x, axis=-1)
    return x + _A_ref(xn, A_mem0, A_Wq, A_Wk, A_Wv, A_Wo, A_seg_len) + _B_ref(xn, B_w_gate, B_w_up, B_w_down)
```

```cpp
#include <hip/hip_runtime.h>
#include <hip/hip_bf16.h>
typedef unsigned short u16;
__device__ __forceinline__ float b2f(u16 v) { return __builtin_bit_cast(float, (unsigned)v << 16); }
__device__ __forceinline__ u16 f2b(float f) { unsigned u = __builtin_bit_cast(unsigned, f); return (u16)((u + 0x7fffu + ((u >> 16) & 1u)) >> 16); }
constexpr int S = 2048, D = 640, H = 32, E = 192, HE = H * E, M = 8, SEG = 32, NSEG = 64, F = 1536;

__global__ void k_ln(const u16* x, u16* xn) {
  int row = blockIdx.x, t = threadIdx.x;
  float v[10], s = 0;
  for (int i = 0; i < 10; ++i) { v[i] = b2f(x[row * D + t + 64 * i]); s += v[i]; }
  for (int o = 32; o; o >>= 1) s += __shfl_xor(s, o);
  float m = s / D, q = 0;
  for (int i = 0; i < 10; ++i) q += (v[i] - m) * (v[i] - m);
  for (int o = 32; o; o >>= 1) q += __shfl_xor(q, o);
  float r = 1.0f / sqrtf(q / D + 1e-5f);
  for (int i = 0; i < 10; ++i) xn[row * D + t + 64 * i] = f2b((v[i] - m) * r);
}
__global__ void k_gemm(const u16* A, int lda, const u16* B, int ldb, u16* C, int ldc, int Mr, int K) {
  __shared__ float As[16][65], Bs[16][65];
  int tx = threadIdx.x & 15, ty = threadIdx.x >> 4;
  int r0 = blockIdx.y * 64, c0 = blockIdx.x * 64;
  float acc[4][4] = {};
  for (int k0 = 0; k0 < K; k0 += 16) {
    for (int i = threadIdx.x; i < 1024; i += 256) {
      int r = i >> 4, k = i & 15; As[k][r] = (r0 + r < Mr) ? b2f(A[(size_t)(r0 + r) * lda + k0 + k]) : 0.f;
      int kk = i >> 6, c = i & 63; Bs[kk][c] = b2f(B[(size_t)(k0 + kk) * ldb + c0 + c]);
    }
    __syncthreads();
    for (int k = 0; k < 16; ++k)
      for (int i = 0; i < 4; ++i) for (int j = 0; j < 4; ++j) acc[i][j] += As[k][ty * 4 + i] * Bs[k][tx * 4 + j];
    __syncthreads();
  }
  for (int i = 0; i < 4; ++i) for (int j = 0; j < 4; ++j) if (r0 + ty * 4 + i < Mr) C[(size_t)(r0 + ty * 4 + i) * ldc + c0 + tx * 4 + j] = f2b(acc[i][j]);
}
__global__ void __launch_bounds__(1024) k_gemv8(const u16* A, const u16* B, u16* C, int K, int N) {
  __shared__ float red[64][8][17];
  int cl = threadIdx.x & 15, c = blockIdx.x * 16 + cl, w = threadIdx.x >> 4;
  float acc[8] = {};
  for (int k = w; k < K; k += 64) { float b = b2f(B[(size_t)k * N + c]); for (int i = 0; i < 8; ++i) acc[i] += b2f(A[i * K + k]) * b; }
  for (int i = 0; i < 8; ++i) red[w][i][cl] = acc[i];
  __syncthreads();
  if (threadIdx.x < 128) { int i = threadIdx.x >> 4; float s = 0; for (int g = 0; g < 64; ++g) s += red[g][i][cl]; C[i * N + c] = f2b(s); }
}
__global__ void __launch_bounds__(256) k_attn(const u16* qm, const u16* km, const u16* vm, const u16* Q, const u16* K, const u16* V, u16* Ot, u16* Om, int s) {
  __shared__ float q[40][193], k[40][193], v[40][193], p[40][41];
  int h = blockIdx.x, t = threadIdx.x;
  for (int i = t; i < 40 * E; i += 256) { int r = i / E, e = i % E;
    size_t o = r < M ? (size_t)r * HE + h * E + e : (size_t)(s * SEG + r - M) * HE + h * E + e;
    q[r][e] = b2f(r < M ? qm[o] : Q[o]); k[r][e] = b2f(r < M ? km[o] : K[o]); v[r][e] = b2f(r < M ? vm[o] : V[o]); }
  __syncthreads();
  for (int i = t; i < 1600; i += 256) { int a = i / 40, b = i % 40; float sum = 0; for (int e = 0; e < E; ++e) sum += q[a][e] * k[b][e];
    p[a][b] = (a < M || a >= b) ? sum * 0.07216878364870323f : -1e9f; }
  __syncthreads();
  if (t < 40) { float mx = -3e38f; for (int b = 0; b < 40; ++b) mx = fmaxf(mx, p[t][b]); float l = 0; for (int b = 0; b < 40; ++b) { float e = __expf(p[t][b] - mx); p[t][b] = e; l += e; }
    for (int b = 0; b < 40; ++b) p[t][b] = b2f(f2b(p[t][b] / l)); }
  __syncthreads();
  for (int i = t; i < 40 * E; i += 256) { int r = i / E, e = i % E; float sum = 0; for (int b = 0; b < 40; ++b) sum += p[r][b] * v[b][e];
    if (r < M) Om[(size_t)r * HE + h * E + e] = f2b(sum); else Ot[(size_t)(s * SEG + r - M) * HE + h * E + e] = f2b(sum); }
}
__global__ void k_glu(const u16* g, const u16* u, u16* o, int n) { int i = blockIdx.x * 256 + threadIdx.x; if (i < n) { float a = b2f(g[i]); o[i] = f2b(a / (1.f + __expf(-a)) * b2f(u[i])); } }
__global__ void k_fin(const u16* x, const u16* a, const u16* b, u16* o, int n) { int i = blockIdx.x * 256 + threadIdx.x; if (i < n) o[i] = f2b(b2f(x[i]) + b2f(a[i]) + b2f(b[i])); }

extern "C" void kernel_launch(void* const* d_in, const int* in_sizes, int n_in, void* d_out, int out_size, void* d_ws, size_t ws_size, hipStream_t stream) {
  const u16* x = (const u16*)d_in[0]; const u16* mem0 = (const u16*)d_in[1];
  const u16 *Wq = (const u16*)d_in[2], *Wk = (const u16*)d_in[3], *Wv = (const u16*)d_in[4], *Wo = (const u16*)d_in[5];
  const u16 *wg = (const u16*)d_in[8], *wu = (const u16*)d_in[9], *wd = (const u16*)d_in[10];
  u16* w = (u16*)d_ws;
  u16* xn = w; w += S * D;
  u16 *Q = w; w += (size_t)S * HE; u16* K = w; w += (size_t)S * HE; u16* V = w; w += (size_t)S * HE; u16* O = w; w += (size_t)S * HE;
  u16 *G = w; w += S * F; u16* U = w; w += S * F; u16* GU = w; w += S * F; u16* A = w; w += S * D; u16* B = w; w += S * D;
  u16 *qm = w; w += M * HE; u16* km = w; w += M * HE; u16* vm = w; w += M * HE; u16* om = w; w += M * HE; u16* mem = w; w += M * D;
  k_ln<<<S, 64, 0, stream>>>(x, xn);
  k_gemm<<<dim3(HE / 64, S / 64), 256, 0, stream>>>(xn, D, Wq, HE, Q, HE, S, D);
  k_gemm<<<dim3(HE / 64, S / 64), 256, 0, stream>>>(xn, D, Wk, HE, K, HE, S, D);
  k_gemm<<<dim3(HE / 64, S / 64), 256, 0, stream>>>(xn, D, Wv, HE, V, HE, S, D);
  k_gemm<<<dim3(F / 64, S / 64), 256, 0, stream>>>(xn, D, wg, F, G, F, S, D);
  k_gemm<<<dim3(F / 64, S / 64), 256, 0, stream>>>(xn, D, wu, F, U, F, S, D);
  k_glu<<<S * F / 256, 256, 0, stream>>>(G, U, GU, S * F);
  k_gemm<<<dim3(D / 64, S / 64), 256, 0, stream>>>(GU, F, wd, D, B, D, S, F);
  for (int s = 0; s < NSEG; ++s) {
    const u16* m = s == 0 ? mem0 : mem;
    k_gemv8<<<HE / 16, 1024, 0, stream>>>(m, Wq, qm, D, HE);
    k_gemv8<<<HE / 16, 1024, 0, stream>>>(m, Wk, km, D, HE);
    k_gemv8<<<HE / 16, 1024, 0, stream>>>(m, Wv, vm, D, HE);
    k_attn<<<H, 256, 0, stream>>>(qm, km, vm, Q, K, V, O, om, s);
    k_gemv8<<<D / 16, 1024, 0, stream>>>(om, Wo, mem, HE, D);
  }
  k_gemm<<<dim3(D / 64, S / 64), 256, 0, stream>>>(O, HE, Wo, D, A, D, S, HE);
  k_fin<<<S * D / 256, 256, 0, stream>>>(x, A, B, (u16*)d_out, S * D);
}
```

```cpp
#include <hip/hip_runtime.h>
#include <hip/hip_bf16.h>
typedef unsigned short u16;
typedef unsigned v4u __attribute__((ext_vector_type(4)));
typedef unsigned v2u __attribute__((ext_vector_type(2)));
typedef float f2 __attribute__((ext_vector_type(2)));
typedef __bf16 b2 __attribute__((ext_vector_type(2)));
constexpr unsigned L_ABORT = 4195456u;
constexpr unsigned L_CLAIM = 4178944u;
constexpr unsigned L_CTRL = 4194304u;
constexpr unsigned L_CTRL_BYTES = 100352u;
constexpr unsigned L_DBG = 5242880u;
constexpr unsigned L_DONE = 4182016u;
constexpr unsigned L_EPOCH = 4177920u;
constexpr unsigned L_FO = 155189248u;
constexpr unsigned L_FO_SEG = 81920u;
constexpr unsigned L_G = 148897792u;
constexpr unsigned L_G_SEG = 98304u;
constexpr unsigned L_HF_BASE = 294912u;
constexpr unsigned L_MEM_BASE = 0u;
constexpr unsigned L_MEM_REP = 524288u;
constexpr unsigned L_MEM_SLOT = 32768u;
constexpr unsigned L_PAGE0_INIT = 327680u;
constexpr unsigned L_PM = 147849216u;
constexpr unsigned L_PM_SEG = 16384u;
constexpr unsigned L_PRG_C = 4288512u;
constexpr unsigned L_PRG_F = 4284416u;
constexpr unsigned L_PRG_PT = 4259840u;
constexpr unsigned L_PRG_QK = 4196352u;
constexpr unsigned L_PRG_U = 4227072u;
constexpr unsigned L_PRG_W2 = 4268032u;
constexpr unsigned L_PROBE_OFF = 1966080u;
constexpr unsigned L_PT = 143654912u;
constexpr unsigned L_PT_SEG = 65536u;
constexpr unsigned L_P_BASE = 98304u;
constexpr unsigned L_P_SLOT = 65536u;
constexpr unsigned L_QK = 9437184u;
constexpr unsigned L_QK_SEG = 786432u;
constexpr unsigned L_TUNE = 4178048u;
constexpr unsigned L_U = 59768832u;
constexpr unsigned L_U_SEG = 1310720u;
constexpr unsigned L_W2_BASE = 161480704u;
constexpr unsigned L_W2_PAGES = 22u;
constexpr unsigned L_W2_SLOT = 655360u;
constexpr unsigned L_WS_USED = 207618048u;
constexpr unsigned L_XN = 6291456u;
constexpr unsigned L_XN_SEG = 40960u;
constexpr int MREP = 2;
#define TUNING 0

constexpr int D = 640;
__device__ __forceinline__ f2 up2(unsigned w) { return f2{__builtin_bit_cast(float, w << 16), __builtin_bit_cast(float, w & 0xffff0000u)}; }
__device__ __forceinline__ unsigned dn2(f2 v) { return __builtin_bit_cast(unsigned, __builtin_convertvector(v, b2)); }
__device__ __forceinline__ float rb(float f) { return (float)(__bf16)f; }
__device__ __forceinline__ float quad_sum(float v) {
  v += __builtin_bit_cast(float, __builtin_amdgcn_update_dpp(0, __builtin_bit_cast(int, v), 0xB1, 0xf, 0xf, true));
  v += __builtin_bit_cast(float, __builtin_amdgcn_update_dpp(0, __builtin_bit_cast(int, v), 0x4E, 0xf, 0xf, true));
  return v;
}

__device__ __forceinline__ void set_up(const u16* x, const u16* mem0, unsigned char* ws_raw, int w) {
  unsigned char* ws = ws_raw + ((0 - (size_t)ws_raw) & ((2u << 20) - 1));
  const int tid = threadIdx.x, lane = tid & 63, wave = __builtin_amdgcn_readfirstlane(tid >> 6);
  const __amdgpu_buffer_rsrc_t rs = __builtin_amdgcn_make_buffer_rsrc(ws, 0, L_WS_USED, 0x00020000);
  const unsigned tag = *(const unsigned*)(ws + L_EPOCH) + 1;
  {
    const int tile = w * 4 + wave, tok = lane >> 2, g = (lane & 3) ^ ((tok >> 2) & 2);
    const v4u* src = (const v4u*)(x + (tile * 16 + tok) * D) + g;
    v4u p[20];
#pragma unroll
    for (int k = 0; k < 20; ++k) p[k] = src[4 * k];
    const v4u ones = {~0u, ~0u, ~0u, ~0u}, zero = {0, 0, 0, 0};
    constexpr int n0 = L_W2_SLOT / 4096, n1 = n0 + (L_PAGE0_INIT - L_MEM_SLOT) / 4096, n2 = n1 + (MREP - 1) * 16, n3 = n2 + (L_CTRL_BYTES + 4095) / 4096;
    for (int u = w; u < n3; u += 32) {
      const int off = u < n0 ? L_W2_BASE + u * 4096 : u < n1 ? L_MEM_SLOT + (u - n0) * 4096
                    : u < n2 ? (((u - n1) >> 4) + 1) * L_MEM_REP + L_MEM_SLOT + ((u - n1) & 15) * 4096 : L_CTRL + (u - n2) * 4096;
      __builtin_amdgcn_raw_buffer_store_b128(u < n2 ? ones : zero, rs, tid * 16 + off, 0, 16);
    }
    __builtin_amdgcn_raw_buffer_store_b128(ones, rs, tid * 16 + L_PT + 62 * L_PT_SEG + w * 4096, 0, 16);
    if (w < 8) __builtin_amdgcn_raw_buffer_store_b128(ones, rs, tid * 16 + L_PM + 62 * L_PM_SEG + w * 4096, 0, 16);
    f2 f[80], s2 = {0.f, 0.f};
#pragma unroll
    for (int i = 0; i < 80; ++i) { f[i] = up2(p[i >> 2][i & 3]); s2 += f[i]; }
    const float mean = quad_sum(s2.x + s2.y) * (1.f / D);
    f2 q2 = {0.f, 0.f};
#pragma unroll
    for (int i = 0; i < 80; ++i) { f[i] -= mean; q2 = __builtin_elementwise_fma(f[i], f[i], q2); }
    const float var = quad_sum(q2.x + q2.y) * (1.f / D);
    const float inv = 1.f / rb(sqrtf(rb(rb(var) + 1e-5f))), off = (mean - rb(mean)) * inv;
#pragma unroll
    for (int k = 0; k < 20; ++k) {
      v4u o;
#pragma unroll
      for (int i = 0; i < 4; ++i) o[i] = dn2(__builtin_elementwise_fma(f[4 * k + i], f2{inv, inv}, f2{off, off}));
      __builtin_amdgcn_raw_buffer_store_b128(o, rs, lane * 16 + L_XN + tile * 20480 + k * 1024, 0, 16);
    }
  }
  if (w >= 27) {
    const int kk = (w - 27) * 2 + (tid >> 7), r = lane & 15, G = lane >> 4, i = (r >> 2) * 2 + (r & 1), bb = (r >> 1) & 1;
    const unsigned* src = (const unsigned*)(mem0 + i * D + kk * 64 + G * 16 + bb * 2);
    const v4u val = {min(src[0], ~1u), min(src[2], ~1u), min(src[4], ~1u), min(src[6], ~1u)};
    for (int c = 0; c < MREP; ++c) __builtin_amdgcn_raw_buffer_store_b128(val, rs, c * L_MEM_REP + (kk >= 5 ? kk + 11 : kk) * 1024 + (wave & 1) * 8192 + lane * 16, 0, 16);
  }
  asm volatile("s_waitcnt vmcnt(0)" ::: "memory");
  __syncthreads();
  if (tid == 0) __builtin_amdgcn_raw_buffer_store_b64(v2u{tag, 0u}, rs, 0, L_DONE + w * 128, 16);
  if (w <= (int)L_W2_PAGES) {
   if (wave == 0) {
    unsigned m0 = ~0u, m1 = ~0u;
#pragma unroll 1
    for (int i = 0; i < 32 && (i < 8 || (i & 1) || max(m0, m1) - min(m0, m1) < 130 || min(m0, m1) > 600); ++i) {
      const int off = (w ? L_W2_BASE + (w - 1) * (2 << 20) : 0) + L_PROBE_OFF + (i & 1) * 8192 + ((i >> 1) & 1) * 16384;
      unsigned long long t0, t1;
      asm volatile("s_memtime %0\n\ts_waitcnt lgkmcnt(0)" : "=s"(t0) :: "memory");
      __builtin_amdgcn_raw_buffer_store_b32(0u, rs, (lane & 31) * 4, off, 16);
      asm volatile("s_waitcnt vmcnt(0)\n\ts_memtime %0\n\ts_waitcnt lgkmcnt(0)" : "=s"(t1) :: "memory");
      if (i & 1) m1 = min(m1, (unsigned)(t1 - t0)); else m0 = min(m0, (unsigned)(t1 - t0));
    }
    unsigned xcc; asm volatile("s_getreg_b32 %0, hwreg(HW_REG_XCC_ID)" : "=s"(xcc));
    const unsigned par = ((m1 < m0 ? 1u : 0u) ^ ((xcc >> 2) & 1u));
    if (lane == 0) __builtin_amdgcn_raw_buffer_store_b64(v2u{tag, par}, rs, 0, L_DONE + (32 + w) * 128, 16);
   }
   __syncthreads();
  }
}

__global__ void __launch_bounds__(256, 1) k_main(const u16* x, const u16* Wq, const u16* Wk, const u16* Wv, const u16* Wo, const u16* wg, const u16* wu,
                                                 const u16* wd, u16* out, unsigned char* ws, const u16* mem0
                                                 ) {
  __shared__ __attribute__((aligned(16))) unsigned char lds[163840];
  if (blockIdx.x >= 224) set_up(x, mem0, ws, blockIdx.x - 224);
  asm volatile(
    "s_load_dwordx16 s[64:79], s[0:1], 0x0\n"
    "s_load_dwordx4 s[80:83], s[0:1], 0x40\n"
    "v_lshrrev_b32 v2, 6, v0\n"
    "v_and_b32 v1, 63, v0\n"
    "s_getpc_b64 s[12:13]\n"
    ".Lcode0:\n"
    "s_and_b32 s13, s13, 0xffff\n"
    "s_mov_b32 s14, .Lcode1-.Lcode0\n"
    "s_mov_b32 s15, 0x20000\n"
    "s_lshr_b32 s16, s2, 3\n"
    "s_lshl_b32 s16, s16, 12\n"
    "v_lshl_add_u32 v3, v0, 4, s16\n"
    "buffer_load_dwordx4 a[252:255], v3, s[12:15], 0 offen\n"
    "v_readfirstlane_b32 s21, v2\n"
    "s_getreg_b32 s40, hwreg(HW_REG_XCC_ID)\n"
    "s_and_b32 s40, s40, 7\n"
    "s_lshl_b32 s40, s40, 5\n"
    "s_lshr_b32 s5, s2, 3\n"
    "s_add_u32 s5, s5, s40\n"
    "s_mov_b32 s6, 0\n"
    "s_mov_b32 s25, 400000\n"
    "s_mov_b32 s58, 0\n"
    "s_waitcnt lgkmcnt(0)\n"
    "s_add_u32 s82, s82, 0x1fffff\n"
    "s_addc_u32 s83, s83, 0\n"
    "s_and_b32 s82, s82, 0xffe00000\n"
    "s_mov_b32 s8, s82\n"
    "s_and_b32 s9, s83, 0xffff\n"
    "s_mov_b32 s10, 207618048\n"
    "s_mov_b32 s11, 0x20000\n"
    "s_add_u32 s42, s82, 4177920\n"
    "s_addc_u32 s43, s83, 0\n"
    "s_load_dword s3, s[42:43], 0x0\n"
    ".Ldispatch:\n"
    "s_lshr_b32 s41, s5, 5\n"
    "s_and_b32 s42, s5, 31\n"
    "s_cmp_lg_u32 s41, 4\n"
    "s_cbranch_scc1 .Ldl_1\n"
    "s_add_u32 s20, s42, 0\n"
    "s_cmp_lt_u32 s42, 16\n"
    "s_cbranch_scc1 .Lrole_U\n"
    "s_add_u32 s20, s42, -16\n"
    "s_cmp_lt_u32 s42, 25\n"
    "s_cbranch_scc1 .Lrole_P\n"
    "s_add_u32 s20, s42, -25\n"
    "s_cmp_lt_u32 s42, 30\n"
    "s_cbranch_scc1 .Lrole_O\n"
    "s_add_u32 s20, s42, -30\n"
    "s_cmp_lt_u32 s42, 32\n"
    "s_cbranch_scc1 .Lrole_D\n"
    ".Ldl_1:\n"
    "s_cmp_lg_u32 s41, 5\n"
    "s_cbranch_scc1 .Ldl_2\n"
    "s_add_u32 s20, s42, 16\n"
    "s_cmp_lt_u32 s42, 16\n"
    "s_cbranch_scc1 .Lrole_U\n"
    "s_add_u32 s20, s42, -7\n"
    "s_cmp_lt_u32 s42, 25\n"
    "s_cbranch_scc1 .Lrole_P\n"
    "s_add_u32 s20, s42, -20\n"
    "s_cmp_lt_u32 s42, 30\n"
    "s_cbranch_scc1 .Lrole_O\n"
    "s_add_u32 s20, s42, -28\n"
    "s_cmp_lt_u32 s42, 32\n"
    "s_cbranch_scc1 .Lrole_D\n"
    ".Ldl_2:\n"
    "s_cmp_lg_u32 s41, 6\n"
    "s_cbranch_scc1 .Ldl_3\n"
    "s_add_u32 s20, s42, 32\n"
    "s_cmp_lt_u32 s42, 16\n"
    "s_cbranch_scc1 .Lrole_U\n"
    "s_add_u32 s20, s42, 2\n"
    "s_cmp_lt_u32 s42, 25\n"
    "s_cbranch_scc1 .Lrole_P\n"
    "s_add_u32 s20, s42, -15\n"
    "s_cmp_lt_u32 s42, 30\n"
    "s_cbranch_scc1 .Lrole_O\n"
    "s_add_u32 s20, s42, -26\n"
    "s_cmp_lt_u32 s42, 32\n"
    "s_cbranch_scc1 .Lrole_D\n"
    ".Ldl_3:\n"
    "s_cmp_lg_u32 s41, 7\n"
    "s_cbranch_scc1 .Ldl_4\n"
    "s_add_u32 s20, s42, 48\n"
    "s_cmp_lt_u32 s42, 16\n"
    "s_cbranch_scc1 .Lrole_U\n"
    "s_add_u32 s20, s42, 11\n"
    "s_cmp_lt_u32 s42, 25\n"
    "s_cbranch_scc1 .Lrole_P\n"
    "s_add_u32 s20, s42, -10\n"
    "s_cmp_lt_u32 s42, 30\n"
    "s_cbranch_scc1 .Lrole_O\n"
    "s_add_u32 s20, s42, -24\n"
    "s_cmp_lt_u32 s42, 32\n"
    "s_cbranch_scc1 .Lrole_D\n"
    ".Ldl_4:\n"
    "s_cmp_lg_u32 s41, 0\n"
    "s_cbranch_scc1 .Ldl_5\n"
    "s_add_u32 s20, s42, 0\n"
    "s_cmp_lt_u32 s42, 8\n"
    "s_cbranch_scc1 .Lrole_S\n"
    "s_add_u32 s20, s42, -8\n"
    "s_cmp_lt_u32 s42, 16\n"
    "s_cbranch_scc1 .Lrole_W\n"
    "s_add_u32 s20, s42, -16\n"
    "s_cmp_lt_u32 s42, 19\n"
    "s_cbranch_scc1 .Lrole_F\n"
    "s_add_u32 s20, s42, 17\n"
    "s_cmp_lt_u32 s42, 22\n"
    "s_cbranch_scc1 .Lrole_P\n"
    "s_add_u32 s20, s42, -22\n"
    "s_cmp_lt_u32 s42, 32\n"
    "s_cbranch_scc1 .Lrole_C\n"
    ".Ldl_5:\n"
    "s_cmp_lg_u32 s41, 1\n"
    "s_cbranch_scc1 .Ldl_6\n"
    "s_add_u32 s20, s42, 8\n"
    "s_cmp_lt_u32 s42, 8\n"
    "s_cbranch_scc1 .Lrole_S\n"
    "s_add_u32 s20, s42, 0\n"
    "s_cmp_lt_u32 s42, 16\n"
    "s_cbranch_scc1 .Lrole_W\n"
    "s_add_u32 s20, s42, -13\n"
    "s_cmp_lt_u32 s42, 19\n"
    "s_cbranch_scc1 .Lrole_F\n"
    "s_add_u32 s20, s42, 20\n"
    "s_cmp_lt_u32 s42, 22\n"
    "s_cbranch_scc1 .Lrole_P\n"
    "s_add_u32 s20, s42, -12\n"
    "s_cmp_lt_u32 s42, 32\n"
    "s_cbranch_scc1 .Lrole_C\n"
    ".Ldl_6:\n"
    "s_cmp_lg_u32 s41, 2\n"
    "s_cbranch_scc1 .Ldl_7\n"
    "s_add_u32 s20, s42, 16\n"
    "s_cmp_lt_u32 s42, 8\n"
    "s_cbranch_scc1 .Lrole_S\n"
    "s_add_u32 s20, s42, 8\n"
    "s_cmp_lt_u32 s42, 16\n"
    "s_cbranch_scc1 .Lrole_W\n"
    "s_add_u32 s20, s42, -10\n"
    "s_cmp_lt_u32 s42, 19\n"
    "s_cbranch_scc1 .Lrole_F\n"
    "s_add_u32 s20, s42, 23\n"
    "s_cmp_lt_u32 s42, 22\n"
    "s_cbranch_scc1 .Lrole_P\n"
    "s_add_u32 s20, s42, -2\n"
    "s_cmp_lt_u32 s42, 32\n"
    "s_cbranch_scc1 .Lrole_C\n"
    ".Ldl_7:\n"
    "s_cmp_lg_u32 s41, 3\n"
    "s_cbranch_scc1 .Ldl_8\n"
    "s_add_u32 s20, s42, 24\n"
    "s_cmp_lt_u32 s42, 8\n"
    "s_cbranch_scc1 .Lrole_S\n"
    "s_add_u32 s20, s42, 16\n"
    "s_cmp_lt_u32 s42, 16\n"
    "s_cbranch_scc1 .Lrole_W\n"
    "s_add_u32 s20, s42, -7\n"
    "s_cmp_lt_u32 s42, 19\n"
    "s_cbranch_scc1 .Lrole_F\n"
    "s_add_u32 s20, s42, 26\n"
    "s_cmp_lt_u32 s42, 22\n"
    "s_cbranch_scc1 .Lrole_P\n"
    "s_add_u32 s20, s42, 8\n"
    "s_cmp_lt_u32 s42, 32\n"
    "s_cbranch_scc1 .Lrole_C\n"
    ".Ldl_8:\n"
    ".Lend:\n"
    "s_endpgm\n"
    ".Lrole_I:\n"
    "s_waitcnt lgkmcnt(0)\n"
    "s_add_u32 s4, s3, 1\n"
    "v_mov_b32 v2, s3\n"
    "s_or_b32 s41, s21, s6\n"
    "s_cbranch_scc1 .Lcl_9\n"
    "s_lshl_b32 s41, s5, 2\n"
    "s_add_u32 s41, s41, 4178944\n"
    "v_mov_b32 v3, s41\n"
    "v_mov_b32 v2, s4\n"
    "s_mov_b64 exec, 1\n"
    "buffer_atomic_swap v2, v3, s[8:11], 0 offen sc0 sc1\n"
    "s_mov_b64 exec, -1\n"
    ".Lcl_9:\n"
    "v_min_u32 v6, 31, v1\n"
    "v_lshlrev_b32 v6, 7, v6\n"
    "v_add_u32 v6, 4182016, v6\n"
    "buffer_load_dwordx2 v[4:5], v6, s[8:11], 0 offen sc1\n"
    "s_waitcnt vmcnt(0)\n"
    "v_cmp_ne_u32 vcc, s4, v4\n"
    "s_nop 1\n"
    "s_cmp_eq_u32 vcc_lo, 0\n"
    "s_cbranch_scc1 .Lrx_12\n"
    ".Lrp_10:\n"
    "v_min_u32 v6, 31, v1\n"
    "v_lshlrev_b32 v6, 7, v6\n"
    "v_add_u32 v6, 4182016, v6\n"
    "buffer_load_dwordx2 v[4:5], v6, s[8:11], 0 offen sc1\n"
    "s_waitcnt vmcnt(0)\n"
    "v_cmp_ne_u32 vcc, s4, v4\n"
    "s_nop 1\n"
    "s_cmp_eq_u32 vcc_lo, 0\n"
    "s_cbranch_scc1 .Lro_11\n"
    "s_sleep 2\n"
    "s_sub_u32 s25, s25, 1\n"
    "s_cbranch_scc0 .Lrp_10\n"
    "s_branch .Lfail\n"
    ".Lro_11:\n"
    ".Lrx_12:\n"
    "s_cmp_lg_u32 s21, 0\n"
    "s_cbranch_scc1 .Lgt_13\n"
    "v_mov_b32 v3, 0\n"
    "v_readfirstlane_b32 s40, v2\n"
    "s_nop 1\n"
    "v_mov_b32 v2, s40\n"
    "ds_write_b32 v3, v2\n"
    "s_waitcnt lgkmcnt(0)\n"
    ".Lgt_13:\n"
    "s_barrier\n"
    "v_mov_b32 v3, 0\n"
    "ds_read_b32 v2, v3\n"
    "s_waitcnt lgkmcnt(0)\n"
    "v_readfirstlane_b32 s40, v2\n"
    "s_barrier\n"
    "s_cmp_eq_u32 s40, s4\n"
    "s_cbranch_scc1 .Llost\n"
    "s_endpgm\n"
    ".Llost:\n"
    "s_waitcnt vmcnt(0)\n"
    "s_mov_b32 s6, 1\n"
    "s_cmp_lg_u32 s21, 0\n"
    "s_cbranch_scc1 .Lfound\n"
    "s_mov_b64 exec, 1\n"
    "s_mov_b32 s5, -1\n"
    ".Lscan:\n"
    "s_add_u32 s5, s5, 1\n"
    "s_cmp_ge_u32 s5, 256\n"
    "s_cbranch_scc1 .Lscanned\n"
    "s_lshl_b32 s40, s5, 2\n"
    "s_add_u32 s40, s40, 4178944\n"
    "v_mov_b32 v2, s40\n"
    "v_mov_b32 v3, s4\n"
    "buffer_atomic_swap v3, v2, s[8:11], 0 offen sc0 sc1\n"
    "s_waitcnt vmcnt(0)\n"
    "v_readfirstlane_b32 s40, v3\n"
    "s_nop 0\n"
    "s_cmp_eq_u32 s40, s4\n"
    "s_cbranch_scc1 .Lscan\n"
    ".Lscanned:\n"
    "v_mov_b32 v3, s5\n"
    "v_mov_b32 v2, 0\n"
    "ds_write_b32 v2, v3\n"
    "s_mov_b64 exec, -1\n"
    "s_waitcnt lgkmcnt(0)\n"
    ".Lfound:\n"
    "s_barrier\n"
    "v_mov_b32 v2, 0\n"
    "ds_read_b32 v3, v2\n"
    "s_waitcnt lgkmcnt(0)\n"
    "v_readfirstlane_b32 s5, v3\n"
    "s_barrier\n"
    "s_nop 3\n"
    "s_branch .Ldispatch\n"
    ".Lfail:\n"
    "s_mov_b64 exec, 1\n"
    "v_mov_b32 v2, 4195456\n"
    "v_mov_b32 v3, 1\n"
    "buffer_store_dword v3, v2, s[8:11], 0 offen sc1\n"
    "s_endpgm\n"
    ".Lrole_S:\n"
    "s_cmp_lt_u32 s21, 2\n"
    "s_cselect_b64 s[44:45], s[66:67], s[68:69]\n"
    "s_mov_b32 s12, s44\n"
    "s_and_b32 s13, s45, 0xffff\n"
    "s_mov_b32 s14, 7864320\n"
    "s_mov_b32 s15, 0x20000\n"
    "s_lshr_b32 s40, s21, 1\n"
    "s_mul_i32 s40, s40, 5\n"
    "s_and_b32 s41, s21, 1\n"
    "s_lshl_b32 s42, s41, 1\n"
    "s_add_u32 s26, s40, s42\n"
    "s_mul_i32 s42, s41, 3\n"
    "s_sub_u32 s42, 3, s42\n"
    "s_add_u32 s27, s40, s42\n"
    "s_sub_u32 s28, 5, s40\n"
    "s_mul_i32 s41, s20, 384\n"
    "s_mul_i32 s49, s28, 786432\n"
    "s_sub_u32 s48, 3932160, s49\n"
    "s_add_u32 s48, s48, s41\n"
    "s_add_u32 s49, s49, s41\n"
    "s_mul_i32 s29, s21, 6144\n"
    "s_add_u32 s29, s29, 0\n"
    "s_and_b32 s40, s21, 1\n"
    "s_mul_i32 s41, s40, 384\n"
    "v_add_u32 v8, s41, v1\n"
    "s_add_u32 s41, s41, 64\n"
    "v_mul_u32_u24 v0, 2731, v8\n"
    "v_lshrrev_b32 v0, 16, v0\n"
    "v_mul_u32_u24 v14, 24, v0\n"
    "v_sub_u32 v8, v8, v14\n"
    "v_mul_u32_u24 v0, 12288, v0\n"
    "v_lshl_add_u32 v8, v8, 4, v0\n"
    "v_add_u32 v9, s41, v1\n"
    "s_add_u32 s41, s41, 64\n"
    "v_mul_u32_u24 v0, 2731, v9\n"
    "v_lshrrev_b32 v0, 16, v0\n"
    "v_mul_u32_u24 v14, 24, v0\n"
    "v_sub_u32 v9, v9, v14\n"
    "v_mul_u32_u24 v0, 12288, v0\n"
    "v_lshl_add_u32 v9, v9, 4, v0\n"
    "v_add_u32 v10, s41, v1\n"
    "s_add_u32 s41, s41, 64\n"
    "v_mul_u32_u24 v0, 2731, v10\n"
    "v_lshrrev_b32 v0, 16, v0\n"
    "v_mul_u32_u24 v14, 24, v0\n"
    "v_sub_u32 v10, v10, v14\n"
    "v_mul_u32_u24 v0, 12288, v0\n"
    "v_lshl_add_u32 v10, v10, 4, v0\n"
    "v_add_u32 v11, s41, v1\n"
    "s_add_u32 s41, s41, 64\n"
    "v_mul_u32_u24 v0, 2731, v11\n"
    "v_lshrrev_b32 v0, 16, v0\n"
    "v_mul_u32_u24 v14, 24, v0\n"
    "v_sub_u32 v11, v11, v14\n"
    "v_mul_u32_u24 v0, 12288, v0\n"
    "v_lshl_add_u32 v11, v11, 4, v0\n"
    "v_add_u32 v12, s41, v1\n"
    "s_add_u32 s41, s41, 64\n"
    "v_mul_u32_u24 v0, 2731, v12\n"
    "v_lshrrev_b32 v0, 16, v0\n"
    "v_mul_u32_u24 v14, 24, v0\n"
    "v_sub_u32 v12, v12, v14\n"
    "v_mul_u32_u24 v0, 12288, v0\n"
    "v_lshl_add_u32 v12, v12, 4, v0\n"
    "v_add_u32 v13, s41, v1\n"
    "s_add_u32 s41, s41, 64\n"
    "v_mul_u32_u24 v0, 2731, v13\n"
    "v_lshrrev_b32 v0, 16, v0\n"
    "v_mul_u32_u24 v14, 24, v0\n"
    "v_sub_u32 v13, v13, v14\n"
    "v_mul_u32_u24 v0, 12288, v0\n"
    "v_lshl_add_u32 v13, v13, 4, v0\n"
    "s_mul_i32 s40, s40, 5952\n"
    "s_sub_u32 s40, s29, s40\n"
    "v_bfe_u32 v14, v1, 4, 2\n"
    "v_lshlrev_b32 v14, 3, v14\n"
    "v_bfe_u32 v0, v1, 2, 2\n"
    "v_lshl_add_u32 v14, v0, 0, v14\n"
    "v_mul_u32_u24 v14, 384, v14\n"
    "v_and_b32 v0, 3, v1\n"
    "v_mul_u32_u24 v0, 8, v0\n"
    "v_add3_u32 v14, v14, v0, s40\n"
    "v_add_u32 v15, 57344, v14\n"
    "v_add_u32 v16, 114688, v14\n"
    "s_and_b32 s40, s21, 1\n"
    "s_cmp_lg_u32 s40, 0\n"
    "s_cbranch_scc1 .Lpn_15\n"
    "s_add_u32 s40, s48, 0\n"
    "s_add_u32 m0, s29, 0\n"
    "s_nop 0\n"
    "buffer_load_dwordx4 v8, s[12:15], s40 offen lds\n"
    "s_add_u32 m0, s29, 1024\n"
    "s_nop 0\n"
    "buffer_load_dwordx4 v9, s[12:15], s40 offen lds\n"
    "s_add_u32 m0, s29, 2048\n"
    "s_nop 0\n"
    "buffer_load_dwordx4 v10, s[12:15], s40 offen lds\n"
    "s_add_u32 m0, s29, 3072\n"
    "s_nop 0\n"
    "buffer_load_dwordx4 v11, s[12:15], s40 offen lds\n"
    "s_add_u32 m0, s29, 4096\n"
    "s_nop 0\n"
    "buffer_load_dwordx4 v12, s[12:15], s40 offen lds\n"
    "s_add_u32 m0, s29, 5120\n"
    "s_nop 0\n"
    "buffer_load_dwordx4 v13, s[12:15], s40 offen lds\n"
    "s_add_u32 s40, s48, 393216\n"
    "s_add_u32 m0, s29, 24576\n"
    "s_nop 0\n"
    "buffer_load_dwordx4 v8, s[12:15], s40 offen lds\n"
    "s_add_u32 m0, s29, 25600\n"
    "s_nop 0\n"
    "buffer_load_dwordx4 v9, s[12:15], s40 offen lds\n"
    "s_add_u32 m0, s29, 26624\n"
    "s_nop 0\n"
    "buffer_load_dwordx4 v10, s[12:15], s40 offen lds\n"
    "s_add_u32 m0, s29, 27648\n"
    "s_nop 0\n"
    "buffer_load_dwordx4 v11, s[12:15], s40 offen lds\n"
    "s_add_u32 m0, s29, 28672\n"
    "s_nop 0\n"
    "buffer_load_dwordx4 v12, s[12:15], s40 offen lds\n"
    "s_add_u32 m0, s29, 29696\n"
    "s_nop 0\n"
    "buffer_load_dwordx4 v13, s[12:15], s40 offen lds\n"
    "s_add_u32 s40, s48, 786432\n"
    "s_add_u32 m0, s29, 49152\n"
    "s_nop 0\n"
    "buffer_load_dwordx4 v8, s[12:15], s40 offen lds\n"
    "s_add_u32 m0, s29, 50176\n"
    "s_nop 0\n"
    "buffer_load_dwordx4 v9, s[12:15], s40 offen lds\n"
    "s_add_u32 m0, s29, 51200\n"
    "s_nop 0\n"
    "buffer_load_dwordx4 v10, s[12:15], s40 offen lds\n"
    "s_add_u32 m0, s29, 52224\n"
    "s_nop 0\n"
    "buffer_load_dwordx4 v11, s[12:15], s40 offen lds\n"
    "s_add_u32 m0, s29, 53248\n"
    "s_nop 0\n"
    "buffer_load_dwordx4 v12, s[12:15], s40 offen lds\n"
    "s_add_u32 m0, s29, 54272\n"
    "s_nop 0\n"
    "buffer_load_dwordx4 v13, s[12:15], s40 offen lds\n"
    "s_add_u32 s40, s48, 1179648\n"
    "s_add_u32 m0, s29, 73728\n"
    "s_nop 0\n"
    "buffer_load_dwordx4 v8, s[12:15], s40 offen lds\n"
    "s_add_u32 m0, s29, 74752\n"
    "s_nop 0\n"
    "buffer_load_dwordx4 v9, s[12:15], s40 offen lds\n"
    "s_add_u32 m0, s29, 75776\n"
    "s_nop 0\n"
    "buffer_load_dwordx4 v10, s[12:15], s40 offen lds\n"
    "s_add_u32 m0, s29, 76800\n"
    "s_nop 0\n"
    "buffer_load_dwordx4 v11, s[12:15], s40 offen lds\n"
    "s_add_u32 m0, s29, 77824\n"
    "s_nop 0\n"
    "buffer_load_dwordx4 v12, s[12:15], s40 offen lds\n"
    "s_add_u32 m0, s29, 78848\n"
    "s_nop 0\n"
    "buffer_load_dwordx4 v13, s[12:15], s40 offen lds\n"
    "s_add_u32 s40, s48, 1572864\n"
    "s_add_u32 m0, s29, 98304\n"
    "s_nop 0\n"
    "buffer_load_dwordx4 v8, s[12:15], s40 offen lds\n"
    "s_add_u32 m0, s29, 99328\n"
    "s_nop 0\n"
    "buffer_load_dwordx4 v9, s[12:15], s40 offen lds\n"
    "s_add_u32 m0, s29, 100352\n"
    "s_nop 0\n"
    "buffer_load_dwordx4 v10, s[12:15], s40 offen lds\n"
    "s_add_u32 m0, s29, 101376\n"
    "s_nop 0\n"
    "buffer_load_dwordx4 v11, s[12:15], s40 offen lds\n"
    "s_add_u32 m0, s29, 102400\n"
    "s_nop 0\n"
    "buffer_load_dwordx4 v12, s[12:15], s40 offen lds\n"
    "s_add_u32 m0, s29, 103424\n"
    "s_nop 0\n"
    "buffer_load_dwordx4 v13, s[12:15], s40 offen lds\n"
    "s_add_u32 s40, s48, 1966080\n"
    "s_add_u32 m0, s29, 122880\n"
    "s_nop 0\n"
    "buffer_load_dwordx4 v8, s[12:15], s40 offen lds\n"
    "s_add_u32 m0, s29, 123904\n"
    "s_nop 0\n"
    "buffer_load_dwordx4 v9, s[12:15], s40 offen lds\n"
    "s_add_u32 m0, s29, 124928\n"
    "s_nop 0\n"
    "buffer_load_dwordx4 v10, s[12:15], s40 offen lds\n"
    "s_add_u32 m0, s29, 125952\n"
    "s_nop 0\n"
    "buffer_load_dwordx4 v11, s[12:15], s40 offen lds\n"
    "s_add_u32 m0, s29, 126976\n"
    "s_nop 0\n"
    "buffer_load_dwordx4 v12, s[12:15], s40 offen lds\n"
    "s_add_u32 m0, s29, 128000\n"
    "s_nop 0\n"
    "buffer_load_dwordx4 v13, s[12:15], s40 offen lds\n"
    "s_waitcnt vmcnt(30)\n"
    "s_barrier\n"
    "ds_read_b64_tr_b16 v[22:23], v14 offset:0\n"
    "ds_read_b64_tr_b16 v[24:25], v14 offset:1536\n"
    "ds_read_b64_tr_b16 v[30:31], v14 offset:32\n"
    "ds_read_b64_tr_b16 v[32:33], v14 offset:1568\n"
    "ds_read_b64_tr_b16 v[38:39], v14 offset:64\n"
    "ds_read_b64_tr_b16 v[40:41], v14 offset:1600\n"
    "ds_read_b64_tr_b16 v[46:47], v14 offset:96\n"
    "ds_read_b64_tr_b16 v[48:49], v14 offset:1632\n"
    "ds_read_b64_tr_b16 v[54:55], v14 offset:128\n"
    "ds_read_b64_tr_b16 v[56:57], v14 offset:1664\n"
    "ds_read_b64_tr_b16 v[62:63], v14 offset:160\n"
    "ds_read_b64_tr_b16 v[64:65], v14 offset:1696\n"
    "s_waitcnt lgkmcnt(0)\n"
    "s_waitcnt vmcnt(24)\n"
    "s_barrier\n"
    "ds_read_b64_tr_b16 v[26:27], v14 offset:24576\n"
    "ds_read_b64_tr_b16 v[28:29], v14 offset:26112\n"
    "ds_read_b64_tr_b16 v[34:35], v14 offset:24608\n"
    "ds_read_b64_tr_b16 v[36:37], v14 offset:26144\n"
    "ds_read_b64_tr_b16 v[42:43], v14 offset:24640\n"
    "ds_read_b64_tr_b16 v[44:45], v14 offset:26176\n"
    "ds_read_b64_tr_b16 v[50:51], v14 offset:24672\n"
    "ds_read_b64_tr_b16 v[52:53], v14 offset:26208\n"
    "ds_read_b64_tr_b16 v[58:59], v14 offset:24704\n"
    "ds_read_b64_tr_b16 v[60:61], v14 offset:26240\n"
    "ds_read_b64_tr_b16 v[66:67], v14 offset:24736\n"
    "ds_read_b64_tr_b16 v[68:69], v14 offset:26272\n"
    "s_add_u32 s40, s48, 2359296\n"
    "s_add_u32 m0, s29, 0\n"
    "s_nop 0\n"
    "buffer_load_dwordx4 v8, s[12:15], s40 offen lds\n"
    "s_add_u32 m0, s29, 1024\n"
    "s_nop 0\n"
    "buffer_load_dwordx4 v9, s[12:15], s40 offen lds\n"
    "s_add_u32 m0, s29, 2048\n"
    "s_nop 0\n"
    "buffer_load_dwordx4 v10, s[12:15], s40 offen lds\n"
    "s_add_u32 m0, s29, 3072\n"
    "s_nop 0\n"
    "buffer_load_dwordx4 v11, s[12:15], s40 offen lds\n"
    "s_add_u32 m0, s29, 4096\n"
    "s_nop 0\n"
    "buffer_load_dwordx4 v12, s[12:15], s40 offen lds\n"
    "s_add_u32 m0, s29, 5120\n"
    "s_nop 0\n"
    "buffer_load_dwordx4 v13, s[12:15], s40 offen lds\n"
    "s_waitcnt lgkmcnt(0)\n"
    "s_waitcnt vmcnt(24)\n"
    "s_barrier\n"
    "ds_read_b64_tr_b16 a[0:1], v14 offset:49152\n"
    "ds_read_b64_tr_b16 a[2:3], v14 offset:50688\n"
    "ds_read_b64_tr_b16 a[8:9], v14 offset:49184\n"
    "ds_read_b64_tr_b16 a[10:11], v14 offset:50720\n"
    "ds_read_b64_tr_b16 a[16:17], v14 offset:49216\n"
    "ds_read_b64_tr_b16 a[18:19], v14 offset:50752\n"
    "ds_read_b64_tr_b16 a[24:25], v14 offset:49248\n"
    "ds_read_b64_tr_b16 a[26:27], v14 offset:50784\n"
    "ds_read_b64_tr_b16 a[32:33], v14 offset:49280\n"
    "ds_read_b64_tr_b16 a[34:35], v14 offset:50816\n"
    "ds_read_b64_tr_b16 a[40:41], v14 offset:49312\n"
    "ds_read_b64_tr_b16 a[42:43], v14 offset:50848\n"
    "s_add_u32 s40, s48, 2752512\n"
    "s_add_u32 m0, s29, 24576\n"
    "s_nop 0\n"
    "buffer_load_dwordx4 v8, s[12:15], s40 offen lds\n"
    "s_add_u32 m0, s29, 25600\n"
    "s_nop 0\n"
    "buffer_load_dwordx4 v9, s[12:15], s40 offen lds\n"
    "s_add_u32 m0, s29, 26624\n"
    "s_nop 0\n"
    "buffer_load_dwordx4 v10, s[12:15], s40 offen lds\n"
    "s_add_u32 m0, s29, 27648\n"
    "s_nop 0\n"
    "buffer_load_dwordx4 v11, s[12:15], s40 offen lds\n"
    "s_add_u32 m0, s29, 28672\n"
    "s_nop 0\n"
    "buffer_load_dwordx4 v12, s[12:15], s40 offen lds\n"
    "s_add_u32 m0, s29, 29696\n"
    "s_nop 0\n"
    "buffer_load_dwordx4 v13, s[12:15], s40 offen lds\n"
    "s_waitcnt lgkmcnt(0)\n"
    "s_waitcnt vmcnt(24)\n"
    "s_barrier\n"
    "ds_read_b64_tr_b16 a[4:5], v15 offset:16384\n"
    "ds_read_b64_tr_b16 a[6:7], v15 offset:17920\n"
    "ds_read_b64_tr_b16 a[12:13], v15 offset:16416\n"
    "ds_read_b64_tr_b16 a[14:15], v15 offset:17952\n"
    "ds_read_b64_tr_b16 a[20:21], v15 offset:16448\n"
    "ds_read_b64_tr_b16 a[22:23], v15 offset:17984\n"
    "ds_read_b64_tr_b16 a[28:29], v15 offset:16480\n"
    "ds_read_b64_tr_b16 a[30:31], v15 offset:18016\n"
    "ds_read_b64_tr_b16 a[36:37], v15 offset:16512\n"
    "ds_read_b64_tr_b16 a[38:39], v15 offset:18048\n"
    "ds_read_b64_tr_b16 a[44:45], v15 offset:16544\n"
    "ds_read_b64_tr_b16 a[46:47], v15 offset:18080\n"
    "s_add_u32 s40, s48, 3145728\n"
    "s_add_u32 m0, s29, 49152\n"
    "s_nop 0\n"
    "buffer_load_dwordx4 v8, s[12:15], s40 offen lds\n"
    "s_add_u32 m0, s29, 50176\n"
    "s_nop 0\n"
    "buffer_load_dwordx4 v9, s[12:15], s40 offen lds\n"
    "s_add_u32 m0, s29, 51200\n"
    "s_nop 0\n"
    "buffer_load_dwordx4 v10, s[12:15], s40 offen lds\n"
    "s_add_u32 m0, s29, 52224\n"
    "s_nop 0\n"
    "buffer_load_dwordx4 v11, s[12:15], s40 offen lds\n"
    "s_add_u32 m0, s29, 53248\n"
    "s_nop 0\n"
    "buffer_load_dwordx4 v12, s[12:15], s40 offen lds\n"
    "s_add_u32 m0, s29, 54272\n"
    "s_nop 0\n"
    "buffer_load_dwordx4 v13, s[12:15], s40 offen lds\n"
    "s_waitcnt lgkmcnt(0)\n"
    "s_waitcnt vmcnt(24)\n"
    "s_barrier\n"
    "ds_read_b64_tr_b16 a[48:49], v15 offset:40960\n"
    "ds_read_b64_tr_b16 a[50:51], v15 offset:42496\n"
    "ds_read_b64_tr_b16 a[56:57], v15 offset:40992\n"
    "ds_read_b64_tr_b16 a[58:59], v15 offset:42528\n"
    "ds_read_b64_tr_b16 a[64:65], v15 offset:41024\n"
    "ds_read_b64_tr_b16 a[66:67], v15 offset:42560\n"
    "ds_read_b64_tr_b16 a[72:73], v15 offset:41056\n"
    "ds_read_b64_tr_b16 a[74:75], v15 offset:42592\n"
    "ds_read_b64_tr_b16 a[80:81], v15 offset:41088\n"
    "ds_read_b64_tr_b16 a[82:83], v15 offset:42624\n"
    "ds_read_b64_tr_b16 a[88:89], v15 offset:41120\n"
    "ds_read_b64_tr_b16 a[90:91], v15 offset:42656\n"
    "s_add_u32 s40, s48, 3538944\n"
    "s_add_u32 m0, s29, 73728\n"
    "s_nop 0\n"
    "buffer_load_dwordx4 v8, s[12:15], s40 offen lds\n"
    "s_add_u32 m0, s29, 74752\n"
    "s_nop 0\n"
    "buffer_load_dwordx4 v9, s[12:15], s40 offen lds\n"
    "s_add_u32 m0, s29, 75776\n"
    "s_nop 0\n"
    "buffer_load_dwordx4 v10, s[12:15], s40 offen lds\n"
    "s_add_u32 m0, s29, 76800\n"
    "s_nop 0\n"
    "buffer_load_dwordx4 v11, s[12:15], s40 offen lds\n"
    "s_add_u32 m0, s29, 77824\n"
    "s_nop 0\n"
    "buffer_load_dwordx4 v12, s[12:15], s40 offen lds\n"
    "s_add_u32 m0, s29, 78848\n"
    "s_nop 0\n"
    "buffer_load_dwordx4 v13, s[12:15], s40 offen lds\n"
    "s_waitcnt lgkmcnt(0)\n"
    "s_waitcnt vmcnt(24)\n"
    "s_barrier\n"
    "ds_read_b64_tr_b16 a[52:53], v16 offset:8192\n"
    "ds_read_b64_tr_b16 a[54:55], v16 offset:9728\n"
    "ds_read_b64_tr_b16 a[60:61], v16 offset:8224\n"
    "ds_read_b64_tr_b16 a[62:63], v16 offset:9760\n"
    "ds_read_b64_tr_b16 a[68:69], v16 offset:8256\n"
    "ds_read_b64_tr_b16 a[70:71], v16 offset:9792\n"
    "ds_read_b64_tr_b16 a[76:77], v16 offset:8288\n"
    "ds_read_b64_tr_b16 a[78:79], v16 offset:9824\n"
    "ds_read_b64_tr_b16 a[84:85], v16 offset:8320\n"
    "ds_read_b64_tr_b16 a[86:87], v16 offset:9856\n"
    "ds_read_b64_tr_b16 a[92:93], v16 offset:8352\n"
    "ds_read_b64_tr_b16 a[94:95], v16 offset:9888\n"
    "s_add_u32 s40, s49, 0\n"
    "s_add_u32 m0, s29, 98304\n"
    "s_nop 0\n"
    "buffer_load_dwordx4 v8, s[12:15], s40 offen lds\n"
    "s_add_u32 m0, s29, 99328\n"
    "s_nop 0\n"
    "buffer_load_dwordx4 v9, s[12:15], s40 offen lds\n"
    "s_add_u32 m0, s29, 100352\n"
    "s_nop 0\n"
    "buffer_load_dwordx4 v10, s[12:15], s40 offen lds\n"
    "s_add_u32 m0, s29, 101376\n"
    "s_nop 0\n"
    "buffer_load_dwordx4 v11, s[12:15], s40 offen lds\n"
    "s_add_u32 m0, s29, 102400\n"
    "s_nop 0\n"
    "buffer_load_dwordx4 v12, s[12:15], s40 offen lds\n"
    "s_add_u32 m0, s29, 103424\n"
    "s_nop 0\n"
    "buffer_load_dwordx4 v13, s[12:15], s40 offen lds\n"
    "s_waitcnt lgkmcnt(0)\n"
    "s_waitcnt vmcnt(24)\n"
    "s_barrier\n"
    "ds_read_b64_tr_b16 a[96:97], v14 offset:0\n"
    "ds_read_b64_tr_b16 a[98:99], v14 offset:1536\n"
    "ds_read_b64_tr_b16 a[104:105], v14 offset:32\n"
    "ds_read_b64_tr_b16 a[106:107], v14 offset:1568\n"
    "ds_read_b64_tr_b16 a[112:113], v14 offset:64\n"
    "ds_read_b64_tr_b16 a[114:115], v14 offset:1600\n"
    "ds_read_b64_tr_b16 a[120:121], v14 offset:96\n"
    "ds_read_b64_tr_b16 a[122:123], v14 offset:1632\n"
    "ds_read_b64_tr_b16 a[128:129], v14 offset:128\n"
    "ds_read_b64_tr_b16 a[130:131], v14 offset:1664\n"
    "ds_read_b64_tr_b16 a[136:137], v14 offset:160\n"
    "ds_read_b64_tr_b16 a[138:139], v14 offset:1696\n"
    "s_add_u32 s40, s49, 393216\n"
    "s_add_u32 m0, s29, 122880\n"
    "s_nop 0\n"
    "buffer_load_dwordx4 v8, s[12:15], s40 offen lds\n"
    "s_add_u32 m0, s29, 123904\n"
    "s_nop 0\n"
    "buffer_load_dwordx4 v9, s[12:15], s40 offen lds\n"
    "s_add_u32 m0, s29, 124928\n"
    "s_nop 0\n"
    "buffer_load_dwordx4 v10, s[12:15], s40 offen lds\n"
    "s_add_u32 m0, s29, 125952\n"
    "s_nop 0\n"
    "buffer_load_dwordx4 v11, s[12:15], s40 offen lds\n"
    "s_add_u32 m0, s29, 126976\n"
    "s_nop 0\n"
    "buffer_load_dwordx4 v12, s[12:15], s40 offen lds\n"
    "s_add_u32 m0, s29, 128000\n"
    "s_nop 0\n"
    "buffer_load_dwordx4 v13, s[12:15], s40 offen lds\n"
    "s_waitcnt lgkmcnt(0)\n"
    "s_waitcnt vmcnt(24)\n"
    "s_barrier\n"
    "ds_read_b64_tr_b16 a[100:101], v14 offset:24576\n"
    "ds_read_b64_tr_b16 a[102:103], v14 offset:26112\n"
    "ds_read_b64_tr_b16 a[108:109], v14 offset:24608\n"
    "ds_read_b64_tr_b16 a[110:111], v14 offset:26144\n"
    "ds_read_b64_tr_b16 a[116:117], v14 offset:24640\n"
    "ds_read_b64_tr_b16 a[118:119], v14 offset:26176\n"
    "ds_read_b64_tr_b16 a[124:125], v14 offset:24672\n"
    "ds_read_b64_tr_b16 a[126:127], v14 offset:26208\n"
    "ds_read_b64_tr_b16 a[132:133], v14 offset:24704\n"
    "ds_read_b64_tr_b16 a[134:135], v14 offset:26240\n"
    "ds_read_b64_tr_b16 a[140:141], v14 offset:24736\n"
    "ds_read_b64_tr_b16 a[142:143], v14 offset:26272\n"
    "s_add_u32 s40, s49, 786432\n"
    "s_add_u32 m0, s29, 0\n"
    "s_nop 0\n"
    "buffer_load_dwordx4 v8, s[12:15], s40 offen lds\n"
    "s_add_u32 m0, s29, 1024\n"
    "s_nop 0\n"
    "buffer_load_dwordx4 v9, s[12:15], s40 offen lds\n"
    "s_add_u32 m0, s29, 2048\n"
    "s_nop 0\n"
    "buffer_load_dwordx4 v10, s[12:15], s40 offen lds\n"
    "s_add_u32 m0, s29, 3072\n"
    "s_nop 0\n"
    "buffer_load_dwordx4 v11, s[12:15], s40 offen lds\n"
    "s_add_u32 m0, s29, 4096\n"
    "s_nop 0\n"
    "buffer_load_dwordx4 v12, s[12:15], s40 offen lds\n"
    "s_add_u32 m0, s29, 5120\n"
    "s_nop 0\n"
    "buffer_load_dwordx4 v13, s[12:15], s40 offen lds\n"
    "s_waitcnt lgkmcnt(0)\n"
    "s_waitcnt vmcnt(24)\n"
    "s_barrier\n"
    "ds_read_b64_tr_b16 a[144:145], v14 offset:49152\n"
    "ds_read_b64_tr_b16 a[146:147], v14 offset:50688\n"
    "ds_read_b64_tr_b16 a[152:153], v14 offset:49184\n"
    "ds_read_b64_tr_b16 a[154:155], v14 offset:50720\n"
    "ds_read_b64_tr_b16 v[78:79], v14 offset:49216\n"
    "ds_read_b64_tr_b16 v[80:81], v14 offset:50752\n"
    "ds_read_b64_tr_b16 a[160:161], v14 offset:49248\n"
    "ds_read_b64_tr_b16 a[162:163], v14 offset:50784\n"
    "ds_read_b64_tr_b16 a[168:169], v14 offset:49280\n"
    "ds_read_b64_tr_b16 a[170:171], v14 offset:50816\n"
    "ds_read_b64_tr_b16 a[176:177], v14 offset:49312\n"
    "ds_read_b64_tr_b16 a[178:179], v14 offset:50848\n"
    "s_add_u32 s40, s49, 1179648\n"
    "s_add_u32 m0, s29, 24576\n"
    "s_nop 0\n"
    "buffer_load_dwordx4 v8, s[12:15], s40 offen lds\n"
    "s_add_u32 m0, s29, 25600\n"
    "s_nop 0\n"
    "buffer_load_dwordx4 v9, s[12:15], s40 offen lds\n"
    "s_add_u32 m0, s29, 26624\n"
    "s_nop 0\n"
    "buffer_load_dwordx4 v10, s[12:15], s40 offen lds\n"
    "s_add_u32 m0, s29, 27648\n"
    "s_nop 0\n"
    "buffer_load_dwordx4 v11, s[12:15], s40 offen lds\n"
    "s_add_u32 m0, s29, 28672\n"
    "s_nop 0\n"
    "buffer_load_dwordx4 v12, s[12:15], s40 offen lds\n"
    "s_add_u32 m0, s29, 29696\n"
    "s_nop 0\n"
    "buffer_load_dwordx4 v13, s[12:15], s40 offen lds\n"
    "s_waitcnt lgkmcnt(0)\n"
    "s_waitcnt vmcnt(24)\n"
    "s_barrier\n"
    "ds_read_b64_tr_b16 a[148:149], v15 offset:16384\n"
    "ds_read_b64_tr_b16 a[150:151], v15 offset:17920\n"
    "ds_read_b64_tr_b16 a[156:157], v15 offset:16416\n"
    "ds_read_b64_tr_b16 a[158:159], v15 offset:17952\n"
    "ds_read_b64_tr_b16 v[82:83], v15 offset:16448\n"
    "ds_read_b64_tr_b16 v[84:85], v15 offset:17984\n"
    "ds_read_b64_tr_b16 a[164:165], v15 offset:16480\n"
    "ds_read_b64_tr_b16 a[166:167], v15 offset:18016\n"
    "ds_read_b64_tr_b16 a[172:173], v15 offset:16512\n"
    "ds_read_b64_tr_b16 a[174:175], v15 offset:18048\n"
    "ds_read_b64_tr_b16 a[180:181], v15 offset:16544\n"
    "ds_read_b64_tr_b16 a[182:183], v15 offset:18080\n"
    "s_add_u32 s40, s49, 1572864\n"
    "s_add_u32 m0, s29, 49152\n"
    "s_nop 0\n"
    "buffer_load_dwordx4 v8, s[12:15], s40 offen lds\n"
    "s_add_u32 m0, s29, 50176\n"
    "s_nop 0\n"
    "buffer_load_dwordx4 v9, s[12:15], s40 offen lds\n"
    "s_add_u32 m0, s29, 51200\n"
    "s_nop 0\n"
    "buffer_load_dwordx4 v10, s[12:15], s40 offen lds\n"
    "s_add_u32 m0, s29, 52224\n"
    "s_nop 0\n"
    "buffer_load_dwordx4 v11, s[12:15], s40 offen lds\n"
    "s_add_u32 m0, s29, 53248\n"
    "s_nop 0\n"
    "buffer_load_dwordx4 v12, s[12:15], s40 offen lds\n"
    "s_add_u32 m0, s29, 54272\n"
    "s_nop 0\n"
    "buffer_load_dwordx4 v13, s[12:15], s40 offen lds\n"
    "s_waitcnt lgkmcnt(0)\n"
    "s_waitcnt vmcnt(24)\n"
    "s_barrier\n"
    "ds_read_b64_tr_b16 a[184:185], v15 offset:40960\n"
    "ds_read_b64_tr_b16 a[186:187], v15 offset:42496\n"
    "ds_read_b64_tr_b16 a[192:193], v15 offset:40992\n"
    "ds_read_b64_tr_b16 a[194:195], v15 offset:42528\n"
    "ds_read_b64_tr_b16 v[86:87], v15 offset:41024\n"
    "ds_read_b64_tr_b16 v[88:89], v15 offset:42560\n"
    "ds_read_b64_tr_b16 a[200:201], v15 offset:41056\n"
    "ds_read_b64_tr_b16 a[202:203], v15 offset:42592\n"
    "ds_read_b64_tr_b16 a[208:209], v15 offset:41088\n"
    "ds_read_b64_tr_b16 a[210:211], v15 offset:42624\n"
    "ds_read_b64_tr_b16 a[216:217], v15 offset:41120\n"
    "ds_read_b64_tr_b16 a[218:219], v15 offset:42656\n"
    "s_add_u32 s40, s49, 1966080\n"
    "s_add_u32 m0, s29, 73728\n"
    "s_nop 0\n"
    "buffer_load_dwordx4 v8, s[12:15], s40 offen lds\n"
    "s_add_u32 m0, s29, 74752\n"
    "s_nop 0\n"
    "buffer_load_dwordx4 v9, s[12:15], s40 offen lds\n"
    "s_add_u32 m0, s29, 75776\n"
    "s_nop 0\n"
    "buffer_load_dwordx4 v10, s[12:15], s40 offen lds\n"
    "s_add_u32 m0, s29, 76800\n"
    "s_nop 0\n"
    "buffer_load_dwordx4 v11, s[12:15], s40 offen lds\n"
    "s_add_u32 m0, s29, 77824\n"
    "s_nop 0\n"
    "buffer_load_dwordx4 v12, s[12:15], s40 offen lds\n"
    "s_add_u32 m0, s29, 78848\n"
    "s_nop 0\n"
    "buffer_load_dwordx4 v13, s[12:15], s40 offen lds\n"
    "s_waitcnt lgkmcnt(0)\n"
    "s_add_u32 s4, s3, 1\n"
    "v_mov_b32 v2, s3\n"
    "s_or_b32 s41, s21, s6\n"
    "s_cbranch_scc1 .Lcl_16\n"
    "s_lshl_b32 s41, s5, 2\n"
    "s_add_u32 s41, s41, 4178944\n"
    "v_mov_b32 v3, s41\n"
    "v_mov_b32 v2, s4\n"
    "s_mov_b64 exec, 1\n"
    "buffer_atomic_swap v2, v3, s[8:11], 0 offen sc0 sc1\n"
    "s_mov_b64 exec, -1\n"
    ".Lcl_16:\n"
    "v_min_u32 v6, 54, v1\n"
    "v_lshlrev_b32 v6, 7, v6\n"
    "v_add_u32 v6, 4182016, v6\n"
    "buffer_load_dwordx2 v[4:5], v6, s[8:11], 0 offen sc1\n"
    "s_waitcnt lgkmcnt(0)\n"
    "s_waitcnt vmcnt(25)\n"
    "s_barrier\n"
    "ds_read_b64_tr_b16 a[188:189], v16 offset:8192\n"
    "ds_read_b64_tr_b16 a[190:191], v16 offset:9728\n"
    "ds_read_b64_tr_b16 a[196:197], v16 offset:8224\n"
    "ds_read_b64_tr_b16 a[198:199], v16 offset:9760\n"
    "ds_read_b64_tr_b16 v[90:91], v16 offset:8256\n"
    "ds_read_b64_tr_b16 v[92:93], v16 offset:9792\n"
    "ds_read_b64_tr_b16 a[204:205], v16 offset:8288\n"
    "ds_read_b64_tr_b16 a[206:207], v16 offset:9824\n"
    "ds_read_b64_tr_b16 a[212:213], v16 offset:8320\n"
    "ds_read_b64_tr_b16 a[214:215], v16 offset:9856\n"
    "ds_read_b64_tr_b16 a[220:221], v16 offset:8352\n"
    "ds_read_b64_tr_b16 a[222:223], v16 offset:9888\n"
    "s_add_u32 s40, s49, 2359296\n"
    "s_add_u32 m0, s29, 98304\n"
    "s_nop 0\n"
    "buffer_load_dwordx4 v8, s[12:15], s40 offen lds\n"
    "s_add_u32 m0, s29, 99328\n"
    "s_nop 0\n"
    "buffer_load_dwordx4 v9, s[12:15], s40 offen lds\n"
    "s_add_u32 m0, s29, 100352\n"
    "s_nop 0\n"
    "buffer_load_dwordx4 v10, s[12:15], s40 offen lds\n"
    "s_add_u32 m0, s29, 101376\n"
    "s_nop 0\n"
    "buffer_load_dwordx4 v11, s[12:15], s40 offen lds\n"
    "s_add_u32 m0, s29, 102400\n"
    "s_nop 0\n"
    "buffer_load_dwordx4 v12, s[12:15], s40 offen lds\n"
    "s_add_u32 m0, s29, 103424\n"
    "s_nop 0\n"
    "buffer_load_dwordx4 v13, s[12:15], s40 offen lds\n"
    "s_waitcnt lgkmcnt(0)\n"
    "s_waitcnt vmcnt(25)\n"
    "s_barrier\n"
    "ds_read_b64_tr_b16 a[224:225], v14 offset:0\n"
    "ds_read_b64_tr_b16 a[226:227], v14 offset:1536\n"
    "ds_read_b64_tr_b16 a[232:233], v14 offset:32\n"
    "ds_read_b64_tr_b16 a[234:235], v14 offset:1568\n"
    "ds_read_b64_tr_b16 v[94:95], v14 offset:64\n"
    "ds_read_b64_tr_b16 v[96:97], v14 offset:1600\n"
    "ds_read_b64_tr_b16 a[240:241], v14 offset:96\n"
    "ds_read_b64_tr_b16 a[242:243], v14 offset:1632\n"
    "ds_read_b64_tr_b16 a[248:249], v14 offset:128\n"
    "ds_read_b64_tr_b16 a[250:251], v14 offset:1664\n"
    "ds_read_b64_tr_b16 v[120:121], v14 offset:160\n"
    "ds_read_b64_tr_b16 v[122:123], v14 offset:1696\n"
    "s_add_u32 s40, s49, 2752512\n"
    "s_add_u32 m0, s29, 122880\n"
    "s_nop 0\n"
    "buffer_load_dwordx4 v8, s[12:15], s40 offen lds\n"
    "s_add_u32 m0, s29, 123904\n"
    "s_nop 0\n"
    "buffer_load_dwordx4 v9, s[12:15], s40 offen lds\n"
    "s_add_u32 m0, s29, 124928\n"
    "s_nop 0\n"
    "buffer_load_dwordx4 v10, s[12:15], s40 offen lds\n"
    "s_add_u32 m0, s29, 125952\n"
    "s_nop 0\n"
    "buffer_load_dwordx4 v11, s[12:15], s40 offen lds\n"
    "s_add_u32 m0, s29, 126976\n"
    "s_nop 0\n"
    "buffer_load_dwordx4 v12, s[12:15], s40 offen lds\n"
    "s_add_u32 m0, s29, 128000\n"
    "s_nop 0\n"
    "buffer_load_dwordx4 v13, s[12:15], s40 offen lds\n"
    "s_waitcnt lgkmcnt(0)\n"
    "s_waitcnt vmcnt(25)\n"
    "s_barrier\n"
    "ds_read_b64_tr_b16 a[228:229], v14 offset:24576\n"
    "ds_read_b64_tr_b16 a[230:231], v14 offset:26112\n"
    "ds_read_b64_tr_b16 a[236:237], v14 offset:24608\n"
    "ds_read_b64_tr_b16 a[238:239], v14 offset:26144\n"
    "ds_read_b64_tr_b16 v[98:99], v14 offset:24640\n"
    "ds_read_b64_tr_b16 v[100:101], v14 offset:26176\n"
    "ds_read_b64_tr_b16 a[244:245], v14 offset:24672\n"
    "ds_read_b64_tr_b16 a[246:247], v14 offset:26208\n"
    "ds_read_b64_tr_b16 a[252:253], v14 offset:24704\n"
    "ds_read_b64_tr_b16 a[254:255], v14 offset:26240\n"
    "ds_read_b64_tr_b16 v[124:125], v14 offset:24736\n"
    "ds_read_b64_tr_b16 v[126:127], v14 offset:26272\n"
    "s_add_u32 s40, s49, 3145728\n"
    "s_add_u32 m0, s29, 0\n"
    "s_nop 0\n"
    "buffer_load_dwordx4 v8, s[12:15], s40 offen lds\n"
    "s_add_u32 m0, s29, 1024\n"
    "s_nop 0\n"
    "buffer_load_dwordx4 v9, s[12:15], s40 offen lds\n"
    "s_add_u32 m0, s29, 2048\n"
    "s_nop 0\n"
    "buffer_load_dwordx4 v10, s[12:15], s40 offen lds\n"
    "s_add_u32 m0, s29, 3072\n"
    "s_nop 0\n"
    "buffer_load_dwordx4 v11, s[12:15], s40 offen lds\n"
    "s_add_u32 m0, s29, 4096\n"
    "s_nop 0\n"
    "buffer_load_dwordx4 v12, s[12:15], s40 offen lds\n"
    "s_add_u32 m0, s29, 5120\n"
    "s_nop 0\n"
    "buffer_load_dwordx4 v13, s[12:15], s40 offen lds\n"
    "s_waitcnt lgkmcnt(0)\n"
    "s_waitcnt vmcnt(25)\n"
    "s_barrier\n"
    "ds_read_b64_tr_b16 v[128:129], v14 offset:49152\n"
    "ds_read_b64_tr_b16 v[130:131], v14 offset:50688\n"
    "ds_read_b64_tr_b16 v[136:137], v14 offset:49184\n"
    "ds_read_b64_tr_b16 v[138:139], v14 offset:50720\n"
    "ds_read_b64_tr_b16 v[102:103], v14 offset:49216\n"
    "ds_read_b64_tr_b16 v[104:105], v14 offset:50752\n"
    "ds_read_b64_tr_b16 v[144:145], v14 offset:49248\n"
    "ds_read_b64_tr_b16 v[146:147], v14 offset:50784\n"
    "ds_read_b64_tr_b16 v[152:153], v14 offset:49280\n"
    "ds_read_b64_tr_b16 v[154:155], v14 offset:50816\n"
    "ds_read_b64_tr_b16 v[160:161], v14 offset:49312\n"
    "ds_read_b64_tr_b16 v[162:163], v14 offset:50848\n"
    "s_add_u32 s40, s49, 3538944\n"
    "s_add_u32 m0, s29, 24576\n"
    "s_nop 0\n"
    "buffer_load_dwordx4 v8, s[12:15], s40 offen lds\n"
    "s_add_u32 m0, s29, 25600\n"
    "s_nop 0\n"
    "buffer_load_dwordx4 v9, s[12:15], s40 offen lds\n"
    "s_add_u32 m0, s29, 26624\n"
    "s_nop 0\n"
    "buffer_load_dwordx4 v10, s[12:15], s40 offen lds\n"
    "s_add_u32 m0, s29, 27648\n"
    "s_nop 0\n"
    "buffer_load_dwordx4 v11, s[12:15], s40 offen lds\n"
    "s_add_u32 m0, s29, 28672\n"
    "s_nop 0\n"
    "buffer_load_dwordx4 v12, s[12:15], s40 offen lds\n"
    "s_add_u32 m0, s29, 29696\n"
    "s_nop 0\n"
    "buffer_load_dwordx4 v13, s[12:15], s40 offen lds\n"
    "s_waitcnt lgkmcnt(0)\n"
    "s_waitcnt vmcnt(25)\n"
    "s_barrier\n"
    "ds_read_b64_tr_b16 v[132:133], v15 offset:16384\n"
    "ds_read_b64_tr_b16 v[134:135], v15 offset:17920\n"
    "ds_read_b64_tr_b16 v[140:141], v15 offset:16416\n"
    "ds_read_b64_tr_b16 v[142:143], v15 offset:17952\n"
    "ds_read_b64_tr_b16 v[106:107], v15 offset:16448\n"
    "ds_read_b64_tr_b16 v[108:109], v15 offset:17984\n"
    "ds_read_b64_tr_b16 v[148:149], v15 offset:16480\n"
    "ds_read_b64_tr_b16 v[150:151], v15 offset:18016\n"
    "ds_read_b64_tr_b16 v[156:157], v15 offset:16512\n"
    "ds_read_b64_tr_b16 v[158:159], v15 offset:18048\n"
    "ds_read_b64_tr_b16 v[164:165], v15 offset:16544\n"
    "ds_read_b64_tr_b16 v[166:167], v15 offset:18080\n"
    "s_waitcnt lgkmcnt(0)\n"
    "s_waitcnt vmcnt(18)\n"
    "s_barrier\n"
    "ds_read_b64_tr_b16 v[168:169], v15 offset:40960\n"
    "ds_read_b64_tr_b16 v[170:171], v15 offset:42496\n"
    "ds_read_b64_tr_b16 v[176:177], v15 offset:40992\n"
    "ds_read_b64_tr_b16 v[178:179], v15 offset:42528\n"
    "ds_read_b64_tr_b16 v[184:185], v15 offset:41024\n"
    "ds_read_b64_tr_b16 v[186:187], v15 offset:42560\n"
    "ds_read_b64_tr_b16 v[192:193], v15 offset:41056\n"
    "ds_read_b64_tr_b16 v[194:195], v15 offset:42592\n"
    "ds_read_b64_tr_b16 v[200:201], v15 offset:41088\n"
    "ds_read_b64_tr_b16 v[202:203], v15 offset:42624\n"
    "ds_read_b64_tr_b16 v[208:209], v15 offset:41120\n"
    "ds_read_b64_tr_b16 v[210:211], v15 offset:42656\n"
    "s_waitcnt lgkmcnt(0)\n"
    "s_waitcnt vmcnt(12)\n"
    "s_barrier\n"
    "ds_read_b64_tr_b16 v[172:173], v16 offset:8192\n"
    "ds_read_b64_tr_b16 v[174:175], v16 offset:9728\n"
    "ds_read_b64_tr_b16 v[180:181], v16 offset:8224\n"
    "ds_read_b64_tr_b16 v[182:183], v16 offset:9760\n"
    "ds_read_b64_tr_b16 v[188:189], v16 offset:8256\n"
    "ds_read_b64_tr_b16 v[190:191], v16 offset:9792\n"
    "ds_read_b64_tr_b16 v[196:197], v16 offset:8288\n"
    "ds_read_b64_tr_b16 v[198:199], v16 offset:9824\n"
    "ds_read_b64_tr_b16 v[204:205], v16 offset:8320\n"
    "ds_read_b64_tr_b16 v[206:207], v16 offset:9856\n"
    "ds_read_b64_tr_b16 v[212:213], v16 offset:8352\n"
    "ds_read_b64_tr_b16 v[214:215], v16 offset:9888\n"
    "s_waitcnt lgkmcnt(0)\n"
    "s_waitcnt vmcnt(6)\n"
    "s_barrier\n"
    "ds_read_b64_tr_b16 v[216:217], v14 offset:0\n"
    "ds_read_b64_tr_b16 v[218:219], v14 offset:1536\n"
    "ds_read_b64_tr_b16 v[70:71], v14 offset:32\n"
    "ds_read_b64_tr_b16 v[72:73], v14 offset:1568\n"
    "ds_read_b64_tr_b16 v[224:225], v14 offset:64\n"
    "ds_read_b64_tr_b16 v[226:227], v14 offset:1600\n"
    "ds_read_b64_tr_b16 v[232:233], v14 offset:96\n"
    "ds_read_b64_tr_b16 v[234:235], v14 offset:1632\n"
    "ds_read_b64_tr_b16 v[240:241], v14 offset:128\n"
    "ds_read_b64_tr_b16 v[242:243], v14 offset:1664\n"
    "ds_read_b64_tr_b16 v[248:249], v14 offset:160\n"
    "ds_read_b64_tr_b16 v[250:251], v14 offset:1696\n"
    "s_waitcnt lgkmcnt(0)\n"
    "s_waitcnt vmcnt(0)\n"
    "s_barrier\n"
    "ds_read_b64_tr_b16 v[220:221], v14 offset:24576\n"
    "ds_read_b64_tr_b16 v[222:223], v14 offset:26112\n"
    "ds_read_b64_tr_b16 v[74:75], v14 offset:24608\n"
    "ds_read_b64_tr_b16 v[76:77], v14 offset:26144\n"
    "ds_read_b64_tr_b16 v[228:229], v14 offset:24640\n"
    "ds_read_b64_tr_b16 v[230:231], v14 offset:26176\n"
    "ds_read_b64_tr_b16 v[236:237], v14 offset:24672\n"
    "ds_read_b64_tr_b16 v[238:239], v14 offset:26208\n"
    "ds_read_b64_tr_b16 v[244:245], v14 offset:24704\n"
    "ds_read_b64_tr_b16 v[246:247], v14 offset:26240\n"
    "ds_read_b64_tr_b16 v[252:253], v14 offset:24736\n"
    "ds_read_b64_tr_b16 v[254:255], v14 offset:26272\n"
    "s_waitcnt lgkmcnt(0)\n"
    "v_cmp_ne_u32 vcc, s4, v4\n"
    "s_nop 1\n"
    "s_cmp_eq_u32 vcc_lo, 0\n"
    "s_cbranch_scc1 .Lrx_19\n"
    ".Lrp_17:\n"
    "v_min_u32 v6, 54, v1\n"
    "v_lshlrev_b32 v6, 7, v6\n"
    "v_add_u32 v6, 4182016, v6\n"
    "buffer_load_dwordx2 v[4:5], v6, s[8:11], 0 offen sc1\n"
    "s_waitcnt vmcnt(0)\n"
    "v_cmp_ne_u32 vcc, s4, v4\n"
    "s_nop 1\n"
    "s_cmp_eq_u32 vcc_lo, 0\n"
    "s_cbranch_scc1 .Lro_18\n"
    "s_sleep 2\n"
    "s_sub_u32 s25, s25, 1\n"
    "s_cbranch_scc0 .Lrp_17\n"
    "s_branch .Lfail\n"
    ".Lro_18:\n"
    ".Lrx_19:\n"
    "v_cmp_ne_u32 vcc, s4, v4\n"
    "s_nop 1\n"
    "s_cmp_eq_u32 vcc_hi, 0\n"
    "s_cbranch_scc1 .Lrx_22\n"
    ".Lrp_20:\n"
    "v_min_u32 v6, 54, v1\n"
    "v_lshlrev_b32 v6, 7, v6\n"
    "v_add_u32 v6, 4182016, v6\n"
    "buffer_load_dwordx2 v[4:5], v6, s[8:11], 0 offen sc1\n"
    "s_waitcnt vmcnt(0)\n"
    "v_cmp_ne_u32 vcc, s4, v4\n"
    "s_nop 1\n"
    "s_cmp_eq_u32 vcc_hi, 0\n"
    "s_cbranch_scc1 .Lro_21\n"
    "s_sleep 2\n"
    "s_sub_u32 s25, s25, 1\n"
    "s_cbranch_scc0 .Lrp_20\n"
    "s_branch .Lfail\n"
    ".Lro_21:\n"
    ".Lrx_22:\n"
    "v_cmp_ne_u32 vcc, 0, v5\n"
    "s_nop 1\n"
    "s_and_b32 s22, vcc_hi, 1\n"
    "s_lshl_b32 s22, s22, 13\n"
    "s_lshr_b32 s23, vcc_hi, 1\n"
    "s_branch .Lpw_14\n"
    ".Lpn_15:\n"
    "s_add_u32 s40, s48, 0\n"
    "s_add_u32 m0, s29, 0\n"
    "s_nop 0\n"
    "buffer_load_dwordx4 v8, s[12:15], s40 offen lds\n"
    "s_add_u32 m0, s29, 1024\n"
    "s_nop 0\n"
    "buffer_load_dwordx4 v9, s[12:15], s40 offen lds\n"
    "s_add_u32 m0, s29, 2048\n"
    "s_nop 0\n"
    "buffer_load_dwordx4 v10, s[12:15], s40 offen lds\n"
    "s_add_u32 m0, s29, 3072\n"
    "s_nop 0\n"
    "buffer_load_dwordx4 v11, s[12:15], s40 offen lds\n"
    "s_add_u32 m0, s29, 4096\n"
    "s_nop 0\n"
    "buffer_load_dwordx4 v12, s[12:15], s40 offen lds\n"
    "s_add_u32 m0, s29, 5120\n"
    "s_nop 0\n"
    "buffer_load_dwordx4 v13, s[12:15], s40 offen lds\n"
    "s_add_u32 s40, s48, 393216\n"
    "s_add_u32 m0, s29, 24576\n"
    "s_nop 0\n"
    "buffer_load_dwordx4 v8, s[12:15], s40 offen lds\n"
    "s_add_u32 m0, s29, 25600\n"
    "s_nop 0\n"
    "buffer_load_dwordx4 v9, s[12:15], s40 offen lds\n"
    "s_add_u32 m0, s29, 26624\n"
    "s_nop 0\n"
    "buffer_load_dwordx4 v10, s[12:15], s40 offen lds\n"
    "s_add_u32 m0, s29, 27648\n"
    "s_nop 0\n"
    "buffer_load_dwordx4 v11, s[12:15], s40 offen lds\n"
    "s_add_u32 m0, s29, 28672\n"
    "s_nop 0\n"
    "buffer_load_dwordx4 v12, s[12:15], s40 offen lds\n"
    "s_add_u32 m0, s29, 29696\n"
    "s_nop 0\n"
    "buffer_load_dwordx4 v13, s[12:15], s40 offen lds\n"
    "s_add_u32 s40, s48, 786432\n"
    "s_add_u32 m0, s29, 49152\n"
    "s_nop 0\n"
    "buffer_load_dwordx4 v8, s[12:15], s40 offen lds\n"
    "s_add_u32 m0, s29, 50176\n"
    "s_nop 0\n"
    "buffer_load_dwordx4 v9, s[12:15], s40 offen lds\n"
    "s_add_u32 m0, s29, 51200\n"
    "s_nop 0\n"
    "buffer_load_dwordx4 v10, s[12:15], s40 offen lds\n"
    "s_add_u32 m0, s29, 52224\n"
    "s_nop 0\n"
    "buffer_load_dwordx4 v11, s[12:15], s40 offen lds\n"
    "s_add_u32 m0, s29, 53248\n"
    "s_nop 0\n"
    "buffer_load_dwordx4 v12, s[12:15], s40 offen lds\n"
    "s_add_u32 m0, s29, 54272\n"
    "s_nop 0\n"
    "buffer_load_dwordx4 v13, s[12:15], s40 offen lds\n"
    "s_add_u32 s40, s48, 1179648\n"
    "s_add_u32 m0, s29, 73728\n"
    "s_nop 0\n"
    "buffer_load_dwordx4 v8, s[12:15], s40 offen lds\n"
    "s_add_u32 m0, s29, 74752\n"
    "s_nop 0\n"
    "buffer_load_dwordx4 v9, s[12:15], s40 offen lds\n"
    "s_add_u32 m0, s29, 75776\n"
    "s_nop 0\n"
    "buffer_load_dwordx4 v10, s[12:15], s40 offen lds\n"
    "s_add_u32 m0, s29, 76800\n"
    "s_nop 0\n"
    "buffer_load_dwordx4 v11, s[12:15], s40 offen lds\n"
    "s_add_u32 m0, s29, 77824\n"
    "s_nop 0\n"
    "buffer_load_dwordx4 v12, s[12:15], s40 offen lds\n"
    "s_add_u32 m0, s29, 78848\n"
    "s_nop 0\n"
    "buffer_load_dwordx4 v13, s[12:15], s40 offen lds\n"
    "s_add_u32 s40, s48, 1572864\n"
    "s_add_u32 m0, s29, 98304\n"
    "s_nop 0\n"
    "buffer_load_dwordx4 v8, s[12:15], s40 offen lds\n"
    "s_add_u32 m0, s29, 99328\n"
    "s_nop 0\n"
    "buffer_load_dwordx4 v9, s[12:15], s40 offen lds\n"
    "s_add_u32 m0, s29, 100352\n"
    "s_nop 0\n"
    "buffer_load_dwordx4 v10, s[12:15], s40 offen lds\n"
    "s_add_u32 m0, s29, 101376\n"
    "s_nop 0\n"
    "buffer_load_dwordx4 v11, s[12:15], s40 offen lds\n"
    "s_add_u32 m0, s29, 102400\n"
    "s_nop 0\n"
    "buffer_load_dwordx4 v12, s[12:15], s40 offen lds\n"
    "s_add_u32 m0, s29, 103424\n"
    "s_nop 0\n"
    "buffer_load_dwordx4 v13, s[12:15], s40 offen lds\n"
    "s_add_u32 s40, s48, 1966080\n"
    "s_add_u32 m0, s29, 122880\n"
    "s_nop 0\n"
    "buffer_load_dwordx4 v8, s[12:15], s40 offen lds\n"
    "s_add_u32 m0, s29, 123904\n"
    "s_nop 0\n"
    "buffer_load_dwordx4 v9, s[12:15], s40 offen lds\n"
    "s_add_u32 m0, s29, 124928\n"
    "s_nop 0\n"
    "buffer_load_dwordx4 v10, s[12:15], s40 offen lds\n"
    "s_add_u32 m0, s29, 125952\n"
    "s_nop 0\n"
    "buffer_load_dwordx4 v11, s[12:15], s40 offen lds\n"
    "s_add_u32 m0, s29, 126976\n"
    "s_nop 0\n"
    "buffer_load_dwordx4 v12, s[12:15], s40 offen lds\n"
    "s_add_u32 m0, s29, 128000\n"
    "s_nop 0\n"
    "buffer_load_dwordx4 v13, s[12:15], s40 offen lds\n"
    "s_waitcnt vmcnt(30)\n"
    "s_barrier\n"
    "ds_read_b64_tr_b16 a[96:97], v14 offset:0\n"
    "ds_read_b64_tr_b16 a[98:99], v14 offset:1536\n"
    "ds_read_b64_tr_b16 a[104:105], v14 offset:32\n"
    "ds_read_b64_tr_b16 a[106:107], v14 offset:1568\n"
    "ds_read_b64_tr_b16 a[112:113], v14 offset:64\n"
    "ds_read_b64_tr_b16 a[114:115], v14 offset:1600\n"
    "ds_read_b64_tr_b16 a[120:121], v14 offset:96\n"
    "ds_read_b64_tr_b16 a[122:123], v14 offset:1632\n"
    "ds_read_b64_tr_b16 a[128:129], v14 offset:128\n"
    "ds_read_b64_tr_b16 a[130:131], v14 offset:1664\n"
    "ds_read_b64_tr_b16 a[136:137], v14 offset:160\n"
    "ds_read_b64_tr_b16 a[138:139], v14 offset:1696\n"
    "s_waitcnt lgkmcnt(0)\n"
    "s_waitcnt vmcnt(24)\n"
    "s_barrier\n"
    "ds_read_b64_tr_b16 a[100:101], v14 offset:24576\n"
    "ds_read_b64_tr_b16 a[102:103], v14 offset:26112\n"
    "ds_read_b64_tr_b16 a[108:109], v14 offset:24608\n"
    "ds_read_b64_tr_b16 a[110:111], v14 offset:26144\n"
    "ds_read_b64_tr_b16 a[116:117], v14 offset:24640\n"
    "ds_read_b64_tr_b16 a[118:119], v14 offset:26176\n"
    "ds_read_b64_tr_b16 a[124:125], v14 offset:24672\n"
    "ds_read_b64_tr_b16 a[126:127], v14 offset:26208\n"
    "ds_read_b64_tr_b16 a[132:133], v14 offset:24704\n"
    "ds_read_b64_tr_b16 a[134:135], v14 offset:26240\n"
    "ds_read_b64_tr_b16 a[140:141], v14 offset:24736\n"
    "ds_read_b64_tr_b16 a[142:143], v14 offset:26272\n"
    "s_add_u32 s40, s48, 2359296\n"
    "s_add_u32 m0, s29, 0\n"
    "s_nop 0\n"
    "buffer_load_dwordx4 v8, s[12:15], s40 offen lds\n"
    "s_add_u32 m0, s29, 1024\n"
    "s_nop 0\n"
    "buffer_load_dwordx4 v9, s[12:15], s40 offen lds\n"
    "s_add_u32 m0, s29, 2048\n"
    "s_nop 0\n"
    "buffer_load_dwordx4 v10, s[12:15], s40 offen lds\n"
    "s_add_u32 m0, s29, 3072\n"
    "s_nop 0\n"
    "buffer_load_dwordx4 v11, s[12:15], s40 offen lds\n"
    "s_add_u32 m0, s29, 4096\n"
    "s_nop 0\n"
    "buffer_load_dwordx4 v12, s[12:15], s40 offen lds\n"
    "s_add_u32 m0, s29, 5120\n"
    "s_nop 0\n"
    "buffer_load_dwordx4 v13, s[12:15], s40 offen lds\n"
    "s_waitcnt lgkmcnt(0)\n"
    "s_waitcnt vmcnt(24)\n"
    "s_barrier\n"
    "ds_read_b64_tr_b16 a[144:145], v14 offset:49152\n"
    "ds_read_b64_tr_b16 a[146:147], v14 offset:50688\n"
    "ds_read_b64_tr_b16 a[152:153], v14 offset:49184\n"
    "ds_read_b64_tr_b16 a[154:155], v14 offset:50720\n"
    "ds_read_b64_tr_b16 v[78:79], v14 offset:49216\n"
    "ds_read_b64_tr_b16 v[80:81], v14 offset:50752\n"
    "ds_read_b64_tr_b16 a[160:161], v14 offset:49248\n"
    "ds_read_b64_tr_b16 a[162:163], v14 offset:50784\n"
    "ds_read_b64_tr_b16 a[168:169], v14 offset:49280\n"
    "ds_read_b64_tr_b16 a[170:171], v14 offset:50816\n"
    "ds_read_b64_tr_b16 a[176:177], v14 offset:49312\n"
    "ds_read_b64_tr_b16 a[178:179], v14 offset:50848\n"
    "s_add_u32 s40, s48, 2752512\n"
    "s_add_u32 m0, s29, 24576\n"
    "s_nop 0\n"
    "buffer_load_dwordx4 v8, s[12:15], s40 offen lds\n"
    "s_add_u32 m0, s29, 25600\n"
    "s_nop 0\n"
    "buffer_load_dwordx4 v9, s[12:15], s40 offen lds\n"
    "s_add_u32 m0, s29, 26624\n"
    "s_nop 0\n"
    "buffer_load_dwordx4 v10, s[12:15], s40 offen lds\n"
    "s_add_u32 m0, s29, 27648\n"
    "s_nop 0\n"
    "buffer_load_dwordx4 v11, s[12:15], s40 offen lds\n"
    "s_add_u32 m0, s29, 28672\n"
    "s_nop 0\n"
    "buffer_load_dwordx4 v12, s[12:15], s40 offen lds\n"
    "s_add_u32 m0, s29, 29696\n"
    "s_nop 0\n"
    "buffer_load_dwordx4 v13, s[12:15], s40 offen lds\n"
    "s_waitcnt lgkmcnt(0)\n"
    "s_waitcnt vmcnt(24)\n"
    "s_barrier\n"
    "ds_read_b64_tr_b16 a[148:149], v15 offset:16384\n"
    "ds_read_b64_tr_b16 a[150:151], v15 offset:17920\n"
    "ds_read_b64_tr_b16 a[156:157], v15 offset:16416\n"
    "ds_read_b64_tr_b16 a[158:159], v15 offset:17952\n"
    "ds_read_b64_tr_b16 v[82:83], v15 offset:16448\n"
    "ds_read_b64_tr_b16 v[84:85], v15 offset:17984\n"
    "ds_read_b64_tr_b16 a[164:165], v15 offset:16480\n"
    "ds_read_b64_tr_b16 a[166:167], v15 offset:18016\n"
    "ds_read_b64_tr_b16 a[172:173], v15 offset:16512\n"
    "ds_read_b64_tr_b16 a[174:175], v15 offset:18048\n"
    "ds_read_b64_tr_b16 a[180:181], v15 offset:16544\n"
    "ds_read_b64_tr_b16 a[182:183], v15 offset:18080\n"
    "s_add_u32 s40, s48, 3145728\n"
    "s_add_u32 m0, s29, 49152\n"
    "s_nop 0\n"
    "buffer_load_dwordx4 v8, s[12:15], s40 offen lds\n"
    "s_add_u32 m0, s29, 50176\n"
    "s_nop 0\n"
    "buffer_load_dwordx4 v9, s[12:15], s40 offen lds\n"
    "s_add_u32 m0, s29, 51200\n"
    "s_nop 0\n"
    "buffer_load_dwordx4 v10, s[12:15], s40 offen lds\n"
    "s_add_u32 m0, s29, 52224\n"
    "s_nop 0\n"
    "buffer_load_dwordx4 v11, s[12:15], s40 offen lds\n"
    "s_add_u32 m0, s29, 53248\n"
    "s_nop 0\n"
    "buffer_load_dwordx4 v12, s[12:15], s40 offen lds\n"
    "s_add_u32 m0, s29, 54272\n"
    "s_nop 0\n"
    "buffer_load_dwordx4 v13, s[12:15], s40 offen lds\n"
    "s_waitcnt lgkmcnt(0)\n"
    "s_waitcnt vmcnt(24)\n"
    "s_barrier\n"
    "ds_read_b64_tr_b16 v[22:23], v15 offset:40960\n"
    "ds_read_b64_tr_b16 v[24:25], v15 offset:42496\n"
    "ds_read_b64_tr_b16 v[30:31], v15 offset:40992\n"
    "ds_read_b64_tr_b16 v[32:33], v15 offset:42528\n"
    "ds_read_b64_tr_b16 v[38:39], v15 offset:41024\n"
    "ds_read_b64_tr_b16 v[40:41], v15 offset:42560\n"
    "ds_read_b64_tr_b16 v[46:47], v15 offset:41056\n"
    "ds_read_b64_tr_b16 v[48:49], v15 offset:42592\n"
    "ds_read_b64_tr_b16 v[54:55], v15 offset:41088\n"
    "ds_read_b64_tr_b16 v[56:57], v15 offset:42624\n"
    "ds_read_b64_tr_b16 v[62:63], v15 offset:41120\n"
    "ds_read_b64_tr_b16 v[64:65], v15 offset:42656\n"
    "s_add_u32 s40, s48, 3538944\n"
    "s_add_u32 m0, s29, 73728\n"
    "s_nop 0\n"
    "buffer_load_dwordx4 v8, s[12:15], s40 offen lds\n"
    "s_add_u32 m0, s29, 74752\n"
    "s_nop 0\n"
    "buffer_load_dwordx4 v9, s[12:15], s40 offen lds\n"
    "s_add_u32 m0, s29, 75776\n"
    "s_nop 0\n"
    "buffer_load_dwordx4 v10, s[12:15], s40 offen lds\n"
    "s_add_u32 m0, s29, 76800\n"
    "s_nop 0\n"
    "buffer_load_dwordx4 v11, s[12:15], s40 offen lds\n"
    "s_add_u32 m0, s29, 77824\n"
    "s_nop 0\n"
    "buffer_load_dwordx4 v12, s[12:15], s40 offen lds\n"
    "s_add_u32 m0, s29, 78848\n"
    "s_nop 0\n"
    "buffer_load_dwordx4 v13, s[12:15], s40 offen lds\n"
    "s_waitcnt lgkmcnt(0)\n"
    "s_waitcnt vmcnt(24)\n"
    "s_barrier\n"
    "ds_read_b64_tr_b16 v[26:27], v16 offset:8192\n"
    "ds_read_b64_tr_b16 v[28:29], v16 offset:9728\n"
    "ds_read_b64_tr_b16 v[34:35], v16 offset:8224\n"
    "ds_read_b64_tr_b16 v[36:37], v16 offset:9760\n"
    "ds_read_b64_tr_b16 v[42:43], v16 offset:8256\n"
    "ds_read_b64_tr_b16 v[44:45], v16 offset:9792\n"
    "ds_read_b64_tr_b16 v[50:51], v16 offset:8288\n"
    "ds_read_b64_tr_b16 v[52:53], v16 offset:9824\n"
    "ds_read_b64_tr_b16 v[58:59], v16 offset:8320\n"
    "ds_read_b64_tr_b16 v[60:61], v16 offset:9856\n"
    "ds_read_b64_tr_b16 v[66:67], v16 offset:8352\n"
    "ds_read_b64_tr_b16 v[68:69], v16 offset:9888\n"
    "s_add_u32 s40, s49, 0\n"
    "s_add_u32 m0, s29, 98304\n"
    "s_nop 0\n"
    "buffer_load_dwordx4 v8, s[12:15], s40 offen lds\n"
    "s_add_u32 m0, s29, 99328\n"
    "s_nop 0\n"
    "buffer_load_dwordx4 v9, s[12:15], s40 offen lds\n"
    "s_add_u32 m0, s29, 100352\n"
    "s_nop 0\n"
    "buffer_load_dwordx4 v10, s[12:15], s40 offen lds\n"
    "s_add_u32 m0, s29, 101376\n"
    "s_nop 0\n"
    "buffer_load_dwordx4 v11, s[12:15], s40 offen lds\n"
    "s_add_u32 m0, s29, 102400\n"
    "s_nop 0\n"
    "buffer_load_dwordx4 v12, s[12:15], s40 offen lds\n"
    "s_add_u32 m0, s29, 103424\n"
    "s_nop 0\n"
    "buffer_load_dwordx4 v13, s[12:15], s40 offen lds\n"
    "s_waitcnt lgkmcnt(0)\n"
    "s_waitcnt vmcnt(24)\n"
    "s_barrier\n"
    "ds_read_b64_tr_b16 a[0:1], v14 offset:0\n"
    "ds_read_b64_tr_b16 a[2:3], v14 offset:1536\n"
    "ds_read_b64_tr_b16 a[8:9], v14 offset:32\n"
    "ds_read_b64_tr_b16 a[10:11], v14 offset:1568\n"
    "ds_read_b64_tr_b16 a[16:17], v14 offset:64\n"
    "ds_read_b64_tr_b16 a[18:19], v14 offset:1600\n"
    "ds_read_b64_tr_b16 a[24:25], v14 offset:96\n"
    "ds_read_b64_tr_b16 a[26:27], v14 offset:1632\n"
    "ds_read_b64_tr_b16 a[32:33], v14 offset:128\n"
    "ds_read_b64_tr_b16 a[34:35], v14 offset:1664\n"
    "ds_read_b64_tr_b16 a[40:41], v14 offset:160\n"
    "ds_read_b64_tr_b16 a[42:43], v14 offset:1696\n"
    "s_add_u32 s40, s49, 393216\n"
    "s_add_u32 m0, s29, 122880\n"
    "s_nop 0\n"
    "buffer_load_dwordx4 v8, s[12:15], s40 offen lds\n"
    "s_add_u32 m0, s29, 123904\n"
    "s_nop 0\n"
    "buffer_load_dwordx4 v9, s[12:15], s40 offen lds\n"
    "s_add_u32 m0, s29, 124928\n"
    "s_nop 0\n"
    "buffer_load_dwordx4 v10, s[12:15], s40 offen lds\n"
    "s_add_u32 m0, s29, 125952\n"
    "s_nop 0\n"
    "buffer_load_dwordx4 v11, s[12:15], s40 offen lds\n"
    "s_add_u32 m0, s29, 126976\n"
    "s_nop 0\n"
    "buffer_load_dwordx4 v12, s[12:15], s40 offen lds\n"
    "s_add_u32 m0, s29, 128000\n"
    "s_nop 0\n"
    "buffer_load_dwordx4 v13, s[12:15], s40 offen lds\n"
    "s_waitcnt lgkmcnt(0)\n"
    "s_waitcnt vmcnt(24)\n"
    "s_barrier\n"
    "ds_read_b64_tr_b16 a[4:5], v14 offset:24576\n"
    "ds_read_b64_tr_b16 a[6:7], v14 offset:26112\n"
    "ds_read_b64_tr_b16 a[12:13], v14 offset:24608\n"
    "ds_read_b64_tr_b16 a[14:15], v14 offset:26144\n"
    "ds_read_b64_tr_b16 a[20:21], v14 offset:24640\n"
    "ds_read_b64_tr_b16 a[22:23], v14 offset:26176\n"
    "ds_read_b64_tr_b16 a[28:29], v14 offset:24672\n"
    "ds_read_b64_tr_b16 a[30:31], v14 offset:26208\n"
    "ds_read_b64_tr_b16 a[36:37], v14 offset:24704\n"
    "ds_read_b64_tr_b16 a[38:39], v14 offset:26240\n"
    "ds_read_b64_tr_b16 a[44:45], v14 offset:24736\n"
    "ds_read_b64_tr_b16 a[46:47], v14 offset:26272\n"
    "s_add_u32 s40, s49, 786432\n"
    "s_add_u32 m0, s29, 0\n"
    "s_nop 0\n"
    "buffer_load_dwordx4 v8, s[12:15], s40 offen lds\n"
    "s_add_u32 m0, s29, 1024\n"
    "s_nop 0\n"
    "buffer_load_dwordx4 v9, s[12:15], s40 offen lds\n"
    "s_add_u32 m0, s29, 2048\n"
    "s_nop 0\n"
    "buffer_load_dwordx4 v10, s[12:15], s40 offen lds\n"
    "s_add_u32 m0, s29, 3072\n"
    "s_nop 0\n"
    "buffer_load_dwordx4 v11, s[12:15], s40 offen lds\n"
    "s_add_u32 m0, s29, 4096\n"
    "s_nop 0\n"
    "buffer_load_dwordx4 v12, s[12:15], s40 offen lds\n"
    "s_add_u32 m0, s29, 5120\n"
    "s_nop 0\n"
    "buffer_load_dwordx4 v13, s[12:15], s40 offen lds\n"
    "s_waitcnt lgkmcnt(0)\n"
    "s_waitcnt vmcnt(24)\n"
    "s_barrier\n"
    "ds_read_b64_tr_b16 a[48:49], v14 offset:49152\n"
    "ds_read_b64_tr_b16 a[50:51], v14 offset:50688\n"
    "ds_read_b64_tr_b16 a[56:57], v14 offset:49184\n"
    "ds_read_b64_tr_b16 a[58:59], v14 offset:50720\n"
    "ds_read_b64_tr_b16 a[64:65], v14 offset:49216\n"
    "ds_read_b64_tr_b16 a[66:67], v14 offset:50752\n"
    "ds_read_b64_tr_b16 a[72:73], v14 offset:49248\n"
    "ds_read_b64_tr_b16 a[74:75], v14 offset:50784\n"
    "ds_read_b64_tr_b16 a[80:81], v14 offset:49280\n"
    "ds_read_b64_tr_b16 a[82:83], v14 offset:50816\n"
    "ds_read_b64_tr_b16 a[88:89], v14 offset:49312\n"
    "ds_read_b64_tr_b16 a[90:91], v14 offset:50848\n"
    "s_add_u32 s40, s49, 1179648\n"
    "s_add_u32 m0, s29, 24576\n"
    "s_nop 0\n"
    "buffer_load_dwordx4 v8, s[12:15], s40 offen lds\n"
    "s_add_u32 m0, s29, 25600\n"
    "s_nop 0\n"
    "buffer_load_dwordx4 v9, s[12:15], s40 offen lds\n"
    "s_add_u32 m0, s29, 26624\n"
    "s_nop 0\n"
    "buffer_load_dwordx4 v10, s[12:15], s40 offen lds\n"
    "s_add_u32 m0, s29, 27648\n"
    "s_nop 0\n"
    "buffer_load_dwordx4 v11, s[12:15], s40 offen lds\n"
    "s_add_u32 m0, s29, 28672\n"
    "s_nop 0\n"
    "buffer_load_dwordx4 v12, s[12:15], s40 offen lds\n"
    "s_add_u32 m0, s29, 29696\n"
    "s_nop 0\n"
    "buffer_load_dwordx4 v13, s[12:15], s40 offen lds\n"
    "s_waitcnt lgkmcnt(0)\n"
    "s_waitcnt vmcnt(24)\n"
    "s_barrier\n"
    "ds_read_b64_tr_b16 a[52:53], v15 offset:16384\n"
    "ds_read_b64_tr_b16 a[54:55], v15 offset:17920\n"
    "ds_read_b64_tr_b16 a[60:61], v15 offset:16416\n"
    "ds_read_b64_tr_b16 a[62:63], v15 offset:17952\n"
    "ds_read_b64_tr_b16 a[68:69], v15 offset:16448\n"
    "ds_read_b64_tr_b16 a[70:71], v15 offset:17984\n"
    "ds_read_b64_tr_b16 a[76:77], v15 offset:16480\n"
    "ds_read_b64_tr_b16 a[78:79], v15 offset:18016\n"
    "ds_read_b64_tr_b16 a[84:85], v15 offset:16512\n"
    "ds_read_b64_tr_b16 a[86:87], v15 offset:18048\n"
    "ds_read_b64_tr_b16 a[92:93], v15 offset:16544\n"
    "ds_read_b64_tr_b16 a[94:95], v15 offset:18080\n"
    "s_add_u32 s40, s49, 1572864\n"
    "s_add_u32 m0, s29, 49152\n"
    "s_nop 0\n"
    "buffer_load_dwordx4 v8, s[12:15], s40 offen lds\n"
    "s_add_u32 m0, s29, 50176\n"
    "s_nop 0\n"
    "buffer_load_dwordx4 v9, s[12:15], s40 offen lds\n"
    "s_add_u32 m0, s29, 51200\n"
    "s_nop 0\n"
    "buffer_load_dwordx4 v10, s[12:15], s40 offen lds\n"
    "s_add_u32 m0, s29, 52224\n"
    "s_nop 0\n"
    "buffer_load_dwordx4 v11, s[12:15], s40 offen lds\n"
    "s_add_u32 m0, s29, 53248\n"
    "s_nop 0\n"
    "buffer_load_dwordx4 v12, s[12:15], s40 offen lds\n"
    "s_add_u32 m0, s29, 54272\n"
    "s_nop 0\n"
    "buffer_load_dwordx4 v13, s[12:15], s40 offen lds\n"
    "s_waitcnt lgkmcnt(0)\n"
    "s_waitcnt vmcnt(24)\n"
    "s_barrier\n"
    "ds_read_b64_tr_b16 a[184:185], v15 offset:40960\n"
    "ds_read_b64_tr_b16 a[186:187], v15 offset:42496\n"
    "ds_read_b64_tr_b16 a[192:193], v15 offset:40992\n"
    "ds_read_b64_tr_b16 a[194:195], v15 offset:42528\n"
    "ds_read_b64_tr_b16 v[86:87], v15 offset:41024\n"
    "ds_read_b64_tr_b16 v[88:89], v15 offset:42560\n"
    "ds_read_b64_tr_b16 a[200:201], v15 offset:41056\n"
    "ds_read_b64_tr_b16 a[202:203], v15 offset:42592\n"
    "ds_read_b64_tr_b16 a[208:209], v15 offset:41088\n"
    "ds_read_b64_tr_b16 a[210:211], v15 offset:42624\n"
    "ds_read_b64_tr_b16 a[216:217], v15 offset:41120\n"
    "ds_read_b64_tr_b16 a[218:219], v15 offset:42656\n"
    "s_add_u32 s40, s49, 1966080\n"
    "s_add_u32 m0, s29, 73728\n"
    "s_nop 0\n"
    "buffer_load_dwordx4 v8, s[12:15], s40 offen lds\n"
    "s_add_u32 m0, s29, 74752\n"
    "s_nop 0\n"
    "buffer_load_dwordx4 v9, s[12:15], s40 offen lds\n"
    "s_add_u32 m0, s29, 75776\n"
    "s_nop 0\n"
    "buffer_load_dwordx4 v10, s[12:15], s40 offen lds\n"
    "s_add_u32 m0, s29, 76800\n"
    "s_nop 0\n"
    "buffer_load_dwordx4 v11, s[12:15], s40 offen lds\n"
    "s_add_u32 m0, s29, 77824\n"
    "s_nop 0\n"
    "buffer_load_dwordx4 v12, s[12:15], s40 offen lds\n"
    "s_add_u32 m0, s29, 78848\n"
    "s_nop 0\n"
    "buffer_load_dwordx4 v13, s[12:15], s40 offen lds\n"
    "s_waitcnt lgkmcnt(0)\n"
    "s_add_u32 s4, s3, 1\n"
    "v_mov_b32 v2, s3\n"
    "s_or_b32 s41, s21, s6\n"
    "s_cbranch_scc1 .Lcl_24\n"
    "s_lshl_b32 s41, s5, 2\n"
    "s_add_u32 s41, s41, 4178944\n"
    "v_mov_b32 v3, s41\n"
    "v_mov_b32 v2, s4\n"
    "s_mov_b64 exec, 1\n"
    "buffer_atomic_swap v2, v3, s[8:11], 0 offen sc0 sc1\n"
    "s_mov_b64 exec, -1\n"
    ".Lcl_24:\n"
    "v_min_u32 v6, 54, v1\n"
    "v_lshlrev_b32 v6, 7, v6\n"
    "v_add_u32 v6, 4182016, v6\n"
    "buffer_load_dwordx2 v[4:5], v6, s[8:11], 0 offen sc1\n"
    "s_waitcnt lgkmcnt(0)\n"
    "s_waitcnt vmcnt(25)\n"
    "s_barrier\n"
    "ds_read_b64_tr_b16 a[188:189], v16 offset:8192\n"
    "ds_read_b64_tr_b16 a[190:191], v16 offset:9728\n"
    "ds_read_b64_tr_b16 a[196:197], v16 offset:8224\n"
    "ds_read_b64_tr_b16 a[198:199], v16 offset:9760\n"
    "ds_read_b64_tr_b16 v[90:91], v16 offset:8256\n"
    "ds_read_b64_tr_b16 v[92:93], v16 offset:9792\n"
    "ds_read_b64_tr_b16 a[204:205], v16 offset:8288\n"
    "ds_read_b64_tr_b16 a[206:207], v16 offset:9824\n"
    "ds_read_b64_tr_b16 a[212:213], v16 offset:8320\n"
    "ds_read_b64_tr_b16 a[214:215], v16 offset:9856\n"
    "ds_read_b64_tr_b16 a[220:221], v16 offset:8352\n"
    "ds_read_b64_tr_b16 a[222:223], v16 offset:9888\n"
    "s_add_u32 s40, s49, 2359296\n"
    "s_add_u32 m0, s29, 98304\n"
    "s_nop 0\n"
    "buffer_load_dwordx4 v8, s[12:15], s40 offen lds\n"
    "s_add_u32 m0, s29, 99328\n"
    "s_nop 0\n"
    "buffer_load_dwordx4 v9, s[12:15], s40 offen lds\n"
    "s_add_u32 m0, s29, 100352\n"
    "s_nop 0\n"
    "buffer_load_dwordx4 v10, s[12:15], s40 offen lds\n"
    "s_add_u32 m0, s29, 101376\n"
    "s_nop 0\n"
    "buffer_load_dwordx4 v11, s[12:15], s40 offen lds\n"
    "s_add_u32 m0, s29, 102400\n"
    "s_nop 0\n"
    "buffer_load_dwordx4 v12, s[12:15], s40 offen lds\n"
    "s_add_u32 m0, s29, 103424\n"
    "s_nop 0\n"
    "buffer_load_dwordx4 v13, s[12:15], s40 offen lds\n"
    "s_waitcnt lgkmcnt(0)\n"
    "s_waitcnt vmcnt(25)\n"
    "s_barrier\n"
    "ds_read_b64_tr_b16 a[224:225], v14 offset:0\n"
    "ds_read_b64_tr_b16 a[226:227], v14 offset:1536\n"
    "ds_read_b64_tr_b16 a[232:233], v14 offset:32\n"
    "ds_read_b64_tr_b16 a[234:235], v14 offset:1568\n"
    "ds_read_b64_tr_b16 v[94:95], v14 offset:64\n"
    "ds_read_b64_tr_b16 v[96:97], v14 offset:1600\n"
    "ds_read_b64_tr_b16 a[240:241], v14 offset:96\n"
    "ds_read_b64_tr_b16 a[242:243], v14 offset:1632\n"
    "ds_read_b64_tr_b16 a[248:249], v14 offset:128\n"
    "ds_read_b64_tr_b16 a[250:251], v14 offset:1664\n"
    "ds_read_b64_tr_b16 v[120:121], v14 offset:160\n"
    "ds_read_b64_tr_b16 v[122:123], v14 offset:1696\n"
    "s_add_u32 s40, s49, 2752512\n"
    "s_add_u32 m0, s29, 122880\n"
    "s_nop 0\n"
    "buffer_load_dwordx4 v8, s[12:15], s40 offen lds\n"
    "s_add_u32 m0, s29, 123904\n"
    "s_nop 0\n"
    "buffer_load_dwordx4 v9, s[12:15], s40 offen lds\n"
    "s_add_u32 m0, s29, 124928\n"
    "s_nop 0\n"
    "buffer_load_dwordx4 v10, s[12:15], s40 offen lds\n"
    "s_add_u32 m0, s29, 125952\n"
    "s_nop 0\n"
    "buffer_load_dwordx4 v11, s[12:15], s40 offen lds\n"
    "s_add_u32 m0, s29, 126976\n"
    "s_nop 0\n"
    "buffer_load_dwordx4 v12, s[12:15], s40 offen lds\n"
    "s_add_u32 m0, s29, 128000\n"
    "s_nop 0\n"
    "buffer_load_dwordx4 v13, s[12:15], s40 offen lds\n"
    "s_waitcnt lgkmcnt(0)\n"
    "s_waitcnt vmcnt(25)\n"
    "s_barrier\n"
    "ds_read_b64_tr_b16 a[228:229], v14 offset:24576\n"
    "ds_read_b64_tr_b16 a[230:231], v14 offset:26112\n"
    "ds_read_b64_tr_b16 a[236:237], v14 offset:24608\n"
    "ds_read_b64_tr_b16 a[238:239], v14 offset:26144\n"
    "ds_read_b64_tr_b16 v[98:99], v14 offset:24640\n"
    "ds_read_b64_tr_b16 v[100:101], v14 offset:26176\n"
    "ds_read_b64_tr_b16 a[244:245], v14 offset:24672\n"
    "ds_read_b64_tr_b16 a[246:247], v14 offset:26208\n"
    "ds_read_b64_tr_b16 a[252:253], v14 offset:24704\n"
    "ds_read_b64_tr_b16 a[254:255], v14 offset:26240\n"
    "ds_read_b64_tr_b16 v[124:125], v14 offset:24736\n"
    "ds_read_b64_tr_b16 v[126:127], v14 offset:26272\n"
    "s_add_u32 s40, s49, 3145728\n"
    "s_add_u32 m0, s29, 0\n"
    "s_nop 0\n"
    "buffer_load_dwordx4 v8, s[12:15], s40 offen lds\n"
    "s_add_u32 m0, s29, 1024\n"
    "s_nop 0\n"
    "buffer_load_dwordx4 v9, s[12:15], s40 offen lds\n"
    "s_add_u32 m0, s29, 2048\n"
    "s_nop 0\n"
    "buffer_load_dwordx4 v10, s[12:15], s40 offen lds\n"
    "s_add_u32 m0, s29, 3072\n"
    "s_nop 0\n"
    "buffer_load_dwordx4 v11, s[12:15], s40 offen lds\n"
    "s_add_u32 m0, s29, 4096\n"
    "s_nop 0\n"
    "buffer_load_dwordx4 v12, s[12:15], s40 offen lds\n"
    "s_add_u32 m0, s29, 5120\n"
    "s_nop 0\n"
    "buffer_load_dwordx4 v13, s[12:15], s40 offen lds\n"
    "s_waitcnt lgkmcnt(0)\n"
    "s_waitcnt vmcnt(25)\n"
    "s_barrier\n"
    "ds_read_b64_tr_b16 v[128:129], v14 offset:49152\n"
    "ds_read_b64_tr_b16 v[130:131], v14 offset:50688\n"
    "ds_read_b64_tr_b16 v[136:137], v14 offset:49184\n"
    "ds_read_b64_tr_b16 v[138:139], v14 offset:50720\n"
    "ds_read_b64_tr_b16 v[102:103], v14 offset:49216\n"
    "ds_read_b64_tr_b16 v[104:105], v14 offset:50752\n"
    "ds_read_b64_tr_b16 v[144:145], v14 offset:49248\n"
    "ds_read_b64_tr_b16 v[146:147], v14 offset:50784\n"
    "ds_read_b64_tr_b16 v[152:153], v14 offset:49280\n"
    "ds_read_b64_tr_b16 v[154:155], v14 offset:50816\n"
    "ds_read_b64_tr_b16 v[160:161], v14 offset:49312\n"
    "ds_read_b64_tr_b16 v[162:163], v14 offset:50848\n"
    "s_add_u32 s40, s49, 3538944\n"
    "s_add_u32 m0, s29, 24576\n"
    "s_nop 0\n"
    "buffer_load_dwordx4 v8, s[12:15], s40 offen lds\n"
    "s_add_u32 m0, s29, 25600\n"
    "s_nop 0\n"
    "buffer_load_dwordx4 v9, s[12:15], s40 offen lds\n"
    "s_add_u32 m0, s29, 26624\n"
    "s_nop 0\n"
    "buffer_load_dwordx4 v10, s[12:15], s40 offen lds\n"
    "s_add_u32 m0, s29, 27648\n"
    "s_nop 0\n"
    "buffer_load_dwordx4 v11, s[12:15], s40 offen lds\n"
    "s_add_u32 m0, s29, 28672\n"
    "s_nop 0\n"
    "buffer_load_dwordx4 v12, s[12:15], s40 offen lds\n"
    "s_add_u32 m0, s29, 29696\n"
    "s_nop 0\n"
    "buffer_load_dwordx4 v13, s[12:15], s40 offen lds\n"
    "s_waitcnt lgkmcnt(0)\n"
    "s_waitcnt vmcnt(25)\n"
    "s_barrier\n"
    "ds_read_b64_tr_b16 v[132:133], v15 offset:16384\n"
    "ds_read_b64_tr_b16 v[134:135], v15 offset:17920\n"
    "ds_read_b64_tr_b16 v[140:141], v15 offset:16416\n"
    "ds_read_b64_tr_b16 v[142:143], v15 offset:17952\n"
    "ds_read_b64_tr_b16 v[106:107], v15 offset:16448\n"
    "ds_read_b64_tr_b16 v[108:109], v15 offset:17984\n"
    "ds_read_b64_tr_b16 v[148:149], v15 offset:16480\n"
    "ds_read_b64_tr_b16 v[150:151], v15 offset:18016\n"
    "ds_read_b64_tr_b16 v[156:157], v15 offset:16512\n"
    "ds_read_b64_tr_b16 v[158:159], v15 offset:18048\n"
    "ds_read_b64_tr_b16 v[164:165], v15 offset:16544\n"
    "ds_read_b64_tr_b16 v[166:167], v15 offset:18080\n"
    "s_waitcnt lgkmcnt(0)\n"
    "s_waitcnt vmcnt(18)\n"
    "s_barrier\n"
    "ds_read_b64_tr_b16 v[168:169], v15 offset:40960\n"
    "ds_read_b64_tr_b16 v[170:171], v15 offset:42496\n"
    "ds_read_b64_tr_b16 v[176:177], v15 offset:40992\n"
    "ds_read_b64_tr_b16 v[178:179], v15 offset:42528\n"
    "ds_read_b64_tr_b16 v[184:185], v15 offset:41024\n"
    "ds_read_b64_tr_b16 v[186:187], v15 offset:42560\n"
    "ds_read_b64_tr_b16 v[192:193], v15 offset:41056\n"
    "ds_read_b64_tr_b16 v[194:195], v15 offset:42592\n"
    "ds_read_b64_tr_b16 v[200:201], v15 offset:41088\n"
    "ds_read_b64_tr_b16 v[202:203], v15 offset:42624\n"
    "ds_read_b64_tr_b16 v[208:209], v15 offset:41120\n"
    "ds_read_b64_tr_b16 v[210:211], v15 offset:42656\n"
    "s_waitcnt lgkmcnt(0)\n"
    "s_waitcnt vmcnt(12)\n"
    "s_barrier\n"
    "ds_read_b64_tr_b16 v[172:173], v16 offset:8192\n"
    "ds_read_b64_tr_b16 v[174:175], v16 offset:9728\n"
    "ds_read_b64_tr_b16 v[180:181], v16 offset:8224\n"
    "ds_read_b64_tr_b16 v[182:183], v16 offset:9760\n"
    "ds_read_b64_tr_b16 v[188:189], v16 offset:8256\n"
    "ds_read_b64_tr_b16 v[190:191], v16 offset:9792\n"
    "ds_read_b64_tr_b16 v[196:197], v16 offset:8288\n"
    "ds_read_b64_tr_b16 v[198:199], v16 offset:9824\n"
    "ds_read_b64_tr_b16 v[204:205], v16 offset:8320\n"
    "ds_read_b64_tr_b16 v[206:207], v16 offset:9856\n"
    "ds_read_b64_tr_b16 v[212:213], v16 offset:8352\n"
    "ds_read_b64_tr_b16 v[214:215], v16 offset:9888\n"
    "s_waitcnt lgkmcnt(0)\n"
    "s_waitcnt vmcnt(6)\n"
    "s_barrier\n"
    "ds_read_b64_tr_b16 v[216:217], v14 offset:0\n"
    "ds_read_b64_tr_b16 v[218:219], v14 offset:1536\n"
    "ds_read_b64_tr_b16 v[70:71], v14 offset:32\n"
    "ds_read_b64_tr_b16 v[72:73], v14 offset:1568\n"
    "ds_read_b64_tr_b16 v[224:225], v14 offset:64\n"
    "ds_read_b64_tr_b16 v[226:227], v14 offset:1600\n"
    "ds_read_b64_tr_b16 v[232:233], v14 offset:96\n"
    "ds_read_b64_tr_b16 v[234:235], v14 offset:1632\n"
    "ds_read_b64_tr_b16 v[240:241], v14 offset:128\n"
    "ds_read_b64_tr_b16 v[242:243], v14 offset:1664\n"
    "ds_read_b64_tr_b16 v[248:249], v14 offset:160\n"
    "ds_read_b64_tr_b16 v[250:251], v14 offset:1696\n"
    "s_waitcnt lgkmcnt(0)\n"
    "s_waitcnt vmcnt(0)\n"
    "s_barrier\n"
    "ds_read_b64_tr_b16 v[220:221], v14 offset:24576\n"
    "ds_read_b64_tr_b16 v[222:223], v14 offset:26112\n"
    "ds_read_b64_tr_b16 v[74:75], v14 offset:24608\n"
    "ds_read_b64_tr_b16 v[76:77], v14 offset:26144\n"
    "ds_read_b64_tr_b16 v[228:229], v14 offset:24640\n"
    "ds_read_b64_tr_b16 v[230:231], v14 offset:26176\n"
    "ds_read_b64_tr_b16 v[236:237], v14 offset:24672\n"
    "ds_read_b64_tr_b16 v[238:239], v14 offset:26208\n"
    "ds_read_b64_tr_b16 v[244:245], v14 offset:24704\n"
    "ds_read_b64_tr_b16 v[246:247], v14 offset:26240\n"
    "ds_read_b64_tr_b16 v[252:253], v14 offset:24736\n"
    "ds_read_b64_tr_b16 v[254:255], v14 offset:26272\n"
    "s_waitcnt lgkmcnt(0)\n"
    "v_cmp_ne_u32 vcc, s4, v4\n"
    "s_nop 1\n"
    "s_cmp_eq_u32 vcc_lo, 0\n"
    "s_cbranch_scc1 .Lrx_27\n"
    ".Lrp_25:\n"
    "v_min_u32 v6, 54, v1\n"
    "v_lshlrev_b32 v6, 7, v6\n"
    "v_add_u32 v6, 4182016, v6\n"
    "buffer_load_dwordx2 v[4:5], v6, s[8:11], 0 offen sc1\n"
    "s_waitcnt vmcnt(0)\n"
    "v_cmp_ne_u32 vcc, s4, v4\n"
    "s_nop 1\n"
    "s_cmp_eq_u32 vcc_lo, 0\n"
    "s_cbranch_scc1 .Lro_26\n"
    "s_sleep 2\n"
    "s_sub_u32 s25, s25, 1\n"
    "s_cbranch_scc0 .Lrp_25\n"
    "s_branch .Lfail\n"
    ".Lro_26:\n"
    ".Lrx_27:\n"
    "v_cmp_ne_u32 vcc, s4, v4\n"
    "s_nop 1\n"
    "s_cmp_eq_u32 vcc_hi, 0\n"
    "s_cbranch_scc1 .Lrx_30\n"
    ".Lrp_28:\n"
    "v_min_u32 v6, 54, v1\n"
    "v_lshlrev_b32 v6, 7, v6\n"
    "v_add_u32 v6, 4182016, v6\n"
    "buffer_load_dwordx2 v[4:5], v6, s[8:11], 0 offen sc1\n"
    "s_waitcnt vmcnt(0)\n"
    "v_cmp_ne_u32 vcc, s4, v4\n"
    "s_nop 1\n"
    "s_cmp_eq_u32 vcc_hi, 0\n"
    "s_cbranch_scc1 .Lro_29\n"
    "s_sleep 2\n"
    "s_sub_u32 s25, s25, 1\n"
    "s_cbranch_scc0 .Lrp_28\n"
    "s_branch .Lfail\n"
    ".Lro_29:\n"
    ".Lrx_30:\n"
    "v_cmp_ne_u32 vcc, 0, v5\n"
    "s_nop 1\n"
    "s_and_b32 s22, vcc_hi, 1\n"
    "s_lshl_b32 s22, s22, 13\n"
    "s_lshr_b32 s23, vcc_hi, 1\n"
    ".Lpw_14:\n"
    "s_cmp_lg_u32 s21, 0\n"
    "s_cbranch_scc1 .Lgt_31\n"
    "v_mov_b32 v3, 159744\n"
    "v_readfirstlane_b32 s40, v2\n"
    "s_nop 1\n"
    "v_mov_b32 v2, s40\n"
    "ds_write_b32 v3, v2\n"
    "s_waitcnt lgkmcnt(0)\n"
    ".Lgt_31:\n"
    "s_barrier\n"
    "v_mov_b32 v3, 159744\n"
    "ds_read_b32 v2, v3\n"
    "s_waitcnt lgkmcnt(0)\n"
    "v_readfirstlane_b32 s40, v2\n"
    "s_barrier\n"
    "s_cmp_eq_u32 s40, s4\n"
    "s_cbranch_scc1 .Llost\n"
    "s_lshr_b32 s40, s21, 1\n"
    "s_lshl_b32 s40, s40, 14\n"
    "s_and_b32 s41, s21, 1\n"
    "s_lshl_b32 s41, s41, 11\n"
    "s_add_u32 s40, s40, s41\n"
    "s_add_u32 s40, s40, s22\n"
    "s_lshr_b32 s41, s5, 5\n"
    "s_and_b32 s41, s41, 1\n"
    "s_mul_i32 s41, s41, 524288\n"
    "s_add_u32 s40, s40, s41\n"
    "v_lshl_add_u32 v3, v1, 4, s40\n"
    "s_lshl_b32 s40, s26, 10\n"
    "s_add_u32 s40, s40, 139264\n"
    "v_lshl_add_u32 v4, v1, 4, s40\n"
    "s_lshl_b32 s40, s27, 10\n"
    "s_add_u32 s40, s40, 139264\n"
    "v_lshl_add_u32 v5, v1, 4, s40\n"
    "s_lshl_b32 s40, s28, 10\n"
    "s_add_u32 s40, s40, 139264\n"
    "v_lshl_add_u32 v6, v1, 4, s40\n"
    "s_mul_i32 s40, s21, 22528\n"
    "v_lshl_add_u32 v7, v1, 4, s40\n"
    "ds_write_b128 v7, v[22:25] offset:0\n"
    "ds_write_b128 v7, v[26:29] offset:1024\n"
    "ds_write_b128 v7, v[30:33] offset:2048\n"
    "ds_write_b128 v7, v[34:37] offset:3072\n"
    "ds_write_b128 v7, v[38:41] offset:4096\n"
    "ds_write_b128 v7, v[42:45] offset:5120\n"
    "ds_write_b128 v7, v[46:49] offset:6144\n"
    "ds_write_b128 v7, v[50:53] offset:7168\n"
    "ds_write_b128 v7, v[54:57] offset:8192\n"
    "ds_write_b128 v7, v[58:61] offset:9216\n"
    "ds_write_b128 v7, v[62:65] offset:10240\n"
    "ds_write_b128 v7, v[66:69] offset:11264\n"
    "ds_write_b128 v7, v[70:73] offset:12288\n"
    "ds_write_b128 v7, v[74:77] offset:13312\n"
    "ds_write_b128 v7, v[78:81] offset:14336\n"
    "ds_write_b128 v7, v[82:85] offset:15360\n"
    "ds_write_b128 v7, v[86:89] offset:16384\n"
    "ds_write_b128 v7, v[90:93] offset:17408\n"
    "ds_write_b128 v7, v[94:97] offset:18432\n"
    "ds_write_b128 v7, v[98:101] offset:19456\n"
    "ds_write_b128 v7, v[102:105] offset:20480\n"
    "ds_write_b128 v7, v[106:109] offset:21504\n"
    "v_and_b32 v0, 2, v1\n"
    "v_cmp_ne_u32 vcc, 0, v0\n"
    "v_mov_b32 v2, 0x4444\n"
    "v_mov_b32 v0, 0xeeee\n"
    "v_cndmask_b32 v2, v2, v0, vcc\n"
    "s_mov_b32 s56, 0x3dd53b95\n"
    "s_mov_b32 s57, 0x3dd53b95\n"
    "s_and_b32 s40, s21, 1\n"
    "s_cmp_lt_u32 s21, 2\n"
    "s_cbranch_scc1 .LSq_32\n"
    "v_bfe_u32 v8, v1, 2, 1\n"
    "v_lshlrev_b32 v8, 8, v8\n"
    "v_bfe_u32 v0, v1, 3, 1\n"
    "v_lshl_add_u32 v8, v0, 6, v8\n"
    "v_bfe_u32 v0, v1, 0, 2\n"
    "v_lshl_add_u32 v8, v0, 4, v8\n"
    "v_bfe_u32 v0, v1, 4, 2\n"
    "v_lshl_add_u32 v8, v0, 2, v8\n"
    "s_lshl_b32 s40, s40, 9\n"
    "s_add_u32 s40, s40, 152576\n"
    "s_branch .LSk_33\n"
    ".LSq_32:\n"
    "v_bfe_u32 v8, v1, 3, 1\n"
    "v_lshlrev_b32 v8, 9, v8\n"
    "v_bfe_u32 v0, v1, 2, 1\n"
    "v_lshl_add_u32 v8, v0, 6, v8\n"
    "v_bfe_u32 v0, v1, 0, 1\n"
    "v_lshl_add_u32 v8, v0, 5, v8\n"
    "v_bfe_u32 v0, v1, 4, 2\n"
    "v_lshl_add_u32 v8, v0, 3, v8\n"
    "v_bfe_u32 v0, v1, 1, 1\n"
    "v_lshl_add_u32 v8, v0, 2, v8\n"
    "s_lshl_b32 s40, s40, 8\n"
    "s_add_u32 s40, s40, 149504\n"
    ".LSk_33:\n"
    "v_add_u32 v8, s40, v8\n"
    "v_lshlrev_b32 v9, 3, v1\n"
    "v_add_u32 v9, 149504, v9\n"
    "v_bfe_u32 v10, v1, 2, 4\n"
    "v_lshlrev_b32 v10, 4, v10\n"
    "v_bfe_u32 v0, v1, 0, 1\n"
    "v_lshl_add_u32 v10, v0, 3, v10\n"
    "v_add_u32 v10, 152576, v10\n"
    "s_mov_b32 s48, 0xff00ff00\n"
    "s_mov_b32 s50, 0xaaaaaaaa\n"
    "s_mov_b32 s49, 0xff00ff00\n"
    "s_mov_b32 s51, 0xaaaaaaaa\n"
    "s_lshr_b32 s40, s20, 4\n"
    "s_lshl_b32 s40, s40, 14\n"
    "s_and_b32 s41, s20, 15\n"
    "s_lshl_b32 s41, s41, 9\n"
    "s_add_u32 s40, s40, s41\n"
    "s_add_u32 s42, s40, s22\n"
    "s_lshl_b32 s43, s20, 7\n"
    "s_add_u32 s43, s43, s22\n"
    "s_add_u32 s43, s43, 32256\n"
    "v_lshl_add_u32 v16, v1, 4, s42\n"
    "v_lshl_add_u32 v0, v1, 4, s43\n"
    "v_cmp_lt_u32 vcc, 31, v1\n"
    "s_nop 1\n"
    "v_cndmask_b32 v16, v16, v0, vcc\n"
    "v_cmp_lt_u32 vcc, 39, v1\n"
    "v_mov_b32 v0, 0x7ff00000\n"
    "s_nop 0\n"
    "v_cndmask_b32 v16, v16, v0, vcc\n"
    "s_cmp_eq_u32 s21, 0\n"
    "s_nop 0\n"
    "s_cbranch_scc1 .LSw0_34\n"
    "v_lshlrev_b32 v11, 4, v1\n"
    "v_add_u32 v11, 102400, v11\n"
    "v_add_u32 v12, 1024, v11\n"
    "v_xor_b32 v12, 16, v12\n"
    "s_sub_u32 s40, s21, 1\n"
    "s_and_b32 s40, s40, 1\n"
    "s_lshl_b32 s41, s40, 10\n"
    "s_add_u32 s41, s41, 90112\n"
    "v_xor_b32 v0, s40, v1\n"
    "v_lshl_add_u32 v13, v0, 4, s41\n"
    "s_lshl_b32 s41, s40, 5\n"
    "s_add_u32 s41, s41, s20\n"
    "s_lshl_b32 s42, s41, 10\n"
    "v_lshl_add_u32 v14, v1, 4, s42\n"
    "s_lshl_b32 s42, s41, 8\n"
    "v_bfe_u32 v15, v1, 0, 4\n"
    "v_lshlrev_b32 v15, 4, v15\n"
    "v_bfe_u32 v0, v1, 4, 1\n"
    "v_lshl_add_u32 v15, v0, 3, v15\n"
    "v_add_u32 v15, s42, v15\n"
    "v_cmp_lt_u32 s[80:81], 31, v1\n"
    "v_mov_b32 v0, 0x7ff00000\n"
    "s_nop 0\n"
    "v_cndmask_b32 v15, v15, v0, s[80:81]\n"
    "s_lshl_b32 s42, s41, 7\n"
    "s_add_u32 s42, s42, 4259840\n"
    "v_mov_b32 v17, s42\n"
    "s_lshl_b32 s41, s40, 4\n"
    "v_and_b32 v119, 15, v1\n"
    "v_add_u32 v119, s41, v119\n"
    "v_bfe_u32 v118, v1, 4, 2\n"
    "v_lshlrev_b32 v118, 2, v118\n"
    "v_add_u32 v0, 0, v118\n"
    "v_cmp_gt_u32 s[64:65], v0, v119\n"
    "v_add_u32 v0, 1, v118\n"
    "v_cmp_gt_u32 s[66:67], v0, v119\n"
    "v_add_u32 v0, 2, v118\n"
    "v_cmp_gt_u32 s[68:69], v0, v119\n"
    "v_add_u32 v0, 3, v118\n"
    "v_cmp_gt_u32 s[70:71], v0, v119\n"
    "v_add_u32 v0, 16, v118\n"
    "v_cmp_gt_u32 s[72:73], v0, v119\n"
    "v_add_u32 v0, 17, v118\n"
    "v_cmp_gt_u32 s[74:75], v0, v119\n"
    "v_add_u32 v0, 18, v118\n"
    "v_cmp_gt_u32 s[76:77], v0, v119\n"
    "v_add_u32 v0, 19, v118\n"
    "v_cmp_gt_u32 s[78:79], v0, v119\n"
    "s_cmp_lg_u32 s21, 3\n"
    "s_nop 3\n"
    "s_cbranch_scc1 .LSwd_35\n"
    "v_min_u32 v0, 5, v1\n"
    "s_mul_i32 s40, s20, 768\n"
    "s_add_u32 s40, s40, 4196352\n"
    "v_lshl_add_u32 v13, v0, 7, s40\n"
    "s_add_u32 s40, s22, 294912\n"
    "v_and_b32 v0, 31, v1\n"
    "v_lshl_add_u32 v15, v0, 7, s40\n"
    "v_mov_b32 v11, 90112\n"
    "v_mov_b32 v12, 90112\n"
    "v_mov_b32 v17, 0x7ff00000\n"
    "s_nop 1\n"
    "s_branch .LSwd_35\n"
    ".LSw0_34:\n"
    "v_bfe_u32 v11, v1, 0, 1\n"
    "v_lshlrev_b32 v11, 10, v11\n"
    "v_bfe_u32 v0, v1, 3, 3\n"
    "v_lshl_add_u32 v11, v0, 7, v11\n"
    "v_bfe_u32 v0, v1, 1, 1\n"
    "v_lshl_add_u32 v11, v0, 6, v11\n"
    "v_bfe_u32 v0, v1, 2, 1\n"
    "v_lshl_add_u32 v11, v0, 5, v11\n"
    "v_bfe_u32 v0, v1, 0, 1\n"
    "v_lshl_add_u32 v11, v0, 4, v11\n"
    "v_add_u32 v11, 102400, v11\n"
    "v_xor_b32 v12, 16, v11\n"
    "v_bfe_u32 v14, v1, 5, 1\n"
    "v_lshlrev_b32 v14, 7, v14\n"
    "v_bfe_u32 v0, v1, 3, 1\n"
    "v_lshl_add_u32 v14, v0, 6, v14\n"
    "v_bfe_u32 v0, v1, 4, 1\n"
    "v_lshl_add_u32 v14, v0, 5, v14\n"
    "v_bfe_u32 v0, v1, 0, 3\n"
    "v_lshl_add_u32 v14, v0, 2, v14\n"
    "v_add_u32 v14, s42, v14\n"
    "v_bfe_u32 v15, v1, 4, 2\n"
    "v_lshlrev_b32 v15, 5, v15\n"
    "v_bfe_u32 v0, v1, 0, 1\n"
    "v_lshl_add_u32 v15, v0, 4, v15\n"
    "v_bfe_u32 v0, v1, 1, 2\n"
    "v_lshl_add_u32 v15, v0, 2, v15\n"
    "v_add_u32 v15, s43, v15\n"
    "v_and_b32 v0, 8, v1\n"
    "v_cmp_ne_u32 vcc, 0, v0\n"
    "v_mov_b32 v0, 0x7ff00000\n"
    "s_nop 0\n"
    "v_cndmask_b32 v15, v15, v0, vcc\n"
    "s_lshl_b32 s40, s20, 7\n"
    "s_add_u32 s40, s40, s22\n"
    "s_add_u32 s40, s40, 294912\n"
    "v_mov_b32 v17, s40\n"
    "s_nop 1\n"
    ".LSwd_35:\n"
    "v_cmp_ne_u32 vcc, 0, v1\n"
    "v_mov_b32 v0, 0x7ff00000\n"
    "s_nop 0\n"
    "v_cndmask_b32 v17, v17, v0, vcc\n"
    "v_lshlrev_b32 v0, 4, v1\n"
    "s_mov_b32 s24, 0\n"
    "s_mov_b32 s30, 0\n"
    "s_mov_b32 s31, 98304\n"
    "s_mov_b32 s34, 229376\n"
    "s_mov_b32 s53, 420\n"
    "s_mov_b32 s36, 143654912\n"
    "s_mov_b32 s37, 147849216\n"
    "s_mov_b32 s38, 24576\n"
    "s_mov_b32 s35, 90112\n"
    "s_mul_i32 s39, s20, 24576\n"
    "s_add_u32 s39, s39, 9437184\n"
    "s_mul_i32 s40, s21, 3072\n"
    "s_add_u32 s55, s40, 90112\n"
    "s_add_u32 s29, s40, s39\n"
    "s_waitcnt lgkmcnt(0)\n"
    "s_cmp_lg_u32 s21, 3\n"
    "s_cbranch_scc1 .LS_first\n"
    "s_mov_b32 s41, 1\n"
    ".Lpl_36:\n"
    "buffer_load_dword v118, v13, s[8:11], 0 offen sc1\n"
    "s_waitcnt vmcnt(0)\n"
    "v_cmp_gt_u32 vcc, s41, v118\n"
    "s_nop 1\n"
    "s_cbranch_vccz .Lpd_37\n"
    "s_sleep 1\n"
    "s_sub_u32 s25, s25, 1\n"
    "s_cbranch_scc0 .Lpl_36\n"
    "s_branch .Lfail\n"
    ".Lpd_37:\n"
    "s_add_u32 m0, s35, 0\n"
    "s_add_u32 s40, s39, 0\n"
    "buffer_load_dwordx4 v0, s[8:11], s40 offen offset:0 sc1 lds\n"
    "buffer_load_dwordx4 v0, s[8:11], s40 offen offset:1024 sc1 lds\n"
    "buffer_load_dwordx4 v0, s[8:11], s40 offen offset:2048 sc1 lds\n"
    "buffer_load_dwordx4 v0, s[8:11], s40 offen offset:3072 sc1 lds\n"
    "s_add_u32 m0, s35, 4096\n"
    "s_add_u32 s40, s39, 4096\n"
    "buffer_load_dwordx4 v0, s[8:11], s40 offen offset:0 sc1 lds\n"
    "buffer_load_dwordx4 v0, s[8:11], s40 offen offset:1024 sc1 lds\n"
    "buffer_load_dwordx4 v0, s[8:11], s40 offen offset:2048 sc1 lds\n"
    "buffer_load_dwordx4 v0, s[8:11], s40 offen offset:3072 sc1 lds\n"
    "s_add_u32 m0, s35, 8192\n"
    "s_add_u32 s40, s39, 8192\n"
    "buffer_load_dwordx4 v0, s[8:11], s40 offen offset:0 sc1 lds\n"
    "buffer_load_dwordx4 v0, s[8:11], s40 offen offset:1024 sc1 lds\n"
    "buffer_load_dwordx4 v0, s[8:11], s40 offen offset:2048 sc1 lds\n"
    "buffer_load_dwordx4 v0, s[8:11], s40 offen offset:3072 sc1 lds\n"
    "s_add_u32 m0, s35, 12288\n"
    "s_add_u32 s40, s39, 12288\n"
    "buffer_load_dwordx4 v0, s[8:11], s40 offen offset:0 sc1 lds\n"
    "buffer_load_dwordx4 v0, s[8:11], s40 offen offset:1024 sc1 lds\n"
    "buffer_load_dwordx4 v0, s[8:11], s40 offen offset:2048 sc1 lds\n"
    "buffer_load_dwordx4 v0, s[8:11], s40 offen offset:3072 sc1 lds\n"
    "s_add_u32 m0, s35, 16384\n"
    "s_add_u32 s40, s39, 16384\n"
    "buffer_load_dwordx4 v0, s[8:11], s40 offen offset:0 sc1 lds\n"
    "buffer_load_dwordx4 v0, s[8:11], s40 offen offset:1024 sc1 lds\n"
    "buffer_load_dwordx4 v0, s[8:11], s40 offen offset:2048 sc1 lds\n"
    "buffer_load_dwordx4 v0, s[8:11], s40 offen offset:3072 sc1 lds\n"
    "s_add_u32 m0, s35, 20480\n"
    "s_add_u32 s40, s39, 20480\n"
    "buffer_load_dwordx4 v0, s[8:11], s40 offen offset:0 sc1 lds\n"
    "buffer_load_dwordx4 v0, s[8:11], s40 offen offset:1024 sc1 lds\n"
    "buffer_load_dwordx4 v0, s[8:11], s40 offen offset:2048 sc1 lds\n"
    "buffer_load_dwordx4 v0, s[8:11], s40 offen offset:3072 sc1 lds\n"
    "s_add_u32 s39, s39, 786432\n"
    "s_xor_b32 s35, s35, 40960\n"
    "v_mov_b32 v14, 0\n"
    ".LS_first:\n"
    "s_mov_b32 s82, 40\n"
    "s_mov_b32 s83, 0x100000\n"
    "s_memrealtime s[42:43]\n"
    "s_waitcnt lgkmcnt(0)\n"
    "s_mul_i32 s59, s42, 6\n"
    "s_sub_u32 s59, s59, 0x10000\n"
    ".LS_idle:\n"
    "ds_read_b128 v[70:73], v7 offset:0\n"
    "ds_read_b128 v[74:77], v7 offset:1024\n"
    "ds_read_b128 v[78:81], v7 offset:2048\n"
    "ds_read_b128 v[82:85], v7 offset:3072\n"
    "ds_read_b128 v[86:89], v7 offset:4096\n"
    "ds_read_b128 v[90:93], v7 offset:5120\n"
    "ds_read_b128 v[94:97], v7 offset:6144\n"
    "ds_read_b128 v[98:101], v7 offset:7168\n"
    "ds_read_b128 v[102:105], v7 offset:8192\n"
    "ds_read_b128 v[106:109], v7 offset:9216\n"
    "ds_read_b128 v[110:113], v7 offset:10240\n"
    "ds_read_b128 v[114:117], v7 offset:11264\n"
    "s_and_b32 s33, s24, 2\n"
    "s_lshl_b32 s33, s33, 13\n"
    "s_and_b32 s40, s24, 1\n"
    "s_lshl_b32 s40, s40, 12\n"
    "s_add_u32 s33, s33, s40\n"
    "s_add_u32 s52, s31, 524288\n"
    "s_add_u32 s41, s24, s4\n"
    "v_mov_b32 v119, s41\n"
    "v_mov_b64 v[22:23], 0\n"
    "v_mov_b64 v[24:25], 0\n"
    "v_mov_b64 v[26:27], 0\n"
    "v_mov_b64 v[28:29], 0\n"
    "v_mov_b64 v[30:31], 0\n"
    "v_mov_b64 v[32:33], 0\n"
    "v_mov_b64 v[34:35], 0\n"
    "v_mov_b64 v[36:37], 0\n"
    "v_mov_b64 v[38:39], 0\n"
    "v_mov_b64 v[40:41], 0\n"
    "v_mov_b64 v[42:43], 0\n"
    "v_mov_b64 v[44:45], 0\n"
    "v_mov_b32 v19, 0xff800000\n"
    "v_mov_b64 v[20:21], 0\n"
    "v_cndmask_b32 v18, 0, v19, s[48:49]\n"
    "v_cndmask_b32 v19, 0, v19, s[48:49]\n"
    "s_cmp_lg_u32 s21, 3\n"
    "s_waitcnt lgkmcnt(0)\n"
    "s_cbranch_scc1 .LS_go\n"
    "s_lshr_b32 s41, s82, 0\n"
    "s_add_u32 s41, s41, s59\n"
    "s_add_u32 s41, s41, 394\n"
    ".Lwt_38:\n"
    "s_memrealtime s[42:43]\n"
    "s_waitcnt lgkmcnt(0)\n"
    "s_mul_i32 s42, s42, 6\n"
    "s_sub_u32 s40, s41, s42\n"
    "s_cmp_gt_i32 s40, 120\n"
    "s_cbranch_scc1 .Lwt_38\n"
    "s_cmp_lt_i32 s40, 50\n"
    "s_cbranch_scc1 .Lwd_40\n"
    "s_memrealtime s[12:13]\n"
    "s_nop 2\n"
    "s_memrealtime s[14:15]\n"
    "s_nop 2\n"
    "s_memrealtime s[16:17]\n"
    "s_nop 2\n"
    "s_memrealtime s[18:19]\n"
    "s_nop 2\n"
    "s_memrealtime s[44:45]\n"
    "s_nop 2\n"
    "s_memrealtime s[46:47]\n"
    "s_waitcnt lgkmcnt(0)\n"
    "s_add_u32 s40, s12, s14\n"
    "s_add_u32 s40, s40, s16\n"
    "s_add_u32 s40, s40, s18\n"
    "s_add_u32 s40, s40, s44\n"
    "s_add_u32 s40, s40, s46\n"
    "s_sub_u32 s40, s41, s40\n"
    "s_sub_u32 s40, s40, 56\n"
    "s_max_i32 s40, s40, 0\n"
    "s_min_i32 s40, s40, 88\n"
    "s_lshl_b32 s40, s40, 2\n"
    "s_getpc_b64 s[42:43]\n"
    ".Lwh_39:\n"
    "s_sub_u32 s40, .Lwd_40-.Lwh_39, s40\n"
    "s_add_u32 s42, s42, s40\n"
    "s_addc_u32 s43, s43, 0\n"
    "s_setpc_b64 s[42:43]\n"
    "s_nop 0\n"
    "s_nop 0\n"
    "s_nop 0\n"
    "s_nop 0\n"
    "s_nop 0\n"
    "s_nop 0\n"
    "s_nop 0\n"
    "s_nop 0\n"
    "s_nop 0\n"
    "s_nop 0\n"
    "s_nop 0\n"
    "s_nop 0\n"
    "s_nop 0\n"
    "s_nop 0\n"
    "s_nop 0\n"
    "s_nop 0\n"
    "s_nop 0\n"
    "s_nop 0\n"
    "s_nop 0\n"
    "s_nop 0\n"
    "s_nop 0\n"
    "s_nop 0\n"
    "s_nop 0\n"
    "s_nop 0\n"
    "s_nop 0\n"
    "s_nop 0\n"
    "s_nop 0\n"
    "s_nop 0\n"
    "s_nop 0\n"
    "s_nop 0\n"
    "s_nop 0\n"
    "s_nop 0\n"
    "s_nop 0\n"
    "s_nop 0\n"
    "s_nop 0\n"
    "s_nop 0\n"
    "s_nop 0\n"
    "s_nop 0\n"
    "s_nop 0\n"
    "s_nop 0\n"
    "s_nop 0\n"
    "s_nop 0\n"
    "s_nop 0\n"
    "s_nop 0\n"
    "s_nop 0\n"
    "s_nop 0\n"
    "s_nop 0\n"
    "s_nop 0\n"
    "s_nop 0\n"
    "s_nop 0\n"
    "s_nop 0\n"
    "s_nop 0\n"
    "s_nop 0\n"
    "s_nop 0\n"
    "s_nop 0\n"
    "s_nop 0\n"
    "s_nop 0\n"
    "s_nop 0\n"
    "s_nop 0\n"
    "s_nop 0\n"
    "s_nop 0\n"
    "s_nop 0\n"
    "s_nop 0\n"
    "s_nop 0\n"
    "s_nop 0\n"
    "s_nop 0\n"
    "s_nop 0\n"
    "s_nop 0\n"
    "s_nop 0\n"
    "s_nop 0\n"
    "s_nop 0\n"
    "s_nop 0\n"
    "s_nop 0\n"
    "s_nop 0\n"
    "s_nop 0\n"
    "s_nop 0\n"
    "s_nop 0\n"
    "s_nop 0\n"
    "s_nop 0\n"
    "s_nop 0\n"
    "s_nop 0\n"
    "s_nop 0\n"
    "s_nop 0\n"
    "s_nop 0\n"
    "s_nop 0\n"
    "s_nop 0\n"
    "s_nop 0\n"
    "s_nop 0\n"
    ".Lwd_40:\n"
    ".LS_go:\n"
    "s_barrier\n"
    "buffer_load_dwordx4 v[46:49], v3, s[8:11], s30 offen offset:0 sc1\n"
    "s_nop 9\n"
    "buffer_load_dwordx4 v[50:53], v3, s[8:11], s30 offen offset:1024 sc1\n"
    "s_nop 9\n"
    "buffer_load_dwordx4 v[54:57], v3, s[8:11], s30 offen offset:2048 sc1\n"
    "s_mov_b32 m0, s55\n"
    "s_nop 15\n"
    "s_memtime s[42:43]\n"
    "s_memrealtime s[12:13]\n"
    "s_nop 2\n"
    "s_memrealtime s[14:15]\n"
    "s_nop 2\n"
    "s_memrealtime s[16:17]\n"
    "s_nop 2\n"
    "s_memrealtime s[18:19]\n"
    "s_nop 2\n"
    "s_memrealtime s[44:45]\n"
    "s_nop 2\n"
    "s_memrealtime s[46:47]\n"
    "s_waitcnt lgkmcnt(0)\n"
    "s_add_u32 s54, s12, s14\n"
    "s_add_u32 s54, s54, s16\n"
    "s_add_u32 s54, s54, s18\n"
    "s_add_u32 s54, s54, s44\n"
    "s_add_u32 s54, s54, s46\n"
    ".LS_have:\n"
    "s_waitcnt vmcnt(2)\n"
    "v_smfmac_f32_16x16x64_bf16 v[22:25], v[46:49], v[70:77], v2\n"
    "ds_write_b128 v4, v[46:49]\n"
    "v_smfmac_f32_16x16x64_bf16 v[26:29], v[46:49], v[78:85], v2\n"
    "v_max3_u32 v118, v46, v47, v48\n"
    "v_smfmac_f32_16x16x64_bf16 v[30:33], v[46:49], v[86:93], v2\n"
    "v_max_u32 v118, v118, v49\n"
    "v_smfmac_f32_16x16x64_bf16 v[34:37], v[46:49], v[94:101], v2\n"
    "s_waitcnt vmcnt(1)\n"
    "ds_write_b128 v4, v[50:53] offset:1024\n"
    "v_smfmac_f32_16x16x64_bf16 v[38:41], v[46:49], v[102:109], v2\n"
    "v_max3_u32 v118, v118, v50, v51\n"
    "v_smfmac_f32_16x16x64_bf16 v[42:45], v[46:49], v[110:117], v2\n"
    "v_max3_u32 v118, v118, v52, v53\n"
    "v_smfmac_f32_16x16x64_bf16 v[22:25], v[50:53], a[0:7], v2\n"
    "s_waitcnt vmcnt(0)\n"
    "ds_write_b128 v4, v[54:57] offset:2048\n"
    "v_smfmac_f32_16x16x64_bf16 v[26:29], v[50:53], a[8:15], v2\n"
    "v_max3_u32 v118, v118, v54, v55\n"
    "v_smfmac_f32_16x16x64_bf16 v[30:33], v[50:53], a[16:23], v2\n"
    "v_max3_u32 v118, v118, v56, v57\n"
    "v_smfmac_f32_16x16x64_bf16 v[34:37], v[50:53], a[24:31], v2\n"
    "v_cmp_eq_u32 vcc, -1, v118\n"
    "v_smfmac_f32_16x16x64_bf16 v[38:41], v[50:53], a[32:39], v2\n"
    "v_smfmac_f32_16x16x64_bf16 v[42:45], v[50:53], a[40:47], v2\n"
    "s_cbranch_vccnz .LS_again\n"
    "s_waitcnt lgkmcnt(0)\n"
    "s_barrier\n"
    "ds_read_b128 v[58:61], v5 offset:0\n"
    "s_memtime s[12:13]\n"
    "v_smfmac_f32_16x16x64_bf16 v[22:25], v[54:57], a[48:55], v2\n"
    "ds_read_b128 v[62:65], v5 offset:1024\n"
    "v_smfmac_f32_16x16x64_bf16 v[26:29], v[54:57], a[56:63], v2\n"
    "v_smfmac_f32_16x16x64_bf16 v[30:33], v[54:57], a[64:71], v2\n"
    "v_smfmac_f32_16x16x64_bf16 v[34:37], v[54:57], a[72:79], v2\n"
    "v_smfmac_f32_16x16x64_bf16 v[38:41], v[54:57], a[80:87], v2\n"
    "s_waitcnt lgkmcnt(0)\n"
    "s_sub_u32 s40, s12, s42\n"
    "s_lshr_b32 s40, s40, 2\n"
    "ds_read_b128 v[66:69], v6 offset:0\n"
    "v_smfmac_f32_16x16x64_bf16 v[42:45], v[54:57], a[88:95], v2\n"
    "s_add_u32 s54, s54, s40\n"
    "s_add_u32 s40, s54, s53\n"
    "ds_read_b128 v[46:49], v6 offset:1024\n"
    "v_smfmac_f32_16x16x64_bf16 v[22:25], v[58:61], a[96:103], v2\n"
    "s_cmp_lg_u32 s21, 0\n"
    "v_mov_b32 v118, s40\n"
    "ds_read_b128 v[50:53], v6 offset:2048\n"
    "v_smfmac_f32_16x16x64_bf16 v[26:29], v[58:61], a[104:111], v2\n"
    "ds_read_b128 v[54:57], v6 offset:3072\n"
    "v_smfmac_f32_16x16x64_bf16 v[30:33], v[58:61], a[112:119], v2\n"
    "s_cbranch_scc1 .LS_nohf\n"
    "buffer_store_dwordx2 v[118:119], v17, s[8:11], s33 offen sc1\n"
    ".LS_nohf:\n"
    "v_smfmac_f32_16x16x64_bf16 v[34:37], v[58:61], a[120:127], v2\n"
    "ds_read_b128 v[70:73], v7 offset:12288\n"
    "v_smfmac_f32_16x16x64_bf16 v[38:41], v[58:61], a[128:135], v2\n"
    "ds_read_b128 v[74:77], v7 offset:13312\n"
    "v_smfmac_f32_16x16x64_bf16 v[42:45], v[58:61], a[136:143], v2\n"
    "ds_read_b128 v[58:61], v6 offset:4096\n"
    "v_smfmac_f32_16x16x64_bf16 v[22:25], v[62:65], a[144:151], v2\n"
    "ds_read_b128 v[78:81], v7 offset:14336\n"
    "s_waitcnt lgkmcnt(7)\n"
    "v_smfmac_f32_16x16x64_bf16 v[22:25], v[66:69], a[184:191], v2\n"
    "ds_read_b128 v[82:85], v7 offset:15360\n"
    "s_waitcnt lgkmcnt(7)\n"
    "v_smfmac_f32_16x16x64_bf16 v[22:25], v[46:49], a[224:231], v2\n"
    "ds_read_b128 v[86:89], v7 offset:16384\n"
    "s_waitcnt lgkmcnt(7)\n"
    "v_smfmac_f32_16x16x64_bf16 v[22:25], v[50:53], v[128:135], v2\n"
    "ds_read_b128 v[90:93], v7 offset:17408\n"
    "s_waitcnt lgkmcnt(7)\n"
    "v_smfmac_f32_16x16x64_bf16 v[22:25], v[54:57], v[168:175], v2\n"
    "ds_read_b128 v[94:97], v7 offset:18432\n"
    "s_waitcnt lgkmcnt(5)\n"
    "v_smfmac_f32_16x16x64_bf16 v[22:25], v[58:61], v[216:223], v2\n"
    "ds_read_b128 v[98:101], v7 offset:19456\n"
    "v_smfmac_f32_16x16x64_bf16 v[26:29], v[62:65], a[152:159], v2\n"
    "ds_read_b128 v[102:105], v7 offset:20480\n"
    "v_smfmac_f32_16x16x64_bf16 v[26:29], v[66:69], a[192:199], v2\n"
    "ds_read_b128 v[106:109], v7 offset:21504\n"
    "v_mov_b64 v[110:111], 0\n"
    "v_smfmac_f32_16x16x64_bf16 v[26:29], v[46:49], a[232:239], v2\n"
    "v_mov_b64 v[112:113], 0\n"
    "v_smfmac_f32_16x16x64_bf16 v[26:29], v[50:53], v[136:143], v2\n"
    "v_mov_b64 v[114:115], 0\n"
    "v_smfmac_f32_16x16x64_bf16 v[26:29], v[54:57], v[176:183], v2\n"
    "v_mov_b64 v[116:117], 0\n"
    "v_smfmac_f32_16x16x64_bf16 v[26:29], v[58:61], v[70:77], v2\n"
    "s_waitcnt lgkmcnt(6)\n"
    "v_smfmac_f32_16x16x64_bf16 v[30:33], v[62:65], v[78:85], v2\n"
    "v_add_f32 v22, v22, v24\n"
    "s_waitcnt lgkmcnt(4)\n"
    "v_smfmac_f32_16x16x64_bf16 v[30:33], v[66:69], v[86:93], v2\n"
    "ds_read_b128 v[70:73], v11 offset:0\n"
    "v_add_f32 v23, v23, v25\n"
    "s_waitcnt lgkmcnt(3)\n"
    "v_smfmac_f32_16x16x64_bf16 v[30:33], v[46:49], v[94:101], v2\n"
    "ds_read_b128 v[74:77], v11 offset:6144\n"
    "v_cvt_pk_bf16_f32 v22, v22, v23\n"
    "s_waitcnt lgkmcnt(2)\n"
    "v_smfmac_f32_16x16x64_bf16 v[30:33], v[50:53], v[102:109], v2\n"
    "ds_write_b32 v8, v22 offset:0\n"
    "v_smfmac_f32_16x16x64_bf16 v[30:33], v[54:57], v[184:191], v2\n"
    "ds_read_b128 v[78:81], v12 offset:0\n"
    "v_smfmac_f32_16x16x64_bf16 v[30:33], v[58:61], v[224:231], v2\n"
    "v_add_f32 v26, v26, v28\n"
    "v_smfmac_f32_16x16x64_bf16 v[34:37], v[62:65], a[160:167], v2\n"
    "ds_read_b128 v[82:85], v12 offset:6144\n"
    "v_add_f32 v27, v27, v29\n"
    "v_smfmac_f32_16x16x64_bf16 v[34:37], v[66:69], a[200:207], v2\n"
    "v_cvt_pk_bf16_f32 v26, v26, v27\n"
    "v_smfmac_f32_16x16x64_bf16 v[34:37], v[46:49], a[240:247], v2\n"
    "ds_write_b32 v8, v26 offset:128\n"
    "v_smfmac_f32_16x16x64_bf16 v[34:37], v[50:53], v[144:151], v2\n"
    "ds_read_b128 v[86:89], v11 offset:2048\n"
    "v_smfmac_f32_16x16x64_bf16 v[34:37], v[54:57], v[192:199], v2\n"
    "v_add_f32 v30, v30, v32\n"
    "v_smfmac_f32_16x16x64_bf16 v[34:37], v[58:61], v[232:239], v2\n"
    "ds_read_b128 v[90:93], v11 offset:8192\n"
    "v_add_f32 v31, v31, v33\n"
    "v_smfmac_f32_16x16x64_bf16 v[38:41], v[62:65], a[168:175], v2\n"
    "v_cvt_pk_bf16_f32 v30, v30, v31\n"
    "s_nop 0\n"
    "v_smfmac_f32_16x16x64_bf16 v[38:41], v[66:69], a[208:215], v2\n"
    "ds_write_b32 v8, v30 offset:1024\n"
    "s_nop 0\n"
    "v_smfmac_f32_16x16x64_bf16 v[38:41], v[46:49], a[248:255], v2\n"
    "ds_read_b128 v[94:97], v12 offset:2048\n"
    "v_add_f32 v34, v34, v36\n"
    "v_smfmac_f32_16x16x64_bf16 v[38:41], v[50:53], v[152:159], v2\n"
    "ds_read_b128 v[98:101], v12 offset:8192\n"
    "v_add_f32 v35, v35, v37\n"
    "v_smfmac_f32_16x16x64_bf16 v[38:41], v[54:57], v[200:207], v2\n"
    "v_cvt_pk_bf16_f32 v34, v34, v35\n"
    "ds_write_b32 v8, v34 offset:1152\n"
    "v_smfmac_f32_16x16x64_bf16 v[38:41], v[58:61], v[240:247], v2\n"
    "v_smfmac_f32_16x16x64_bf16 v[42:45], v[62:65], a[176:183], v2\n"
    "v_smfmac_f32_16x16x64_bf16 v[42:45], v[66:69], a[216:223], v2\n"
    "s_cmp_lg_u32 s21, 0\n"
    "v_smfmac_f32_16x16x64_bf16 v[42:45], v[46:49], v[120:127], v2\n"
    "s_waitcnt lgkmcnt(0)\n"
    "s_barrier\n"
    "s_cbranch_scc1 .LS_other\n"
    "v_smfmac_f32_16x16x64_bf16 v[42:45], v[50:53], v[160:167], v2\n"
    "ds_read_b64_tr_b16 v[102:103], v9 offset:0\n"
    "ds_read_b64_tr_b16 v[104:105], v9 offset:512\n"
    "v_smfmac_f32_16x16x64_bf16 v[42:45], v[54:57], v[208:215], v2\n"
    "ds_read_b64_tr_b16 v[106:107], v9 offset:1024\n"
    "v_add_f32 v38, v38, v40\n"
    "v_add_f32 v39, v39, v41\n"
    "v_smfmac_f32_16x16x64_bf16 v[42:45], v[58:61], v[248:255], v2\n"
    "ds_read_b64_tr_b16 v[108:109], v9 offset:1536\n"
    "v_cvt_pk_bf16_f32 v38, v38, v39\n"
    "ds_write_b32 v8, v38 offset:2048\n"
    "ds_read_b64_tr_b16 v[22:23], v10 offset:0\n"
    "ds_read_b64_tr_b16 v[24:25], v10 offset:256\n"
    "ds_read_b64_tr_b16 v[26:27], v10 offset:512\n"
    "ds_read_b64_tr_b16 v[28:29], v10 offset:768\n"
    "s_nop 0\n"
    "v_pk_add_f32 v[42:43], v[42:43], v[44:45]\n"
    "v_cvt_pk_bf16_f32 v42, v42, v43\n"
    "ds_write_b32 v8, v42 offset:2176\n"
    "s_waitcnt lgkmcnt(8)\n"
    "v_smfmac_f32_16x16x64_bf16 v[110:113], v[102:105], v[70:77], v2\n"
    "ds_read_b64_tr_b16 v[30:31], v10 offset:1024\n"
    "ds_read_b64_tr_b16 v[32:33], v10 offset:1280\n"
    "v_smfmac_f32_16x16x64_bf16 v[114:117], v[102:105], v[78:85], v2\n"
    "ds_read_b64_tr_b16 v[34:35], v10 offset:1536\n"
    "ds_read_b64_tr_b16 v[36:37], v10 offset:1792\n"
    "s_waitcnt lgkmcnt(10)\n"
    "v_smfmac_f32_16x16x64_bf16 v[110:113], v[106:109], v[86:93], v2\n"
    "ds_read_b128 v[62:65], v12 offset:4096\n"
    "ds_read_b128 v[66:69], v12 offset:10240\n"
    "s_waitcnt lgkmcnt(6)\n"
    "s_barrier\n"
    "ds_read_b64_tr_b16 v[58:59], v9 offset:2048\n"
    "ds_read_b64_tr_b16 v[60:61], v9 offset:2560\n"
    "v_smfmac_f32_16x16x64_bf16 v[114:117], v[106:109], v[94:101], v2\n"
    "ds_read_b128 v[46:49], v11 offset:4096\n"
    "ds_read_b128 v[50:53], v11 offset:10240\n"
    "v_smfmac_f32_16x16x64_bf16 v[18:21], v[102:105], v[22:29], v2\n"
    "ds_read_b64_tr_b16 v[38:39], v10 offset:2048\n"
    "ds_read_b64_tr_b16 v[40:41], v10 offset:2304\n"
    "s_waitcnt lgkmcnt(8)\n"
    "v_smfmac_f32_16x16x64_bf16 v[18:21], v[106:109], v[30:37], v2\n"
    "ds_read_b64_tr_b16 v[42:43], v10 offset:2560\n"
    "ds_read_b64_tr_b16 v[44:45], v10 offset:2816\n"
    "s_waitcnt lgkmcnt(6)\n"
    "v_smfmac_f32_16x16x64_bf16 v[114:117], v[58:61], v[62:69], v2\n"
    "s_waitcnt lgkmcnt(4)\n"
    "v_smfmac_f32_16x16x64_bf16 v[110:113], v[58:61], v[46:53], v2\n"
    "s_waitcnt lgkmcnt(0)\n"
    "v_smfmac_f32_16x16x64_bf16 v[18:21], v[58:61], v[38:45], v2\n"
    "s_nop 3\n"
    "v_pk_add_f32 v[114:115], v[114:115], v[116:117]\n"
    "s_nop 0\n"
    "v_pk_add_f32 v[110:111], v[110:111], v[112:113]\n"
    "s_nop 0\n"
    "v_pk_add_f32 v[18:19], v[18:19], v[20:21]\n"
    "v_max3_f32 v70, v110, v114, v18\n"
    "v_max3_f32 v71, v111, v115, v19\n"
    "v_pk_mul_f32 v[110:111], v[110:111], s[56:57]\n"
    "v_max_f32_dpp v70, v70, v70 row_ror:8 row_mask:0xf bank_mask:0xf\n"
    "v_max_f32_dpp v71, v71, v71 row_ror:8 row_mask:0xf bank_mask:0xf\n"
    "v_pk_mul_f32 v[114:115], v[114:115], s[56:57]\n"
    "v_max_f32_dpp v70, v70, v70 row_ror:4 row_mask:0xf bank_mask:0xf\n"
    "v_max_f32_dpp v71, v71, v71 row_ror:4 row_mask:0xf bank_mask:0xf\n"
    "v_pk_mul_f32 v[18:19], v[18:19], s[56:57]\n"
    "v_max_f32_dpp v70, v70, v70 row_ror:2 row_mask:0xf bank_mask:0xf\n"
    "v_max_f32_dpp v71, v71, v71 row_ror:2 row_mask:0xf bank_mask:0xf\n"
    "s_nop 0\n"
    "v_max_f32_dpp v70, v70, v70 row_ror:1 row_mask:0xf bank_mask:0xf\n"
    "v_max_f32_dpp v71, v71, v71 row_ror:1 row_mask:0xf bank_mask:0xf\n"
    "v_pk_fma_f32 v[18:19], v[70:71], s[56:57], v[18:19] neg_lo:[1,0,0] neg_hi:[1,0,0]\n"
    "v_pk_fma_f32 v[110:111], v[70:71], s[56:57], v[110:111] neg_lo:[1,0,0] neg_hi:[1,0,0]\n"
    "v_pk_fma_f32 v[114:115], v[70:71], s[56:57], v[114:115] neg_lo:[1,0,0] neg_hi:[1,0,0]\n"
    "v_exp_f32 v18, v18\n"
    "v_exp_f32 v19, v19\n"
    "v_exp_f32 v110, v110\n"
    "v_exp_f32 v111, v111\n"
    "v_exp_f32 v114, v114\n"
    "v_exp_f32 v115, v115\n"
    "v_pk_add_f32 v[70:71], v[110:111], v[18:19]\n"
    "v_pk_add_f32 v[70:71], v[70:71], v[114:115]\n"
    "v_mov_b32_dpp v72, v18 quad_perm:[1,0,3,2] row_mask:0xf bank_mask:0xf\n"
    "v_mov_b32_dpp v73, v19 quad_perm:[1,0,3,2] row_mask:0xf bank_mask:0xf\n"
    "v_add_f32_dpp v70, v70, v70 row_ror:8 row_mask:0xf bank_mask:0xf\n"
    "v_add_f32_dpp v71, v71, v71 row_ror:8 row_mask:0xf bank_mask:0xf\n"
    "v_cndmask_b32 v18, v18, v73, s[50:51]\n"
    "v_add_f32_dpp v70, v70, v70 row_ror:4 row_mask:0xf bank_mask:0xf\n"
    "v_add_f32_dpp v71, v71, v71 row_ror:4 row_mask:0xf bank_mask:0xf\n"
    "v_cndmask_b32 v19, v72, v19, s[50:51]\n"
    "v_add_f32_dpp v70, v70, v70 row_ror:2 row_mask:0xf bank_mask:0xf\n"
    "v_add_f32_dpp v71, v71, v71 row_ror:2 row_mask:0xf bank_mask:0xf\n"
    "s_nop 0\n"
    "v_add_f32_dpp v70, v70, v70 row_ror:1 row_mask:0xf bank_mask:0xf\n"
    "v_add_f32_dpp v71, v71, v71 row_ror:1 row_mask:0xf bank_mask:0xf\n"
    "v_rcp_f32 v70, v70\n"
    "v_rcp_f32 v71, v71\n"
    "s_nop 0\n"
    "v_cndmask_b32 v72, v70, v71, s[50:51]\n"
    "v_pk_mul_f32 v[18:19], v[18:19], v[72:73] op_sel_hi:[1,0]\n"
    "v_cvt_pk_bf16_f32 v72, v18, v19\n"
    "buffer_store_dword v72, v15, s[8:11], s31 offen offset:512 sc1\n"
    "v_pk_mul_f32 v[110:111], v[110:111], v[70:71]\n"
    "v_pk_mul_f32 v[114:115], v[114:115], v[70:71]\n"
    "v_cvt_pk_bf16_f32 v70, v110, v114\n"
    "buffer_store_dword v70, v14, s[8:11], s31 offen offset:0 sc1\n"
    "v_cvt_pk_bf16_f32 v71, v111, v115\n"
    "buffer_store_dword v71, v14, s[8:11], s31 offen offset:256 sc1\n"
    "s_memrealtime s[12:13]\n"
    "s_nop 2\n"
    "s_memrealtime s[14:15]\n"
    "s_nop 2\n"
    "s_memrealtime s[16:17]\n"
    "s_nop 2\n"
    "s_memrealtime s[18:19]\n"
    "s_nop 2\n"
    "s_memrealtime s[44:45]\n"
    "s_nop 2\n"
    "s_memrealtime s[46:47]\n"
    "s_waitcnt lgkmcnt(0)\n"
    "s_add_u32 s40, s12, s14\n"
    "s_add_u32 s40, s40, s16\n"
    "s_add_u32 s40, s40, s18\n"
    "s_add_u32 s40, s40, s44\n"
    "s_add_u32 s40, s40, s46\n"
    "s_sub_u32 s53, s40, s54\n"
    ".LS_next:\n"
    "s_add_u32 s24, s24, 1\n"
    "s_add_u32 s30, s30, 32768\n"
    "s_cmp_eq_u32 s30, 98304\n"
    "s_cselect_b32 s30, 0, s30\n"
    "s_add_u32 s31, s31, 65536\n"
    "s_cmp_eq_u32 s31, 294912\n"
    "s_cselect_b32 s31, 98304, s31\n"
    "s_add_u32 s34, s34, 65536\n"
    "s_cmp_eq_u32 s34, 294912\n"
    "s_cselect_b32 s34, 98304, s34\n"
    "v_add_u32 v11, s38, v11\n"
    "v_add_u32 v12, s38, v12\n"
    "s_sub_u32 s38, 0, s38\n"
    "s_cmp_eq_u32 s24, 1\n"
    "s_cselect_b32 s40, 0, s38\n"
    "s_cselect_b32 s41, 2, 1\n"
    "s_add_u32 s55, s55, s40\n"
    "s_cmp_lt_u32 s24, 63\n"
    "s_cselect_b32 s41, s41, 0\n"
    "s_mul_i32 s41, s41, 786432\n"
    "s_add_u32 s29, s29, s41\n"
    "s_cmp_lt_u32 s24, 64\n"
    "s_cbranch_scc1 .LS_idle\n"
    "s_endpgm\n"
    ".LS_again:\n"
    "s_nop 7\n"
    "s_add_u32 s53, s53, 10\n"
    "v_mov_b64 v[22:23], 0\n"
    "v_mov_b64 v[24:25], 0\n"
    "v_mov_b64 v[26:27], 0\n"
    "v_mov_b64 v[28:29], 0\n"
    "v_mov_b64 v[30:31], 0\n"
    "v_mov_b64 v[32:33], 0\n"
    "v_mov_b64 v[34:35], 0\n"
    "v_mov_b64 v[36:37], 0\n"
    "v_mov_b64 v[38:39], 0\n"
    "v_mov_b64 v[40:41], 0\n"
    "v_mov_b64 v[42:43], 0\n"
    "v_mov_b64 v[44:45], 0\n"
    ".LS_poll:\n"
    "buffer_load_dwordx4 v[46:49], v3, s[8:11], s30 offen offset:0 sc1\n"
    "buffer_load_dwordx4 v[50:53], v3, s[8:11], s30 offen offset:1024 sc1\n"
    "buffer_load_dwordx4 v[54:57], v3, s[8:11], s30 offen offset:2048 sc1\n"
    "s_waitcnt vmcnt(2)\n"
    "v_max3_u32 v118, v46, v47, v48\n"
    "v_max_u32 v118, v118, v49\n"
    "s_waitcnt vmcnt(1)\n"
    "v_max3_u32 v118, v118, v50, v51\n"
    "v_max3_u32 v118, v118, v52, v53\n"
    "s_waitcnt vmcnt(0)\n"
    "v_max3_u32 v118, v118, v54, v55\n"
    "v_max3_u32 v118, v118, v56, v57\n"
    "v_cmp_eq_u32 vcc, -1, v118\n"
    "s_cbranch_vccz .LS_have\n"
    "s_sub_u32 s25, s25, 1\n"
    "s_nop 2\n"
    "s_cbranch_scc0 .LS_poll\n"
    "s_branch .Lfail\n"
    ".LS_other:\n"
    "v_smfmac_f32_16x16x64_bf16 v[42:45], v[50:53], v[160:167], v2\n"
    "s_cmp_eq_u32 s21, 3\n"
    "v_smfmac_f32_16x16x64_bf16 v[42:45], v[54:57], v[208:215], v2\n"
    "v_add_f32 v38, v38, v40\n"
    "v_add_f32 v39, v39, v41\n"
    "v_smfmac_f32_16x16x64_bf16 v[42:45], v[58:61], v[248:255], v2\n"
    "v_cvt_pk_bf16_f32 v38, v38, v39\n"
    "ds_write_b32 v8, v38 offset:2048\n"
    "s_nop 5\n"
    "v_pk_add_f32 v[42:43], v[42:43], v[44:45]\n"
    "v_cvt_pk_bf16_f32 v42, v42, v43\n"
    "ds_write_b32 v8, v42 offset:2176\n"
    "s_waitcnt lgkmcnt(0)\n"
    "s_barrier\n"
    "s_cbranch_scc1 .LS_w3\n"
    "s_cmp_lg_u32 s24, 63\n"
    "s_cbranch_scc1 .LS_notlast\n"
    "v_mov_b32 v118, 64\n"
    "buffer_store_dword v118, v17, s[8:11], 0 offen sc1\n"
    "v_mov_b32 v17, 0x7ff00000\n"
    ".LS_notlast:\n"
    "s_nop 9\n"
    "ds_read_b128 v[102:105], v13 offset:0\n"
    "ds_read_b128 v[106:109], v13 offset:2048\n"
    "ds_read_b128 v[58:61], v13 offset:4096\n"
    "ds_read_b64_tr_b16 v[22:23], v10 offset:0\n"
    "ds_read_b64_tr_b16 v[24:25], v10 offset:256\n"
    "ds_read_b64_tr_b16 v[26:27], v10 offset:512\n"
    "ds_read_b64_tr_b16 v[28:29], v10 offset:768\n"
    "ds_read_b64_tr_b16 v[30:31], v10 offset:1024\n"
    "ds_read_b64_tr_b16 v[32:33], v10 offset:1280\n"
    "ds_read_b64_tr_b16 v[34:35], v10 offset:1536\n"
    "ds_read_b64_tr_b16 v[36:37], v10 offset:1792\n"
    "ds_read_b128 v[62:65], v12 offset:4096\n"
    "ds_read_b128 v[66:69], v12 offset:10240\n"
    "ds_read_b128 v[46:49], v11 offset:4096\n"
    "ds_read_b128 v[50:53], v11 offset:10240\n"
    "ds_read_b64_tr_b16 v[38:39], v10 offset:2048\n"
    "ds_read_b64_tr_b16 v[40:41], v10 offset:2304\n"
    "ds_read_b64_tr_b16 v[42:43], v10 offset:2560\n"
    "ds_read_b64_tr_b16 v[44:45], v10 offset:2816\n"
    "s_waitcnt lgkmcnt(15)\n"
    "v_mfma_f32_16x16x32_bf16 v[110:113], v[70:73], v[102:105], 0\n"
    "v_mfma_f32_16x16x32_bf16 v[114:117], v[78:81], v[102:105], 0\n"
    "s_waitcnt lgkmcnt(14)\n"
    "v_mfma_f32_16x16x32_bf16 v[18:21], v[22:25], v[102:105], 0\n"
    "v_mfma_f32_16x16x32_bf16 v[110:113], v[86:89], v[106:109], v[110:113]\n"
    "v_mfma_f32_16x16x32_bf16 v[114:117], v[94:97], v[106:109], v[114:117]\n"
    "s_waitcnt lgkmcnt(10)\n"
    "v_mfma_f32_16x16x32_bf16 v[18:21], v[30:33], v[106:109], v[18:21]\n"
    "s_waitcnt lgkmcnt(5)\n"
    "v_mfma_f32_16x16x32_bf16 v[110:113], v[46:49], v[58:61], v[110:113]\n"
    "v_mfma_f32_16x16x32_bf16 v[114:117], v[62:65], v[58:61], v[114:117]\n"
    "s_waitcnt lgkmcnt(2)\n"
    "v_mfma_f32_16x16x32_bf16 v[18:21], v[38:41], v[58:61], v[18:21]\n"
    "ds_read_b128 v[102:105], v13 offset:6144\n"
    "ds_read_b128 v[106:109], v13 offset:8192\n"
    "ds_read_b128 v[58:61], v13 offset:10240\n"
    "s_waitcnt lgkmcnt(2)\n"
    "v_mfma_f32_16x16x32_bf16 v[110:113], v[74:77], v[102:105], v[110:113]\n"
    "v_mfma_f32_16x16x32_bf16 v[114:117], v[82:85], v[102:105], v[114:117]\n"
    "v_mfma_f32_16x16x32_bf16 v[18:21], v[26:29], v[102:105], v[18:21]\n"
    "s_waitcnt lgkmcnt(1)\n"
    "v_mfma_f32_16x16x32_bf16 v[110:113], v[90:93], v[106:109], v[110:113]\n"
    "v_mfma_f32_16x16x32_bf16 v[114:117], v[98:101], v[106:109], v[114:117]\n"
    "v_mfma_f32_16x16x32_bf16 v[18:21], v[34:37], v[106:109], v[18:21]\n"
    "s_waitcnt lgkmcnt(0)\n"
    "v_mfma_f32_16x16x32_bf16 v[110:113], v[50:53], v[58:61], v[110:113]\n"
    "v_mfma_f32_16x16x32_bf16 v[114:117], v[66:69], v[58:61], v[114:117]\n"
    "v_mfma_f32_16x16x32_bf16 v[18:21], v[42:45], v[58:61], v[18:21]\n"
    "v_add_u32 v13, s38, v13\n"
    "v_mov_b32 v118, 0xff800000\n"
    "s_nop 3\n"
    "v_cndmask_b32 v110, v110, v118, s[64:65]\n"
    "v_cndmask_b32 v111, v111, v118, s[66:67]\n"
    "v_cndmask_b32 v112, v112, v118, s[68:69]\n"
    "v_cndmask_b32 v113, v113, v118, s[70:71]\n"
    "v_cndmask_b32 v114, v114, v118, s[72:73]\n"
    "v_cndmask_b32 v115, v115, v118, s[74:75]\n"
    "v_cndmask_b32 v116, v116, v118, s[76:77]\n"
    "v_cndmask_b32 v117, v117, v118, s[78:79]\n"
    "v_cndmask_b32 v18, v18, v118, s[80:81]\n"
    "v_cndmask_b32 v19, v19, v118, s[80:81]\n"
    "v_cndmask_b32 v20, v20, v118, s[80:81]\n"
    "v_cndmask_b32 v21, v21, v118, s[80:81]\n"
    "v_max3_f32 v70, v110, v111, v112\n"
    "v_max3_f32 v70, v70, v113, v114\n"
    "v_max3_f32 v70, v70, v115, v116\n"
    "v_max3_f32 v70, v70, v117, v18\n"
    "v_max3_f32 v70, v70, v19, v20\n"
    "v_max_f32 v70, v70, v21\n"
    "v_mov_b32 v71, v70\n"
    "s_nop 1\n"
    "v_permlane16_swap_b32 v70, v71\n"
    "v_max_f32 v70, v70, v71\n"
    "v_mov_b32 v71, v70\n"
    "s_nop 1\n"
    "v_permlane32_swap_b32 v70, v71\n"
    "v_max_f32 v70, v70, v71\n"
    "v_mul_f32 v70, s56, v70\n"
    "v_pk_fma_f32 v[110:111], v[110:111], s[56:57], v[70:71] op_sel_hi:[1,1,0] neg_lo:[0,0,1] neg_hi:[0,0,1]\n"
    "v_pk_fma_f32 v[112:113], v[112:113], s[56:57], v[70:71] op_sel_hi:[1,1,0] neg_lo:[0,0,1] neg_hi:[0,0,1]\n"
    "v_pk_fma_f32 v[114:115], v[114:115], s[56:57], v[70:71] op_sel_hi:[1,1,0] neg_lo:[0,0,1] neg_hi:[0,0,1]\n"
    "v_pk_fma_f32 v[116:117], v[116:117], s[56:57], v[70:71] op_sel_hi:[1,1,0] neg_lo:[0,0,1] neg_hi:[0,0,1]\n"
    "v_pk_fma_f32 v[18:19], v[18:19], s[56:57], v[70:71] op_sel_hi:[1,1,0] neg_lo:[0,0,1] neg_hi:[0,0,1]\n"
    "v_pk_fma_f32 v[20:21], v[20:21], s[56:57], v[70:71] op_sel_hi:[1,1,0] neg_lo:[0,0,1] neg_hi:[0,0,1]\n"
    "v_exp_f32 v110, v110\n"
    "v_exp_f32 v111, v111\n"
    "v_exp_f32 v112, v112\n"
    "v_exp_f32 v113, v113\n"
    "v_exp_f32 v114, v114\n"
    "v_exp_f32 v115, v115\n"
    "v_exp_f32 v116, v116\n"
    "v_exp_f32 v117, v117\n"
    "v_exp_f32 v18, v18\n"
    "v_exp_f32 v19, v19\n"
    "v_exp_f32 v20, v20\n"
    "v_exp_f32 v21, v21\n"
    "v_pk_add_f32 v[70:71], v[110:111], v[112:113]\n"
    "v_pk_add_f32 v[70:71], v[70:71], v[114:115]\n"
    "v_pk_add_f32 v[70:71], v[70:71], v[116:117]\n"
    "v_pk_add_f32 v[70:71], v[70:71], v[18:19]\n"
    "v_pk_add_f32 v[70:71], v[70:71], v[20:21]\n"
    "v_add_f32 v70, v70, v71\n"
    "v_mov_b32 v71, v70\n"
    "s_nop 1\n"
    "v_permlane16_swap_b32 v70, v71\n"
    "v_add_f32 v70, v70, v71\n"
    "v_mov_b32 v71, v70\n"
    "s_nop 1\n"
    "v_permlane32_swap_b32 v70, v71\n"
    "v_add_f32 v70, v70, v71\n"
    "v_rcp_f32 v70, v70\n"
    "s_nop 0\n"
    "v_pk_mul_f32 v[110:111], v[110:111], v[70:71] op_sel_hi:[1,0]\n"
    "v_pk_mul_f32 v[112:113], v[112:113], v[70:71] op_sel_hi:[1,0]\n"
    "v_pk_mul_f32 v[114:115], v[114:115], v[70:71] op_sel_hi:[1,0]\n"
    "v_pk_mul_f32 v[116:117], v[116:117], v[70:71] op_sel_hi:[1,0]\n"
    "v_pk_mul_f32 v[18:19], v[18:19], v[70:71] op_sel_hi:[1,0]\n"
    "v_pk_mul_f32 v[20:21], v[20:21], v[70:71] op_sel_hi:[1,0]\n"
    "v_cvt_pk_bf16_f32 v72, v110, v111\n"
    "v_cvt_pk_bf16_f32 v73, v112, v113\n"
    "v_cvt_pk_bf16_f32 v74, v114, v115\n"
    "v_cvt_pk_bf16_f32 v75, v116, v117\n"
    "v_cvt_pk_bf16_f32 v76, v18, v19\n"
    "v_cvt_pk_bf16_f32 v77, v20, v21\n"
    "v_mov_b32 v118, s24\n"
    "buffer_store_dword v118, v17, s[8:11], 0 offen sc1\n"
    ".LS_noword:\n"
    "buffer_store_dwordx4 v[72:75], v14, s[8:11], s36 offen sc1\n"
    "buffer_store_dwordx2 v[76:77], v15, s[8:11], s37 offen sc1\n"
    "s_add_u32 s36, s36, 65536\n"
    "s_add_u32 s37, s37, 16384\n"
    "s_branch .LS_next\n"
    ".LS_w3:\n"
    "s_cmp_eq_u32 s24, 63\n"
    "s_cbranch_scc1 .LS_next\n"
    "s_mov_b32 exec_hi, 0\n"
    "buffer_load_dwordx2 v[70:71], v15, s[8:11], s33 offen sc1\n"
    "s_mov_b32 exec_hi, -1\n"
    "v_mov_b64 v[74:75], -1\n"
    "v_mov_b64 v[76:77], -1\n"
    "buffer_store_dwordx4 v[74:77], v16, s[8:11], s34 offen sc1\n"
    "s_xor_b32 s40, s33, 16384\n"
    "s_lshl_b64 exec, 1, s20\n"
    "buffer_store_dwordx4 v[74:77], v15, s[8:11], s40 offen sc1\n"
    "s_mov_b64 exec, -1\n"
    "s_cmp_lg_u32 s24, 0\n"
    "s_cbranch_scc1 .LS_w3k\n"
    "s_mov_b32 s41, 2\n"
    ".Lpl_41:\n"
    "buffer_load_dword v14, v13, s[8:11], 0 offen sc1\n"
    "s_waitcnt vmcnt(0)\n"
    "v_cmp_gt_u32 vcc, s41, v14\n"
    "s_nop 1\n"
    "s_cbranch_vccz .Lpd_42\n"
    "s_sleep 1\n"
    "s_sub_u32 s25, s25, 1\n"
    "s_cbranch_scc0 .Lpl_41\n"
    "s_branch .Lfail\n"
    ".Lpd_42:\n"
    ".LS_w3k:\n"
    "s_add_u32 m0, s35, 0\n"
    "s_add_u32 s40, s39, 0\n"
    "buffer_load_dwordx4 v0, s[8:11], s40 offen offset:0 sc1 lds\n"
    "buffer_load_dwordx4 v0, s[8:11], s40 offen offset:1024 sc1 lds\n"
    "buffer_load_dwordx4 v0, s[8:11], s40 offen offset:2048 sc1 lds\n"
    "buffer_load_dwordx4 v0, s[8:11], s40 offen offset:3072 sc1 lds\n"
    "s_add_u32 m0, s35, 4096\n"
    "s_add_u32 s40, s39, 4096\n"
    "buffer_load_dwordx4 v0, s[8:11], s40 offen offset:0 sc1 lds\n"
    "buffer_load_dwordx4 v0, s[8:11], s40 offen offset:1024 sc1 lds\n"
    "buffer_load_dwordx4 v0, s[8:11], s40 offen offset:2048 sc1 lds\n"
    "buffer_load_dwordx4 v0, s[8:11], s40 offen offset:3072 sc1 lds\n"
    "s_add_u32 m0, s35, 8192\n"
    "s_add_u32 s40, s39, 8192\n"
    "buffer_load_dwordx4 v0, s[8:11], s40 offen offset:0 sc1 lds\n"
    "buffer_load_dwordx4 v0, s[8:11], s40 offen offset:1024 sc1 lds\n"
    "buffer_load_dwordx4 v0, s[8:11], s40 offen offset:2048 sc1 lds\n"
    "buffer_load_dwordx4 v0, s[8:11], s40 offen offset:3072 sc1 lds\n"
    "s_add_u32 m0, s35, 12288\n"
    "s_add_u32 s40, s39, 12288\n"
    "buffer_load_dwordx4 v0, s[8:11], s40 offen offset:0 sc1 lds\n"
    "buffer_load_dwordx4 v0, s[8:11], s40 offen offset:1024 sc1 lds\n"
    "buffer_load_dwordx4 v0, s[8:11], s40 offen offset:2048 sc1 lds\n"
    "buffer_load_dwordx4 v0, s[8:11], s40 offen offset:3072 sc1 lds\n"
    "s_nop 15\n"
    "s_nop 15\n"
    "s_nop 15\n"
    "s_nop 15\n"
    "s_add_u32 m0, s35, 16384\n"
    "s_add_u32 s40, s39, 16384\n"
    "buffer_load_dwordx4 v0, s[8:11], s40 offen offset:0 sc1 lds\n"
    "buffer_load_dwordx4 v0, s[8:11], s40 offen offset:1024 sc1 lds\n"
    "buffer_load_dwordx4 v0, s[8:11], s40 offen offset:2048 sc1 lds\n"
    "buffer_load_dwordx4 v0, s[8:11], s40 offen offset:3072 sc1 lds\n"
    "s_add_u32 m0, s35, 20480\n"
    "s_add_u32 s40, s39, 20480\n"
    "buffer_load_dwordx4 v0, s[8:11], s40 offen offset:0 sc1 lds\n"
    "buffer_load_dwordx4 v0, s[8:11], s40 offen offset:1024 sc1 lds\n"
    "buffer_load_dwordx4 v0, s[8:11], s40 offen offset:2048 sc1 lds\n"
    "buffer_load_dwordx4 v0, s[8:11], s40 offen offset:3072 sc1 lds\n"
    "s_add_u32 s39, s39, 786432\n"
    "s_xor_b32 s35, s35, 40960\n"
    "s_add_u32 s41, s24, s4\n"
    "s_memrealtime s[42:43]\n"
    "s_waitcnt vmcnt(26)\n"
    ".Lhp_43:\n"
    "v_cmp_ne_u32 vcc, s41, v71\n"
    "s_nop 1\n"
    "s_cmp_eq_u32 vcc_lo, 0\n"
    "s_cbranch_scc1 .Lhd_44\n"
    "s_mov_b32 exec_hi, 0\n"
    "buffer_load_dwordx2 v[70:71], v15, s[8:11], s33 offen sc1\n"
    "s_mov_b32 exec_hi, -1\n"
    "s_waitcnt lgkmcnt(0)\n"
    "s_memrealtime s[42:43]\n"
    "s_waitcnt vmcnt(0)\n"
    "s_sub_u32 s25, s25, 1\n"
    "s_cbranch_scc0 .Lhp_43\n"
    "s_branch .Lfail\n"
    ".Lhd_44:\n"
    "s_waitcnt lgkmcnt(0)\n"
    "s_mul_i32 s42, s42, 6\n"
    "v_subrev_u32 v70, s42, v70\n"
    "s_nop 1\n"
    "v_max_i32_dpp v70, v70, v70 row_ror:8 row_mask:0xf bank_mask:0xf\n"
    "s_nop 1\n"
    "v_max_i32_dpp v70, v70, v70 row_ror:4 row_mask:0xf bank_mask:0xf\n"
    "s_nop 1\n"
    "v_max_i32_dpp v70, v70, v70 row_ror:2 row_mask:0xf bank_mask:0xf\n"
    "s_nop 1\n"
    "v_max_i32_dpp v70, v70, v70 row_ror:1 row_mask:0xf bank_mask:0xf\n"
    "s_nop 0\n"
    "v_readlane_b32 s40, v70, 0\n"
    "v_readlane_b32 s41, v70, 16\n"
    "s_nop 0\n"
    "s_max_i32 s40, s40, s41\n"
    "s_min_i32 s40, s40, 2000\n"
    "s_add_u32 s40, s40, s42\n"
    "s_sub_u32 s41, s40, s59\n"
    "s_mov_b32 s59, s40\n"
    "s_sub_u32 s41, s41, s82\n"
    "s_add_u32 s83, s83, 4\n"
    "s_min_u32 s83, s83, s41\n"
    "s_sub_u32 s41, s41, s83\n"
    "s_cmp_gt_u32 s41, 90\n"
    "s_cselect_b32 s41, 12, 0\n"
    "s_add_u32 s82, s82, s41\n"
    "s_cmp_lt_u32 s24, 46\n"
    "s_cselect_b32 s41, 1, 4\n"
    "s_sub_u32 s82, s82, s41\n"
    "s_cmp_lt_u32 s24, 46\n"
    "s_cselect_b32 s41, 40, 0\n"
    "s_max_i32 s82, s82, s41\n"
    "s_min_i32 s82, s82, 100\n"
    "v_lshlrev_b32 v74, 8, v0\n"
    "s_mov_b64 exec, 63\n"
    "buffer_load_dword v58, v74, s[8:11], s39 offen\n"
    "s_mov_b64 exec, -1\n"
    "s_add_u32 s41, s24, 3\n"
    "s_min_u32 s41, s41, 64\n"
    "v_cmp_gt_u32 vcc, s41, v14\n"
    "s_cbranch_vccz .LS_w3go\n"
    "s_nop 3\n"
    ".Lpl_45:\n"
    "buffer_load_dword v14, v13, s[8:11], 0 offen sc1\n"
    "s_waitcnt vmcnt(0)\n"
    "v_cmp_gt_u32 vcc, s41, v14\n"
    "s_nop 1\n"
    "s_cbranch_vccz .Lpd_46\n"
    "s_sleep 1\n"
    "s_sub_u32 s25, s25, 1\n"
    "s_cbranch_scc0 .Lpl_45\n"
    "s_branch .Lfail\n"
    ".Lpd_46:\n"
    ".LS_w3go:\n"
    "v_cmp_ge_u32 vcc, s41, v14\n"
    "s_cbranch_vccz .LS_next\n"
    "buffer_load_dword v14, v13, s[8:11], 0 offen sc1\n"
    "s_nop 2\n"
    "s_branch .LS_next\n"
    ".Lrole_W:\n"
    "s_mov_b32 s12, s70\n"
    "s_and_b32 s13, s71, 0xffff\n"
    "s_mov_b32 s14, 7864320\n"
    "s_mov_b32 s15, 0x20000\n"
    "s_mov_b32 s16, s72\n"
    "s_and_b32 s17, s73, 0xffff\n"
    "s_mov_b32 s18, 7864320\n"
    "s_mov_b32 s19, 0x20000\n"
    "s_lshr_b32 s40, s21, 1\n"
    "s_mul_i32 s40, s40, 5\n"
    "s_and_b32 s41, s21, 1\n"
    "s_lshl_b32 s42, s41, 1\n"
    "s_add_u32 s26, s40, s42\n"
    "s_mul_i32 s42, s41, 3\n"
    "s_sub_u32 s42, 3, s42\n"
    "s_add_u32 s27, s40, s42\n"
    "s_sub_u32 s28, 5, s40\n"
    "s_mul_i32 s48, s20, 384\n"
    "s_mul_i32 s51, s20, 245760\n"
    "s_mul_i32 s40, s21, 320\n"
    "s_add_u32 s51, s51, s40\n"
    "s_mul_i32 s29, s21, 3072\n"
    "s_add_u32 s29, s29, 0\n"
    "s_and_b32 s40, s21, 3\n"
    "s_mul_i32 s41, s40, 192\n"
    "v_add_u32 v168, s41, v1\n"
    "s_add_u32 s41, s41, 64\n"
    "v_mul_u32_u24 v0, 2731, v168\n"
    "v_lshrrev_b32 v0, 16, v0\n"
    "v_mul_u32_u24 v171, 24, v0\n"
    "v_sub_u32 v168, v168, v171\n"
    "v_mul_u32_u24 v0, 12288, v0\n"
    "v_lshl_add_u32 v168, v168, 4, v0\n"
    "v_add_u32 v169, s41, v1\n"
    "s_add_u32 s41, s41, 64\n"
    "v_mul_u32_u24 v0, 2731, v169\n"
    "v_lshrrev_b32 v0, 16, v0\n"
    "v_mul_u32_u24 v171, 24, v0\n"
    "v_sub_u32 v169, v169, v171\n"
    "v_mul_u32_u24 v0, 12288, v0\n"
    "v_lshl_add_u32 v169, v169, 4, v0\n"
    "v_add_u32 v170, s41, v1\n"
    "s_add_u32 s41, s41, 64\n"
    "v_mul_u32_u24 v0, 2731, v170\n"
    "v_lshrrev_b32 v0, 16, v0\n"
    "v_mul_u32_u24 v171, 24, v0\n"
    "v_sub_u32 v170, v170, v171\n"
    "v_mul_u32_u24 v0, 12288, v0\n"
    "v_lshl_add_u32 v170, v170, 4, v0\n"
    "s_mul_i32 s40, s40, 2976\n"
    "s_sub_u32 s40, s29, s40\n"
    "v_bfe_u32 v171, v1, 4, 2\n"
    "v_lshlrev_b32 v171, 3, v171\n"
    "v_bfe_u32 v0, v1, 2, 2\n"
    "v_lshl_add_u32 v171, v0, 0, v171\n"
    "v_mul_u32_u24 v171, 384, v171\n"
    "v_and_b32 v0, 3, v1\n"
    "v_mul_u32_u24 v0, 8, v0\n"
    "v_add3_u32 v171, v171, v0, s40\n"
    "v_add_u32 v172, 57344, v171\n"
    "v_add_u32 v173, 114688, v171\n"
    "s_mul_i32 s49, s21, 10240\n"
    "s_add_u32 s49, s49, 0\n"
    "s_and_b32 s40, s21, 0\n"
    "s_mul_i32 s41, s40, 640\n"
    "v_add_u32 v8, s41, v1\n"
    "s_add_u32 s41, s41, 64\n"
    "v_mul_u32_u24 v0, 3277, v8\n"
    "v_lshrrev_b32 v0, 16, v0\n"
    "v_mul_u32_u24 v18, 20, v0\n"
    "v_sub_u32 v8, v8, v18\n"
    "v_lshrrev_b32 v18, 4, v0\n"
    "v_mul_u32_u24 v18, 32, v18\n"
    "v_add_u32 v0, v0, v18\n"
    "v_mul_u32_u24 v0, 1280, v0\n"
    "v_lshl_add_u32 v8, v8, 4, v0\n"
    "v_add_u32 v9, s41, v1\n"
    "s_add_u32 s41, s41, 64\n"
    "v_mul_u32_u24 v0, 3277, v9\n"
    "v_lshrrev_b32 v0, 16, v0\n"
    "v_mul_u32_u24 v18, 20, v0\n"
    "v_sub_u32 v9, v9, v18\n"
    "v_lshrrev_b32 v18, 4, v0\n"
    "v_mul_u32_u24 v18, 32, v18\n"
    "v_add_u32 v0, v0, v18\n"
    "v_mul_u32_u24 v0, 1280, v0\n"
    "v_lshl_add_u32 v9, v9, 4, v0\n"
    "v_add_u32 v10, s41, v1\n"
    "s_add_u32 s41, s41, 64\n"
    "v_mul_u32_u24 v0, 3277, v10\n"
    "v_lshrrev_b32 v0, 16, v0\n"
    "v_mul_u32_u24 v18, 20, v0\n"
    "v_sub_u32 v10, v10, v18\n"
    "v_lshrrev_b32 v18, 4, v0\n"
    "v_mul_u32_u24 v18, 32, v18\n"
    "v_add_u32 v0, v0, v18\n"
    "v_mul_u32_u24 v0, 1280, v0\n"
    "v_lshl_add_u32 v10, v10, 4, v0\n"
    "v_add_u32 v11, s41, v1\n"
    "s_add_u32 s41, s41, 64\n"
    "v_mul_u32_u24 v0, 3277, v11\n"
    "v_lshrrev_b32 v0, 16, v0\n"
    "v_mul_u32_u24 v18, 20, v0\n"
    "v_sub_u32 v11, v11, v18\n"
    "v_lshrrev_b32 v18, 4, v0\n"
    "v_mul_u32_u24 v18, 32, v18\n"
    "v_add_u32 v0, v0, v18\n"
    "v_mul_u32_u24 v0, 1280, v0\n"
    "v_lshl_add_u32 v11, v11, 4, v0\n"
    "v_add_u32 v12, s41, v1\n"
    "s_add_u32 s41, s41, 64\n"
    "v_mul_u32_u24 v0, 3277, v12\n"
    "v_lshrrev_b32 v0, 16, v0\n"
    "v_mul_u32_u24 v18, 20, v0\n"
    "v_sub_u32 v12, v12, v18\n"
    "v_lshrrev_b32 v18, 4, v0\n"
    "v_mul_u32_u24 v18, 32, v18\n"
    "v_add_u32 v0, v0, v18\n"
    "v_mul_u32_u24 v0, 1280, v0\n"
    "v_lshl_add_u32 v12, v12, 4, v0\n"
    "v_add_u32 v13, s41, v1\n"
    "s_add_u32 s41, s41, 64\n"
    "v_mul_u32_u24 v0, 3277, v13\n"
    "v_lshrrev_b32 v0, 16, v0\n"
    "v_mul_u32_u24 v18, 20, v0\n"
    "v_sub_u32 v13, v13, v18\n"
    "v_lshrrev_b32 v18, 4, v0\n"
    "v_mul_u32_u24 v18, 32, v18\n"
    "v_add_u32 v0, v0, v18\n"
    "v_mul_u32_u24 v0, 1280, v0\n"
    "v_lshl_add_u32 v13, v13, 4, v0\n"
    "v_add_u32 v14, s41, v1\n"
    "s_add_u32 s41, s41, 64\n"
    "v_mul_u32_u24 v0, 3277, v14\n"
    "v_lshrrev_b32 v0, 16, v0\n"
    "v_mul_u32_u24 v18, 20, v0\n"
    "v_sub_u32 v14, v14, v18\n"
    "v_lshrrev_b32 v18, 4, v0\n"
    "v_mul_u32_u24 v18, 32, v18\n"
    "v_add_u32 v0, v0, v18\n"
    "v_mul_u32_u24 v0, 1280, v0\n"
    "v_lshl_add_u32 v14, v14, 4, v0\n"
    "v_add_u32 v15, s41, v1\n"
    "s_add_u32 s41, s41, 64\n"
    "v_mul_u32_u24 v0, 3277, v15\n"
    "v_lshrrev_b32 v0, 16, v0\n"
    "v_mul_u32_u24 v18, 20, v0\n"
    "v_sub_u32 v15, v15, v18\n"
    "v_lshrrev_b32 v18, 4, v0\n"
    "v_mul_u32_u24 v18, 32, v18\n"
    "v_add_u32 v0, v0, v18\n"
    "v_mul_u32_u24 v0, 1280, v0\n"
    "v_lshl_add_u32 v15, v15, 4, v0\n"
    "v_add_u32 v16, s41, v1\n"
    "s_add_u32 s41, s41, 64\n"
    "v_mul_u32_u24 v0, 3277, v16\n"
    "v_lshrrev_b32 v0, 16, v0\n"
    "v_mul_u32_u24 v18, 20, v0\n"
    "v_sub_u32 v16, v16, v18\n"
    "v_lshrrev_b32 v18, 4, v0\n"
    "v_mul_u32_u24 v18, 32, v18\n"
    "v_add_u32 v0, v0, v18\n"
    "v_mul_u32_u24 v0, 1280, v0\n"
    "v_lshl_add_u32 v16, v16, 4, v0\n"
    "v_add_u32 v17, s41, v1\n"
    "s_add_u32 s41, s41, 64\n"
    "v_mul_u32_u24 v0, 3277, v17\n"
    "v_lshrrev_b32 v0, 16, v0\n"
    "v_mul_u32_u24 v18, 20, v0\n"
    "v_sub_u32 v17, v17, v18\n"
    "v_lshrrev_b32 v18, 4, v0\n"
    "v_mul_u32_u24 v18, 32, v18\n"
    "v_add_u32 v0, v0, v18\n"
    "v_mul_u32_u24 v0, 1280, v0\n"
    "v_lshl_add_u32 v17, v17, 4, v0\n"
    "s_mul_i32 s40, s40, 9920\n"
    "s_sub_u32 s40, s49, s40\n"
    "v_bfe_u32 v18, v1, 4, 2\n"
    "v_lshlrev_b32 v18, 3, v18\n"
    "v_bfe_u32 v0, v1, 2, 2\n"
    "v_lshl_add_u32 v18, v0, 0, v18\n"
    "v_mul_u32_u24 v18, 320, v18\n"
    "v_and_b32 v0, 3, v1\n"
    "v_mul_u32_u24 v0, 8, v0\n"
    "v_add3_u32 v18, v18, v0, s40\n"
    "v_add_u32 v19, 57344, v18\n"
    "v_add_u32 v20, 114688, v18\n"
    "s_and_b32 s40, s21, 3\n"
    "s_cmp_lg_u32 s40, 0\n"
    "s_cbranch_scc1 .Lpn_48\n"
    "s_add_u32 s40, s48, 0\n"
    "s_add_u32 m0, s29, 0\n"
    "s_nop 0\n"
    "buffer_load_dwordx4 v168, s[12:15], s40 offen lds\n"
    "s_add_u32 m0, s29, 1024\n"
    "s_nop 0\n"
    "buffer_load_dwordx4 v169, s[12:15], s40 offen lds\n"
    "s_add_u32 m0, s29, 2048\n"
    "s_nop 0\n"
    "buffer_load_dwordx4 v170, s[12:15], s40 offen lds\n"
    "s_add_u32 s40, s48, 393216\n"
    "s_add_u32 m0, s29, 12288\n"
    "s_nop 0\n"
    "buffer_load_dwordx4 v168, s[12:15], s40 offen lds\n"
    "s_add_u32 m0, s29, 13312\n"
    "s_nop 0\n"
    "buffer_load_dwordx4 v169, s[12:15], s40 offen lds\n"
    "s_add_u32 m0, s29, 14336\n"
    "s_nop 0\n"
    "buffer_load_dwordx4 v170, s[12:15], s40 offen lds\n"
    "s_add_u32 s40, s48, 786432\n"
    "s_add_u32 m0, s29, 24576\n"
    "s_nop 0\n"
    "buffer_load_dwordx4 v168, s[12:15], s40 offen lds\n"
    "s_add_u32 m0, s29, 25600\n"
    "s_nop 0\n"
    "buffer_load_dwordx4 v169, s[12:15], s40 offen lds\n"
    "s_add_u32 m0, s29, 26624\n"
    "s_nop 0\n"
    "buffer_load_dwordx4 v170, s[12:15], s40 offen lds\n"
    "s_add_u32 s40, s48, 1179648\n"
    "s_add_u32 m0, s29, 36864\n"
    "s_nop 0\n"
    "buffer_load_dwordx4 v168, s[12:15], s40 offen lds\n"
    "s_add_u32 m0, s29, 37888\n"
    "s_nop 0\n"
    "buffer_load_dwordx4 v169, s[12:15], s40 offen lds\n"
    "s_add_u32 m0, s29, 38912\n"
    "s_nop 0\n"
    "buffer_load_dwordx4 v170, s[12:15], s40 offen lds\n"
    "s_add_u32 s40, s48, 1572864\n"
    "s_add_u32 m0, s29, 49152\n"
    "s_nop 0\n"
    "buffer_load_dwordx4 v168, s[12:15], s40 offen lds\n"
    "s_add_u32 m0, s29, 50176\n"
    "s_nop 0\n"
    "buffer_load_dwordx4 v169, s[12:15], s40 offen lds\n"
    "s_add_u32 m0, s29, 51200\n"
    "s_nop 0\n"
    "buffer_load_dwordx4 v170, s[12:15], s40 offen lds\n"
    "s_add_u32 s40, s48, 1966080\n"
    "s_add_u32 m0, s29, 61440\n"
    "s_nop 0\n"
    "buffer_load_dwordx4 v168, s[12:15], s40 offen lds\n"
    "s_add_u32 m0, s29, 62464\n"
    "s_nop 0\n"
    "buffer_load_dwordx4 v169, s[12:15], s40 offen lds\n"
    "s_add_u32 m0, s29, 63488\n"
    "s_nop 0\n"
    "buffer_load_dwordx4 v170, s[12:15], s40 offen lds\n"
    "s_add_u32 s40, s48, 2359296\n"
    "s_add_u32 m0, s29, 73728\n"
    "s_nop 0\n"
    "buffer_load_dwordx4 v168, s[12:15], s40 offen lds\n"
    "s_add_u32 m0, s29, 74752\n"
    "s_nop 0\n"
    "buffer_load_dwordx4 v169, s[12:15], s40 offen lds\n"
    "s_add_u32 m0, s29, 75776\n"
    "s_nop 0\n"
    "buffer_load_dwordx4 v170, s[12:15], s40 offen lds\n"
    "s_add_u32 s40, s48, 2752512\n"
    "s_add_u32 m0, s29, 86016\n"
    "s_nop 0\n"
    "buffer_load_dwordx4 v168, s[12:15], s40 offen lds\n"
    "s_add_u32 m0, s29, 87040\n"
    "s_nop 0\n"
    "buffer_load_dwordx4 v169, s[12:15], s40 offen lds\n"
    "s_add_u32 m0, s29, 88064\n"
    "s_nop 0\n"
    "buffer_load_dwordx4 v170, s[12:15], s40 offen lds\n"
    "s_add_u32 s40, s48, 3145728\n"
    "s_add_u32 m0, s29, 98304\n"
    "s_nop 0\n"
    "buffer_load_dwordx4 v168, s[12:15], s40 offen lds\n"
    "s_add_u32 m0, s29, 99328\n"
    "s_nop 0\n"
    "buffer_load_dwordx4 v169, s[12:15], s40 offen lds\n"
    "s_add_u32 m0, s29, 100352\n"
    "s_nop 0\n"
    "buffer_load_dwordx4 v170, s[12:15], s40 offen lds\n"
    "s_add_u32 s40, s48, 3538944\n"
    "s_add_u32 m0, s29, 110592\n"
    "s_nop 0\n"
    "buffer_load_dwordx4 v168, s[12:15], s40 offen lds\n"
    "s_add_u32 m0, s29, 111616\n"
    "s_nop 0\n"
    "buffer_load_dwordx4 v169, s[12:15], s40 offen lds\n"
    "s_add_u32 m0, s29, 112640\n"
    "s_nop 0\n"
    "buffer_load_dwordx4 v170, s[12:15], s40 offen lds\n"
    "s_add_u32 s40, s48, 3932160\n"
    "s_add_u32 m0, s29, 122880\n"
    "s_nop 0\n"
    "buffer_load_dwordx4 v168, s[12:15], s40 offen lds\n"
    "s_add_u32 m0, s29, 123904\n"
    "s_nop 0\n"
    "buffer_load_dwordx4 v169, s[12:15], s40 offen lds\n"
    "s_add_u32 m0, s29, 124928\n"
    "s_nop 0\n"
    "buffer_load_dwordx4 v170, s[12:15], s40 offen lds\n"
    "s_add_u32 s40, s48, 4325376\n"
    "s_add_u32 m0, s29, 135168\n"
    "s_nop 0\n"
    "buffer_load_dwordx4 v168, s[12:15], s40 offen lds\n"
    "s_add_u32 m0, s29, 136192\n"
    "s_nop 0\n"
    "buffer_load_dwordx4 v169, s[12:15], s40 offen lds\n"
    "s_add_u32 m0, s29, 137216\n"
    "s_nop 0\n"
    "buffer_load_dwordx4 v170, s[12:15], s40 offen lds\n"
    "s_add_u32 s40, s48, 4718592\n"
    "s_add_u32 m0, s29, 147456\n"
    "s_nop 0\n"
    "buffer_load_dwordx4 v168, s[12:15], s40 offen lds\n"
    "s_add_u32 m0, s29, 148480\n"
    "s_nop 0\n"
    "buffer_load_dwordx4 v169, s[12:15], s40 offen lds\n"
    "s_add_u32 m0, s29, 149504\n"
    "s_nop 0\n"
    "buffer_load_dwordx4 v170, s[12:15], s40 offen lds\n"
    "s_waitcnt vmcnt(36)\n"
    "s_barrier\n"
    "ds_read_b64_tr_b16 v[24:25], v171 offset:0\n"
    "ds_read_b64_tr_b16 v[26:27], v171 offset:1536\n"
    "ds_read_b64_tr_b16 v[32:33], v171 offset:32\n"
    "ds_read_b64_tr_b16 v[34:35], v171 offset:1568\n"
    "ds_read_b64_tr_b16 v[40:41], v171 offset:64\n"
    "ds_read_b64_tr_b16 v[42:43], v171 offset:1600\n"
    "s_waitcnt lgkmcnt(0)\n"
    "s_waitcnt vmcnt(33)\n"
    "s_barrier\n"
    "ds_read_b64_tr_b16 v[28:29], v171 offset:12288\n"
    "ds_read_b64_tr_b16 v[30:31], v171 offset:13824\n"
    "ds_read_b64_tr_b16 v[36:37], v171 offset:12320\n"
    "ds_read_b64_tr_b16 v[38:39], v171 offset:13856\n"
    "ds_read_b64_tr_b16 v[44:45], v171 offset:12352\n"
    "ds_read_b64_tr_b16 v[46:47], v171 offset:13888\n"
    "s_add_u32 s40, s48, 5111808\n"
    "s_add_u32 m0, s29, 0\n"
    "s_nop 0\n"
    "buffer_load_dwordx4 v168, s[12:15], s40 offen lds\n"
    "s_add_u32 m0, s29, 1024\n"
    "s_nop 0\n"
    "buffer_load_dwordx4 v169, s[12:15], s40 offen lds\n"
    "s_add_u32 m0, s29, 2048\n"
    "s_nop 0\n"
    "buffer_load_dwordx4 v170, s[12:15], s40 offen lds\n"
    "s_waitcnt lgkmcnt(0)\n"
    "s_waitcnt vmcnt(33)\n"
    "s_barrier\n"
    "ds_read_b64_tr_b16 v[48:49], v171 offset:24576\n"
    "ds_read_b64_tr_b16 v[50:51], v171 offset:26112\n"
    "ds_read_b64_tr_b16 v[56:57], v171 offset:24608\n"
    "ds_read_b64_tr_b16 v[58:59], v171 offset:26144\n"
    "ds_read_b64_tr_b16 v[64:65], v171 offset:24640\n"
    "ds_read_b64_tr_b16 v[66:67], v171 offset:26176\n"
    "s_add_u32 s40, s48, 5505024\n"
    "s_add_u32 m0, s29, 12288\n"
    "s_nop 0\n"
    "buffer_load_dwordx4 v168, s[12:15], s40 offen lds\n"
    "s_add_u32 m0, s29, 13312\n"
    "s_nop 0\n"
    "buffer_load_dwordx4 v169, s[12:15], s40 offen lds\n"
    "s_add_u32 m0, s29, 14336\n"
    "s_nop 0\n"
    "buffer_load_dwordx4 v170, s[12:15], s40 offen lds\n"
    "s_waitcnt lgkmcnt(0)\n"
    "s_waitcnt vmcnt(33)\n"
    "s_barrier\n"
    "ds_read_b64_tr_b16 v[52:53], v171 offset:36864\n"
    "ds_read_b64_tr_b16 v[54:55], v171 offset:38400\n"
    "ds_read_b64_tr_b16 v[60:61], v171 offset:36896\n"
    "ds_read_b64_tr_b16 v[62:63], v171 offset:38432\n"
    "ds_read_b64_tr_b16 v[68:69], v171 offset:36928\n"
    "ds_read_b64_tr_b16 v[70:71], v171 offset:38464\n"
    "s_add_u32 s40, s48, 5898240\n"
    "s_add_u32 m0, s29, 24576\n"
    "s_nop 0\n"
    "buffer_load_dwordx4 v168, s[12:15], s40 offen lds\n"
    "s_add_u32 m0, s29, 25600\n"
    "s_nop 0\n"
    "buffer_load_dwordx4 v169, s[12:15], s40 offen lds\n"
    "s_add_u32 m0, s29, 26624\n"
    "s_nop 0\n"
    "buffer_load_dwordx4 v170, s[12:15], s40 offen lds\n"
    "s_waitcnt lgkmcnt(0)\n"
    "s_waitcnt vmcnt(33)\n"
    "s_barrier\n"
    "ds_read_b64_tr_b16 v[72:73], v171 offset:49152\n"
    "ds_read_b64_tr_b16 v[74:75], v171 offset:50688\n"
    "ds_read_b64_tr_b16 a[0:1], v171 offset:49184\n"
    "ds_read_b64_tr_b16 a[2:3], v171 offset:50720\n"
    "ds_read_b64_tr_b16 a[8:9], v171 offset:49216\n"
    "ds_read_b64_tr_b16 a[10:11], v171 offset:50752\n"
    "s_add_u32 s40, s48, 6291456\n"
    "s_add_u32 m0, s29, 36864\n"
    "s_nop 0\n"
    "buffer_load_dwordx4 v168, s[12:15], s40 offen lds\n"
    "s_add_u32 m0, s29, 37888\n"
    "s_nop 0\n"
    "buffer_load_dwordx4 v169, s[12:15], s40 offen lds\n"
    "s_add_u32 m0, s29, 38912\n"
    "s_nop 0\n"
    "buffer_load_dwordx4 v170, s[12:15], s40 offen lds\n"
    "s_waitcnt lgkmcnt(0)\n"
    "s_waitcnt vmcnt(33)\n"
    "s_barrier\n"
    "ds_read_b64_tr_b16 v[76:77], v172 offset:4096\n"
    "ds_read_b64_tr_b16 v[78:79], v172 offset:5632\n"
    "ds_read_b64_tr_b16 a[4:5], v172 offset:4128\n"
    "ds_read_b64_tr_b16 a[6:7], v172 offset:5664\n"
    "ds_read_b64_tr_b16 a[12:13], v172 offset:4160\n"
    "ds_read_b64_tr_b16 a[14:15], v172 offset:5696\n"
    "s_add_u32 s40, s48, 6684672\n"
    "s_add_u32 m0, s29, 49152\n"
    "s_nop 0\n"
    "buffer_load_dwordx4 v168, s[12:15], s40 offen lds\n"
    "s_add_u32 m0, s29, 50176\n"
    "s_nop 0\n"
    "buffer_load_dwordx4 v169, s[12:15], s40 offen lds\n"
    "s_add_u32 m0, s29, 51200\n"
    "s_nop 0\n"
    "buffer_load_dwordx4 v170, s[12:15], s40 offen lds\n"
    "s_waitcnt lgkmcnt(0)\n"
    "s_waitcnt vmcnt(33)\n"
    "s_barrier\n"
    "ds_read_b64_tr_b16 a[16:17], v172 offset:16384\n"
    "ds_read_b64_tr_b16 a[18:19], v172 offset:17920\n"
    "ds_read_b64_tr_b16 a[72:73], v172 offset:16416\n"
    "ds_read_b64_tr_b16 a[74:75], v172 offset:17952\n"
    "ds_read_b64_tr_b16 a[128:129], v172 offset:16448\n"
    "ds_read_b64_tr_b16 a[130:131], v172 offset:17984\n"
    "s_add_u32 s40, s48, 7077888\n"
    "s_add_u32 m0, s29, 61440\n"
    "s_nop 0\n"
    "buffer_load_dwordx4 v168, s[12:15], s40 offen lds\n"
    "s_add_u32 m0, s29, 62464\n"
    "s_nop 0\n"
    "buffer_load_dwordx4 v169, s[12:15], s40 offen lds\n"
    "s_add_u32 m0, s29, 63488\n"
    "s_nop 0\n"
    "buffer_load_dwordx4 v170, s[12:15], s40 offen lds\n"
    "s_waitcnt lgkmcnt(0)\n"
    "s_waitcnt vmcnt(33)\n"
    "s_barrier\n"
    "ds_read_b64_tr_b16 a[20:21], v172 offset:28672\n"
    "ds_read_b64_tr_b16 a[22:23], v172 offset:30208\n"
    "ds_read_b64_tr_b16 a[76:77], v172 offset:28704\n"
    "ds_read_b64_tr_b16 a[78:79], v172 offset:30240\n"
    "ds_read_b64_tr_b16 a[132:133], v172 offset:28736\n"
    "ds_read_b64_tr_b16 a[134:135], v172 offset:30272\n"
    "s_add_u32 s40, s48, 7471104\n"
    "s_add_u32 m0, s29, 73728\n"
    "s_nop 0\n"
    "buffer_load_dwordx4 v168, s[12:15], s40 offen lds\n"
    "s_add_u32 m0, s29, 74752\n"
    "s_nop 0\n"
    "buffer_load_dwordx4 v169, s[12:15], s40 offen lds\n"
    "s_add_u32 m0, s29, 75776\n"
    "s_nop 0\n"
    "buffer_load_dwordx4 v170, s[12:15], s40 offen lds\n"
    "s_waitcnt lgkmcnt(0)\n"
    "s_waitcnt vmcnt(33)\n"
    "s_barrier\n"
    "ds_read_b64_tr_b16 a[24:25], v172 offset:40960\n"
    "ds_read_b64_tr_b16 a[26:27], v172 offset:42496\n"
    "ds_read_b64_tr_b16 a[80:81], v172 offset:40992\n"
    "ds_read_b64_tr_b16 a[82:83], v172 offset:42528\n"
    "ds_read_b64_tr_b16 a[136:137], v172 offset:41024\n"
    "ds_read_b64_tr_b16 a[138:139], v172 offset:42560\n"
    "s_waitcnt lgkmcnt(0)\n"
    "s_waitcnt vmcnt(30)\n"
    "s_barrier\n"
    "ds_read_b64_tr_b16 a[28:29], v172 offset:53248\n"
    "ds_read_b64_tr_b16 a[30:31], v172 offset:54784\n"
    "ds_read_b64_tr_b16 a[84:85], v172 offset:53280\n"
    "ds_read_b64_tr_b16 a[86:87], v172 offset:54816\n"
    "ds_read_b64_tr_b16 a[140:141], v172 offset:53312\n"
    "ds_read_b64_tr_b16 a[142:143], v172 offset:54848\n"
    "s_waitcnt lgkmcnt(0)\n"
    "s_waitcnt vmcnt(27)\n"
    "s_barrier\n"
    "ds_read_b64_tr_b16 a[32:33], v173 offset:8192\n"
    "ds_read_b64_tr_b16 a[34:35], v173 offset:9728\n"
    "ds_read_b64_tr_b16 a[88:89], v173 offset:8224\n"
    "ds_read_b64_tr_b16 a[90:91], v173 offset:9760\n"
    "ds_read_b64_tr_b16 a[144:145], v173 offset:8256\n"
    "ds_read_b64_tr_b16 a[146:147], v173 offset:9792\n"
    "s_waitcnt lgkmcnt(0)\n"
    "s_waitcnt vmcnt(24)\n"
    "s_barrier\n"
    "ds_read_b64_tr_b16 a[36:37], v173 offset:20480\n"
    "ds_read_b64_tr_b16 a[38:39], v173 offset:22016\n"
    "ds_read_b64_tr_b16 a[92:93], v173 offset:20512\n"
    "ds_read_b64_tr_b16 a[94:95], v173 offset:22048\n"
    "ds_read_b64_tr_b16 a[148:149], v173 offset:20544\n"
    "ds_read_b64_tr_b16 a[150:151], v173 offset:22080\n"
    "s_add_u32 s40, s51, 0\n"
    "s_add_u32 m0, s49, 86016\n"
    "s_nop 0\n"
    "buffer_load_dwordx4 v8, s[16:19], s40 offen lds\n"
    "s_add_u32 m0, s49, 87040\n"
    "s_nop 0\n"
    "buffer_load_dwordx4 v9, s[16:19], s40 offen lds\n"
    "s_add_u32 m0, s49, 88064\n"
    "s_nop 0\n"
    "buffer_load_dwordx4 v10, s[16:19], s40 offen lds\n"
    "s_add_u32 m0, s49, 89088\n"
    "s_nop 0\n"
    "buffer_load_dwordx4 v11, s[16:19], s40 offen lds\n"
    "s_add_u32 m0, s49, 90112\n"
    "s_nop 0\n"
    "buffer_load_dwordx4 v12, s[16:19], s40 offen lds\n"
    "s_add_u32 m0, s49, 91136\n"
    "s_nop 0\n"
    "buffer_load_dwordx4 v13, s[16:19], s40 offen lds\n"
    "s_add_u32 m0, s49, 92160\n"
    "s_nop 0\n"
    "buffer_load_dwordx4 v14, s[16:19], s40 offen lds\n"
    "s_add_u32 m0, s49, 93184\n"
    "s_nop 0\n"
    "buffer_load_dwordx4 v15, s[16:19], s40 offen lds\n"
    "s_add_u32 m0, s49, 94208\n"
    "s_nop 0\n"
    "buffer_load_dwordx4 v16, s[16:19], s40 offen lds\n"
    "s_add_u32 m0, s49, 95232\n"
    "s_nop 0\n"
    "buffer_load_dwordx4 v17, s[16:19], s40 offen lds\n"
    "s_waitcnt lgkmcnt(0)\n"
    "s_waitcnt vmcnt(31)\n"
    "s_barrier\n"
    "ds_read_b64_tr_b16 a[40:41], v173 offset:32768\n"
    "ds_read_b64_tr_b16 a[42:43], v173 offset:34304\n"
    "ds_read_b64_tr_b16 a[96:97], v173 offset:32800\n"
    "ds_read_b64_tr_b16 a[98:99], v173 offset:34336\n"
    "ds_read_b64_tr_b16 a[152:153], v173 offset:32832\n"
    "ds_read_b64_tr_b16 a[154:155], v173 offset:34368\n"
    "s_waitcnt lgkmcnt(0)\n"
    "s_waitcnt vmcnt(28)\n"
    "s_barrier\n"
    "ds_read_b64_tr_b16 a[44:45], v171 offset:0\n"
    "ds_read_b64_tr_b16 a[46:47], v171 offset:1536\n"
    "ds_read_b64_tr_b16 a[100:101], v171 offset:32\n"
    "ds_read_b64_tr_b16 a[102:103], v171 offset:1568\n"
    "ds_read_b64_tr_b16 a[156:157], v171 offset:64\n"
    "ds_read_b64_tr_b16 a[158:159], v171 offset:1600\n"
    "s_waitcnt lgkmcnt(0)\n"
    "s_waitcnt vmcnt(25)\n"
    "s_barrier\n"
    "ds_read_b64_tr_b16 a[48:49], v171 offset:12288\n"
    "ds_read_b64_tr_b16 a[50:51], v171 offset:13824\n"
    "ds_read_b64_tr_b16 a[104:105], v171 offset:12320\n"
    "ds_read_b64_tr_b16 a[106:107], v171 offset:13856\n"
    "ds_read_b64_tr_b16 a[160:161], v171 offset:12352\n"
    "ds_read_b64_tr_b16 a[162:163], v171 offset:13888\n"
    "s_waitcnt lgkmcnt(0)\n"
    "s_waitcnt vmcnt(22)\n"
    "s_barrier\n"
    "ds_read_b64_tr_b16 a[52:53], v171 offset:24576\n"
    "ds_read_b64_tr_b16 a[54:55], v171 offset:26112\n"
    "ds_read_b64_tr_b16 a[108:109], v171 offset:24608\n"
    "ds_read_b64_tr_b16 a[110:111], v171 offset:26144\n"
    "ds_read_b64_tr_b16 a[164:165], v171 offset:24640\n"
    "ds_read_b64_tr_b16 a[166:167], v171 offset:26176\n"
    "s_waitcnt lgkmcnt(0)\n"
    "s_waitcnt vmcnt(19)\n"
    "s_barrier\n"
    "ds_read_b64_tr_b16 a[56:57], v171 offset:36864\n"
    "ds_read_b64_tr_b16 a[58:59], v171 offset:38400\n"
    "ds_read_b64_tr_b16 a[112:113], v171 offset:36896\n"
    "ds_read_b64_tr_b16 a[114:115], v171 offset:38432\n"
    "ds_read_b64_tr_b16 a[168:169], v171 offset:36928\n"
    "ds_read_b64_tr_b16 a[170:171], v171 offset:38464\n"
    "s_waitcnt lgkmcnt(0)\n"
    "s_waitcnt vmcnt(16)\n"
    "s_barrier\n"
    "ds_read_b64_tr_b16 a[60:61], v171 offset:49152\n"
    "ds_read_b64_tr_b16 a[62:63], v171 offset:50688\n"
    "ds_read_b64_tr_b16 a[116:117], v171 offset:49184\n"
    "ds_read_b64_tr_b16 a[118:119], v171 offset:50720\n"
    "ds_read_b64_tr_b16 a[172:173], v171 offset:49216\n"
    "ds_read_b64_tr_b16 a[174:175], v171 offset:50752\n"
    "s_add_u32 s40, s51, 122880\n"
    "s_add_u32 m0, s49, 0\n"
    "s_nop 0\n"
    "buffer_load_dwordx4 v8, s[16:19], s40 offen lds\n"
    "s_add_u32 m0, s49, 1024\n"
    "s_nop 0\n"
    "buffer_load_dwordx4 v9, s[16:19], s40 offen lds\n"
    "s_add_u32 m0, s49, 2048\n"
    "s_nop 0\n"
    "buffer_load_dwordx4 v10, s[16:19], s40 offen lds\n"
    "s_add_u32 m0, s49, 3072\n"
    "s_nop 0\n"
    "buffer_load_dwordx4 v11, s[16:19], s40 offen lds\n"
    "s_add_u32 m0, s49, 4096\n"
    "s_nop 0\n"
    "buffer_load_dwordx4 v12, s[16:19], s40 offen lds\n"
    "s_add_u32 m0, s49, 5120\n"
    "s_nop 0\n"
    "buffer_load_dwordx4 v13, s[16:19], s40 offen lds\n"
    "s_add_u32 m0, s49, 6144\n"
    "s_nop 0\n"
    "buffer_load_dwordx4 v14, s[16:19], s40 offen lds\n"
    "s_add_u32 m0, s49, 7168\n"
    "s_nop 0\n"
    "buffer_load_dwordx4 v15, s[16:19], s40 offen lds\n"
    "s_add_u32 m0, s49, 8192\n"
    "s_nop 0\n"
    "buffer_load_dwordx4 v16, s[16:19], s40 offen lds\n"
    "s_add_u32 m0, s49, 9216\n"
    "s_nop 0\n"
    "buffer_load_dwordx4 v17, s[16:19], s40 offen lds\n"
    "s_waitcnt lgkmcnt(0)\n"
    "s_add_u32 s4, s3, 1\n"
    "v_mov_b32 v2, s3\n"
    "s_or_b32 s41, s21, s6\n"
    "s_cbranch_scc1 .Lcl_49\n"
    "s_lshl_b32 s41, s5, 2\n"
    "s_add_u32 s41, s41, 4178944\n"
    "v_mov_b32 v3, s41\n"
    "v_mov_b32 v2, s4\n"
    "s_mov_b64 exec, 1\n"
    "buffer_atomic_swap v2, v3, s[8:11], 0 offen sc0 sc1\n"
    "s_mov_b64 exec, -1\n"
    ".Lcl_49:\n"
    "v_min_u32 v6, 54, v1\n"
    "v_lshlrev_b32 v6, 7, v6\n"
    "v_add_u32 v6, 4182016, v6\n"
    "buffer_load_dwordx2 v[4:5], v6, s[8:11], 0 offen sc1\n"
    "s_waitcnt lgkmcnt(0)\n"
    "s_waitcnt vmcnt(24)\n"
    "s_barrier\n"
    "ds_read_b64_tr_b16 a[64:65], v172 offset:4096\n"
    "ds_read_b64_tr_b16 a[66:67], v172 offset:5632\n"
    "ds_read_b64_tr_b16 a[120:121], v172 offset:4128\n"
    "ds_read_b64_tr_b16 a[122:123], v172 offset:5664\n"
    "ds_read_b64_tr_b16 a[176:177], v172 offset:4160\n"
    "ds_read_b64_tr_b16 a[178:179], v172 offset:5696\n"
    "s_waitcnt lgkmcnt(0)\n"
    "s_waitcnt vmcnt(21)\n"
    "s_barrier\n"
    "ds_read_b64_tr_b16 a[68:69], v172 offset:16384\n"
    "ds_read_b64_tr_b16 a[70:71], v172 offset:17920\n"
    "ds_read_b64_tr_b16 a[124:125], v172 offset:16416\n"
    "ds_read_b64_tr_b16 a[126:127], v172 offset:17952\n"
    "ds_read_b64_tr_b16 a[180:181], v172 offset:16448\n"
    "ds_read_b64_tr_b16 a[182:183], v172 offset:17984\n"
    "s_waitcnt lgkmcnt(0)\n"
    "s_waitcnt vmcnt(11)\n"
    "s_barrier\n"
    "ds_read_b64_tr_b16 v[80:81], v19 offset:28672\n"
    "ds_read_b64_tr_b16 v[82:83], v19 offset:29952\n"
    "ds_read_b64_tr_b16 v[88:89], v19 offset:28704\n"
    "ds_read_b64_tr_b16 v[90:91], v19 offset:29984\n"
    "ds_read_b64_tr_b16 v[96:97], v19 offset:28736\n"
    "ds_read_b64_tr_b16 v[98:99], v19 offset:30016\n"
    "ds_read_b64_tr_b16 v[104:105], v19 offset:28768\n"
    "ds_read_b64_tr_b16 v[106:107], v19 offset:30048\n"
    "ds_read_b64_tr_b16 v[112:113], v19 offset:28800\n"
    "ds_read_b64_tr_b16 v[114:115], v19 offset:30080\n"
    "ds_read_b64_tr_b16 v[120:121], v19 offset:28832\n"
    "ds_read_b64_tr_b16 v[122:123], v19 offset:30112\n"
    "ds_read_b64_tr_b16 v[128:129], v19 offset:28864\n"
    "ds_read_b64_tr_b16 v[130:131], v19 offset:30144\n"
    "ds_read_b64_tr_b16 a[184:185], v19 offset:28896\n"
    "ds_read_b64_tr_b16 a[186:187], v19 offset:30176\n"
    "ds_read_b64_tr_b16 v[208:209], v19 offset:28928\n"
    "ds_read_b64_tr_b16 v[210:211], v19 offset:30208\n"
    "ds_read_b64_tr_b16 v[232:233], v19 offset:28960\n"
    "ds_read_b64_tr_b16 v[234:235], v19 offset:30240\n"
    "s_add_u32 s40, s51, 20480\n"
    "s_add_u32 m0, s49, 40960\n"
    "s_nop 0\n"
    "buffer_load_dwordx4 v8, s[16:19], s40 offen lds\n"
    "s_add_u32 m0, s49, 41984\n"
    "s_nop 0\n"
    "buffer_load_dwordx4 v9, s[16:19], s40 offen lds\n"
    "s_add_u32 m0, s49, 43008\n"
    "s_nop 0\n"
    "buffer_load_dwordx4 v10, s[16:19], s40 offen lds\n"
    "s_add_u32 m0, s49, 44032\n"
    "s_nop 0\n"
    "buffer_load_dwordx4 v11, s[16:19], s40 offen lds\n"
    "s_add_u32 m0, s49, 45056\n"
    "s_nop 0\n"
    "buffer_load_dwordx4 v12, s[16:19], s40 offen lds\n"
    "s_add_u32 m0, s49, 46080\n"
    "s_nop 0\n"
    "buffer_load_dwordx4 v13, s[16:19], s40 offen lds\n"
    "s_add_u32 m0, s49, 47104\n"
    "s_nop 0\n"
    "buffer_load_dwordx4 v14, s[16:19], s40 offen lds\n"
    "s_add_u32 m0, s49, 48128\n"
    "s_nop 0\n"
    "buffer_load_dwordx4 v15, s[16:19], s40 offen lds\n"
    "s_add_u32 m0, s49, 49152\n"
    "s_nop 0\n"
    "buffer_load_dwordx4 v16, s[16:19], s40 offen lds\n"
    "s_add_u32 m0, s49, 50176\n"
    "s_nop 0\n"
    "buffer_load_dwordx4 v17, s[16:19], s40 offen lds\n"
    "s_waitcnt lgkmcnt(0)\n"
    "s_waitcnt vmcnt(11)\n"
    "s_barrier\n"
    "ds_read_b64_tr_b16 v[84:85], v18 offset:0\n"
    "ds_read_b64_tr_b16 v[86:87], v18 offset:1280\n"
    "ds_read_b64_tr_b16 v[92:93], v18 offset:32\n"
    "ds_read_b64_tr_b16 v[94:95], v18 offset:1312\n"
    "ds_read_b64_tr_b16 v[100:101], v18 offset:64\n"
    "ds_read_b64_tr_b16 v[102:103], v18 offset:1344\n"
    "ds_read_b64_tr_b16 v[108:109], v18 offset:96\n"
    "ds_read_b64_tr_b16 v[110:111], v18 offset:1376\n"
    "ds_read_b64_tr_b16 v[116:117], v18 offset:128\n"
    "ds_read_b64_tr_b16 v[118:119], v18 offset:1408\n"
    "ds_read_b64_tr_b16 v[124:125], v18 offset:160\n"
    "ds_read_b64_tr_b16 v[126:127], v18 offset:1440\n"
    "ds_read_b64_tr_b16 v[132:133], v18 offset:192\n"
    "ds_read_b64_tr_b16 v[134:135], v18 offset:1472\n"
    "ds_read_b64_tr_b16 a[188:189], v18 offset:224\n"
    "ds_read_b64_tr_b16 a[190:191], v18 offset:1504\n"
    "ds_read_b64_tr_b16 v[212:213], v18 offset:256\n"
    "ds_read_b64_tr_b16 v[214:215], v18 offset:1536\n"
    "ds_read_b64_tr_b16 v[236:237], v18 offset:288\n"
    "ds_read_b64_tr_b16 v[238:239], v18 offset:1568\n"
    "s_add_u32 s40, s51, 143360\n"
    "s_add_u32 m0, s49, 81920\n"
    "s_nop 0\n"
    "buffer_load_dwordx4 v8, s[16:19], s40 offen lds\n"
    "s_add_u32 m0, s49, 82944\n"
    "s_nop 0\n"
    "buffer_load_dwordx4 v9, s[16:19], s40 offen lds\n"
    "s_add_u32 m0, s49, 83968\n"
    "s_nop 0\n"
    "buffer_load_dwordx4 v10, s[16:19], s40 offen lds\n"
    "s_add_u32 m0, s49, 84992\n"
    "s_nop 0\n"
    "buffer_load_dwordx4 v11, s[16:19], s40 offen lds\n"
    "s_add_u32 m0, s49, 86016\n"
    "s_nop 0\n"
    "buffer_load_dwordx4 v12, s[16:19], s40 offen lds\n"
    "s_add_u32 m0, s49, 87040\n"
    "s_nop 0\n"
    "buffer_load_dwordx4 v13, s[16:19], s40 offen lds\n"
    "s_add_u32 m0, s49, 88064\n"
    "s_nop 0\n"
    "buffer_load_dwordx4 v14, s[16:19], s40 offen lds\n"
    "s_add_u32 m0, s49, 89088\n"
    "s_nop 0\n"
    "buffer_load_dwordx4 v15, s[16:19], s40 offen lds\n"
    "s_add_u32 m0, s49, 90112\n"
    "s_nop 0\n"
    "buffer_load_dwordx4 v16, s[16:19], s40 offen lds\n"
    "s_add_u32 m0, s49, 91136\n"
    "s_nop 0\n"
    "buffer_load_dwordx4 v17, s[16:19], s40 offen lds\n"
    "s_waitcnt lgkmcnt(0)\n"
    "s_waitcnt vmcnt(10)\n"
    "s_barrier\n"
    "ds_read_b64_tr_b16 a[192:193], v18 offset:40960\n"
    "ds_read_b64_tr_b16 a[194:195], v18 offset:42240\n"
    "ds_read_b64_tr_b16 a[200:201], v18 offset:40992\n"
    "ds_read_b64_tr_b16 a[202:203], v18 offset:42272\n"
    "ds_read_b64_tr_b16 a[208:209], v18 offset:41024\n"
    "ds_read_b64_tr_b16 a[210:211], v18 offset:42304\n"
    "ds_read_b64_tr_b16 a[216:217], v18 offset:41056\n"
    "ds_read_b64_tr_b16 a[218:219], v18 offset:42336\n"
    "ds_read_b64_tr_b16 a[224:225], v18 offset:41088\n"
    "ds_read_b64_tr_b16 a[226:227], v18 offset:42368\n"
    "ds_read_b64_tr_b16 a[232:233], v18 offset:41120\n"
    "ds_read_b64_tr_b16 a[234:235], v18 offset:42400\n"
    "ds_read_b64_tr_b16 v[136:137], v18 offset:41152\n"
    "ds_read_b64_tr_b16 v[138:139], v18 offset:42432\n"
    "ds_read_b64_tr_b16 v[152:153], v18 offset:41184\n"
    "ds_read_b64_tr_b16 v[154:155], v18 offset:42464\n"
    "ds_read_b64_tr_b16 v[216:217], v18 offset:41216\n"
    "ds_read_b64_tr_b16 v[218:219], v18 offset:42496\n"
    "ds_read_b64_tr_b16 v[240:241], v18 offset:41248\n"
    "ds_read_b64_tr_b16 v[242:243], v18 offset:42528\n"
    "s_add_u32 s40, s51, 40960\n"
    "s_add_u32 m0, s49, 0\n"
    "s_nop 0\n"
    "buffer_load_dwordx4 v8, s[16:19], s40 offen lds\n"
    "s_add_u32 m0, s49, 1024\n"
    "s_nop 0\n"
    "buffer_load_dwordx4 v9, s[16:19], s40 offen lds\n"
    "s_add_u32 m0, s49, 2048\n"
    "s_nop 0\n"
    "buffer_load_dwordx4 v10, s[16:19], s40 offen lds\n"
    "s_add_u32 m0, s49, 3072\n"
    "s_nop 0\n"
    "buffer_load_dwordx4 v11, s[16:19], s40 offen lds\n"
    "s_add_u32 m0, s49, 4096\n"
    "s_nop 0\n"
    "buffer_load_dwordx4 v12, s[16:19], s40 offen lds\n"
    "s_add_u32 m0, s49, 5120\n"
    "s_nop 0\n"
    "buffer_load_dwordx4 v13, s[16:19], s40 offen lds\n"
    "s_add_u32 m0, s49, 6144\n"
    "s_nop 0\n"
    "buffer_load_dwordx4 v14, s[16:19], s40 offen lds\n"
    "s_add_u32 m0, s49, 7168\n"
    "s_nop 0\n"
    "buffer_load_dwordx4 v15, s[16:19], s40 offen lds\n"
    "s_add_u32 m0, s49, 8192\n"
    "s_nop 0\n"
    "buffer_load_dwordx4 v16, s[16:19], s40 offen lds\n"
    "s_add_u32 m0, s49, 9216\n"
    "s_nop 0\n"
    "buffer_load_dwordx4 v17, s[16:19], s40 offen lds\n"
    "s_waitcnt lgkmcnt(0)\n"
    "s_waitcnt vmcnt(10)\n"
    "s_barrier\n"
    "ds_read_b64_tr_b16 a[196:197], v19 offset:24576\n"
    "ds_read_b64_tr_b16 a[198:199], v19 offset:25856\n"
    "ds_read_b64_tr_b16 a[204:205], v19 offset:24608\n"
    "ds_read_b64_tr_b16 a[206:207], v19 offset:25888\n"
    "ds_read_b64_tr_b16 a[212:213], v19 offset:24640\n"
    "ds_read_b64_tr_b16 a[214:215], v19 offset:25920\n"
    "ds_read_b64_tr_b16 a[220:221], v19 offset:24672\n"
    "ds_read_b64_tr_b16 a[222:223], v19 offset:25952\n"
    "ds_read_b64_tr_b16 a[228:229], v19 offset:24704\n"
    "ds_read_b64_tr_b16 a[230:231], v19 offset:25984\n"
    "ds_read_b64_tr_b16 a[236:237], v19 offset:24736\n"
    "ds_read_b64_tr_b16 a[238:239], v19 offset:26016\n"
    "ds_read_b64_tr_b16 v[140:141], v19 offset:24768\n"
    "ds_read_b64_tr_b16 v[142:143], v19 offset:26048\n"
    "ds_read_b64_tr_b16 v[156:157], v19 offset:24800\n"
    "ds_read_b64_tr_b16 v[158:159], v19 offset:26080\n"
    "ds_read_b64_tr_b16 v[220:221], v19 offset:24832\n"
    "ds_read_b64_tr_b16 v[222:223], v19 offset:26112\n"
    "ds_read_b64_tr_b16 v[244:245], v19 offset:24864\n"
    "ds_read_b64_tr_b16 v[246:247], v19 offset:26144\n"
    "s_add_u32 s40, s51, 163840\n"
    "s_add_u32 m0, s49, 40960\n"
    "s_nop 0\n"
    "buffer_load_dwordx4 v8, s[16:19], s40 offen lds\n"
    "s_add_u32 m0, s49, 41984\n"
    "s_nop 0\n"
    "buffer_load_dwordx4 v9, s[16:19], s40 offen lds\n"
    "s_add_u32 m0, s49, 43008\n"
    "s_nop 0\n"
    "buffer_load_dwordx4 v10, s[16:19], s40 offen lds\n"
    "s_add_u32 m0, s49, 44032\n"
    "s_nop 0\n"
    "buffer_load_dwordx4 v11, s[16:19], s40 offen lds\n"
    "s_add_u32 m0, s49, 45056\n"
    "s_nop 0\n"
    "buffer_load_dwordx4 v12, s[16:19], s40 offen lds\n"
    "s_add_u32 m0, s49, 46080\n"
    "s_nop 0\n"
    "buffer_load_dwordx4 v13, s[16:19], s40 offen lds\n"
    "s_add_u32 m0, s49, 47104\n"
    "s_nop 0\n"
    "buffer_load_dwordx4 v14, s[16:19], s40 offen lds\n"
    "s_add_u32 m0, s49, 48128\n"
    "s_nop 0\n"
    "buffer_load_dwordx4 v15, s[16:19], s40 offen lds\n"
    "s_add_u32 m0, s49, 49152\n"
    "s_nop 0\n"
    "buffer_load_dwordx4 v16, s[16:19], s40 offen lds\n"
    "s_add_u32 m0, s49, 50176\n"
    "s_nop 0\n"
    "buffer_load_dwordx4 v17, s[16:19], s40 offen lds\n"
    "s_waitcnt lgkmcnt(0)\n"
    "s_waitcnt vmcnt(10)\n"
    "s_barrier\n"
    "ds_read_b64_tr_b16 a[240:241], v18 offset:0\n"
    "ds_read_b64_tr_b16 a[242:243], v18 offset:1280\n"
    "ds_read_b64_tr_b16 a[248:249], v18 offset:32\n"
    "ds_read_b64_tr_b16 a[250:251], v18 offset:1312\n"
    "ds_read_b64_tr_b16 v[176:177], v18 offset:64\n"
    "ds_read_b64_tr_b16 v[178:179], v18 offset:1344\n"
    "ds_read_b64_tr_b16 v[184:185], v18 offset:96\n"
    "ds_read_b64_tr_b16 v[186:187], v18 offset:1376\n"
    "ds_read_b64_tr_b16 v[192:193], v18 offset:128\n"
    "ds_read_b64_tr_b16 v[194:195], v18 offset:1408\n"
    "ds_read_b64_tr_b16 v[200:201], v18 offset:160\n"
    "ds_read_b64_tr_b16 v[202:203], v18 offset:1440\n"
    "ds_read_b64_tr_b16 v[144:145], v18 offset:192\n"
    "ds_read_b64_tr_b16 v[146:147], v18 offset:1472\n"
    "ds_read_b64_tr_b16 v[160:161], v18 offset:224\n"
    "ds_read_b64_tr_b16 v[162:163], v18 offset:1504\n"
    "ds_read_b64_tr_b16 v[224:225], v18 offset:256\n"
    "ds_read_b64_tr_b16 v[226:227], v18 offset:1536\n"
    "ds_read_b64_tr_b16 v[248:249], v18 offset:288\n"
    "ds_read_b64_tr_b16 v[250:251], v18 offset:1568\n"
    "s_waitcnt lgkmcnt(0)\n"
    "s_waitcnt vmcnt(0)\n"
    "s_barrier\n"
    "ds_read_b64_tr_b16 a[244:245], v18 offset:40960\n"
    "ds_read_b64_tr_b16 a[246:247], v18 offset:42240\n"
    "ds_read_b64_tr_b16 a[252:253], v18 offset:40992\n"
    "ds_read_b64_tr_b16 a[254:255], v18 offset:42272\n"
    "ds_read_b64_tr_b16 v[180:181], v18 offset:41024\n"
    "ds_read_b64_tr_b16 v[182:183], v18 offset:42304\n"
    "ds_read_b64_tr_b16 v[188:189], v18 offset:41056\n"
    "ds_read_b64_tr_b16 v[190:191], v18 offset:42336\n"
    "ds_read_b64_tr_b16 v[196:197], v18 offset:41088\n"
    "ds_read_b64_tr_b16 v[198:199], v18 offset:42368\n"
    "ds_read_b64_tr_b16 v[204:205], v18 offset:41120\n"
    "ds_read_b64_tr_b16 v[206:207], v18 offset:42400\n"
    "ds_read_b64_tr_b16 v[148:149], v18 offset:41152\n"
    "ds_read_b64_tr_b16 v[150:151], v18 offset:42432\n"
    "ds_read_b64_tr_b16 v[164:165], v18 offset:41184\n"
    "ds_read_b64_tr_b16 v[166:167], v18 offset:42464\n"
    "ds_read_b64_tr_b16 v[228:229], v18 offset:41216\n"
    "ds_read_b64_tr_b16 v[230:231], v18 offset:42496\n"
    "ds_read_b64_tr_b16 v[252:253], v18 offset:41248\n"
    "ds_read_b64_tr_b16 v[254:255], v18 offset:42528\n"
    "s_waitcnt lgkmcnt(0)\n"
    "v_cmp_ne_u32 vcc, s4, v4\n"
    "s_nop 1\n"
    "s_cmp_eq_u32 vcc_lo, 0\n"
    "s_cbranch_scc1 .Lrx_52\n"
    ".Lrp_50:\n"
    "v_min_u32 v6, 54, v1\n"
    "v_lshlrev_b32 v6, 7, v6\n"
    "v_add_u32 v6, 4182016, v6\n"
    "buffer_load_dwordx2 v[4:5], v6, s[8:11], 0 offen sc1\n"
    "s_waitcnt vmcnt(0)\n"
    "v_cmp_ne_u32 vcc, s4, v4\n"
    "s_nop 1\n"
    "s_cmp_eq_u32 vcc_lo, 0\n"
    "s_cbranch_scc1 .Lro_51\n"
    "s_sleep 2\n"
    "s_sub_u32 s25, s25, 1\n"
    "s_cbranch_scc0 .Lrp_50\n"
    "s_branch .Lfail\n"
    ".Lro_51:\n"
    ".Lrx_52:\n"
    "v_cmp_ne_u32 vcc, s4, v4\n"
    "s_nop 1\n"
    "s_cmp_eq_u32 vcc_hi, 0\n"
    "s_cbranch_scc1 .Lrx_55\n"
    ".Lrp_53:\n"
    "v_min_u32 v6, 54, v1\n"
    "v_lshlrev_b32 v6, 7, v6\n"
    "v_add_u32 v6, 4182016, v6\n"
    "buffer_load_dwordx2 v[4:5], v6, s[8:11], 0 offen sc1\n"
    "s_waitcnt vmcnt(0)\n"
    "v_cmp_ne_u32 vcc, s4, v4\n"
    "s_nop 1\n"
    "s_cmp_eq_u32 vcc_hi, 0\n"
    "s_cbranch_scc1 .Lro_54\n"
    "s_sleep 2\n"
    "s_sub_u32 s25, s25, 1\n"
    "s_cbranch_scc0 .Lrp_53\n"
    "s_branch .Lfail\n"
    ".Lro_54:\n"
    ".Lrx_55:\n"
    "v_cmp_ne_u32 vcc, 0, v5\n"
    "s_nop 1\n"
    "s_and_b32 s22, vcc_hi, 1\n"
    "s_lshl_b32 s22, s22, 13\n"
    "s_lshr_b32 s23, vcc_hi, 1\n"
    "s_branch .Lpw_47\n"
    ".Lpn_48:\n"
    "s_and_b32 s40, s21, 3\n"
    "s_cmp_lg_u32 s40, 1\n"
    "s_cbranch_scc1 .Lpn_56\n"
    "s_add_u32 s40, s48, 0\n"
    "s_add_u32 m0, s29, 0\n"
    "s_nop 0\n"
    "buffer_load_dwordx4 v168, s[12:15], s40 offen lds\n"
    "s_add_u32 m0, s29, 1024\n"
    "s_nop 0\n"
    "buffer_load_dwordx4 v169, s[12:15], s40 offen lds\n"
    "s_add_u32 m0, s29, 2048\n"
    "s_nop 0\n"
    "buffer_load_dwordx4 v170, s[12:15], s40 offen lds\n"
    "s_add_u32 s40, s48, 393216\n"
    "s_add_u32 m0, s29, 12288\n"
    "s_nop 0\n"
    "buffer_load_dwordx4 v168, s[12:15], s40 offen lds\n"
    "s_add_u32 m0, s29, 13312\n"
    "s_nop 0\n"
    "buffer_load_dwordx4 v169, s[12:15], s40 offen lds\n"
    "s_add_u32 m0, s29, 14336\n"
    "s_nop 0\n"
    "buffer_load_dwordx4 v170, s[12:15], s40 offen lds\n"
    "s_add_u32 s40, s48, 786432\n"
    "s_add_u32 m0, s29, 24576\n"
    "s_nop 0\n"
    "buffer_load_dwordx4 v168, s[12:15], s40 offen lds\n"
    "s_add_u32 m0, s29, 25600\n"
    "s_nop 0\n"
    "buffer_load_dwordx4 v169, s[12:15], s40 offen lds\n"
    "s_add_u32 m0, s29, 26624\n"
    "s_nop 0\n"
    "buffer_load_dwordx4 v170, s[12:15], s40 offen lds\n"
    "s_add_u32 s40, s48, 1179648\n"
    "s_add_u32 m0, s29, 36864\n"
    "s_nop 0\n"
    "buffer_load_dwordx4 v168, s[12:15], s40 offen lds\n"
    "s_add_u32 m0, s29, 37888\n"
    "s_nop 0\n"
    "buffer_load_dwordx4 v169, s[12:15], s40 offen lds\n"
    "s_add_u32 m0, s29, 38912\n"
    "s_nop 0\n"
    "buffer_load_dwordx4 v170, s[12:15], s40 offen lds\n"
    "s_add_u32 s40, s48, 1572864\n"
    "s_add_u32 m0, s29, 49152\n"
    "s_nop 0\n"
    "buffer_load_dwordx4 v168, s[12:15], s40 offen lds\n"
    "s_add_u32 m0, s29, 50176\n"
    "s_nop 0\n"
    "buffer_load_dwordx4 v169, s[12:15], s40 offen lds\n"
    "s_add_u32 m0, s29, 51200\n"
    "s_nop 0\n"
    "buffer_load_dwordx4 v170, s[12:15], s40 offen lds\n"
    "s_add_u32 s40, s48, 1966080\n"
    "s_add_u32 m0, s29, 61440\n"
    "s_nop 0\n"
    "buffer_load_dwordx4 v168, s[12:15], s40 offen lds\n"
    "s_add_u32 m0, s29, 62464\n"
    "s_nop 0\n"
    "buffer_load_dwordx4 v169, s[12:15], s40 offen lds\n"
    "s_add_u32 m0, s29, 63488\n"
    "s_nop 0\n"
    "buffer_load_dwordx4 v170, s[12:15], s40 offen lds\n"
    "s_add_u32 s40, s48, 2359296\n"
    "s_add_u32 m0, s29, 73728\n"
    "s_nop 0\n"
    "buffer_load_dwordx4 v168, s[12:15], s40 offen lds\n"
    "s_add_u32 m0, s29, 74752\n"
    "s_nop 0\n"
    "buffer_load_dwordx4 v169, s[12:15], s40 offen lds\n"
    "s_add_u32 m0, s29, 75776\n"
    "s_nop 0\n"
    "buffer_load_dwordx4 v170, s[12:15], s40 offen lds\n"
    "s_add_u32 s40, s48, 2752512\n"
    "s_add_u32 m0, s29, 86016\n"
    "s_nop 0\n"
    "buffer_load_dwordx4 v168, s[12:15], s40 offen lds\n"
    "s_add_u32 m0, s29, 87040\n"
    "s_nop 0\n"
    "buffer_load_dwordx4 v169, s[12:15], s40 offen lds\n"
    "s_add_u32 m0, s29, 88064\n"
    "s_nop 0\n"
    "buffer_load_dwordx4 v170, s[12:15], s40 offen lds\n"
    "s_add_u32 s40, s48, 3145728\n"
    "s_add_u32 m0, s29, 98304\n"
    "s_nop 0\n"
    "buffer_load_dwordx4 v168, s[12:15], s40 offen lds\n"
    "s_add_u32 m0, s29, 99328\n"
    "s_nop 0\n"
    "buffer_load_dwordx4 v169, s[12:15], s40 offen lds\n"
    "s_add_u32 m0, s29, 100352\n"
    "s_nop 0\n"
    "buffer_load_dwordx4 v170, s[12:15], s40 offen lds\n"
    "s_add_u32 s40, s48, 3538944\n"
    "s_add_u32 m0, s29, 110592\n"
    "s_nop 0\n"
    "buffer_load_dwordx4 v168, s[12:15], s40 offen lds\n"
    "s_add_u32 m0, s29, 111616\n"
    "s_nop 0\n"
    "buffer_load_dwordx4 v169, s[12:15], s40 offen lds\n"
    "s_add_u32 m0, s29, 112640\n"
    "s_nop 0\n"
    "buffer_load_dwordx4 v170, s[12:15], s40 offen lds\n"
    "s_add_u32 s40, s48, 3932160\n"
    "s_add_u32 m0, s29, 122880\n"
    "s_nop 0\n"
    "buffer_load_dwordx4 v168, s[12:15], s40 offen lds\n"
    "s_add_u32 m0, s29, 123904\n"
    "s_nop 0\n"
    "buffer_load_dwordx4 v169, s[12:15], s40 offen lds\n"
    "s_add_u32 m0, s29, 124928\n"
    "s_nop 0\n"
    "buffer_load_dwordx4 v170, s[12:15], s40 offen lds\n"
    "s_add_u32 s40, s48, 4325376\n"
    "s_add_u32 m0, s29, 135168\n"
    "s_nop 0\n"
    "buffer_load_dwordx4 v168, s[12:15], s40 offen lds\n"
    "s_add_u32 m0, s29, 136192\n"
    "s_nop 0\n"
    "buffer_load_dwordx4 v169, s[12:15], s40 offen lds\n"
    "s_add_u32 m0, s29, 137216\n"
    "s_nop 0\n"
    "buffer_load_dwordx4 v170, s[12:15], s40 offen lds\n"
    "s_add_u32 s40, s48, 4718592\n"
    "s_add_u32 m0, s29, 147456\n"
    "s_nop 0\n"
    "buffer_load_dwordx4 v168, s[12:15], s40 offen lds\n"
    "s_add_u32 m0, s29, 148480\n"
    "s_nop 0\n"
    "buffer_load_dwordx4 v169, s[12:15], s40 offen lds\n"
    "s_add_u32 m0, s29, 149504\n"
    "s_nop 0\n"
    "buffer_load_dwordx4 v170, s[12:15], s40 offen lds\n"
    "s_waitcnt vmcnt(36)\n"
    "s_barrier\n"
    "ds_read_b64_tr_b16 a[16:17], v171 offset:0\n"
    "ds_read_b64_tr_b16 a[18:19], v171 offset:1536\n"
    "ds_read_b64_tr_b16 a[72:73], v171 offset:32\n"
    "ds_read_b64_tr_b16 a[74:75], v171 offset:1568\n"
    "ds_read_b64_tr_b16 a[128:129], v171 offset:64\n"
    "ds_read_b64_tr_b16 a[130:131], v171 offset:1600\n"
    "s_waitcnt lgkmcnt(0)\n"
    "s_waitcnt vmcnt(33)\n"
    "s_barrier\n"
    "ds_read_b64_tr_b16 a[20:21], v171 offset:12288\n"
    "ds_read_b64_tr_b16 a[22:23], v171 offset:13824\n"
    "ds_read_b64_tr_b16 a[76:77], v171 offset:12320\n"
    "ds_read_b64_tr_b16 a[78:79], v171 offset:13856\n"
    "ds_read_b64_tr_b16 a[132:133], v171 offset:12352\n"
    "ds_read_b64_tr_b16 a[134:135], v171 offset:13888\n"
    "s_add_u32 s40, s48, 5111808\n"
    "s_add_u32 m0, s29, 0\n"
    "s_nop 0\n"
    "buffer_load_dwordx4 v168, s[12:15], s40 offen lds\n"
    "s_add_u32 m0, s29, 1024\n"
    "s_nop 0\n"
    "buffer_load_dwordx4 v169, s[12:15], s40 offen lds\n"
    "s_add_u32 m0, s29, 2048\n"
    "s_nop 0\n"
    "buffer_load_dwordx4 v170, s[12:15], s40 offen lds\n"
    "s_waitcnt lgkmcnt(0)\n"
    "s_waitcnt vmcnt(33)\n"
    "s_barrier\n"
    "ds_read_b64_tr_b16 a[24:25], v171 offset:24576\n"
    "ds_read_b64_tr_b16 a[26:27], v171 offset:26112\n"
    "ds_read_b64_tr_b16 a[80:81], v171 offset:24608\n"
    "ds_read_b64_tr_b16 a[82:83], v171 offset:26144\n"
    "ds_read_b64_tr_b16 a[136:137], v171 offset:24640\n"
    "ds_read_b64_tr_b16 a[138:139], v171 offset:26176\n"
    "s_add_u32 s40, s48, 5505024\n"
    "s_add_u32 m0, s29, 12288\n"
    "s_nop 0\n"
    "buffer_load_dwordx4 v168, s[12:15], s40 offen lds\n"
    "s_add_u32 m0, s29, 13312\n"
    "s_nop 0\n"
    "buffer_load_dwordx4 v169, s[12:15], s40 offen lds\n"
    "s_add_u32 m0, s29, 14336\n"
    "s_nop 0\n"
    "buffer_load_dwordx4 v170, s[12:15], s40 offen lds\n"
    "s_waitcnt lgkmcnt(0)\n"
    "s_waitcnt vmcnt(33)\n"
    "s_barrier\n"
    "ds_read_b64_tr_b16 a[28:29], v171 offset:36864\n"
    "ds_read_b64_tr_b16 a[30:31], v171 offset:38400\n"
    "ds_read_b64_tr_b16 a[84:85], v171 offset:36896\n"
    "ds_read_b64_tr_b16 a[86:87], v171 offset:38432\n"
    "ds_read_b64_tr_b16 a[140:141], v171 offset:36928\n"
    "ds_read_b64_tr_b16 a[142:143], v171 offset:38464\n"
    "s_add_u32 s40, s48, 5898240\n"
    "s_add_u32 m0, s29, 24576\n"
    "s_nop 0\n"
    "buffer_load_dwordx4 v168, s[12:15], s40 offen lds\n"
    "s_add_u32 m0, s29, 25600\n"
    "s_nop 0\n"
    "buffer_load_dwordx4 v169, s[12:15], s40 offen lds\n"
    "s_add_u32 m0, s29, 26624\n"
    "s_nop 0\n"
    "buffer_load_dwordx4 v170, s[12:15], s40 offen lds\n"
    "s_waitcnt lgkmcnt(0)\n"
    "s_waitcnt vmcnt(33)\n"
    "s_barrier\n"
    "ds_read_b64_tr_b16 v[24:25], v171 offset:49152\n"
    "ds_read_b64_tr_b16 v[26:27], v171 offset:50688\n"
    "ds_read_b64_tr_b16 v[32:33], v171 offset:49184\n"
    "ds_read_b64_tr_b16 v[34:35], v171 offset:50720\n"
    "ds_read_b64_tr_b16 v[40:41], v171 offset:49216\n"
    "ds_read_b64_tr_b16 v[42:43], v171 offset:50752\n"
    "s_add_u32 s40, s48, 6291456\n"
    "s_add_u32 m0, s29, 36864\n"
    "s_nop 0\n"
    "buffer_load_dwordx4 v168, s[12:15], s40 offen lds\n"
    "s_add_u32 m0, s29, 37888\n"
    "s_nop 0\n"
    "buffer_load_dwordx4 v169, s[12:15], s40 offen lds\n"
    "s_add_u32 m0, s29, 38912\n"
    "s_nop 0\n"
    "buffer_load_dwordx4 v170, s[12:15], s40 offen lds\n"
    "s_waitcnt lgkmcnt(0)\n"
    "s_waitcnt vmcnt(33)\n"
    "s_barrier\n"
    "ds_read_b64_tr_b16 v[28:29], v172 offset:4096\n"
    "ds_read_b64_tr_b16 v[30:31], v172 offset:5632\n"
    "ds_read_b64_tr_b16 v[36:37], v172 offset:4128\n"
    "ds_read_b64_tr_b16 v[38:39], v172 offset:5664\n"
    "ds_read_b64_tr_b16 v[44:45], v172 offset:4160\n"
    "ds_read_b64_tr_b16 v[46:47], v172 offset:5696\n"
    "s_add_u32 s40, s48, 6684672\n"
    "s_add_u32 m0, s29, 49152\n"
    "s_nop 0\n"
    "buffer_load_dwordx4 v168, s[12:15], s40 offen lds\n"
    "s_add_u32 m0, s29, 50176\n"
    "s_nop 0\n"
    "buffer_load_dwordx4 v169, s[12:15], s40 offen lds\n"
    "s_add_u32 m0, s29, 51200\n"
    "s_nop 0\n"
    "buffer_load_dwordx4 v170, s[12:15], s40 offen lds\n"
    "s_waitcnt lgkmcnt(0)\n"
    "s_waitcnt vmcnt(33)\n"
    "s_barrier\n"
    "ds_read_b64_tr_b16 v[48:49], v172 offset:16384\n"
    "ds_read_b64_tr_b16 v[50:51], v172 offset:17920\n"
    "ds_read_b64_tr_b16 v[56:57], v172 offset:16416\n"
    "ds_read_b64_tr_b16 v[58:59], v172 offset:17952\n"
    "ds_read_b64_tr_b16 v[64:65], v172 offset:16448\n"
    "ds_read_b64_tr_b16 v[66:67], v172 offset:17984\n"
    "s_add_u32 s40, s48, 7077888\n"
    "s_add_u32 m0, s29, 61440\n"
    "s_nop 0\n"
    "buffer_load_dwordx4 v168, s[12:15], s40 offen lds\n"
    "s_add_u32 m0, s29, 62464\n"
    "s_nop 0\n"
    "buffer_load_dwordx4 v169, s[12:15], s40 offen lds\n"
    "s_add_u32 m0, s29, 63488\n"
    "s_nop 0\n"
    "buffer_load_dwordx4 v170, s[12:15], s40 offen lds\n"
    "s_waitcnt lgkmcnt(0)\n"
    "s_waitcnt vmcnt(33)\n"
    "s_barrier\n"
    "ds_read_b64_tr_b16 v[52:53], v172 offset:28672\n"
    "ds_read_b64_tr_b16 v[54:55], v172 offset:30208\n"
    "ds_read_b64_tr_b16 v[60:61], v172 offset:28704\n"
    "ds_read_b64_tr_b16 v[62:63], v172 offset:30240\n"
    "ds_read_b64_tr_b16 v[68:69], v172 offset:28736\n"
    "ds_read_b64_tr_b16 v[70:71], v172 offset:30272\n"
    "s_add_u32 s40, s48, 7471104\n"
    "s_add_u32 m0, s29, 73728\n"
    "s_nop 0\n"
    "buffer_load_dwordx4 v168, s[12:15], s40 offen lds\n"
    "s_add_u32 m0, s29, 74752\n"
    "s_nop 0\n"
    "buffer_load_dwordx4 v169, s[12:15], s40 offen lds\n"
    "s_add_u32 m0, s29, 75776\n"
    "s_nop 0\n"
    "buffer_load_dwordx4 v170, s[12:15], s40 offen lds\n"
    "s_waitcnt lgkmcnt(0)\n"
    "s_waitcnt vmcnt(33)\n"
    "s_barrier\n"
    "ds_read_b64_tr_b16 v[72:73], v172 offset:40960\n"
    "ds_read_b64_tr_b16 v[74:75], v172 offset:42496\n"
    "ds_read_b64_tr_b16 a[0:1], v172 offset:40992\n"
    "ds_read_b64_tr_b16 a[2:3], v172 offset:42528\n"
    "ds_read_b64_tr_b16 a[8:9], v172 offset:41024\n"
    "ds_read_b64_tr_b16 a[10:11], v172 offset:42560\n"
    "s_waitcnt lgkmcnt(0)\n"
    "s_waitcnt vmcnt(30)\n"
    "s_barrier\n"
    "ds_read_b64_tr_b16 v[76:77], v172 offset:53248\n"
    "ds_read_b64_tr_b16 v[78:79], v172 offset:54784\n"
    "ds_read_b64_tr_b16 a[4:5], v172 offset:53280\n"
    "ds_read_b64_tr_b16 a[6:7], v172 offset:54816\n"
    "ds_read_b64_tr_b16 a[12:13], v172 offset:53312\n"
    "ds_read_b64_tr_b16 a[14:15], v172 offset:54848\n"
    "s_waitcnt lgkmcnt(0)\n"
    "s_waitcnt vmcnt(27)\n"
    "s_barrier\n"
    "ds_read_b64_tr_b16 a[32:33], v173 offset:8192\n"
    "ds_read_b64_tr_b16 a[34:35], v173 offset:9728\n"
    "ds_read_b64_tr_b16 a[88:89], v173 offset:8224\n"
    "ds_read_b64_tr_b16 a[90:91], v173 offset:9760\n"
    "ds_read_b64_tr_b16 a[144:145], v173 offset:8256\n"
    "ds_read_b64_tr_b16 a[146:147], v173 offset:9792\n"
    "s_waitcnt lgkmcnt(0)\n"
    "s_waitcnt vmcnt(24)\n"
    "s_barrier\n"
    "ds_read_b64_tr_b16 a[36:37], v173 offset:20480\n"
    "ds_read_b64_tr_b16 a[38:39], v173 offset:22016\n"
    "ds_read_b64_tr_b16 a[92:93], v173 offset:20512\n"
    "ds_read_b64_tr_b16 a[94:95], v173 offset:22048\n"
    "ds_read_b64_tr_b16 a[148:149], v173 offset:20544\n"
    "ds_read_b64_tr_b16 a[150:151], v173 offset:22080\n"
    "s_add_u32 s40, s51, 0\n"
    "s_add_u32 m0, s49, 86016\n"
    "s_nop 0\n"
    "buffer_load_dwordx4 v8, s[16:19], s40 offen lds\n"
    "s_add_u32 m0, s49, 87040\n"
    "s_nop 0\n"
    "buffer_load_dwordx4 v9, s[16:19], s40 offen lds\n"
    "s_add_u32 m0, s49, 88064\n"
    "s_nop 0\n"
    "buffer_load_dwordx4 v10, s[16:19], s40 offen lds\n"
    "s_add_u32 m0, s49, 89088\n"
    "s_nop 0\n"
    "buffer_load_dwordx4 v11, s[16:19], s40 offen lds\n"
    "s_add_u32 m0, s49, 90112\n"
    "s_nop 0\n"
    "buffer_load_dwordx4 v12, s[16:19], s40 offen lds\n"
    "s_add_u32 m0, s49, 91136\n"
    "s_nop 0\n"
    "buffer_load_dwordx4 v13, s[16:19], s40 offen lds\n"
    "s_add_u32 m0, s49, 92160\n"
    "s_nop 0\n"
    "buffer_load_dwordx4 v14, s[16:19], s40 offen lds\n"
    "s_add_u32 m0, s49, 93184\n"
    "s_nop 0\n"
    "buffer_load_dwordx4 v15, s[16:19], s40 offen lds\n"
    "s_add_u32 m0, s49, 94208\n"
    "s_nop 0\n"
    "buffer_load_dwordx4 v16, s[16:19], s40 offen lds\n"
    "s_add_u32 m0, s49, 95232\n"
    "s_nop 0\n"
    "buffer_load_dwordx4 v17, s[16:19], s40 offen lds\n"
    "s_waitcnt lgkmcnt(0)\n"
    "s_waitcnt vmcnt(31)\n"
    "s_barrier\n"
    "ds_read_b64_tr_b16 a[40:41], v173 offset:32768\n"
    "ds_read_b64_tr_b16 a[42:43], v173 offset:34304\n"
    "ds_read_b64_tr_b16 a[96:97], v173 offset:32800\n"
    "ds_read_b64_tr_b16 a[98:99], v173 offset:34336\n"
    "ds_read_b64_tr_b16 a[152:153], v173 offset:32832\n"
    "ds_read_b64_tr_b16 a[154:155], v173 offset:34368\n"
    "s_waitcnt lgkmcnt(0)\n"
    "s_waitcnt vmcnt(28)\n"
    "s_barrier\n"
    "ds_read_b64_tr_b16 a[44:45], v171 offset:0\n"
    "ds_read_b64_tr_b16 a[46:47], v171 offset:1536\n"
    "ds_read_b64_tr_b16 a[100:101], v171 offset:32\n"
    "ds_read_b64_tr_b16 a[102:103], v171 offset:1568\n"
    "ds_read_b64_tr_b16 a[156:157], v171 offset:64\n"
    "ds_read_b64_tr_b16 a[158:159], v171 offset:1600\n"
    "s_waitcnt lgkmcnt(0)\n"
    "s_waitcnt vmcnt(25)\n"
    "s_barrier\n"
    "ds_read_b64_tr_b16 a[48:49], v171 offset:12288\n"
    "ds_read_b64_tr_b16 a[50:51], v171 offset:13824\n"
    "ds_read_b64_tr_b16 a[104:105], v171 offset:12320\n"
    "ds_read_b64_tr_b16 a[106:107], v171 offset:13856\n"
    "ds_read_b64_tr_b16 a[160:161], v171 offset:12352\n"
    "ds_read_b64_tr_b16 a[162:163], v171 offset:13888\n"
    "s_waitcnt lgkmcnt(0)\n"
    "s_waitcnt vmcnt(22)\n"
    "s_barrier\n"
    "ds_read_b64_tr_b16 a[52:53], v171 offset:24576\n"
    "ds_read_b64_tr_b16 a[54:55], v171 offset:26112\n"
    "ds_read_b64_tr_b16 a[108:109], v171 offset:24608\n"
    "ds_read_b64_tr_b16 a[110:111], v171 offset:26144\n"
    "ds_read_b64_tr_b16 a[164:165], v171 offset:24640\n"
    "ds_read_b64_tr_b16 a[166:167], v171 offset:26176\n"
    "s_waitcnt lgkmcnt(0)\n"
    "s_waitcnt vmcnt(19)\n"
    "s_barrier\n"
    "ds_read_b64_tr_b16 a[56:57], v171 offset:36864\n"
    "ds_read_b64_tr_b16 a[58:59], v171 offset:38400\n"
    "ds_read_b64_tr_b16 a[112:113], v171 offset:36896\n"
    "ds_read_b64_tr_b16 a[114:115], v171 offset:38432\n"
    "ds_read_b64_tr_b16 a[168:169], v171 offset:36928\n"
    "ds_read_b64_tr_b16 a[170:171], v171 offset:38464\n"
    "s_waitcnt lgkmcnt(0)\n"
    "s_waitcnt vmcnt(16)\n"
    "s_barrier\n"
    "ds_read_b64_tr_b16 a[60:61], v171 offset:49152\n"
    "ds_read_b64_tr_b16 a[62:63], v171 offset:50688\n"
    "ds_read_b64_tr_b16 a[116:117], v171 offset:49184\n"
    "ds_read_b64_tr_b16 a[118:119], v171 offset:50720\n"
    "ds_read_b64_tr_b16 a[172:173], v171 offset:49216\n"
    "ds_read_b64_tr_b16 a[174:175], v171 offset:50752\n"
    "s_add_u32 s40, s51, 122880\n"
    "s_add_u32 m0, s49, 0\n"
    "s_nop 0\n"
    "buffer_load_dwordx4 v8, s[16:19], s40 offen lds\n"
    "s_add_u32 m0, s49, 1024\n"
    "s_nop 0\n"
    "buffer_load_dwordx4 v9, s[16:19], s40 offen lds\n"
    "s_add_u32 m0, s49, 2048\n"
    "s_nop 0\n"
    "buffer_load_dwordx4 v10, s[16:19], s40 offen lds\n"
    "s_add_u32 m0, s49, 3072\n"
    "s_nop 0\n"
    "buffer_load_dwordx4 v11, s[16:19], s40 offen lds\n"
    "s_add_u32 m0, s49, 4096\n"
    "s_nop 0\n"
    "buffer_load_dwordx4 v12, s[16:19], s40 offen lds\n"
    "s_add_u32 m0, s49, 5120\n"
    "s_nop 0\n"
    "buffer_load_dwordx4 v13, s[16:19], s40 offen lds\n"
    "s_add_u32 m0, s49, 6144\n"
    "s_nop 0\n"
    "buffer_load_dwordx4 v14, s[16:19], s40 offen lds\n"
    "s_add_u32 m0, s49, 7168\n"
    "s_nop 0\n"
    "buffer_load_dwordx4 v15, s[16:19], s40 offen lds\n"
    "s_add_u32 m0, s49, 8192\n"
    "s_nop 0\n"
    "buffer_load_dwordx4 v16, s[16:19], s40 offen lds\n"
    "s_add_u32 m0, s49, 9216\n"
    "s_nop 0\n"
    "buffer_load_dwordx4 v17, s[16:19], s40 offen lds\n"
    "s_waitcnt lgkmcnt(0)\n"
    "s_add_u32 s4, s3, 1\n"
    "v_mov_b32 v2, s3\n"
    "s_or_b32 s41, s21, s6\n"
    "s_cbranch_scc1 .Lcl_57\n"
    "s_lshl_b32 s41, s5, 2\n"
    "s_add_u32 s41, s41, 4178944\n"
    "v_mov_b32 v3, s41\n"
    "v_mov_b32 v2, s4\n"
    "s_mov_b64 exec, 1\n"
    "buffer_atomic_swap v2, v3, s[8:11], 0 offen sc0 sc1\n"
    "s_mov_b64 exec, -1\n"
    ".Lcl_57:\n"
    "v_min_u32 v6, 54, v1\n"
    "v_lshlrev_b32 v6, 7, v6\n"
    "v_add_u32 v6, 4182016, v6\n"
    "buffer_load_dwordx2 v[4:5], v6, s[8:11], 0 offen sc1\n"
    "s_waitcnt lgkmcnt(0)\n"
    "s_waitcnt vmcnt(24)\n"
    "s_barrier\n"
    "ds_read_b64_tr_b16 a[64:65], v172 offset:4096\n"
    "ds_read_b64_tr_b16 a[66:67], v172 offset:5632\n"
    "ds_read_b64_tr_b16 a[120:121], v172 offset:4128\n"
    "ds_read_b64_tr_b16 a[122:123], v172 offset:5664\n"
    "ds_read_b64_tr_b16 a[176:177], v172 offset:4160\n"
    "ds_read_b64_tr_b16 a[178:179], v172 offset:5696\n"
    "s_waitcnt lgkmcnt(0)\n"
    "s_waitcnt vmcnt(21)\n"
    "s_barrier\n"
    "ds_read_b64_tr_b16 a[68:69], v172 offset:16384\n"
    "ds_read_b64_tr_b16 a[70:71], v172 offset:17920\n"
    "ds_read_b64_tr_b16 a[124:125], v172 offset:16416\n"
    "ds_read_b64_tr_b16 a[126:127], v172 offset:17952\n"
    "ds_read_b64_tr_b16 a[180:181], v172 offset:16448\n"
    "ds_read_b64_tr_b16 a[182:183], v172 offset:17984\n"
    "s_waitcnt lgkmcnt(0)\n"
    "s_waitcnt vmcnt(11)\n"
    "s_barrier\n"
    "ds_read_b64_tr_b16 v[80:81], v19 offset:28672\n"
    "ds_read_b64_tr_b16 v[82:83], v19 offset:29952\n"
    "ds_read_b64_tr_b16 v[88:89], v19 offset:28704\n"
    "ds_read_b64_tr_b16 v[90:91], v19 offset:29984\n"
    "ds_read_b64_tr_b16 v[96:97], v19 offset:28736\n"
    "ds_read_b64_tr_b16 v[98:99], v19 offset:30016\n"
    "ds_read_b64_tr_b16 v[104:105], v19 offset:28768\n"
    "ds_read_b64_tr_b16 v[106:107], v19 offset:30048\n"
    "ds_read_b64_tr_b16 v[112:113], v19 offset:28800\n"
    "ds_read_b64_tr_b16 v[114:115], v19 offset:30080\n"
    "ds_read_b64_tr_b16 v[120:121], v19 offset:28832\n"
    "ds_read_b64_tr_b16 v[122:123], v19 offset:30112\n"
    "ds_read_b64_tr_b16 v[128:129], v19 offset:28864\n"
    "ds_read_b64_tr_b16 v[130:131], v19 offset:30144\n"
    "ds_read_b64_tr_b16 a[184:185], v19 offset:28896\n"
    "ds_read_b64_tr_b16 a[186:187], v19 offset:30176\n"
    "ds_read_b64_tr_b16 v[208:209], v19 offset:28928\n"
    "ds_read_b64_tr_b16 v[210:211], v19 offset:30208\n"
    "ds_read_b64_tr_b16 v[232:233], v19 offset:28960\n"
    "ds_read_b64_tr_b16 v[234:235], v19 offset:30240\n"
    "s_add_u32 s40, s51, 20480\n"
    "s_add_u32 m0, s49, 40960\n"
    "s_nop 0\n"
    "buffer_load_dwordx4 v8, s[16:19], s40 offen lds\n"
    "s_add_u32 m0, s49, 41984\n"
    "s_nop 0\n"
    "buffer_load_dwordx4 v9, s[16:19], s40 offen lds\n"
    "s_add_u32 m0, s49, 43008\n"
    "s_nop 0\n"
    "buffer_load_dwordx4 v10, s[16:19], s40 offen lds\n"
    "s_add_u32 m0, s49, 44032\n"
    "s_nop 0\n"
    "buffer_load_dwordx4 v11, s[16:19], s40 offen lds\n"
    "s_add_u32 m0, s49, 45056\n"
    "s_nop 0\n"
    "buffer_load_dwordx4 v12, s[16:19], s40 offen lds\n"
    "s_add_u32 m0, s49, 46080\n"
    "s_nop 0\n"
    "buffer_load_dwordx4 v13, s[16:19], s40 offen lds\n"
    "s_add_u32 m0, s49, 47104\n"
    "s_nop 0\n"
    "buffer_load_dwordx4 v14, s[16:19], s40 offen lds\n"
    "s_add_u32 m0, s49, 48128\n"
    "s_nop 0\n"
    "buffer_load_dwordx4 v15, s[16:19], s40 offen lds\n"
    "s_add_u32 m0, s49, 49152\n"
    "s_nop 0\n"
    "buffer_load_dwordx4 v16, s[16:19], s40 offen lds\n"
    "s_add_u32 m0, s49, 50176\n"
    "s_nop 0\n"
    "buffer_load_dwordx4 v17, s[16:19], s40 offen lds\n"
    "s_waitcnt lgkmcnt(0)\n"
    "s_waitcnt vmcnt(11)\n"
    "s_barrier\n"
    "ds_read_b64_tr_b16 v[84:85], v18 offset:0\n"
    "ds_read_b64_tr_b16 v[86:87], v18 offset:1280\n"
    "ds_read_b64_tr_b16 v[92:93], v18 offset:32\n"
    "ds_read_b64_tr_b16 v[94:95], v18 offset:1312\n"
    "ds_read_b64_tr_b16 v[100:101], v18 offset:64\n"
    "ds_read_b64_tr_b16 v[102:103], v18 offset:1344\n"
    "ds_read_b64_tr_b16 v[108:109], v18 offset:96\n"
    "ds_read_b64_tr_b16 v[110:111], v18 offset:1376\n"
    "ds_read_b64_tr_b16 v[116:117], v18 offset:128\n"
    "ds_read_b64_tr_b16 v[118:119], v18 offset:1408\n"
    "ds_read_b64_tr_b16 v[124:125], v18 offset:160\n"
    "ds_read_b64_tr_b16 v[126:127], v18 offset:1440\n"
    "ds_read_b64_tr_b16 v[132:133], v18 offset:192\n"
    "ds_read_b64_tr_b16 v[134:135], v18 offset:1472\n"
    "ds_read_b64_tr_b16 a[188:189], v18 offset:224\n"
    "ds_read_b64_tr_b16 a[190:191], v18 offset:1504\n"
    "ds_read_b64_tr_b16 v[212:213], v18 offset:256\n"
    "ds_read_b64_tr_b16 v[214:215], v18 offset:1536\n"
    "ds_read_b64_tr_b16 v[236:237], v18 offset:288\n"
    "ds_read_b64_tr_b16 v[238:239], v18 offset:1568\n"
    "s_add_u32 s40, s51, 143360\n"
    "s_add_u32 m0, s49, 81920\n"
    "s_nop 0\n"
    "buffer_load_dwordx4 v8, s[16:19], s40 offen lds\n"
    "s_add_u32 m0, s49, 82944\n"
    "s_nop 0\n"
    "buffer_load_dwordx4 v9, s[16:19], s40 offen lds\n"
    "s_add_u32 m0, s49, 83968\n"
    "s_nop 0\n"
    "buffer_load_dwordx4 v10, s[16:19], s40 offen lds\n"
    "s_add_u32 m0, s49, 84992\n"
    "s_nop 0\n"
    "buffer_load_dwordx4 v11, s[16:19], s40 offen lds\n"
    "s_add_u32 m0, s49, 86016\n"
    "s_nop 0\n"
    "buffer_load_dwordx4 v12, s[16:19], s40 offen lds\n"
    "s_add_u32 m0, s49, 87040\n"
    "s_nop 0\n"
    "buffer_load_dwordx4 v13, s[16:19], s40 offen lds\n"
    "s_add_u32 m0, s49, 88064\n"
    "s_nop 0\n"
    "buffer_load_dwordx4 v14, s[16:19], s40 offen lds\n"
    "s_add_u32 m0, s49, 89088\n"
    "s_nop 0\n"
    "buffer_load_dwordx4 v15, s[16:19], s40 offen lds\n"
    "s_add_u32 m0, s49, 90112\n"
    "s_nop 0\n"
    "buffer_load_dwordx4 v16, s[16:19], s40 offen lds\n"
    "s_add_u32 m0, s49, 91136\n"
    "s_nop 0\n"
    "buffer_load_dwordx4 v17, s[16:19], s40 offen lds\n"
    "s_waitcnt lgkmcnt(0)\n"
    "s_waitcnt vmcnt(10)\n"
    "s_barrier\n"
    "ds_read_b64_tr_b16 a[192:193], v18 offset:40960\n"
    "ds_read_b64_tr_b16 a[194:195], v18 offset:42240\n"
    "ds_read_b64_tr_b16 a[200:201], v18 offset:40992\n"
    "ds_read_b64_tr_b16 a[202:203], v18 offset:42272\n"
    "ds_read_b64_tr_b16 a[208:209], v18 offset:41024\n"
    "ds_read_b64_tr_b16 a[210:211], v18 offset:42304\n"
    "ds_read_b64_tr_b16 a[216:217], v18 offset:41056\n"
    "ds_read_b64_tr_b16 a[218:219], v18 offset:42336\n"
    "ds_read_b64_tr_b16 a[224:225], v18 offset:41088\n"
    "ds_read_b64_tr_b16 a[226:227], v18 offset:42368\n"
    "ds_read_b64_tr_b16 a[232:233], v18 offset:41120\n"
    "ds_read_b64_tr_b16 a[234:235], v18 offset:42400\n"
    "ds_read_b64_tr_b16 v[136:137], v18 offset:41152\n"
    "ds_read_b64_tr_b16 v[138:139], v18 offset:42432\n"
    "ds_read_b64_tr_b16 v[152:153], v18 offset:41184\n"
    "ds_read_b64_tr_b16 v[154:155], v18 offset:42464\n"
    "ds_read_b64_tr_b16 v[216:217], v18 offset:41216\n"
    "ds_read_b64_tr_b16 v[218:219], v18 offset:42496\n"
    "ds_read_b64_tr_b16 v[240:241], v18 offset:41248\n"
    "ds_read_b64_tr_b16 v[242:243], v18 offset:42528\n"
    "s_add_u32 s40, s51, 40960\n"
    "s_add_u32 m0, s49, 0\n"
    "s_nop 0\n"
    "buffer_load_dwordx4 v8, s[16:19], s40 offen lds\n"
    "s_add_u32 m0, s49, 1024\n"
    "s_nop 0\n"
    "buffer_load_dwordx4 v9, s[16:19], s40 offen lds\n"
    "s_add_u32 m0, s49, 2048\n"
    "s_nop 0\n"
    "buffer_load_dwordx4 v10, s[16:19], s40 offen lds\n"
    "s_add_u32 m0, s49, 3072\n"
    "s_nop 0\n"
    "buffer_load_dwordx4 v11, s[16:19], s40 offen lds\n"
    "s_add_u32 m0, s49, 4096\n"
    "s_nop 0\n"
    "buffer_load_dwordx4 v12, s[16:19], s40 offen lds\n"
    "s_add_u32 m0, s49, 5120\n"
    "s_nop 0\n"
    "buffer_load_dwordx4 v13, s[16:19], s40 offen lds\n"
    "s_add_u32 m0, s49, 6144\n"
    "s_nop 0\n"
    "buffer_load_dwordx4 v14, s[16:19], s40 offen lds\n"
    "s_add_u32 m0, s49, 7168\n"
    "s_nop 0\n"
    "buffer_load_dwordx4 v15, s[16:19], s40 offen lds\n"
    "s_add_u32 m0, s49, 8192\n"
    "s_nop 0\n"
    "buffer_load_dwordx4 v16, s[16:19], s40 offen lds\n"
    "s_add_u32 m0, s49, 9216\n"
    "s_nop 0\n"
    "buffer_load_dwordx4 v17, s[16:19], s40 offen lds\n"
    "s_waitcnt lgkmcnt(0)\n"
    "s_waitcnt vmcnt(10)\n"
    "s_barrier\n"
    "ds_read_b64_tr_b16 a[196:197], v19 offset:24576\n"
    "ds_read_b64_tr_b16 a[198:199], v19 offset:25856\n"
    "ds_read_b64_tr_b16 a[204:205], v19 offset:24608\n"
    "ds_read_b64_tr_b16 a[206:207], v19 offset:25888\n"
    "ds_read_b64_tr_b16 a[212:213], v19 offset:24640\n"
    "ds_read_b64_tr_b16 a[214:215], v19 offset:25920\n"
    "ds_read_b64_tr_b16 a[220:221], v19 offset:24672\n"
    "ds_read_b64_tr_b16 a[222:223], v19 offset:25952\n"
    "ds_read_b64_tr_b16 a[228:229], v19 offset:24704\n"
    "ds_read_b64_tr_b16 a[230:231], v19 offset:25984\n"
    "ds_read_b64_tr_b16 a[236:237], v19 offset:24736\n"
    "ds_read_b64_tr_b16 a[238:239], v19 offset:26016\n"
    "ds_read_b64_tr_b16 v[140:141], v19 offset:24768\n"
    "ds_read_b64_tr_b16 v[142:143], v19 offset:26048\n"
    "ds_read_b64_tr_b16 v[156:157], v19 offset:24800\n"
    "ds_read_b64_tr_b16 v[158:159], v19 offset:26080\n"
    "ds_read_b64_tr_b16 v[220:221], v19 offset:24832\n"
    "ds_read_b64_tr_b16 v[222:223], v19 offset:26112\n"
    "ds_read_b64_tr_b16 v[244:245], v19 offset:24864\n"
    "ds_read_b64_tr_b16 v[246:247], v19 offset:26144\n"
    "s_add_u32 s40, s51, 163840\n"
    "s_add_u32 m0, s49, 40960\n"
    "s_nop 0\n"
    "buffer_load_dwordx4 v8, s[16:19], s40 offen lds\n"
    "s_add_u32 m0, s49, 41984\n"
    "s_nop 0\n"
    "buffer_load_dwordx4 v9, s[16:19], s40 offen lds\n"
    "s_add_u32 m0, s49, 43008\n"
    "s_nop 0\n"
    "buffer_load_dwordx4 v10, s[16:19], s40 offen lds\n"
    "s_add_u32 m0, s49, 44032\n"
    "s_nop 0\n"
    "buffer_load_dwordx4 v11, s[16:19], s40 offen lds\n"
    "s_add_u32 m0, s49, 45056\n"
    "s_nop 0\n"
    "buffer_load_dwordx4 v12, s[16:19], s40 offen lds\n"
    "s_add_u32 m0, s49, 46080\n"
    "s_nop 0\n"
    "buffer_load_dwordx4 v13, s[16:19], s40 offen lds\n"
    "s_add_u32 m0, s49, 47104\n"
    "s_nop 0\n"
    "buffer_load_dwordx4 v14, s[16:19], s40 offen lds\n"
    "s_add_u32 m0, s49, 48128\n"
    "s_nop 0\n"
    "buffer_load_dwordx4 v15, s[16:19], s40 offen lds\n"
    "s_add_u32 m0, s49, 49152\n"
    "s_nop 0\n"
    "buffer_load_dwordx4 v16, s[16:19], s40 offen lds\n"
    "s_add_u32 m0, s49, 50176\n"
    "s_nop 0\n"
    "buffer_load_dwordx4 v17, s[16:19], s40 offen lds\n"
    "s_waitcnt lgkmcnt(0)\n"
    "s_waitcnt vmcnt(10)\n"
    "s_barrier\n"
    "ds_read_b64_tr_b16 a[240:241], v18 offset:0\n"
    "ds_read_b64_tr_b16 a[242:243], v18 offset:1280\n"
    "ds_read_b64_tr_b16 a[248:249], v18 offset:32\n"
    "ds_read_b64_tr_b16 a[250:251], v18 offset:1312\n"
    "ds_read_b64_tr_b16 v[176:177], v18 offset:64\n"
    "ds_read_b64_tr_b16 v[178:179], v18 offset:1344\n"
    "ds_read_b64_tr_b16 v[184:185], v18 offset:96\n"
    "ds_read_b64_tr_b16 v[186:187], v18 offset:1376\n"
    "ds_read_b64_tr_b16 v[192:193], v18 offset:128\n"
    "ds_read_b64_tr_b16 v[194:195], v18 offset:1408\n"
    "ds_read_b64_tr_b16 v[200:201], v18 offset:160\n"
    "ds_read_b64_tr_b16 v[202:203], v18 offset:1440\n"
    "ds_read_b64_tr_b16 v[144:145], v18 offset:192\n"
    "ds_read_b64_tr_b16 v[146:147], v18 offset:1472\n"
    "ds_read_b64_tr_b16 v[160:161], v18 offset:224\n"
    "ds_read_b64_tr_b16 v[162:163], v18 offset:1504\n"
    "ds_read_b64_tr_b16 v[224:225], v18 offset:256\n"
    "ds_read_b64_tr_b16 v[226:227], v18 offset:1536\n"
    "ds_read_b64_tr_b16 v[248:249], v18 offset:288\n"
    "ds_read_b64_tr_b16 v[250:251], v18 offset:1568\n"
    "s_waitcnt lgkmcnt(0)\n"
    "s_waitcnt vmcnt(0)\n"
    "s_barrier\n"
    "ds_read_b64_tr_b16 a[244:245], v18 offset:40960\n"
    "ds_read_b64_tr_b16 a[246:247], v18 offset:42240\n"
    "ds_read_b64_tr_b16 a[252:253], v18 offset:40992\n"
    "ds_read_b64_tr_b16 a[254:255], v18 offset:42272\n"
    "ds_read_b64_tr_b16 v[180:181], v18 offset:41024\n"
    "ds_read_b64_tr_b16 v[182:183], v18 offset:42304\n"
    "ds_read_b64_tr_b16 v[188:189], v18 offset:41056\n"
    "ds_read_b64_tr_b16 v[190:191], v18 offset:42336\n"
    "ds_read_b64_tr_b16 v[196:197], v18 offset:41088\n"
    "ds_read_b64_tr_b16 v[198:199], v18 offset:42368\n"
    "ds_read_b64_tr_b16 v[204:205], v18 offset:41120\n"
    "ds_read_b64_tr_b16 v[206:207], v18 offset:42400\n"
    "ds_read_b64_tr_b16 v[148:149], v18 offset:41152\n"
    "ds_read_b64_tr_b16 v[150:151], v18 offset:42432\n"
    "ds_read_b64_tr_b16 v[164:165], v18 offset:41184\n"
    "ds_read_b64_tr_b16 v[166:167], v18 offset:42464\n"
    "ds_read_b64_tr_b16 v[228:229], v18 offset:41216\n"
    "ds_read_b64_tr_b16 v[230:231], v18 offset:42496\n"
    "ds_read_b64_tr_b16 v[252:253], v18 offset:41248\n"
    "ds_read_b64_tr_b16 v[254:255], v18 offset:42528\n"
    "s_waitcnt lgkmcnt(0)\n"
    "v_cmp_ne_u32 vcc, s4, v4\n"
    "s_nop 1\n"
    "s_cmp_eq_u32 vcc_lo, 0\n"
    "s_cbranch_scc1 .Lrx_60\n"
    ".Lrp_58:\n"
    "v_min_u32 v6, 54, v1\n"
    "v_lshlrev_b32 v6, 7, v6\n"
    "v_add_u32 v6, 4182016, v6\n"
    "buffer_load_dwordx2 v[4:5], v6, s[8:11], 0 offen sc1\n"
    "s_waitcnt vmcnt(0)\n"
    "v_cmp_ne_u32 vcc, s4, v4\n"
    "s_nop 1\n"
    "s_cmp_eq_u32 vcc_lo, 0\n"
    "s_cbranch_scc1 .Lro_59\n"
    "s_sleep 2\n"
    "s_sub_u32 s25, s25, 1\n"
    "s_cbranch_scc0 .Lrp_58\n"
    "s_branch .Lfail\n"
    ".Lro_59:\n"
    ".Lrx_60:\n"
    "v_cmp_ne_u32 vcc, s4, v4\n"
    "s_nop 1\n"
    "s_cmp_eq_u32 vcc_hi, 0\n"
    "s_cbranch_scc1 .Lrx_63\n"
    ".Lrp_61:\n"
    "v_min_u32 v6, 54, v1\n"
    "v_lshlrev_b32 v6, 7, v6\n"
    "v_add_u32 v6, 4182016, v6\n"
    "buffer_load_dwordx2 v[4:5], v6, s[8:11], 0 offen sc1\n"
    "s_waitcnt vmcnt(0)\n"
    "v_cmp_ne_u32 vcc, s4, v4\n"
    "s_nop 1\n"
    "s_cmp_eq_u32 vcc_hi, 0\n"
    "s_cbranch_scc1 .Lro_62\n"
    "s_sleep 2\n"
    "s_sub_u32 s25, s25, 1\n"
    "s_cbranch_scc0 .Lrp_61\n"
    "s_branch .Lfail\n"
    ".Lro_62:\n"
    ".Lrx_63:\n"
    "v_cmp_ne_u32 vcc, 0, v5\n"
    "s_nop 1\n"
    "s_and_b32 s22, vcc_hi, 1\n"
    "s_lshl_b32 s22, s22, 13\n"
    "s_lshr_b32 s23, vcc_hi, 1\n"
    "s_branch .Lpw_47\n"
    ".Lpn_56:\n"
    "s_and_b32 s40, s21, 3\n"
    "s_cmp_lg_u32 s40, 2\n"
    "s_cbranch_scc1 .Lpn_64\n"
    "s_add_u32 s40, s48, 0\n"
    "s_add_u32 m0, s29, 0\n"
    "s_nop 0\n"
    "buffer_load_dwordx4 v168, s[12:15], s40 offen lds\n"
    "s_add_u32 m0, s29, 1024\n"
    "s_nop 0\n"
    "buffer_load_dwordx4 v169, s[12:15], s40 offen lds\n"
    "s_add_u32 m0, s29, 2048\n"
    "s_nop 0\n"
    "buffer_load_dwordx4 v170, s[12:15], s40 offen lds\n"
    "s_add_u32 s40, s48, 393216\n"
    "s_add_u32 m0, s29, 12288\n"
    "s_nop 0\n"
    "buffer_load_dwordx4 v168, s[12:15], s40 offen lds\n"
    "s_add_u32 m0, s29, 13312\n"
    "s_nop 0\n"
    "buffer_load_dwordx4 v169, s[12:15], s40 offen lds\n"
    "s_add_u32 m0, s29, 14336\n"
    "s_nop 0\n"
    "buffer_load_dwordx4 v170, s[12:15], s40 offen lds\n"
    "s_add_u32 s40, s48, 786432\n"
    "s_add_u32 m0, s29, 24576\n"
    "s_nop 0\n"
    "buffer_load_dwordx4 v168, s[12:15], s40 offen lds\n"
    "s_add_u32 m0, s29, 25600\n"
    "s_nop 0\n"
    "buffer_load_dwordx4 v169, s[12:15], s40 offen lds\n"
    "s_add_u32 m0, s29, 26624\n"
    "s_nop 0\n"
    "buffer_load_dwordx4 v170, s[12:15], s40 offen lds\n"
    "s_add_u32 s40, s48, 1179648\n"
    "s_add_u32 m0, s29, 36864\n"
    "s_nop 0\n"
    "buffer_load_dwordx4 v168, s[12:15], s40 offen lds\n"
    "s_add_u32 m0, s29, 37888\n"
    "s_nop 0\n"
    "buffer_load_dwordx4 v169, s[12:15], s40 offen lds\n"
    "s_add_u32 m0, s29, 38912\n"
    "s_nop 0\n"
    "buffer_load_dwordx4 v170, s[12:15], s40 offen lds\n"
    "s_add_u32 s40, s48, 1572864\n"
    "s_add_u32 m0, s29, 49152\n"
    "s_nop 0\n"
    "buffer_load_dwordx4 v168, s[12:15], s40 offen lds\n"
    "s_add_u32 m0, s29, 50176\n"
    "s_nop 0\n"
    "buffer_load_dwordx4 v169, s[12:15], s40 offen lds\n"
    "s_add_u32 m0, s29, 51200\n"
    "s_nop 0\n"
    "buffer_load_dwordx4 v170, s[12:15], s40 offen lds\n"
    "s_add_u32 s40, s48, 1966080\n"
    "s_add_u32 m0, s29, 61440\n"
    "s_nop 0\n"
    "buffer_load_dwordx4 v168, s[12:15], s40 offen lds\n"
    "s_add_u32 m0, s29, 62464\n"
    "s_nop 0\n"
    "buffer_load_dwordx4 v169, s[12:15], s40 offen lds\n"
    "s_add_u32 m0, s29, 63488\n"
    "s_nop 0\n"
    "buffer_load_dwordx4 v170, s[12:15], s40 offen lds\n"
    "s_add_u32 s40, s48, 2359296\n"
    "s_add_u32 m0, s29, 73728\n"
    "s_nop 0\n"
    "buffer_load_dwordx4 v168, s[12:15], s40 offen lds\n"
    "s_add_u32 m0, s29, 74752\n"
    "s_nop 0\n"
    "buffer_load_dwordx4 v169, s[12:15], s40 offen lds\n"
    "s_add_u32 m0, s29, 75776\n"
    "s_nop 0\n"
    "buffer_load_dwordx4 v170, s[12:15], s40 offen lds\n"
    "s_add_u32 s40, s48, 2752512\n"
    "s_add_u32 m0, s29, 86016\n"
    "s_nop 0\n"
    "buffer_load_dwordx4 v168, s[12:15], s40 offen lds\n"
    "s_add_u32 m0, s29, 87040\n"
    "s_nop 0\n"
    "buffer_load_dwordx4 v169, s[12:15], s40 offen lds\n"
    "s_add_u32 m0, s29, 88064\n"
    "s_nop 0\n"
    "buffer_load_dwordx4 v170, s[12:15], s40 offen lds\n"
    "s_add_u32 s40, s48, 3145728\n"
    "s_add_u32 m0, s29, 98304\n"
    "s_nop 0\n"
    "buffer_load_dwordx4 v168, s[12:15], s40 offen lds\n"
    "s_add_u32 m0, s29, 99328\n"
    "s_nop 0\n"
    "buffer_load_dwordx4 v169, s[12:15], s40 offen lds\n"
    "s_add_u32 m0, s29, 100352\n"
    "s_nop 0\n"
    "buffer_load_dwordx4 v170, s[12:15], s40 offen lds\n"
    "s_add_u32 s40, s48, 3538944\n"
    "s_add_u32 m0, s29, 110592\n"
    "s_nop 0\n"
    "buffer_load_dwordx4 v168, s[12:15], s40 offen lds\n"
    "s_add_u32 m0, s29, 111616\n"
    "s_nop 0\n"
    "buffer_load_dwordx4 v169, s[12:15], s40 offen lds\n"
    "s_add_u32 m0, s29, 112640\n"
    "s_nop 0\n"
    "buffer_load_dwordx4 v170, s[12:15], s40 offen lds\n"
    "s_add_u32 s40, s48, 3932160\n"
    "s_add_u32 m0, s29, 122880\n"
    "s_nop 0\n"
    "buffer_load_dwordx4 v168, s[12:15], s40 offen lds\n"
    "s_add_u32 m0, s29, 123904\n"
    "s_nop 0\n"
    "buffer_load_dwordx4 v169, s[12:15], s40 offen lds\n"
    "s_add_u32 m0, s29, 124928\n"
    "s_nop 0\n"
    "buffer_load_dwordx4 v170, s[12:15], s40 offen lds\n"
    "s_add_u32 s40, s48, 4325376\n"
    "s_add_u32 m0, s29, 135168\n"
    "s_nop 0\n"
    "buffer_load_dwordx4 v168, s[12:15], s40 offen lds\n"
    "s_add_u32 m0, s29, 136192\n"
    "s_nop 0\n"
    "buffer_load_dwordx4 v169, s[12:15], s40 offen lds\n"
    "s_add_u32 m0, s29, 137216\n"
    "s_nop 0\n"
    "buffer_load_dwordx4 v170, s[12:15], s40 offen lds\n"
    "s_add_u32 s40, s48, 4718592\n"
    "s_add_u32 m0, s29, 147456\n"
    "s_nop 0\n"
    "buffer_load_dwordx4 v168, s[12:15], s40 offen lds\n"
    "s_add_u32 m0, s29, 148480\n"
    "s_nop 0\n"
    "buffer_load_dwordx4 v169, s[12:15], s40 offen lds\n"
    "s_add_u32 m0, s29, 149504\n"
    "s_nop 0\n"
    "buffer_load_dwordx4 v170, s[12:15], s40 offen lds\n"
    "s_waitcnt vmcnt(36)\n"
    "s_barrier\n"
    "ds_read_b64_tr_b16 a[32:33], v171 offset:0\n"
    "ds_read_b64_tr_b16 a[34:35], v171 offset:1536\n"
    "ds_read_b64_tr_b16 a[88:89], v171 offset:32\n"
    "ds_read_b64_tr_b16 a[90:91], v171 offset:1568\n"
    "ds_read_b64_tr_b16 a[144:145], v171 offset:64\n"
    "ds_read_b64_tr_b16 a[146:147], v171 offset:1600\n"
    "s_waitcnt lgkmcnt(0)\n"
    "s_waitcnt vmcnt(33)\n"
    "s_barrier\n"
    "ds_read_b64_tr_b16 a[36:37], v171 offset:12288\n"
    "ds_read_b64_tr_b16 a[38:39], v171 offset:13824\n"
    "ds_read_b64_tr_b16 a[92:93], v171 offset:12320\n"
    "ds_read_b64_tr_b16 a[94:95], v171 offset:13856\n"
    "ds_read_b64_tr_b16 a[148:149], v171 offset:12352\n"
    "ds_read_b64_tr_b16 a[150:151], v171 offset:13888\n"
    "s_add_u32 s40, s48, 5111808\n"
    "s_add_u32 m0, s29, 0\n"
    "s_nop 0\n"
    "buffer_load_dwordx4 v168, s[12:15], s40 offen lds\n"
    "s_add_u32 m0, s29, 1024\n"
    "s_nop 0\n"
    "buffer_load_dwordx4 v169, s[12:15], s40 offen lds\n"
    "s_add_u32 m0, s29, 2048\n"
    "s_nop 0\n"
    "buffer_load_dwordx4 v170, s[12:15], s40 offen lds\n"
    "s_waitcnt lgkmcnt(0)\n"
    "s_waitcnt vmcnt(33)\n"
    "s_barrier\n"
    "ds_read_b64_tr_b16 a[40:41], v171 offset:24576\n"
    "ds_read_b64_tr_b16 a[42:43], v171 offset:26112\n"
    "ds_read_b64_tr_b16 a[96:97], v171 offset:24608\n"
    "ds_read_b64_tr_b16 a[98:99], v171 offset:26144\n"
    "ds_read_b64_tr_b16 a[152:153], v171 offset:24640\n"
    "ds_read_b64_tr_b16 a[154:155], v171 offset:26176\n"
    "s_add_u32 s40, s48, 5505024\n"
    "s_add_u32 m0, s29, 12288\n"
    "s_nop 0\n"
    "buffer_load_dwordx4 v168, s[12:15], s40 offen lds\n"
    "s_add_u32 m0, s29, 13312\n"
    "s_nop 0\n"
    "buffer_load_dwordx4 v169, s[12:15], s40 offen lds\n"
    "s_add_u32 m0, s29, 14336\n"
    "s_nop 0\n"
    "buffer_load_dwordx4 v170, s[12:15], s40 offen lds\n"
    "s_waitcnt lgkmcnt(0)\n"
    "s_waitcnt vmcnt(33)\n"
    "s_barrier\n"
    "ds_read_b64_tr_b16 a[44:45], v171 offset:36864\n"
    "ds_read_b64_tr_b16 a[46:47], v171 offset:38400\n"
    "ds_read_b64_tr_b16 a[100:101], v171 offset:36896\n"
    "ds_read_b64_tr_b16 a[102:103], v171 offset:38432\n"
    "ds_read_b64_tr_b16 a[156:157], v171 offset:36928\n"
    "ds_read_b64_tr_b16 a[158:159], v171 offset:38464\n"
    "s_add_u32 s40, s48, 5898240\n"
    "s_add_u32 m0, s29, 24576\n"
    "s_nop 0\n"
    "buffer_load_dwordx4 v168, s[12:15], s40 offen lds\n"
    "s_add_u32 m0, s29, 25600\n"
    "s_nop 0\n"
    "buffer_load_dwordx4 v169, s[12:15], s40 offen lds\n"
    "s_add_u32 m0, s29, 26624\n"
    "s_nop 0\n"
    "buffer_load_dwordx4 v170, s[12:15], s40 offen lds\n"
    "s_waitcnt lgkmcnt(0)\n"
    "s_waitcnt vmcnt(33)\n"
    "s_barrier\n"
    "ds_read_b64_tr_b16 a[48:49], v171 offset:49152\n"
    "ds_read_b64_tr_b16 a[50:51], v171 offset:50688\n"
    "ds_read_b64_tr_b16 a[104:105], v171 offset:49184\n"
    "ds_read_b64_tr_b16 a[106:107], v171 offset:50720\n"
    "ds_read_b64_tr_b16 a[160:161], v171 offset:49216\n"
    "ds_read_b64_tr_b16 a[162:163], v171 offset:50752\n"
    "s_add_u32 s40, s48, 6291456\n"
    "s_add_u32 m0, s29, 36864\n"
    "s_nop 0\n"
    "buffer_load_dwordx4 v168, s[12:15], s40 offen lds\n"
    "s_add_u32 m0, s29, 37888\n"
    "s_nop 0\n"
    "buffer_load_dwordx4 v169, s[12:15], s40 offen lds\n"
    "s_add_u32 m0, s29, 38912\n"
    "s_nop 0\n"
    "buffer_load_dwordx4 v170, s[12:15], s40 offen lds\n"
    "s_waitcnt lgkmcnt(0)\n"
    "s_waitcnt vmcnt(33)\n"
    "s_barrier\n"
    "ds_read_b64_tr_b16 a[52:53], v172 offset:4096\n"
    "ds_read_b64_tr_b16 a[54:55], v172 offset:5632\n"
    "ds_read_b64_tr_b16 a[108:109], v172 offset:4128\n"
    "ds_read_b64_tr_b16 a[110:111], v172 offset:5664\n"
    "ds_read_b64_tr_b16 a[164:165], v172 offset:4160\n"
    "ds_read_b64_tr_b16 a[166:167], v172 offset:5696\n"
    "s_add_u32 s40, s48, 6684672\n"
    "s_add_u32 m0, s29, 49152\n"
    "s_nop 0\n"
    "buffer_load_dwordx4 v168, s[12:15], s40 offen lds\n"
    "s_add_u32 m0, s29, 50176\n"
    "s_nop 0\n"
    "buffer_load_dwordx4 v169, s[12:15], s40 offen lds\n"
    "s_add_u32 m0, s29, 51200\n"
    "s_nop 0\n"
    "buffer_load_dwordx4 v170, s[12:15], s40 offen lds\n"
    "s_waitcnt lgkmcnt(0)\n"
    "s_waitcnt vmcnt(33)\n"
    "s_barrier\n"
    "ds_read_b64_tr_b16 a[56:57], v172 offset:16384\n"
    "ds_read_b64_tr_b16 a[58:59], v172 offset:17920\n"
    "ds_read_b64_tr_b16 a[112:113], v172 offset:16416\n"
    "ds_read_b64_tr_b16 a[114:115], v172 offset:17952\n"
    "ds_read_b64_tr_b16 a[168:169], v172 offset:16448\n"
    "ds_read_b64_tr_b16 a[170:171], v172 offset:17984\n"
    "s_add_u32 s40, s48, 7077888\n"
    "s_add_u32 m0, s29, 61440\n"
    "s_nop 0\n"
    "buffer_load_dwordx4 v168, s[12:15], s40 offen lds\n"
    "s_add_u32 m0, s29, 62464\n"
    "s_nop 0\n"
    "buffer_load_dwordx4 v169, s[12:15], s40 offen lds\n"
    "s_add_u32 m0, s29, 63488\n"
    "s_nop 0\n"
    "buffer_load_dwordx4 v170, s[12:15], s40 offen lds\n"
    "s_waitcnt lgkmcnt(0)\n"
    "s_waitcnt vmcnt(33)\n"
    "s_barrier\n"
    "ds_read_b64_tr_b16 a[60:61], v172 offset:28672\n"
    "ds_read_b64_tr_b16 a[62:63], v172 offset:30208\n"
    "ds_read_b64_tr_b16 a[116:117], v172 offset:28704\n"
    "ds_read_b64_tr_b16 a[118:119], v172 offset:30240\n"
    "ds_read_b64_tr_b16 a[172:173], v172 offset:28736\n"
    "ds_read_b64_tr_b16 a[174:175], v172 offset:30272\n"
    "s_add_u32 s40, s48, 7471104\n"
    "s_add_u32 m0, s29, 73728\n"
    "s_nop 0\n"
    "buffer_load_dwordx4 v168, s[12:15], s40 offen lds\n"
    "s_add_u32 m0, s29, 74752\n"
    "s_nop 0\n"
    "buffer_load_dwordx4 v169, s[12:15], s40 offen lds\n"
    "s_add_u32 m0, s29, 75776\n"
    "s_nop 0\n"
    "buffer_load_dwordx4 v170, s[12:15], s40 offen lds\n"
    "s_waitcnt lgkmcnt(0)\n"
    "s_waitcnt vmcnt(33)\n"
    "s_barrier\n"
    "ds_read_b64_tr_b16 a[64:65], v172 offset:40960\n"
    "ds_read_b64_tr_b16 a[66:67], v172 offset:42496\n"
    "ds_read_b64_tr_b16 a[120:121], v172 offset:40992\n"
    "ds_read_b64_tr_b16 a[122:123], v172 offset:42528\n"
    "ds_read_b64_tr_b16 a[176:177], v172 offset:41024\n"
    "ds_read_b64_tr_b16 a[178:179], v172 offset:42560\n"
    "s_waitcnt lgkmcnt(0)\n"
    "s_waitcnt vmcnt(30)\n"
    "s_barrier\n"
    "ds_read_b64_tr_b16 a[68:69], v172 offset:53248\n"
    "ds_read_b64_tr_b16 a[70:71], v172 offset:54784\n"
    "ds_read_b64_tr_b16 a[124:125], v172 offset:53280\n"
    "ds_read_b64_tr_b16 a[126:127], v172 offset:54816\n"
    "ds_read_b64_tr_b16 a[180:181], v172 offset:53312\n"
    "ds_read_b64_tr_b16 a[182:183], v172 offset:54848\n"
    "s_waitcnt lgkmcnt(0)\n"
    "s_waitcnt vmcnt(27)\n"
    "s_barrier\n"
    "ds_read_b64_tr_b16 v[24:25], v173 offset:8192\n"
    "ds_read_b64_tr_b16 v[26:27], v173 offset:9728\n"
    "ds_read_b64_tr_b16 v[32:33], v173 offset:8224\n"
    "ds_read_b64_tr_b16 v[34:35], v173 offset:9760\n"
    "ds_read_b64_tr_b16 v[40:41], v173 offset:8256\n"
    "ds_read_b64_tr_b16 v[42:43], v173 offset:9792\n"
    "s_waitcnt lgkmcnt(0)\n"
    "s_waitcnt vmcnt(24)\n"
    "s_barrier\n"
    "ds_read_b64_tr_b16 v[28:29], v173 offset:20480\n"
    "ds_read_b64_tr_b16 v[30:31], v173 offset:22016\n"
    "ds_read_b64_tr_b16 v[36:37], v173 offset:20512\n"
    "ds_read_b64_tr_b16 v[38:39], v173 offset:22048\n"
    "ds_read_b64_tr_b16 v[44:45], v173 offset:20544\n"
    "ds_read_b64_tr_b16 v[46:47], v173 offset:22080\n"
    "s_add_u32 s40, s51, 0\n"
    "s_add_u32 m0, s49, 86016\n"
    "s_nop 0\n"
    "buffer_load_dwordx4 v8, s[16:19], s40 offen lds\n"
    "s_add_u32 m0, s49, 87040\n"
    "s_nop 0\n"
    "buffer_load_dwordx4 v9, s[16:19], s40 offen lds\n"
    "s_add_u32 m0, s49, 88064\n"
    "s_nop 0\n"
    "buffer_load_dwordx4 v10, s[16:19], s40 offen lds\n"
    "s_add_u32 m0, s49, 89088\n"
    "s_nop 0\n"
    "buffer_load_dwordx4 v11, s[16:19], s40 offen lds\n"
    "s_add_u32 m0, s49, 90112\n"
    "s_nop 0\n"
    "buffer_load_dwordx4 v12, s[16:19], s40 offen lds\n"
    "s_add_u32 m0, s49, 91136\n"
    "s_nop 0\n"
    "buffer_load_dwordx4 v13, s[16:19], s40 offen lds\n"
    "s_add_u32 m0, s49, 92160\n"
    "s_nop 0\n"
    "buffer_load_dwordx4 v14, s[16:19], s40 offen lds\n"
    "s_add_u32 m0, s49, 93184\n"
    "s_nop 0\n"
    "buffer_load_dwordx4 v15, s[16:19], s40 offen lds\n"
    "s_add_u32 m0, s49, 94208\n"
    "s_nop 0\n"
    "buffer_load_dwordx4 v16, s[16:19], s40 offen lds\n"
    "s_add_u32 m0, s49, 95232\n"
    "s_nop 0\n"
    "buffer_load_dwordx4 v17, s[16:19], s40 offen lds\n"
    "s_waitcnt lgkmcnt(0)\n"
    "s_waitcnt vmcnt(31)\n"
    "s_barrier\n"
    "ds_read_b64_tr_b16 v[48:49], v173 offset:32768\n"
    "ds_read_b64_tr_b16 v[50:51], v173 offset:34304\n"
    "ds_read_b64_tr_b16 v[56:57], v173 offset:32800\n"
    "ds_read_b64_tr_b16 v[58:59], v173 offset:34336\n"
    "ds_read_b64_tr_b16 v[64:65], v173 offset:32832\n"
    "ds_read_b64_tr_b16 v[66:67], v173 offset:34368\n"
    "s_waitcnt lgkmcnt(0)\n"
    "s_waitcnt vmcnt(28)\n"
    "s_barrier\n"
    "ds_read_b64_tr_b16 v[52:53], v171 offset:0\n"
    "ds_read_b64_tr_b16 v[54:55], v171 offset:1536\n"
    "ds_read_b64_tr_b16 v[60:61], v171 offset:32\n"
    "ds_read_b64_tr_b16 v[62:63], v171 offset:1568\n"
    "ds_read_b64_tr_b16 v[68:69], v171 offset:64\n"
    "ds_read_b64_tr_b16 v[70:71], v171 offset:1600\n"
    "s_waitcnt lgkmcnt(0)\n"
    "s_waitcnt vmcnt(25)\n"
    "s_barrier\n"
    "ds_read_b64_tr_b16 v[72:73], v171 offset:12288\n"
    "ds_read_b64_tr_b16 v[74:75], v171 offset:13824\n"
    "ds_read_b64_tr_b16 a[0:1], v171 offset:12320\n"
    "ds_read_b64_tr_b16 a[2:3], v171 offset:13856\n"
    "ds_read_b64_tr_b16 a[8:9], v171 offset:12352\n"
    "ds_read_b64_tr_b16 a[10:11], v171 offset:13888\n"
    "s_waitcnt lgkmcnt(0)\n"
    "s_waitcnt vmcnt(22)\n"
    "s_barrier\n"
    "ds_read_b64_tr_b16 v[76:77], v171 offset:24576\n"
    "ds_read_b64_tr_b16 v[78:79], v171 offset:26112\n"
    "ds_read_b64_tr_b16 a[4:5], v171 offset:24608\n"
    "ds_read_b64_tr_b16 a[6:7], v171 offset:26144\n"
    "ds_read_b64_tr_b16 a[12:13], v171 offset:24640\n"
    "ds_read_b64_tr_b16 a[14:15], v171 offset:26176\n"
    "s_waitcnt lgkmcnt(0)\n"
    "s_waitcnt vmcnt(19)\n"
    "s_barrier\n"
    "ds_read_b64_tr_b16 a[16:17], v171 offset:36864\n"
    "ds_read_b64_tr_b16 a[18:19], v171 offset:38400\n"
    "ds_read_b64_tr_b16 a[72:73], v171 offset:36896\n"
    "ds_read_b64_tr_b16 a[74:75], v171 offset:38432\n"
    "ds_read_b64_tr_b16 a[128:129], v171 offset:36928\n"
    "ds_read_b64_tr_b16 a[130:131], v171 offset:38464\n"
    "s_waitcnt lgkmcnt(0)\n"
    "s_waitcnt vmcnt(16)\n"
    "s_barrier\n"
    "ds_read_b64_tr_b16 a[20:21], v171 offset:49152\n"
    "ds_read_b64_tr_b16 a[22:23], v171 offset:50688\n"
    "ds_read_b64_tr_b16 a[76:77], v171 offset:49184\n"
    "ds_read_b64_tr_b16 a[78:79], v171 offset:50720\n"
    "ds_read_b64_tr_b16 a[132:133], v171 offset:49216\n"
    "ds_read_b64_tr_b16 a[134:135], v171 offset:50752\n"
    "s_add_u32 s40, s51, 122880\n"
    "s_add_u32 m0, s49, 0\n"
    "s_nop 0\n"
    "buffer_load_dwordx4 v8, s[16:19], s40 offen lds\n"
    "s_add_u32 m0, s49, 1024\n"
    "s_nop 0\n"
    "buffer_load_dwordx4 v9, s[16:19], s40 offen lds\n"
    "s_add_u32 m0, s49, 2048\n"
    "s_nop 0\n"
    "buffer_load_dwordx4 v10, s[16:19], s40 offen lds\n"
    "s_add_u32 m0, s49, 3072\n"
    "s_nop 0\n"
    "buffer_load_dwordx4 v11, s[16:19], s40 offen lds\n"
    "s_add_u32 m0, s49, 4096\n"
    "s_nop 0\n"
    "buffer_load_dwordx4 v12, s[16:19], s40 offen lds\n"
    "s_add_u32 m0, s49, 5120\n"
    "s_nop 0\n"
    "buffer_load_dwordx4 v13, s[16:19], s40 offen lds\n"
    "s_add_u32 m0, s49, 6144\n"
    "s_nop 0\n"
    "buffer_load_dwordx4 v14, s[16:19], s40 offen lds\n"
    "s_add_u32 m0, s49, 7168\n"
    "s_nop 0\n"
    "buffer_load_dwordx4 v15, s[16:19], s40 offen lds\n"
    "s_add_u32 m0, s49, 8192\n"
    "s_nop 0\n"
    "buffer_load_dwordx4 v16, s[16:19], s40 offen lds\n"
    "s_add_u32 m0, s49, 9216\n"
    "s_nop 0\n"
    "buffer_load_dwordx4 v17, s[16:19], s40 offen lds\n"
    "s_waitcnt lgkmcnt(0)\n"
    "s_add_u32 s4, s3, 1\n"
    "v_mov_b32 v2, s3\n"
    "s_or_b32 s41, s21, s6\n"
    "s_cbranch_scc1 .Lcl_65\n"
    "s_lshl_b32 s41, s5, 2\n"
    "s_add_u32 s41, s41, 4178944\n"
    "v_mov_b32 v3, s41\n"
    "v_mov_b32 v2, s4\n"
    "s_mov_b64 exec, 1\n"
    "buffer_atomic_swap v2, v3, s[8:11], 0 offen sc0 sc1\n"
    "s_mov_b64 exec, -1\n"
    ".Lcl_65:\n"
    "v_min_u32 v6, 54, v1\n"
    "v_lshlrev_b32 v6, 7, v6\n"
    "v_add_u32 v6, 4182016, v6\n"
    "buffer_load_dwordx2 v[4:5], v6, s[8:11], 0 offen sc1\n"
    "s_waitcnt lgkmcnt(0)\n"
    "s_waitcnt vmcnt(24)\n"
    "s_barrier\n"
    "ds_read_b64_tr_b16 a[24:25], v172 offset:4096\n"
    "ds_read_b64_tr_b16 a[26:27], v172 offset:5632\n"
    "ds_read_b64_tr_b16 a[80:81], v172 offset:4128\n"
    "ds_read_b64_tr_b16 a[82:83], v172 offset:5664\n"
    "ds_read_b64_tr_b16 a[136:137], v172 offset:4160\n"
    "ds_read_b64_tr_b16 a[138:139], v172 offset:5696\n"
    "s_waitcnt lgkmcnt(0)\n"
    "s_waitcnt vmcnt(21)\n"
    "s_barrier\n"
    "ds_read_b64_tr_b16 a[28:29], v172 offset:16384\n"
    "ds_read_b64_tr_b16 a[30:31], v172 offset:17920\n"
    "ds_read_b64_tr_b16 a[84:85], v172 offset:16416\n"
    "ds_read_b64_tr_b16 a[86:87], v172 offset:17952\n"
    "ds_read_b64_tr_b16 a[140:141], v172 offset:16448\n"
    "ds_read_b64_tr_b16 a[142:143], v172 offset:17984\n"
    "s_waitcnt lgkmcnt(0)\n"
    "s_waitcnt vmcnt(11)\n"
    "s_barrier\n"
    "ds_read_b64_tr_b16 v[80:81], v19 offset:28672\n"
    "ds_read_b64_tr_b16 v[82:83], v19 offset:29952\n"
    "ds_read_b64_tr_b16 v[88:89], v19 offset:28704\n"
    "ds_read_b64_tr_b16 v[90:91], v19 offset:29984\n"
    "ds_read_b64_tr_b16 v[96:97], v19 offset:28736\n"
    "ds_read_b64_tr_b16 v[98:99], v19 offset:30016\n"
    "ds_read_b64_tr_b16 v[104:105], v19 offset:28768\n"
    "ds_read_b64_tr_b16 v[106:107], v19 offset:30048\n"
    "ds_read_b64_tr_b16 v[112:113], v19 offset:28800\n"
    "ds_read_b64_tr_b16 v[114:115], v19 offset:30080\n"
    "ds_read_b64_tr_b16 v[120:121], v19 offset:28832\n"
    "ds_read_b64_tr_b16 v[122:123], v19 offset:30112\n"
    "ds_read_b64_tr_b16 v[128:129], v19 offset:28864\n"
    "ds_read_b64_tr_b16 v[130:131], v19 offset:30144\n"
    "ds_read_b64_tr_b16 a[184:185], v19 offset:28896\n"
    "ds_read_b64_tr_b16 a[186:187], v19 offset:30176\n"
    "ds_read_b64_tr_b16 v[208:209], v19 offset:28928\n"
    "ds_read_b64_tr_b16 v[210:211], v19 offset:30208\n"
    "ds_read_b64_tr_b16 v[232:233], v19 offset:28960\n"
    "ds_read_b64_tr_b16 v[234:235], v19 offset:30240\n"
    "s_add_u32 s40, s51, 20480\n"
    "s_add_u32 m0, s49, 40960\n"
    "s_nop 0\n"
    "buffer_load_dwordx4 v8, s[16:19], s40 offen lds\n"
    "s_add_u32 m0, s49, 41984\n"
    "s_nop 0\n"
    "buffer_load_dwordx4 v9, s[16:19], s40 offen lds\n"
    "s_add_u32 m0, s49, 43008\n"
    "s_nop 0\n"
    "buffer_load_dwordx4 v10, s[16:19], s40 offen lds\n"
    "s_add_u32 m0, s49, 44032\n"
    "s_nop 0\n"
    "buffer_load_dwordx4 v11, s[16:19], s40 offen lds\n"
    "s_add_u32 m0, s49, 45056\n"
    "s_nop 0\n"
    "buffer_load_dwordx4 v12, s[16:19], s40 offen lds\n"
    "s_add_u32 m0, s49, 46080\n"
    "s_nop 0\n"
    "buffer_load_dwordx4 v13, s[16:19], s40 offen lds\n"
    "s_add_u32 m0, s49, 47104\n"
    "s_nop 0\n"
    "buffer_load_dwordx4 v14, s[16:19], s40 offen lds\n"
    "s_add_u32 m0, s49, 48128\n"
    "s_nop 0\n"
    "buffer_load_dwordx4 v15, s[16:19], s40 offen lds\n"
    "s_add_u32 m0, s49, 49152\n"
    "s_nop 0\n"
    "buffer_load_dwordx4 v16, s[16:19], s40 offen lds\n"
    "s_add_u32 m0, s49, 50176\n"
    "s_nop 0\n"
    "buffer_load_dwordx4 v17, s[16:19], s40 offen lds\n"
    "s_waitcnt lgkmcnt(0)\n"
    "s_waitcnt vmcnt(11)\n"
    "s_barrier\n"
    "ds_read_b64_tr_b16 v[84:85], v18 offset:0\n"
    "ds_read_b64_tr_b16 v[86:87], v18 offset:1280\n"
    "ds_read_b64_tr_b16 v[92:93], v18 offset:32\n"
    "ds_read_b64_tr_b16 v[94:95], v18 offset:1312\n"
    "ds_read_b64_tr_b16 v[100:101], v18 offset:64\n"
    "ds_read_b64_tr_b16 v[102:103], v18 offset:1344\n"
    "ds_read_b64_tr_b16 v[108:109], v18 offset:96\n"
    "ds_read_b64_tr_b16 v[110:111], v18 offset:1376\n"
    "ds_read_b64_tr_b16 v[116:117], v18 offset:128\n"
    "ds_read_b64_tr_b16 v[118:119], v18 offset:1408\n"
    "ds_read_b64_tr_b16 v[124:125], v18 offset:160\n"
    "ds_read_b64_tr_b16 v[126:127], v18 offset:1440\n"
    "ds_read_b64_tr_b16 v[132:133], v18 offset:192\n"
    "ds_read_b64_tr_b16 v[134:135], v18 offset:1472\n"
    "ds_read_b64_tr_b16 a[188:189], v18 offset:224\n"
    "ds_read_b64_tr_b16 a[190:191], v18 offset:1504\n"
    "ds_read_b64_tr_b16 v[212:213], v18 offset:256\n"
    "ds_read_b64_tr_b16 v[214:215], v18 offset:1536\n"
    "ds_read_b64_tr_b16 v[236:237], v18 offset:288\n"
    "ds_read_b64_tr_b16 v[238:239], v18 offset:1568\n"
    "s_add_u32 s40, s51, 143360\n"
    "s_add_u32 m0, s49, 81920\n"
    "s_nop 0\n"
    "buffer_load_dwordx4 v8, s[16:19], s40 offen lds\n"
    "s_add_u32 m0, s49, 82944\n"
    "s_nop 0\n"
    "buffer_load_dwordx4 v9, s[16:19], s40 offen lds\n"
    "s_add_u32 m0, s49, 83968\n"
    "s_nop 0\n"
    "buffer_load_dwordx4 v10, s[16:19], s40 offen lds\n"
    "s_add_u32 m0, s49, 84992\n"
    "s_nop 0\n"
    "buffer_load_dwordx4 v11, s[16:19], s40 offen lds\n"
    "s_add_u32 m0, s49, 86016\n"
    "s_nop 0\n"
    "buffer_load_dwordx4 v12, s[16:19], s40 offen lds\n"
    "s_add_u32 m0, s49, 87040\n"
    "s_nop 0\n"
    "buffer_load_dwordx4 v13, s[16:19], s40 offen lds\n"
    "s_add_u32 m0, s49, 88064\n"
    "s_nop 0\n"
    "buffer_load_dwordx4 v14, s[16:19], s40 offen lds\n"
    "s_add_u32 m0, s49, 89088\n"
    "s_nop 0\n"
    "buffer_load_dwordx4 v15, s[16:19], s40 offen lds\n"
    "s_add_u32 m0, s49, 90112\n"
    "s_nop 0\n"
    "buffer_load_dwordx4 v16, s[16:19], s40 offen lds\n"
    "s_add_u32 m0, s49, 91136\n"
    "s_nop 0\n"
    "buffer_load_dwordx4 v17, s[16:19], s40 offen lds\n"
    "s_waitcnt lgkmcnt(0)\n"
    "s_waitcnt vmcnt(10)\n"
    "s_barrier\n"
    "ds_read_b64_tr_b16 a[192:193], v18 offset:40960\n"
    "ds_read_b64_tr_b16 a[194:195], v18 offset:42240\n"
    "ds_read_b64_tr_b16 a[200:201], v18 offset:40992\n"
    "ds_read_b64_tr_b16 a[202:203], v18 offset:42272\n"
    "ds_read_b64_tr_b16 a[208:209], v18 offset:41024\n"
    "ds_read_b64_tr_b16 a[210:211], v18 offset:42304\n"
    "ds_read_b64_tr_b16 a[216:217], v18 offset:41056\n"
    "ds_read_b64_tr_b16 a[218:219], v18 offset:42336\n"
    "ds_read_b64_tr_b16 a[224:225], v18 offset:41088\n"
    "ds_read_b64_tr_b16 a[226:227], v18 offset:42368\n"
    "ds_read_b64_tr_b16 a[232:233], v18 offset:41120\n"
    "ds_read_b64_tr_b16 a[234:235], v18 offset:42400\n"
    "ds_read_b64_tr_b16 v[136:137], v18 offset:41152\n"
    "ds_read_b64_tr_b16 v[138:139], v18 offset:42432\n"
    "ds_read_b64_tr_b16 v[152:153], v18 offset:41184\n"
    "ds_read_b64_tr_b16 v[154:155], v18 offset:42464\n"
    "ds_read_b64_tr_b16 v[216:217], v18 offset:41216\n"
    "ds_read_b64_tr_b16 v[218:219], v18 offset:42496\n"
    "ds_read_b64_tr_b16 v[240:241], v18 offset:41248\n"
    "ds_read_b64_tr_b16 v[242:243], v18 offset:42528\n"
    "s_add_u32 s40, s51, 40960\n"
    "s_add_u32 m0, s49, 0\n"
    "s_nop 0\n"
    "buffer_load_dwordx4 v8, s[16:19], s40 offen lds\n"
    "s_add_u32 m0, s49, 1024\n"
    "s_nop 0\n"
    "buffer_load_dwordx4 v9, s[16:19], s40 offen lds\n"
    "s_add_u32 m0, s49, 2048\n"
    "s_nop 0\n"
    "buffer_load_dwordx4 v10, s[16:19], s40 offen lds\n"
    "s_add_u32 m0, s49, 3072\n"
    "s_nop 0\n"
    "buffer_load_dwordx4 v11, s[16:19], s40 offen lds\n"
    "s_add_u32 m0, s49, 4096\n"
    "s_nop 0\n"
    "buffer_load_dwordx4 v12, s[16:19], s40 offen lds\n"
    "s_add_u32 m0, s49, 5120\n"
    "s_nop 0\n"
    "buffer_load_dwordx4 v13, s[16:19], s40 offen lds\n"
    "s_add_u32 m0, s49, 6144\n"
    "s_nop 0\n"
    "buffer_load_dwordx4 v14, s[16:19], s40 offen lds\n"
    "s_add_u32 m0, s49, 7168\n"
    "s_nop 0\n"
    "buffer_load_dwordx4 v15, s[16:19], s40 offen lds\n"
    "s_add_u32 m0, s49, 8192\n"
    "s_nop 0\n"
    "buffer_load_dwordx4 v16, s[16:19], s40 offen lds\n"
    "s_add_u32 m0, s49, 9216\n"
    "s_nop 0\n"
    "buffer_load_dwordx4 v17, s[16:19], s40 offen lds\n"
    "s_waitcnt lgkmcnt(0)\n"
    "s_waitcnt vmcnt(10)\n"
    "s_barrier\n"
    "ds_read_b64_tr_b16 a[196:197], v19 offset:24576\n"
    "ds_read_b64_tr_b16 a[198:199], v19 offset:25856\n"
    "ds_read_b64_tr_b16 a[204:205], v19 offset:24608\n"
    "ds_read_b64_tr_b16 a[206:207], v19 offset:25888\n"
    "ds_read_b64_tr_b16 a[212:213], v19 offset:24640\n"
    "ds_read_b64_tr_b16 a[214:215], v19 offset:25920\n"
    "ds_read_b64_tr_b16 a[220:221], v19 offset:24672\n"
    "ds_read_b64_tr_b16 a[222:223], v19 offset:25952\n"
    "ds_read_b64_tr_b16 a[228:229], v19 offset:24704\n"
    "ds_read_b64_tr_b16 a[230:231], v19 offset:25984\n"
    "ds_read_b64_tr_b16 a[236:237], v19 offset:24736\n"
    "ds_read_b64_tr_b16 a[238:239], v19 offset:26016\n"
    "ds_read_b64_tr_b16 v[140:141], v19 offset:24768\n"
    "ds_read_b64_tr_b16 v[142:143], v19 offset:26048\n"
    "ds_read_b64_tr_b16 v[156:157], v19 offset:24800\n"
    "ds_read_b64_tr_b16 v[158:159], v19 offset:26080\n"
    "ds_read_b64_tr_b16 v[220:221], v19 offset:24832\n"
    "ds_read_b64_tr_b16 v[222:223], v19 offset:26112\n"
    "ds_read_b64_tr_b16 v[244:245], v19 offset:24864\n"
    "ds_read_b64_tr_b16 v[246:247], v19 offset:26144\n"
    "s_add_u32 s40, s51, 163840\n"
    "s_add_u32 m0, s49, 40960\n"
    "s_nop 0\n"
    "buffer_load_dwordx4 v8, s[16:19], s40 offen lds\n"
    "s_add_u32 m0, s49, 41984\n"
    "s_nop 0\n"
    "buffer_load_dwordx4 v9, s[16:19], s40 offen lds\n"
    "s_add_u32 m0, s49, 43008\n"
    "s_nop 0\n"
    "buffer_load_dwordx4 v10, s[16:19], s40 offen lds\n"
    "s_add_u32 m0, s49, 44032\n"
    "s_nop 0\n"
    "buffer_load_dwordx4 v11, s[16:19], s40 offen lds\n"
    "s_add_u32 m0, s49, 45056\n"
    "s_nop 0\n"
    "buffer_load_dwordx4 v12, s[16:19], s40 offen lds\n"
    "s_add_u32 m0, s49, 46080\n"
    "s_nop 0\n"
    "buffer_load_dwordx4 v13, s[16:19], s40 offen lds\n"
    "s_add_u32 m0, s49, 47104\n"
    "s_nop 0\n"
    "buffer_load_dwordx4 v14, s[16:19], s40 offen lds\n"
    "s_add_u32 m0, s49, 48128\n"
    "s_nop 0\n"
    "buffer_load_dwordx4 v15, s[16:19], s40 offen lds\n"
    "s_add_u32 m0, s49, 49152\n"
    "s_nop 0\n"
    "buffer_load_dwordx4 v16, s[16:19], s40 offen lds\n"
    "s_add_u32 m0, s49, 50176\n"
    "s_nop 0\n"
    "buffer_load_dwordx4 v17, s[16:19], s40 offen lds\n"
    "s_waitcnt lgkmcnt(0)\n"
    "s_waitcnt vmcnt(10)\n"
    "s_barrier\n"
    "ds_read_b64_tr_b16 a[240:241], v18 offset:0\n"
    "ds_read_b64_tr_b16 a[242:243], v18 offset:1280\n"
    "ds_read_b64_tr_b16 a[248:249], v18 offset:32\n"
    "ds_read_b64_tr_b16 a[250:251], v18 offset:1312\n"
    "ds_read_b64_tr_b16 v[176:177], v18 offset:64\n"
    "ds_read_b64_tr_b16 v[178:179], v18 offset:1344\n"
    "ds_read_b64_tr_b16 v[184:185], v18 offset:96\n"
    "ds_read_b64_tr_b16 v[186:187], v18 offset:1376\n"
    "ds_read_b64_tr_b16 v[192:193], v18 offset:128\n"
    "ds_read_b64_tr_b16 v[194:195], v18 offset:1408\n"
    "ds_read_b64_tr_b16 v[200:201], v18 offset:160\n"
    "ds_read_b64_tr_b16 v[202:203], v18 offset:1440\n"
    "ds_read_b64_tr_b16 v[144:145], v18 offset:192\n"
    "ds_read_b64_tr_b16 v[146:147], v18 offset:1472\n"
    "ds_read_b64_tr_b16 v[160:161], v18 offset:224\n"
    "ds_read_b64_tr_b16 v[162:163], v18 offset:1504\n"
    "ds_read_b64_tr_b16 v[224:225], v18 offset:256\n"
    "ds_read_b64_tr_b16 v[226:227], v18 offset:1536\n"
    "ds_read_b64_tr_b16 v[248:249], v18 offset:288\n"
    "ds_read_b64_tr_b16 v[250:251], v18 offset:1568\n"
    "s_waitcnt lgkmcnt(0)\n"
    "s_waitcnt vmcnt(0)\n"
    "s_barrier\n"
    "ds_read_b64_tr_b16 a[244:245], v18 offset:40960\n"
    "ds_read_b64_tr_b16 a[246:247], v18 offset:42240\n"
    "ds_read_b64_tr_b16 a[252:253], v18 offset:40992\n"
    "ds_read_b64_tr_b16 a[254:255], v18 offset:42272\n"
    "ds_read_b64_tr_b16 v[180:181], v18 offset:41024\n"
    "ds_read_b64_tr_b16 v[182:183], v18 offset:42304\n"
    "ds_read_b64_tr_b16 v[188:189], v18 offset:41056\n"
    "ds_read_b64_tr_b16 v[190:191], v18 offset:42336\n"
    "ds_read_b64_tr_b16 v[196:197], v18 offset:41088\n"
    "ds_read_b64_tr_b16 v[198:199], v18 offset:42368\n"
    "ds_read_b64_tr_b16 v[204:205], v18 offset:41120\n"
    "ds_read_b64_tr_b16 v[206:207], v18 offset:42400\n"
    "ds_read_b64_tr_b16 v[148:149], v18 offset:41152\n"
    "ds_read_b64_tr_b16 v[150:151], v18 offset:42432\n"
    "ds_read_b64_tr_b16 v[164:165], v18 offset:41184\n"
    "ds_read_b64_tr_b16 v[166:167], v18 offset:42464\n"
    "ds_read_b64_tr_b16 v[228:229], v18 offset:41216\n"
    "ds_read_b64_tr_b16 v[230:231], v18 offset:42496\n"
    "ds_read_b64_tr_b16 v[252:253], v18 offset:41248\n"
    "ds_read_b64_tr_b16 v[254:255], v18 offset:42528\n"
    "s_waitcnt lgkmcnt(0)\n"
    "v_cmp_ne_u32 vcc, s4, v4\n"
    "s_nop 1\n"
    "s_cmp_eq_u32 vcc_lo, 0\n"
    "s_cbranch_scc1 .Lrx_68\n"
    ".Lrp_66:\n"
    "v_min_u32 v6, 54, v1\n"
    "v_lshlrev_b32 v6, 7, v6\n"
    "v_add_u32 v6, 4182016, v6\n"
    "buffer_load_dwordx2 v[4:5], v6, s[8:11], 0 offen sc1\n"
    "s_waitcnt vmcnt(0)\n"
    "v_cmp_ne_u32 vcc, s4, v4\n"
    "s_nop 1\n"
    "s_cmp_eq_u32 vcc_lo, 0\n"
    "s_cbranch_scc1 .Lro_67\n"
    "s_sleep 2\n"
    "s_sub_u32 s25, s25, 1\n"
    "s_cbranch_scc0 .Lrp_66\n"
    "s_branch .Lfail\n"
    ".Lro_67:\n"
    ".Lrx_68:\n"
    "v_cmp_ne_u32 vcc, s4, v4\n"
    "s_nop 1\n"
    "s_cmp_eq_u32 vcc_hi, 0\n"
    "s_cbranch_scc1 .Lrx_71\n"
    ".Lrp_69:\n"
    "v_min_u32 v6, 54, v1\n"
    "v_lshlrev_b32 v6, 7, v6\n"
    "v_add_u32 v6, 4182016, v6\n"
    "buffer_load_dwordx2 v[4:5], v6, s[8:11], 0 offen sc1\n"
    "s_waitcnt vmcnt(0)\n"
    "v_cmp_ne_u32 vcc, s4, v4\n"
    "s_nop 1\n"
    "s_cmp_eq_u32 vcc_hi, 0\n"
    "s_cbranch_scc1 .Lro_70\n"
    "s_sleep 2\n"
    "s_sub_u32 s25, s25, 1\n"
    "s_cbranch_scc0 .Lrp_69\n"
    "s_branch .Lfail\n"
    ".Lro_70:\n"
    ".Lrx_71:\n"
    "v_cmp_ne_u32 vcc, 0, v5\n"
    "s_nop 1\n"
    "s_and_b32 s22, vcc_hi, 1\n"
    "s_lshl_b32 s22, s22, 13\n"
    "s_lshr_b32 s23, vcc_hi, 1\n"
    "s_branch .Lpw_47\n"
    ".Lpn_64:\n"
    "s_add_u32 s40, s48, 0\n"
    "s_add_u32 m0, s29, 0\n"
    "s_nop 0\n"
    "buffer_load_dwordx4 v168, s[12:15], s40 offen lds\n"
    "s_add_u32 m0, s29, 1024\n"
    "s_nop 0\n"
    "buffer_load_dwordx4 v169, s[12:15], s40 offen lds\n"
    "s_add_u32 m0, s29, 2048\n"
    "s_nop 0\n"
    "buffer_load_dwordx4 v170, s[12:15], s40 offen lds\n"
    "s_add_u32 s40, s48, 393216\n"
    "s_add_u32 m0, s29, 12288\n"
    "s_nop 0\n"
    "buffer_load_dwordx4 v168, s[12:15], s40 offen lds\n"
    "s_add_u32 m0, s29, 13312\n"
    "s_nop 0\n"
    "buffer_load_dwordx4 v169, s[12:15], s40 offen lds\n"
    "s_add_u32 m0, s29, 14336\n"
    "s_nop 0\n"
    "buffer_load_dwordx4 v170, s[12:15], s40 offen lds\n"
    "s_add_u32 s40, s48, 786432\n"
    "s_add_u32 m0, s29, 24576\n"
    "s_nop 0\n"
    "buffer_load_dwordx4 v168, s[12:15], s40 offen lds\n"
    "s_add_u32 m0, s29, 25600\n"
    "s_nop 0\n"
    "buffer_load_dwordx4 v169, s[12:15], s40 offen lds\n"
    "s_add_u32 m0, s29, 26624\n"
    "s_nop 0\n"
    "buffer_load_dwordx4 v170, s[12:15], s40 offen lds\n"
    "s_add_u32 s40, s48, 1179648\n"
    "s_add_u32 m0, s29, 36864\n"
    "s_nop 0\n"
    "buffer_load_dwordx4 v168, s[12:15], s40 offen lds\n"
    "s_add_u32 m0, s29, 37888\n"
    "s_nop 0\n"
    "buffer_load_dwordx4 v169, s[12:15], s40 offen lds\n"
    "s_add_u32 m0, s29, 38912\n"
    "s_nop 0\n"
    "buffer_load_dwordx4 v170, s[12:15], s40 offen lds\n"
    "s_add_u32 s40, s48, 1572864\n"
    "s_add_u32 m0, s29, 49152\n"
    "s_nop 0\n"
    "buffer_load_dwordx4 v168, s[12:15], s40 offen lds\n"
    "s_add_u32 m0, s29, 50176\n"
    "s_nop 0\n"
    "buffer_load_dwordx4 v169, s[12:15], s40 offen lds\n"
    "s_add_u32 m0, s29, 51200\n"
    "s_nop 0\n"
    "buffer_load_dwordx4 v170, s[12:15], s40 offen lds\n"
    "s_add_u32 s40, s48, 1966080\n"
    "s_add_u32 m0, s29, 61440\n"
    "s_nop 0\n"
    "buffer_load_dwordx4 v168, s[12:15], s40 offen lds\n"
    "s_add_u32 m0, s29, 62464\n"
    "s_nop 0\n"
    "buffer_load_dwordx4 v169, s[12:15], s40 offen lds\n"
    "s_add_u32 m0, s29, 63488\n"
    "s_nop 0\n"
    "buffer_load_dwordx4 v170, s[12:15], s40 offen lds\n"
    "s_add_u32 s40, s48, 2359296\n"
    "s_add_u32 m0, s29, 73728\n"
    "s_nop 0\n"
    "buffer_load_dwordx4 v168, s[12:15], s40 offen lds\n"
    "s_add_u32 m0, s29, 74752\n"
    "s_nop 0\n"
    "buffer_load_dwordx4 v169, s[12:15], s40 offen lds\n"
    "s_add_u32 m0, s29, 75776\n"
    "s_nop 0\n"
    "buffer_load_dwordx4 v170, s[12:15], s40 offen lds\n"
    "s_add_u32 s40, s48, 2752512\n"
    "s_add_u32 m0, s29, 86016\n"
    "s_nop 0\n"
    "buffer_load_dwordx4 v168, s[12:15], s40 offen lds\n"
    "s_add_u32 m0, s29, 87040\n"
    "s_nop 0\n"
    "buffer_load_dwordx4 v169, s[12:15], s40 offen lds\n"
    "s_add_u32 m0, s29, 88064\n"
    "s_nop 0\n"
    "buffer_load_dwordx4 v170, s[12:15], s40 offen lds\n"
    "s_add_u32 s40, s48, 3145728\n"
    "s_add_u32 m0, s29, 98304\n"
    "s_nop 0\n"
    "buffer_load_dwordx4 v168, s[12:15], s40 offen lds\n"
    "s_add_u32 m0, s29, 99328\n"
    "s_nop 0\n"
    "buffer_load_dwordx4 v169, s[12:15], s40 offen lds\n"
    "s_add_u32 m0, s29, 100352\n"
    "s_nop 0\n"
    "buffer_load_dwordx4 v170, s[12:15], s40 offen lds\n"
    "s_add_u32 s40, s48, 3538944\n"
    "s_add_u32 m0, s29, 110592\n"
    "s_nop 0\n"
    "buffer_load_dwordx4 v168, s[12:15], s40 offen lds\n"
    "s_add_u32 m0, s29, 111616\n"
    "s_nop 0\n"
    "buffer_load_dwordx4 v169, s[12:15], s40 offen lds\n"
    "s_add_u32 m0, s29, 112640\n"
    "s_nop 0\n"
    "buffer_load_dwordx4 v170, s[12:15], s40 offen lds\n"
    "s_add_u32 s40, s48, 3932160\n"
    "s_add_u32 m0, s29, 122880\n"
    "s_nop 0\n"
    "buffer_load_dwordx4 v168, s[12:15], s40 offen lds\n"
    "s_add_u32 m0, s29, 123904\n"
    "s_nop 0\n"
    "buffer_load_dwordx4 v169, s[12:15], s40 offen lds\n"
    "s_add_u32 m0, s29, 124928\n"
    "s_nop 0\n"
    "buffer_load_dwordx4 v170, s[12:15], s40 offen lds\n"
    "s_add_u32 s40, s48, 4325376\n"
    "s_add_u32 m0, s29, 135168\n"
    "s_nop 0\n"
    "buffer_load_dwordx4 v168, s[12:15], s40 offen lds\n"
    "s_add_u32 m0, s29, 136192\n"
    "s_nop 0\n"
    "buffer_load_dwordx4 v169, s[12:15], s40 offen lds\n"
    "s_add_u32 m0, s29, 137216\n"
    "s_nop 0\n"
    "buffer_load_dwordx4 v170, s[12:15], s40 offen lds\n"
    "s_add_u32 s40, s48, 4718592\n"
    "s_add_u32 m0, s29, 147456\n"
    "s_nop 0\n"
    "buffer_load_dwordx4 v168, s[12:15], s40 offen lds\n"
    "s_add_u32 m0, s29, 148480\n"
    "s_nop 0\n"
    "buffer_load_dwordx4 v169, s[12:15], s40 offen lds\n"
    "s_add_u32 m0, s29, 149504\n"
    "s_nop 0\n"
    "buffer_load_dwordx4 v170, s[12:15], s40 offen lds\n"
    "s_waitcnt vmcnt(36)\n"
    "s_barrier\n"
    "ds_read_b64_tr_b16 a[32:33], v171 offset:0\n"
    "ds_read_b64_tr_b16 a[34:35], v171 offset:1536\n"
    "ds_read_b64_tr_b16 a[88:89], v171 offset:32\n"
    "ds_read_b64_tr_b16 a[90:91], v171 offset:1568\n"
    "ds_read_b64_tr_b16 a[144:145], v171 offset:64\n"
    "ds_read_b64_tr_b16 a[146:147], v171 offset:1600\n"
    "s_waitcnt lgkmcnt(0)\n"
    "s_waitcnt vmcnt(33)\n"
    "s_barrier\n"
    "ds_read_b64_tr_b16 a[36:37], v171 offset:12288\n"
    "ds_read_b64_tr_b16 a[38:39], v171 offset:13824\n"
    "ds_read_b64_tr_b16 a[92:93], v171 offset:12320\n"
    "ds_read_b64_tr_b16 a[94:95], v171 offset:13856\n"
    "ds_read_b64_tr_b16 a[148:149], v171 offset:12352\n"
    "ds_read_b64_tr_b16 a[150:151], v171 offset:13888\n"
    "s_add_u32 s40, s48, 5111808\n"
    "s_add_u32 m0, s29, 0\n"
    "s_nop 0\n"
    "buffer_load_dwordx4 v168, s[12:15], s40 offen lds\n"
    "s_add_u32 m0, s29, 1024\n"
    "s_nop 0\n"
    "buffer_load_dwordx4 v169, s[12:15], s40 offen lds\n"
    "s_add_u32 m0, s29, 2048\n"
    "s_nop 0\n"
    "buffer_load_dwordx4 v170, s[12:15], s40 offen lds\n"
    "s_waitcnt lgkmcnt(0)\n"
    "s_waitcnt vmcnt(33)\n"
    "s_barrier\n"
    "ds_read_b64_tr_b16 a[40:41], v171 offset:24576\n"
    "ds_read_b64_tr_b16 a[42:43], v171 offset:26112\n"
    "ds_read_b64_tr_b16 a[96:97], v171 offset:24608\n"
    "ds_read_b64_tr_b16 a[98:99], v171 offset:26144\n"
    "ds_read_b64_tr_b16 a[152:153], v171 offset:24640\n"
    "ds_read_b64_tr_b16 a[154:155], v171 offset:26176\n"
    "s_add_u32 s40, s48, 5505024\n"
    "s_add_u32 m0, s29, 12288\n"
    "s_nop 0\n"
    "buffer_load_dwordx4 v168, s[12:15], s40 offen lds\n"
    "s_add_u32 m0, s29, 13312\n"
    "s_nop 0\n"
    "buffer_load_dwordx4 v169, s[12:15], s40 offen lds\n"
    "s_add_u32 m0, s29, 14336\n"
    "s_nop 0\n"
    "buffer_load_dwordx4 v170, s[12:15], s40 offen lds\n"
    "s_waitcnt lgkmcnt(0)\n"
    "s_waitcnt vmcnt(33)\n"
    "s_barrier\n"
    "ds_read_b64_tr_b16 a[44:45], v171 offset:36864\n"
    "ds_read_b64_tr_b16 a[46:47], v171 offset:38400\n"
    "ds_read_b64_tr_b16 a[100:101], v171 offset:36896\n"
    "ds_read_b64_tr_b16 a[102:103], v171 offset:38432\n"
    "ds_read_b64_tr_b16 a[156:157], v171 offset:36928\n"
    "ds_read_b64_tr_b16 a[158:159], v171 offset:38464\n"
    "s_add_u32 s40, s48, 5898240\n"
    "s_add_u32 m0, s29, 24576\n"
    "s_nop 0\n"
    "buffer_load_dwordx4 v168, s[12:15], s40 offen lds\n"
    "s_add_u32 m0, s29, 25600\n"
    "s_nop 0\n"
    "buffer_load_dwordx4 v169, s[12:15], s40 offen lds\n"
    "s_add_u32 m0, s29, 26624\n"
    "s_nop 0\n"
    "buffer_load_dwordx4 v170, s[12:15], s40 offen lds\n"
    "s_waitcnt lgkmcnt(0)\n"
    "s_waitcnt vmcnt(33)\n"
    "s_barrier\n"
    "ds_read_b64_tr_b16 a[48:49], v171 offset:49152\n"
    "ds_read_b64_tr_b16 a[50:51], v171 offset:50688\n"
    "ds_read_b64_tr_b16 a[104:105], v171 offset:49184\n"
    "ds_read_b64_tr_b16 a[106:107], v171 offset:50720\n"
    "ds_read_b64_tr_b16 a[160:161], v171 offset:49216\n"
    "ds_read_b64_tr_b16 a[162:163], v171 offset:50752\n"
    "s_add_u32 s40, s48, 6291456\n"
    "s_add_u32 m0, s29, 36864\n"
    "s_nop 0\n"
    "buffer_load_dwordx4 v168, s[12:15], s40 offen lds\n"
    "s_add_u32 m0, s29, 37888\n"
    "s_nop 0\n"
    "buffer_load_dwordx4 v169, s[12:15], s40 offen lds\n"
    "s_add_u32 m0, s29, 38912\n"
    "s_nop 0\n"
    "buffer_load_dwordx4 v170, s[12:15], s40 offen lds\n"
    "s_waitcnt lgkmcnt(0)\n"
    "s_waitcnt vmcnt(33)\n"
    "s_barrier\n"
    "ds_read_b64_tr_b16 a[52:53], v172 offset:4096\n"
    "ds_read_b64_tr_b16 a[54:55], v172 offset:5632\n"
    "ds_read_b64_tr_b16 a[108:109], v172 offset:4128\n"
    "ds_read_b64_tr_b16 a[110:111], v172 offset:5664\n"
    "ds_read_b64_tr_b16 a[164:165], v172 offset:4160\n"
    "ds_read_b64_tr_b16 a[166:167], v172 offset:5696\n"
    "s_add_u32 s40, s48, 6684672\n"
    "s_add_u32 m0, s29, 49152\n"
    "s_nop 0\n"
    "buffer_load_dwordx4 v168, s[12:15], s40 offen lds\n"
    "s_add_u32 m0, s29, 50176\n"
    "s_nop 0\n"
    "buffer_load_dwordx4 v169, s[12:15], s40 offen lds\n"
    "s_add_u32 m0, s29, 51200\n"
    "s_nop 0\n"
    "buffer_load_dwordx4 v170, s[12:15], s40 offen lds\n"
    "s_waitcnt lgkmcnt(0)\n"
    "s_waitcnt vmcnt(33)\n"
    "s_barrier\n"
    "ds_read_b64_tr_b16 a[56:57], v172 offset:16384\n"
    "ds_read_b64_tr_b16 a[58:59], v172 offset:17920\n"
    "ds_read_b64_tr_b16 a[112:113], v172 offset:16416\n"
    "ds_read_b64_tr_b16 a[114:115], v172 offset:17952\n"
    "ds_read_b64_tr_b16 a[168:169], v172 offset:16448\n"
    "ds_read_b64_tr_b16 a[170:171], v172 offset:17984\n"
    "s_add_u32 s40, s48, 7077888\n"
    "s_add_u32 m0, s29, 61440\n"
    "s_nop 0\n"
    "buffer_load_dwordx4 v168, s[12:15], s40 offen lds\n"
    "s_add_u32 m0, s29, 62464\n"
    "s_nop 0\n"
    "buffer_load_dwordx4 v169, s[12:15], s40 offen lds\n"
    "s_add_u32 m0, s29, 63488\n"
    "s_nop 0\n"
    "buffer_load_dwordx4 v170, s[12:15], s40 offen lds\n"
    "s_waitcnt lgkmcnt(0)\n"
    "s_waitcnt vmcnt(33)\n"
    "s_barrier\n"
    "ds_read_b64_tr_b16 a[60:61], v172 offset:28672\n"
    "ds_read_b64_tr_b16 a[62:63], v172 offset:30208\n"
    "ds_read_b64_tr_b16 a[116:117], v172 offset:28704\n"
    "ds_read_b64_tr_b16 a[118:119], v172 offset:30240\n"
    "ds_read_b64_tr_b16 a[172:173], v172 offset:28736\n"
    "ds_read_b64_tr_b16 a[174:175], v172 offset:30272\n"
    "s_add_u32 s40, s48, 7471104\n"
    "s_add_u32 m0, s29, 73728\n"
    "s_nop 0\n"
    "buffer_load_dwordx4 v168, s[12:15], s40 offen lds\n"
    "s_add_u32 m0, s29, 74752\n"
    "s_nop 0\n"
    "buffer_load_dwordx4 v169, s[12:15], s40 offen lds\n"
    "s_add_u32 m0, s29, 75776\n"
    "s_nop 0\n"
    "buffer_load_dwordx4 v170, s[12:15], s40 offen lds\n"
    "s_waitcnt lgkmcnt(0)\n"
    "s_waitcnt vmcnt(33)\n"
    "s_barrier\n"
    "ds_read_b64_tr_b16 a[64:65], v172 offset:40960\n"
    "ds_read_b64_tr_b16 a[66:67], v172 offset:42496\n"
    "ds_read_b64_tr_b16 a[120:121], v172 offset:40992\n"
    "ds_read_b64_tr_b16 a[122:123], v172 offset:42528\n"
    "ds_read_b64_tr_b16 a[176:177], v172 offset:41024\n"
    "ds_read_b64_tr_b16 a[178:179], v172 offset:42560\n"
    "s_waitcnt lgkmcnt(0)\n"
    "s_waitcnt vmcnt(30)\n"
    "s_barrier\n"
    "ds_read_b64_tr_b16 a[68:69], v172 offset:53248\n"
    "ds_read_b64_tr_b16 a[70:71], v172 offset:54784\n"
    "ds_read_b64_tr_b16 a[124:125], v172 offset:53280\n"
    "ds_read_b64_tr_b16 a[126:127], v172 offset:54816\n"
    "ds_read_b64_tr_b16 a[180:181], v172 offset:53312\n"
    "ds_read_b64_tr_b16 a[182:183], v172 offset:54848\n"
    "s_waitcnt lgkmcnt(0)\n"
    "s_waitcnt vmcnt(27)\n"
    "s_barrier\n"
    "ds_read_b64_tr_b16 a[16:17], v173 offset:8192\n"
    "ds_read_b64_tr_b16 a[18:19], v173 offset:9728\n"
    "ds_read_b64_tr_b16 a[72:73], v173 offset:8224\n"
    "ds_read_b64_tr_b16 a[74:75], v173 offset:9760\n"
    "ds_read_b64_tr_b16 a[128:129], v173 offset:8256\n"
    "ds_read_b64_tr_b16 a[130:131], v173 offset:9792\n"
    "s_waitcnt lgkmcnt(0)\n"
    "s_waitcnt vmcnt(24)\n"
    "s_barrier\n"
    "ds_read_b64_tr_b16 a[20:21], v173 offset:20480\n"
    "ds_read_b64_tr_b16 a[22:23], v173 offset:22016\n"
    "ds_read_b64_tr_b16 a[76:77], v173 offset:20512\n"
    "ds_read_b64_tr_b16 a[78:79], v173 offset:22048\n"
    "ds_read_b64_tr_b16 a[132:133], v173 offset:20544\n"
    "ds_read_b64_tr_b16 a[134:135], v173 offset:22080\n"
    "s_add_u32 s40, s51, 0\n"
    "s_add_u32 m0, s49, 86016\n"
    "s_nop 0\n"
    "buffer_load_dwordx4 v8, s[16:19], s40 offen lds\n"
    "s_add_u32 m0, s49, 87040\n"
    "s_nop 0\n"
    "buffer_load_dwordx4 v9, s[16:19], s40 offen lds\n"
    "s_add_u32 m0, s49, 88064\n"
    "s_nop 0\n"
    "buffer_load_dwordx4 v10, s[16:19], s40 offen lds\n"
    "s_add_u32 m0, s49, 89088\n"
    "s_nop 0\n"
    "buffer_load_dwordx4 v11, s[16:19], s40 offen lds\n"
    "s_add_u32 m0, s49, 90112\n"
    "s_nop 0\n"
    "buffer_load_dwordx4 v12, s[16:19], s40 offen lds\n"
    "s_add_u32 m0, s49, 91136\n"
    "s_nop 0\n"
    "buffer_load_dwordx4 v13, s[16:19], s40 offen lds\n"
    "s_add_u32 m0, s49, 92160\n"
    "s_nop 0\n"
    "buffer_load_dwordx4 v14, s[16:19], s40 offen lds\n"
    "s_add_u32 m0, s49, 93184\n"
    "s_nop 0\n"
    "buffer_load_dwordx4 v15, s[16:19], s40 offen lds\n"
    "s_add_u32 m0, s49, 94208\n"
    "s_nop 0\n"
    "buffer_load_dwordx4 v16, s[16:19], s40 offen lds\n"
    "s_add_u32 m0, s49, 95232\n"
    "s_nop 0\n"
    "buffer_load_dwordx4 v17, s[16:19], s40 offen lds\n"
    "s_waitcnt lgkmcnt(0)\n"
    "s_waitcnt vmcnt(31)\n"
    "s_barrier\n"
    "ds_read_b64_tr_b16 a[24:25], v173 offset:32768\n"
    "ds_read_b64_tr_b16 a[26:27], v173 offset:34304\n"
    "ds_read_b64_tr_b16 a[80:81], v173 offset:32800\n"
    "ds_read_b64_tr_b16 a[82:83], v173 offset:34336\n"
    "ds_read_b64_tr_b16 a[136:137], v173 offset:32832\n"
    "ds_read_b64_tr_b16 a[138:139], v173 offset:34368\n"
    "s_waitcnt lgkmcnt(0)\n"
    "s_waitcnt vmcnt(28)\n"
    "s_barrier\n"
    "ds_read_b64_tr_b16 a[28:29], v171 offset:0\n"
    "ds_read_b64_tr_b16 a[30:31], v171 offset:1536\n"
    "ds_read_b64_tr_b16 a[84:85], v171 offset:32\n"
    "ds_read_b64_tr_b16 a[86:87], v171 offset:1568\n"
    "ds_read_b64_tr_b16 a[140:141], v171 offset:64\n"
    "ds_read_b64_tr_b16 a[142:143], v171 offset:1600\n"
    "s_waitcnt lgkmcnt(0)\n"
    "s_waitcnt vmcnt(25)\n"
    "s_barrier\n"
    "ds_read_b64_tr_b16 v[24:25], v171 offset:12288\n"
    "ds_read_b64_tr_b16 v[26:27], v171 offset:13824\n"
    "ds_read_b64_tr_b16 v[32:33], v171 offset:12320\n"
    "ds_read_b64_tr_b16 v[34:35], v171 offset:13856\n"
    "ds_read_b64_tr_b16 v[40:41], v171 offset:12352\n"
    "ds_read_b64_tr_b16 v[42:43], v171 offset:13888\n"
    "s_waitcnt lgkmcnt(0)\n"
    "s_waitcnt vmcnt(22)\n"
    "s_barrier\n"
    "ds_read_b64_tr_b16 v[28:29], v171 offset:24576\n"
    "ds_read_b64_tr_b16 v[30:31], v171 offset:26112\n"
    "ds_read_b64_tr_b16 v[36:37], v171 offset:24608\n"
    "ds_read_b64_tr_b16 v[38:39], v171 offset:26144\n"
    "ds_read_b64_tr_b16 v[44:45], v171 offset:24640\n"
    "ds_read_b64_tr_b16 v[46:47], v171 offset:26176\n"
    "s_waitcnt lgkmcnt(0)\n"
    "s_waitcnt vmcnt(19)\n"
    "s_barrier\n"
    "ds_read_b64_tr_b16 v[48:49], v171 offset:36864\n"
    "ds_read_b64_tr_b16 v[50:51], v171 offset:38400\n"
    "ds_read_b64_tr_b16 v[56:57], v171 offset:36896\n"
    "ds_read_b64_tr_b16 v[58:59], v171 offset:38432\n"
    "ds_read_b64_tr_b16 v[64:65], v171 offset:36928\n"
    "ds_read_b64_tr_b16 v[66:67], v171 offset:38464\n"
    "s_waitcnt lgkmcnt(0)\n"
    "s_waitcnt vmcnt(16)\n"
    "s_barrier\n"
    "ds_read_b64_tr_b16 v[52:53], v171 offset:49152\n"
    "ds_read_b64_tr_b16 v[54:55], v171 offset:50688\n"
    "ds_read_b64_tr_b16 v[60:61], v171 offset:49184\n"
    "ds_read_b64_tr_b16 v[62:63], v171 offset:50720\n"
    "ds_read_b64_tr_b16 v[68:69], v171 offset:49216\n"
    "ds_read_b64_tr_b16 v[70:71], v171 offset:50752\n"
    "s_add_u32 s40, s51, 122880\n"
    "s_add_u32 m0, s49, 0\n"
    "s_nop 0\n"
    "buffer_load_dwordx4 v8, s[16:19], s40 offen lds\n"
    "s_add_u32 m0, s49, 1024\n"
    "s_nop 0\n"
    "buffer_load_dwordx4 v9, s[16:19], s40 offen lds\n"
    "s_add_u32 m0, s49, 2048\n"
    "s_nop 0\n"
    "buffer_load_dwordx4 v10, s[16:19], s40 offen lds\n"
    "s_add_u32 m0, s49, 3072\n"
    "s_nop 0\n"
    "buffer_load_dwordx4 v11, s[16:19], s40 offen lds\n"
    "s_add_u32 m0, s49, 4096\n"
    "s_nop 0\n"
    "buffer_load_dwordx4 v12, s[16:19], s40 offen lds\n"
    "s_add_u32 m0, s49, 5120\n"
    "s_nop 0\n"
    "buffer_load_dwordx4 v13, s[16:19], s40 offen lds\n"
    "s_add_u32 m0, s49, 6144\n"
    "s_nop 0\n"
    "buffer_load_dwordx4 v14, s[16:19], s40 offen lds\n"
    "s_add_u32 m0, s49, 7168\n"
    "s_nop 0\n"
    "buffer_load_dwordx4 v15, s[16:19], s40 offen lds\n"
    "s_add_u32 m0, s49, 8192\n"
    "s_nop 0\n"
    "buffer_load_dwordx4 v16, s[16:19], s40 offen lds\n"
    "s_add_u32 m0, s49, 9216\n"
    "s_nop 0\n"
    "buffer_load_dwordx4 v17, s[16:19], s40 offen lds\n"
    "s_waitcnt lgkmcnt(0)\n"
    "s_add_u32 s4, s3, 1\n"
    "v_mov_b32 v2, s3\n"
    "s_or_b32 s41, s21, s6\n"
    "s_cbranch_scc1 .Lcl_73\n"
    "s_lshl_b32 s41, s5, 2\n"
    "s_add_u32 s41, s41, 4178944\n"
    "v_mov_b32 v3, s41\n"
    "v_mov_b32 v2, s4\n"
    "s_mov_b64 exec, 1\n"
    "buffer_atomic_swap v2, v3, s[8:11], 0 offen sc0 sc1\n"
    "s_mov_b64 exec, -1\n"
    ".Lcl_73:\n"
    "v_min_u32 v6, 54, v1\n"
    "v_lshlrev_b32 v6, 7, v6\n"
    "v_add_u32 v6, 4182016, v6\n"
    "buffer_load_dwordx2 v[4:5], v6, s[8:11], 0 offen sc1\n"
    "s_waitcnt lgkmcnt(0)\n"
    "s_waitcnt vmcnt(24)\n"
    "s_barrier\n"
    "ds_read_b64_tr_b16 v[72:73], v172 offset:4096\n"
    "ds_read_b64_tr_b16 v[74:75], v172 offset:5632\n"
    "ds_read_b64_tr_b16 a[0:1], v172 offset:4128\n"
    "ds_read_b64_tr_b16 a[2:3], v172 offset:5664\n"
    "ds_read_b64_tr_b16 a[8:9], v172 offset:4160\n"
    "ds_read_b64_tr_b16 a[10:11], v172 offset:5696\n"
    "s_waitcnt lgkmcnt(0)\n"
    "s_waitcnt vmcnt(21)\n"
    "s_barrier\n"
    "ds_read_b64_tr_b16 v[76:77], v172 offset:16384\n"
    "ds_read_b64_tr_b16 v[78:79], v172 offset:17920\n"
    "ds_read_b64_tr_b16 a[4:5], v172 offset:16416\n"
    "ds_read_b64_tr_b16 a[6:7], v172 offset:17952\n"
    "ds_read_b64_tr_b16 a[12:13], v172 offset:16448\n"
    "ds_read_b64_tr_b16 a[14:15], v172 offset:17984\n"
    "s_waitcnt lgkmcnt(0)\n"
    "s_waitcnt vmcnt(11)\n"
    "s_barrier\n"
    "ds_read_b64_tr_b16 v[80:81], v19 offset:28672\n"
    "ds_read_b64_tr_b16 v[82:83], v19 offset:29952\n"
    "ds_read_b64_tr_b16 v[88:89], v19 offset:28704\n"
    "ds_read_b64_tr_b16 v[90:91], v19 offset:29984\n"
    "ds_read_b64_tr_b16 v[96:97], v19 offset:28736\n"
    "ds_read_b64_tr_b16 v[98:99], v19 offset:30016\n"
    "ds_read_b64_tr_b16 v[104:105], v19 offset:28768\n"
    "ds_read_b64_tr_b16 v[106:107], v19 offset:30048\n"
    "ds_read_b64_tr_b16 v[112:113], v19 offset:28800\n"
    "ds_read_b64_tr_b16 v[114:115], v19 offset:30080\n"
    "ds_read_b64_tr_b16 v[120:121], v19 offset:28832\n"
    "ds_read_b64_tr_b16 v[122:123], v19 offset:30112\n"
    "ds_read_b64_tr_b16 v[128:129], v19 offset:28864\n"
    "ds_read_b64_tr_b16 v[130:131], v19 offset:30144\n"
    "ds_read_b64_tr_b16 a[184:185], v19 offset:28896\n"
    "ds_read_b64_tr_b16 a[186:187], v19 offset:30176\n"
    "ds_read_b64_tr_b16 v[208:209], v19 offset:28928\n"
    "ds_read_b64_tr_b16 v[210:211], v19 offset:30208\n"
    "ds_read_b64_tr_b16 v[232:233], v19 offset:28960\n"
    "ds_read_b64_tr_b16 v[234:235], v19 offset:30240\n"
    "s_add_u32 s40, s51, 20480\n"
    "s_add_u32 m0, s49, 40960\n"
    "s_nop 0\n"
    "buffer_load_dwordx4 v8, s[16:19], s40 offen lds\n"
    "s_add_u32 m0, s49, 41984\n"
    "s_nop 0\n"
    "buffer_load_dwordx4 v9, s[16:19], s40 offen lds\n"
    "s_add_u32 m0, s49, 43008\n"
    "s_nop 0\n"
    "buffer_load_dwordx4 v10, s[16:19], s40 offen lds\n"
    "s_add_u32 m0, s49, 44032\n"
    "s_nop 0\n"
    "buffer_load_dwordx4 v11, s[16:19], s40 offen lds\n"
    "s_add_u32 m0, s49, 45056\n"
    "s_nop 0\n"
    "buffer_load_dwordx4 v12, s[16:19], s40 offen lds\n"
    "s_add_u32 m0, s49, 46080\n"
    "s_nop 0\n"
    "buffer_load_dwordx4 v13, s[16:19], s40 offen lds\n"
    "s_add_u32 m0, s49, 47104\n"
    "s_nop 0\n"
    "buffer_load_dwordx4 v14, s[16:19], s40 offen lds\n"
    "s_add_u32 m0, s49, 48128\n"
    "s_nop 0\n"
    "buffer_load_dwordx4 v15, s[16:19], s40 offen lds\n"
    "s_add_u32 m0, s49, 49152\n"
    "s_nop 0\n"
    "buffer_load_dwordx4 v16, s[16:19], s40 offen lds\n"
    "s_add_u32 m0, s49, 50176\n"
    "s_nop 0\n"
    "buffer_load_dwordx4 v17, s[16:19], s40 offen lds\n"
    "s_waitcnt lgkmcnt(0)\n"
    "s_waitcnt vmcnt(11)\n"
    "s_barrier\n"
    "ds_read_b64_tr_b16 v[84:85], v18 offset:0\n"
    "ds_read_b64_tr_b16 v[86:87], v18 offset:1280\n"
    "ds_read_b64_tr_b16 v[92:93], v18 offset:32\n"
    "ds_read_b64_tr_b16 v[94:95], v18 offset:1312\n"
    "ds_read_b64_tr_b16 v[100:101], v18 offset:64\n"
    "ds_read_b64_tr_b16 v[102:103], v18 offset:1344\n"
    "ds_read_b64_tr_b16 v[108:109], v18 offset:96\n"
    "ds_read_b64_tr_b16 v[110:111], v18 offset:1376\n"
    "ds_read_b64_tr_b16 v[116:117], v18 offset:128\n"
    "ds_read_b64_tr_b16 v[118:119], v18 offset:1408\n"
    "ds_read_b64_tr_b16 v[124:125], v18 offset:160\n"
    "ds_read_b64_tr_b16 v[126:127], v18 offset:1440\n"
    "ds_read_b64_tr_b16 v[132:133], v18 offset:192\n"
    "ds_read_b64_tr_b16 v[134:135], v18 offset:1472\n"
    "ds_read_b64_tr_b16 a[188:189], v18 offset:224\n"
    "ds_read_b64_tr_b16 a[190:191], v18 offset:1504\n"
    "ds_read_b64_tr_b16 v[212:213], v18 offset:256\n"
    "ds_read_b64_tr_b16 v[214:215], v18 offset:1536\n"
    "ds_read_b64_tr_b16 v[236:237], v18 offset:288\n"
    "ds_read_b64_tr_b16 v[238:239], v18 offset:1568\n"
    "s_add_u32 s40, s51, 143360\n"
    "s_add_u32 m0, s49, 81920\n"
    "s_nop 0\n"
    "buffer_load_dwordx4 v8, s[16:19], s40 offen lds\n"
    "s_add_u32 m0, s49, 82944\n"
    "s_nop 0\n"
    "buffer_load_dwordx4 v9, s[16:19], s40 offen lds\n"
    "s_add_u32 m0, s49, 83968\n"
    "s_nop 0\n"
    "buffer_load_dwordx4 v10, s[16:19], s40 offen lds\n"
    "s_add_u32 m0, s49, 84992\n"
    "s_nop 0\n"
    "buffer_load_dwordx4 v11, s[16:19], s40 offen lds\n"
    "s_add_u32 m0, s49, 86016\n"
    "s_nop 0\n"
    "buffer_load_dwordx4 v12, s[16:19], s40 offen lds\n"
    "s_add_u32 m0, s49, 87040\n"
    "s_nop 0\n"
    "buffer_load_dwordx4 v13, s[16:19], s40 offen lds\n"
    "s_add_u32 m0, s49, 88064\n"
    "s_nop 0\n"
    "buffer_load_dwordx4 v14, s[16:19], s40 offen lds\n"
    "s_add_u32 m0, s49, 89088\n"
    "s_nop 0\n"
    "buffer_load_dwordx4 v15, s[16:19], s40 offen lds\n"
    "s_add_u32 m0, s49, 90112\n"
    "s_nop 0\n"
    "buffer_load_dwordx4 v16, s[16:19], s40 offen lds\n"
    "s_add_u32 m0, s49, 91136\n"
    "s_nop 0\n"
    "buffer_load_dwordx4 v17, s[16:19], s40 offen lds\n"
    "s_waitcnt lgkmcnt(0)\n"
    "s_waitcnt vmcnt(10)\n"
    "s_barrier\n"
    "ds_read_b64_tr_b16 a[192:193], v18 offset:40960\n"
    "ds_read_b64_tr_b16 a[194:195], v18 offset:42240\n"
    "ds_read_b64_tr_b16 a[200:201], v18 offset:40992\n"
    "ds_read_b64_tr_b16 a[202:203], v18 offset:42272\n"
    "ds_read_b64_tr_b16 a[208:209], v18 offset:41024\n"
    "ds_read_b64_tr_b16 a[210:211], v18 offset:42304\n"
    "ds_read_b64_tr_b16 a[216:217], v18 offset:41056\n"
    "ds_read_b64_tr_b16 a[218:219], v18 offset:42336\n"
    "ds_read_b64_tr_b16 a[224:225], v18 offset:41088\n"
    "ds_read_b64_tr_b16 a[226:227], v18 offset:42368\n"
    "ds_read_b64_tr_b16 a[232:233], v18 offset:41120\n"
    "ds_read_b64_tr_b16 a[234:235], v18 offset:42400\n"
    "ds_read_b64_tr_b16 v[136:137], v18 offset:41152\n"
    "ds_read_b64_tr_b16 v[138:139], v18 offset:42432\n"
    "ds_read_b64_tr_b16 v[152:153], v18 offset:41184\n"
    "ds_read_b64_tr_b16 v[154:155], v18 offset:42464\n"
    "ds_read_b64_tr_b16 v[216:217], v18 offset:41216\n"
    "ds_read_b64_tr_b16 v[218:219], v18 offset:42496\n"
    "ds_read_b64_tr_b16 v[240:241], v18 offset:41248\n"
    "ds_read_b64_tr_b16 v[242:243], v18 offset:42528\n"
    "s_add_u32 s40, s51, 40960\n"
    "s_add_u32 m0, s49, 0\n"
    "s_nop 0\n"
    "buffer_load_dwordx4 v8, s[16:19], s40 offen lds\n"
    "s_add_u32 m0, s49, 1024\n"
    "s_nop 0\n"
    "buffer_load_dwordx4 v9, s[16:19], s40 offen lds\n"
    "s_add_u32 m0, s49, 2048\n"
    "s_nop 0\n"
    "buffer_load_dwordx4 v10, s[16:19], s40 offen lds\n"
    "s_add_u32 m0, s49, 3072\n"
    "s_nop 0\n"
    "buffer_load_dwordx4 v11, s[16:19], s40 offen lds\n"
    "s_add_u32 m0, s49, 4096\n"
    "s_nop 0\n"
    "buffer_load_dwordx4 v12, s[16:19], s40 offen lds\n"
    "s_add_u32 m0, s49, 5120\n"
    "s_nop 0\n"
    "buffer_load_dwordx4 v13, s[16:19], s40 offen lds\n"
    "s_add_u32 m0, s49, 6144\n"
    "s_nop 0\n"
    "buffer_load_dwordx4 v14, s[16:19], s40 offen lds\n"
    "s_add_u32 m0, s49, 7168\n"
    "s_nop 0\n"
    "buffer_load_dwordx4 v15, s[16:19], s40 offen lds\n"
    "s_add_u32 m0, s49, 8192\n"
    "s_nop 0\n"
    "buffer_load_dwordx4 v16, s[16:19], s40 offen lds\n"
    "s_add_u32 m0, s49, 9216\n"
    "s_nop 0\n"
    "buffer_load_dwordx4 v17, s[16:19], s40 offen lds\n"
    "s_waitcnt lgkmcnt(0)\n"
    "s_waitcnt vmcnt(10)\n"
    "s_barrier\n"
    "ds_read_b64_tr_b16 a[196:197], v19 offset:24576\n"
    "ds_read_b64_tr_b16 a[198:199], v19 offset:25856\n"
    "ds_read_b64_tr_b16 a[204:205], v19 offset:24608\n"
    "ds_read_b64_tr_b16 a[206:207], v19 offset:25888\n"
    "ds_read_b64_tr_b16 a[212:213], v19 offset:24640\n"
    "ds_read_b64_tr_b16 a[214:215], v19 offset:25920\n"
    "ds_read_b64_tr_b16 a[220:221], v19 offset:24672\n"
    "ds_read_b64_tr_b16 a[222:223], v19 offset:25952\n"
    "ds_read_b64_tr_b16 a[228:229], v19 offset:24704\n"
    "ds_read_b64_tr_b16 a[230:231], v19 offset:25984\n"
    "ds_read_b64_tr_b16 a[236:237], v19 offset:24736\n"
    "ds_read_b64_tr_b16 a[238:239], v19 offset:26016\n"
    "ds_read_b64_tr_b16 v[140:141], v19 offset:24768\n"
    "ds_read_b64_tr_b16 v[142:143], v19 offset:26048\n"
    "ds_read_b64_tr_b16 v[156:157], v19 offset:24800\n"
    "ds_read_b64_tr_b16 v[158:159], v19 offset:26080\n"
    "ds_read_b64_tr_b16 v[220:221], v19 offset:24832\n"
    "ds_read_b64_tr_b16 v[222:223], v19 offset:26112\n"
    "ds_read_b64_tr_b16 v[244:245], v19 offset:24864\n"
    "ds_read_b64_tr_b16 v[246:247], v19 offset:26144\n"
    "s_add_u32 s40, s51, 163840\n"
    "s_add_u32 m0, s49, 40960\n"
    "s_nop 0\n"
    "buffer_load_dwordx4 v8, s[16:19], s40 offen lds\n"
    "s_add_u32 m0, s49, 41984\n"
    "s_nop 0\n"
    "buffer_load_dwordx4 v9, s[16:19], s40 offen lds\n"
    "s_add_u32 m0, s49, 43008\n"
    "s_nop 0\n"
    "buffer_load_dwordx4 v10, s[16:19], s40 offen lds\n"
    "s_add_u32 m0, s49, 44032\n"
    "s_nop 0\n"
    "buffer_load_dwordx4 v11, s[16:19], s40 offen lds\n"
    "s_add_u32 m0, s49, 45056\n"
    "s_nop 0\n"
    "buffer_load_dwordx4 v12, s[16:19], s40 offen lds\n"
    "s_add_u32 m0, s49, 46080\n"
    "s_nop 0\n"
    "buffer_load_dwordx4 v13, s[16:19], s40 offen lds\n"
    "s_add_u32 m0, s49, 47104\n"
    "s_nop 0\n"
    "buffer_load_dwordx4 v14, s[16:19], s40 offen lds\n"
    "s_add_u32 m0, s49, 48128\n"
    "s_nop 0\n"
    "buffer_load_dwordx4 v15, s[16:19], s40 offen lds\n"
    "s_add_u32 m0, s49, 49152\n"
    "s_nop 0\n"
    "buffer_load_dwordx4 v16, s[16:19], s40 offen lds\n"
    "s_add_u32 m0, s49, 50176\n"
    "s_nop 0\n"
    "buffer_load_dwordx4 v17, s[16:19], s40 offen lds\n"
    "s_waitcnt lgkmcnt(0)\n"
    "s_waitcnt vmcnt(10)\n"
    "s_barrier\n"
    "ds_read_b64_tr_b16 a[240:241], v18 offset:0\n"
    "ds_read_b64_tr_b16 a[242:243], v18 offset:1280\n"
    "ds_read_b64_tr_b16 a[248:249], v18 offset:32\n"
    "ds_read_b64_tr_b16 a[250:251], v18 offset:1312\n"
    "ds_read_b64_tr_b16 v[176:177], v18 offset:64\n"
    "ds_read_b64_tr_b16 v[178:179], v18 offset:1344\n"
    "ds_read_b64_tr_b16 v[184:185], v18 offset:96\n"
    "ds_read_b64_tr_b16 v[186:187], v18 offset:1376\n"
    "ds_read_b64_tr_b16 v[192:193], v18 offset:128\n"
    "ds_read_b64_tr_b16 v[194:195], v18 offset:1408\n"
    "ds_read_b64_tr_b16 v[200:201], v18 offset:160\n"
    "ds_read_b64_tr_b16 v[202:203], v18 offset:1440\n"
    "ds_read_b64_tr_b16 v[144:145], v18 offset:192\n"
    "ds_read_b64_tr_b16 v[146:147], v18 offset:1472\n"
    "ds_read_b64_tr_b16 v[160:161], v18 offset:224\n"
    "ds_read_b64_tr_b16 v[162:163], v18 offset:1504\n"
    "ds_read_b64_tr_b16 v[224:225], v18 offset:256\n"
    "ds_read_b64_tr_b16 v[226:227], v18 offset:1536\n"
    "ds_read_b64_tr_b16 v[248:249], v18 offset:288\n"
    "ds_read_b64_tr_b16 v[250:251], v18 offset:1568\n"
    "s_waitcnt lgkmcnt(0)\n"
    "s_waitcnt vmcnt(0)\n"
    "s_barrier\n"
    "ds_read_b64_tr_b16 a[244:245], v18 offset:40960\n"
    "ds_read_b64_tr_b16 a[246:247], v18 offset:42240\n"
    "ds_read_b64_tr_b16 a[252:253], v18 offset:40992\n"
    "ds_read_b64_tr_b16 a[254:255], v18 offset:42272\n"
    "ds_read_b64_tr_b16 v[180:181], v18 offset:41024\n"
    "ds_read_b64_tr_b16 v[182:183], v18 offset:42304\n"
    "ds_read_b64_tr_b16 v[188:189], v18 offset:41056\n"
    "ds_read_b64_tr_b16 v[190:191], v18 offset:42336\n"
    "ds_read_b64_tr_b16 v[196:197], v18 offset:41088\n"
    "ds_read_b64_tr_b16 v[198:199], v18 offset:42368\n"
    "ds_read_b64_tr_b16 v[204:205], v18 offset:41120\n"
    "ds_read_b64_tr_b16 v[206:207], v18 offset:42400\n"
    "ds_read_b64_tr_b16 v[148:149], v18 offset:41152\n"
    "ds_read_b64_tr_b16 v[150:151], v18 offset:42432\n"
    "ds_read_b64_tr_b16 v[164:165], v18 offset:41184\n"
    "ds_read_b64_tr_b16 v[166:167], v18 offset:42464\n"
    "ds_read_b64_tr_b16 v[228:229], v18 offset:41216\n"
    "ds_read_b64_tr_b16 v[230:231], v18 offset:42496\n"
    "ds_read_b64_tr_b16 v[252:253], v18 offset:41248\n"
    "ds_read_b64_tr_b16 v[254:255], v18 offset:42528\n"
    "s_waitcnt lgkmcnt(0)\n"
    "v_cmp_ne_u32 vcc, s4, v4\n"
    "s_nop 1\n"
    "s_cmp_eq_u32 vcc_lo, 0\n"
    "s_cbranch_scc1 .Lrx_76\n"
    ".Lrp_74:\n"
    "v_min_u32 v6, 54, v1\n"
    "v_lshlrev_b32 v6, 7, v6\n"
    "v_add_u32 v6, 4182016, v6\n"
    "buffer_load_dwordx2 v[4:5], v6, s[8:11], 0 offen sc1\n"
    "s_waitcnt vmcnt(0)\n"
    "v_cmp_ne_u32 vcc, s4, v4\n"
    "s_nop 1\n"
    "s_cmp_eq_u32 vcc_lo, 0\n"
    "s_cbranch_scc1 .Lro_75\n"
    "s_sleep 2\n"
    "s_sub_u32 s25, s25, 1\n"
    "s_cbranch_scc0 .Lrp_74\n"
    "s_branch .Lfail\n"
    ".Lro_75:\n"
    ".Lrx_76:\n"
    "v_cmp_ne_u32 vcc, s4, v4\n"
    "s_nop 1\n"
    "s_cmp_eq_u32 vcc_hi, 0\n"
    "s_cbranch_scc1 .Lrx_79\n"
    ".Lrp_77:\n"
    "v_min_u32 v6, 54, v1\n"
    "v_lshlrev_b32 v6, 7, v6\n"
    "v_add_u32 v6, 4182016, v6\n"
    "buffer_load_dwordx2 v[4:5], v6, s[8:11], 0 offen sc1\n"
    "s_waitcnt vmcnt(0)\n"
    "v_cmp_ne_u32 vcc, s4, v4\n"
    "s_nop 1\n"
    "s_cmp_eq_u32 vcc_hi, 0\n"
    "s_cbranch_scc1 .Lro_78\n"
    "s_sleep 2\n"
    "s_sub_u32 s25, s25, 1\n"
    "s_cbranch_scc0 .Lrp_77\n"
    "s_branch .Lfail\n"
    ".Lro_78:\n"
    ".Lrx_79:\n"
    "v_cmp_ne_u32 vcc, 0, v5\n"
    "s_nop 1\n"
    "s_and_b32 s22, vcc_hi, 1\n"
    "s_lshl_b32 s22, s22, 13\n"
    "s_lshr_b32 s23, vcc_hi, 1\n"
    ".Lpw_47:\n"
    "s_cmp_lg_u32 s21, 0\n"
    "s_cbranch_scc1 .Lgt_80\n"
    "v_mov_b32 v3, 161792\n"
    "v_readfirstlane_b32 s40, v2\n"
    "s_nop 1\n"
    "v_mov_b32 v2, s40\n"
    "ds_write_b32 v3, v2\n"
    "s_waitcnt lgkmcnt(0)\n"
    ".Lgt_80:\n"
    "s_barrier\n"
    "v_mov_b32 v3, 161792\n"
    "ds_read_b32 v2, v3\n"
    "s_waitcnt lgkmcnt(0)\n"
    "v_readfirstlane_b32 s40, v2\n"
    "s_barrier\n"
    "s_cmp_eq_u32 s40, s4\n"
    "s_cbranch_scc1 .Llost\n"
    "s_lshr_b32 s40, s21, 1\n"
    "s_lshl_b32 s40, s40, 14\n"
    "s_and_b32 s41, s21, 1\n"
    "s_lshl_b32 s41, s41, 11\n"
    "s_add_u32 s40, s40, s41\n"
    "s_add_u32 s40, s40, s22\n"
    "s_lshr_b32 s41, s5, 5\n"
    "s_and_b32 s41, s41, 1\n"
    "s_mul_i32 s41, s41, 524288\n"
    "s_add_u32 s40, s40, s41\n"
    "v_lshl_add_u32 v3, v1, 4, s40\n"
    "s_lshl_b32 s40, s26, 10\n"
    "s_add_u32 s40, s40, 147456\n"
    "v_lshl_add_u32 v4, v1, 4, s40\n"
    "s_lshl_b32 s40, s27, 10\n"
    "s_add_u32 s40, s40, 147456\n"
    "v_lshl_add_u32 v5, v1, 4, s40\n"
    "s_lshl_b32 s40, s28, 10\n"
    "s_add_u32 s40, s40, 147456\n"
    "v_lshl_add_u32 v6, v1, 4, s40\n"
    "s_mul_i32 s40, s21, 36864\n"
    "v_lshl_add_u32 v7, v1, 4, s40\n"
    "ds_write_b128 v7, v[24:27] offset:0\n"
    "ds_write_b128 v7, v[28:31] offset:1024\n"
    "ds_write_b128 v7, v[32:35] offset:2048\n"
    "ds_write_b128 v7, v[36:39] offset:3072\n"
    "ds_write_b128 v7, v[40:43] offset:4096\n"
    "ds_write_b128 v7, v[44:47] offset:5120\n"
    "ds_write_b128 v7, v[48:51] offset:6144\n"
    "ds_write_b128 v7, v[52:55] offset:7168\n"
    "ds_write_b128 v7, v[56:59] offset:8192\n"
    "ds_write_b128 v7, v[60:63] offset:9216\n"
    "ds_write_b128 v7, v[64:67] offset:10240\n"
    "ds_write_b128 v7, v[68:71] offset:11264\n"
    "ds_write_b128 v7, v[72:75] offset:12288\n"
    "ds_write_b128 v7, v[76:79] offset:13312\n"
    "ds_write_b128 v7, v[80:83] offset:14336\n"
    "ds_write_b128 v7, v[84:87] offset:15360\n"
    "ds_write_b128 v7, v[88:91] offset:16384\n"
    "ds_write_b128 v7, v[92:95] offset:17408\n"
    "ds_write_b128 v7, v[96:99] offset:18432\n"
    "ds_write_b128 v7, v[100:103] offset:19456\n"
    "ds_write_b128 v7, v[104:107] offset:20480\n"
    "ds_write_b128 v7, v[108:111] offset:21504\n"
    "ds_write_b128 v7, v[112:115] offset:22528\n"
    "ds_write_b128 v7, v[116:119] offset:23552\n"
    "ds_write_b128 v7, v[120:123] offset:24576\n"
    "ds_write_b128 v7, v[124:127] offset:25600\n"
    "ds_write_b128 v7, v[128:131] offset:26624\n"
    "ds_write_b128 v7, v[132:135] offset:27648\n"
    "ds_write_b128 v7, v[136:139] offset:28672\n"
    "ds_write_b128 v7, v[140:143] offset:29696\n"
    "ds_write_b128 v7, v[144:147] offset:30720\n"
    "ds_write_b128 v7, v[148:151] offset:31744\n"
    "ds_write_b128 v7, v[152:155] offset:32768\n"
    "ds_write_b128 v7, v[156:159] offset:33792\n"
    "ds_write_b128 v7, v[160:163] offset:34816\n"
    "ds_write_b128 v7, v[164:167] offset:35840\n"
    "v_and_b32 v0, 2, v1\n"
    "v_cmp_ne_u32 vcc, 0, v0\n"
    "v_mov_b32 v2, 0x4444\n"
    "v_mov_b32 v0, 0xeeee\n"
    "v_cndmask_b32 v2, v2, v0, vcc\n"
    "v_bfe_u32 v8, v1, 3, 1\n"
    "v_lshlrev_b32 v8, 9, v8\n"
    "v_bfe_u32 v0, v1, 2, 1\n"
    "v_lshl_add_u32 v8, v0, 6, v8\n"
    "v_bfe_u32 v0, v1, 0, 1\n"
    "v_lshl_add_u32 v8, v0, 5, v8\n"
    "v_bfe_u32 v0, v1, 4, 2\n"
    "v_lshl_add_u32 v8, v0, 3, v8\n"
    "v_bfe_u32 v0, v1, 1, 1\n"
    "v_lshl_add_u32 v8, v0, 2, v8\n"
    "s_lshl_b32 s40, s21, 7\n"
    "s_add_u32 s40, s40, 157696\n"
    "v_add_u32 v8, s40, v8\n"
    "v_lshlrev_b32 v9, 3, v1\n"
    "v_add_u32 v9, 157696, v9\n"
    "s_lshl_b32 s40, s20, 8\n"
    "s_mul_i32 s41, s21, 163840\n"
    "s_add_u32 s41, s41, s40\n"
    "v_bfe_u32 v10, v1, 4, 2\n"
    "v_lshlrev_b32 v10, 14, v10\n"
    "v_bfe_u32 v0, v1, 0, 4\n"
    "v_lshl_add_u32 v10, v0, 4, v10\n"
    "v_add_u32 v10, s41, v10\n"
    "v_bfe_u32 v12, v1, 4, 1\n"
    "v_lshlrev_b32 v12, 14, v12\n"
    "v_bfe_u32 v0, v1, 0, 4\n"
    "v_lshl_add_u32 v12, v0, 4, v12\n"
    "v_bfe_u32 v0, v1, 5, 1\n"
    "v_lshl_add_u32 v12, v0, 3, v12\n"
    "v_add_u32 v12, s41, v12\n"
    "s_lshl_b32 s40, s21, 5\n"
    "s_add_u32 s40, s40, s20\n"
    "s_lshl_b32 s40, s40, 7\n"
    "s_add_u32 s40, s40, 4268032\n"
    "v_mov_b32 v14, s40\n"
    "v_cmp_ne_u32 vcc, 0, v1\n"
    "v_mov_b32 v0, 0x7ff00000\n"
    "s_nop 0\n"
    "v_cndmask_b32 v14, v14, v0, vcc\n"
    "s_add_u32 s40, s22, 294912\n"
    "v_and_b32 v0, 31, v1\n"
    "v_lshl_add_u32 v15, v0, 7, s40\n"
    "s_mov_b32 s82, 40\n"
    "s_mov_b32 s83, 0x100000\n"
    "s_memrealtime s[42:43]\n"
    "s_waitcnt lgkmcnt(0)\n"
    "s_mul_i32 s59, s42, 6\n"
    "s_sub_u32 s59, s59, 0x10000\n"
    "s_mov_b32 s24, 0\n"
    "s_mov_b32 s30, 0\n"
    "s_waitcnt lgkmcnt(0)\n"
    ".LW_idle:\n"
    "ds_read_b128 v[120:123], v7 offset:0\n"
    "ds_read_b128 v[124:127], v7 offset:1024\n"
    "ds_read_b128 v[128:131], v7 offset:2048\n"
    "ds_read_b128 v[132:135], v7 offset:3072\n"
    "ds_read_b128 v[136:139], v7 offset:4096\n"
    "ds_read_b128 v[140:143], v7 offset:5120\n"
    "ds_read_b128 v[144:147], v7 offset:6144\n"
    "ds_read_b128 v[148:151], v7 offset:7168\n"
    "ds_read_b128 v[152:155], v7 offset:8192\n"
    "ds_read_b128 v[156:159], v7 offset:9216\n"
    "ds_read_b128 v[160:163], v7 offset:10240\n"
    "ds_read_b128 v[164:167], v7 offset:11264\n"
    "ds_read_b128 v[168:171], v7 offset:12288\n"
    "ds_read_b128 v[172:175], v7 offset:13312\n"
    "s_mul_i32 s40, s24, 43\n"
    "s_lshr_b32 s40, s40, 7\n"
    "s_mul_i32 s31, s24, 655360\n"
    "s_lshl_b32 s41, s40, 17\n"
    "s_add_u32 s31, s31, s41\n"
    "s_lshr_b32 s40, s23, s40\n"
    "s_and_b32 s40, s40, 1\n"
    "s_lshl_b32 s40, s40, 13\n"
    "s_add_u32 s31, s31, s40\n"
    "s_add_u32 s31, s31, 161480704\n"
    "s_or_b32 s40, s31, 0x1fffff\n"
    "s_sub_u32 s40, s40, 131071\n"
    "s_mov_b64 exec, 1\n"
    "buffer_load_dword v11, v1, s[8:11], s40 offen\n"
    "s_mov_b64 exec, -1\n"
    "s_add_u32 s50, s31, 65536\n"
    "s_add_u32 s51, s31, 131072\n"
    "s_waitcnt lgkmcnt(0)\n"
    "s_and_b32 s33, s24, 2\n"
    "s_lshl_b32 s33, s33, 13\n"
    "s_and_b32 s40, s24, 1\n"
    "s_lshl_b32 s40, s40, 12\n"
    "s_add_u32 s33, s33, s40\n"
    "v_mov_b64 v[16:17], 0\n"
    "v_mov_b64 v[18:19], 0\n"
    "v_mov_b64 v[20:21], 0\n"
    "v_mov_b64 v[22:23], 0\n"
    "v_mov_b64 v[24:25], 0\n"
    "v_mov_b64 v[26:27], 0\n"
    "v_mov_b64 v[80:81], 0\n"
    "v_mov_b64 v[82:83], 0\n"
    "v_mov_b64 v[84:85], 0\n"
    "v_mov_b64 v[86:87], 0\n"
    "v_mov_b64 v[88:89], 0\n"
    "v_mov_b64 v[90:91], 0\n"
    "v_mov_b64 v[92:93], 0\n"
    "v_mov_b64 v[94:95], 0\n"
    "v_mov_b64 v[96:97], 0\n"
    "v_mov_b64 v[98:99], 0\n"
    "v_mov_b64 v[100:101], 0\n"
    "v_mov_b64 v[102:103], 0\n"
    "v_mov_b64 v[104:105], 0\n"
    "v_mov_b64 v[106:107], 0\n"
    "v_mov_b64 v[108:109], 0\n"
    "v_mov_b64 v[110:111], 0\n"
    "v_mov_b64 v[112:113], 0\n"
    "v_mov_b64 v[114:115], 0\n"
    "v_mov_b64 v[116:117], 0\n"
    "v_mov_b64 v[118:119], 0\n"
    "s_cmp_lg_u32 s21, 0\n"
    "s_cbranch_scc1 .LW_go\n"
    "s_lshr_b32 s41, s82, 0\n"
    "s_add_u32 s41, s41, s59\n"
    "s_add_u32 s41, s41, 394\n"
    ".Lwt_81:\n"
    "s_memrealtime s[42:43]\n"
    "s_waitcnt lgkmcnt(0)\n"
    "s_mul_i32 s42, s42, 6\n"
    "s_sub_u32 s40, s41, s42\n"
    "s_cmp_gt_i32 s40, 120\n"
    "s_cbranch_scc1 .Lwt_81\n"
    "s_cmp_lt_i32 s40, 50\n"
    "s_cbranch_scc1 .Lwd_83\n"
    "s_memrealtime s[12:13]\n"
    "s_nop 2\n"
    "s_memrealtime s[14:15]\n"
    "s_nop 2\n"
    "s_memrealtime s[16:17]\n"
    "s_nop 2\n"
    "s_memrealtime s[18:19]\n"
    "s_nop 2\n"
    "s_memrealtime s[44:45]\n"
    "s_nop 2\n"
    "s_memrealtime s[46:47]\n"
    "s_waitcnt lgkmcnt(0)\n"
    "s_add_u32 s40, s12, s14\n"
    "s_add_u32 s40, s40, s16\n"
    "s_add_u32 s40, s40, s18\n"
    "s_add_u32 s40, s40, s44\n"
    "s_add_u32 s40, s40, s46\n"
    "s_sub_u32 s40, s41, s40\n"
    "s_sub_u32 s40, s40, 56\n"
    "s_max_i32 s40, s40, 0\n"
    "s_min_i32 s40, s40, 88\n"
    "s_lshl_b32 s40, s40, 2\n"
    "s_getpc_b64 s[42:43]\n"
    ".Lwh_82:\n"
    "s_sub_u32 s40, .Lwd_83-.Lwh_82, s40\n"
    "s_add_u32 s42, s42, s40\n"
    "s_addc_u32 s43, s43, 0\n"
    "s_setpc_b64 s[42:43]\n"
    "s_nop 0\n"
    "s_nop 0\n"
    "s_nop 0\n"
    "s_nop 0\n"
    "s_nop 0\n"
    "s_nop 0\n"
    "s_nop 0\n"
    "s_nop 0\n"
    "s_nop 0\n"
    "s_nop 0\n"
    "s_nop 0\n"
    "s_nop 0\n"
    "s_nop 0\n"
    "s_nop 0\n"
    "s_nop 0\n"
    "s_nop 0\n"
    "s_nop 0\n"
    "s_nop 0\n"
    "s_nop 0\n"
    "s_nop 0\n"
    "s_nop 0\n"
    "s_nop 0\n"
    "s_nop 0\n"
    "s_nop 0\n"
    "s_nop 0\n"
    "s_nop 0\n"
    "s_nop 0\n"
    "s_nop 0\n"
    "s_nop 0\n"
    "s_nop 0\n"
    "s_nop 0\n"
    "s_nop 0\n"
    "s_nop 0\n"
    "s_nop 0\n"
    "s_nop 0\n"
    "s_nop 0\n"
    "s_nop 0\n"
    "s_nop 0\n"
    "s_nop 0\n"
    "s_nop 0\n"
    "s_nop 0\n"
    "s_nop 0\n"
    "s_nop 0\n"
    "s_nop 0\n"
    "s_nop 0\n"
    "s_nop 0\n"
    "s_nop 0\n"
    "s_nop 0\n"
    "s_nop 0\n"
    "s_nop 0\n"
    "s_nop 0\n"
    "s_nop 0\n"
    "s_nop 0\n"
    "s_nop 0\n"
    "s_nop 0\n"
    "s_nop 0\n"
    "s_nop 0\n"
    "s_nop 0\n"
    "s_nop 0\n"
    "s_nop 0\n"
    "s_nop 0\n"
    "s_nop 0\n"
    "s_nop 0\n"
    "s_nop 0\n"
    "s_nop 0\n"
    "s_nop 0\n"
    "s_nop 0\n"
    "s_nop 0\n"
    "s_nop 0\n"
    "s_nop 0\n"
    "s_nop 0\n"
    "s_nop 0\n"
    "s_nop 0\n"
    "s_nop 0\n"
    "s_nop 0\n"
    "s_nop 0\n"
    "s_nop 0\n"
    "s_nop 0\n"
    "s_nop 0\n"
    "s_nop 0\n"
    "s_nop 0\n"
    "s_nop 0\n"
    "s_nop 0\n"
    "s_nop 0\n"
    "s_nop 0\n"
    "s_nop 0\n"
    "s_nop 0\n"
    "s_nop 0\n"
    ".Lwd_83:\n"
    ".LW_go:\n"
    "s_barrier\n"
    "buffer_load_dwordx4 v[40:43], v3, s[8:11], s30 offen offset:0 sc1\n"
    "s_nop 9\n"
    "buffer_load_dwordx4 v[44:47], v3, s[8:11], s30 offen offset:1024 sc1\n"
    "s_nop 9\n"
    "buffer_load_dwordx4 v[48:51], v3, s[8:11], s30 offen offset:2048 sc1\n"
    ".LW_have:\n"
    "s_waitcnt vmcnt(2)\n"
    "v_smfmac_f32_16x16x64_bf16 v[16:19], v[40:43], v[120:127], v2\n"
    "ds_write_b128 v4, v[40:43]\n"
    "v_max3_u32 v0, v40, v41, v42\n"
    "v_max_u32 v0, v0, v43\n"
    "v_smfmac_f32_16x16x64_bf16 v[20:23], v[40:43], v[128:135], v2\n"
    "s_waitcnt vmcnt(1)\n"
    "ds_write_b128 v4, v[44:47] offset:1024\n"
    "v_max3_u32 v0, v0, v44, v45\n"
    "v_max3_u32 v0, v0, v46, v47\n"
    "v_smfmac_f32_16x16x64_bf16 v[24:27], v[40:43], v[136:143], v2\n"
    "s_waitcnt vmcnt(0)\n"
    "ds_write_b128 v4, v[48:51] offset:2048\n"
    "v_max3_u32 v0, v0, v48, v49\n"
    "v_max3_u32 v0, v0, v50, v51\n"
    "v_cmp_eq_u32 vcc, -1, v0\n"
    "v_smfmac_f32_16x16x64_bf16 v[16:19], v[44:47], v[144:151], v2\n"
    "s_cbranch_vccnz .LW_again\n"
    "s_waitcnt lgkmcnt(0)\n"
    "s_barrier\n"
    "ds_read_b128 v[52:55], v5 offset:0\n"
    "ds_read_b128 v[56:59], v5 offset:1024\n"
    "v_smfmac_f32_16x16x64_bf16 v[20:23], v[44:47], v[152:159], v2\n"
    "ds_read_b128 v[60:63], v6 offset:0\n"
    "v_smfmac_f32_16x16x64_bf16 v[24:27], v[44:47], v[160:167], v2\n"
    "ds_read_b128 v[64:67], v6 offset:1024\n"
    "v_smfmac_f32_16x16x64_bf16 v[16:19], v[48:51], v[168:175], v2\n"
    "ds_read_b128 v[68:71], v6 offset:2048\n"
    "v_smfmac_f32_16x16x64_bf16 v[20:23], v[48:51], a[0:7], v2\n"
    "ds_read_b128 v[72:75], v6 offset:3072\n"
    "v_smfmac_f32_16x16x64_bf16 v[24:27], v[48:51], a[8:15], v2\n"
    "ds_read_b128 v[76:79], v6 offset:4096\n"
    "s_waitcnt lgkmcnt(6)\n"
    "v_smfmac_f32_16x16x64_bf16 v[16:19], v[52:55], a[16:23], v2\n"
    "s_waitcnt lgkmcnt(5)\n"
    "v_smfmac_f32_16x16x64_bf16 v[16:19], v[56:59], a[24:31], v2\n"
    "ds_read_b128 v[120:123], v7 offset:14336\n"
    "s_waitcnt lgkmcnt(5)\n"
    "v_smfmac_f32_16x16x64_bf16 v[16:19], v[60:63], a[32:39], v2\n"
    "ds_read_b128 v[124:127], v7 offset:15360\n"
    "s_waitcnt lgkmcnt(5)\n"
    "v_smfmac_f32_16x16x64_bf16 v[16:19], v[64:67], a[40:47], v2\n"
    "ds_read_b128 v[128:131], v7 offset:16384\n"
    "s_waitcnt lgkmcnt(5)\n"
    "v_smfmac_f32_16x16x64_bf16 v[16:19], v[68:71], a[48:55], v2\n"
    "ds_read_b128 v[132:135], v7 offset:17408\n"
    "s_waitcnt lgkmcnt(5)\n"
    "v_smfmac_f32_16x16x64_bf16 v[16:19], v[72:75], a[56:63], v2\n"
    "ds_read_b128 v[136:139], v7 offset:18432\n"
    "s_waitcnt lgkmcnt(5)\n"
    "v_smfmac_f32_16x16x64_bf16 v[16:19], v[76:79], a[64:71], v2\n"
    "ds_read_b128 v[140:143], v7 offset:19456\n"
    "v_smfmac_f32_16x16x64_bf16 v[20:23], v[52:55], a[72:79], v2\n"
    "ds_read_b128 v[144:147], v7 offset:20480\n"
    "v_smfmac_f32_16x16x64_bf16 v[20:23], v[56:59], a[80:87], v2\n"
    "ds_read_b128 v[148:151], v7 offset:21504\n"
    "v_smfmac_f32_16x16x64_bf16 v[20:23], v[60:63], a[88:95], v2\n"
    "ds_read_b128 v[152:155], v7 offset:22528\n"
    "v_smfmac_f32_16x16x64_bf16 v[20:23], v[64:67], a[96:103], v2\n"
    "ds_read_b128 v[156:159], v7 offset:23552\n"
    "v_add_f32 v16, v16, v18\n"
    "v_smfmac_f32_16x16x64_bf16 v[20:23], v[68:71], a[104:111], v2\n"
    "ds_read_b128 v[160:163], v7 offset:24576\n"
    "v_add_f32 v17, v17, v19\n"
    "v_smfmac_f32_16x16x64_bf16 v[20:23], v[72:75], a[112:119], v2\n"
    "v_cvt_pk_bf16_f32 v16, v16, v17\n"
    "v_smfmac_f32_16x16x64_bf16 v[20:23], v[76:79], a[120:127], v2\n"
    "ds_write_b32 v8, v16 offset:0\n"
    "v_smfmac_f32_16x16x64_bf16 v[24:27], v[52:55], a[128:135], v2\n"
    "ds_read_b128 v[164:167], v7 offset:25600\n"
    "v_smfmac_f32_16x16x64_bf16 v[24:27], v[56:59], a[136:143], v2\n"
    "ds_read_b128 v[168:171], v7 offset:26624\n"
    "v_smfmac_f32_16x16x64_bf16 v[24:27], v[60:63], a[144:151], v2\n"
    "ds_read_b128 v[172:175], v7 offset:27648\n"
    "v_smfmac_f32_16x16x64_bf16 v[24:27], v[64:67], a[152:159], v2\n"
    "s_waitcnt lgkmcnt(3)\n"
    "s_barrier\n"
    "ds_read_b64_tr_b16 v[28:29], v9 offset:0\n"
    "ds_read_b64_tr_b16 v[30:31], v9 offset:512\n"
    "v_smfmac_f32_16x16x64_bf16 v[24:27], v[68:71], a[160:167], v2\n"
    "v_add_f32 v20, v20, v22\n"
    "v_smfmac_f32_16x16x64_bf16 v[24:27], v[72:75], a[168:175], v2\n"
    "v_add_f32 v21, v21, v23\n"
    "v_smfmac_f32_16x16x64_bf16 v[24:27], v[76:79], a[176:183], v2\n"
    "v_cvt_pk_bf16_f32 v20, v20, v21\n"
    "s_waitcnt lgkmcnt(0)\n"
    "v_smfmac_f32_16x16x64_bf16 v[80:83], v[28:31], v[120:127], v2\n"
    "ds_write_b32 v8, v20 offset:1024\n"
    "v_smfmac_f32_16x16x64_bf16 v[84:87], v[28:31], v[128:135], v2\n"
    "ds_read_b128 v[120:123], v7 offset:28672\n"
    "v_smfmac_f32_16x16x64_bf16 v[88:91], v[28:31], v[136:143], v2\n"
    "ds_read_b128 v[124:127], v7 offset:29696\n"
    "v_smfmac_f32_16x16x64_bf16 v[92:95], v[28:31], v[144:151], v2\n"
    "v_add_f32 v24, v24, v26\n"
    "v_add_f32 v25, v25, v27\n"
    "v_smfmac_f32_16x16x64_bf16 v[96:99], v[28:31], v[152:159], v2\n"
    "s_waitcnt lgkmcnt(2)\n"
    "s_barrier\n"
    "ds_read_b64_tr_b16 v[32:33], v9 offset:1024\n"
    "ds_read_b64_tr_b16 v[34:35], v9 offset:1536\n"
    "v_smfmac_f32_16x16x64_bf16 v[100:103], v[28:31], v[160:167], v2\n"
    "v_cvt_pk_bf16_f32 v24, v24, v25\n"
    "ds_write_b32 v8, v24 offset:2048\n"
    "v_smfmac_f32_16x16x64_bf16 v[104:107], v[28:31], v[168:175], v2\n"
    "ds_read_b128 v[128:131], v7 offset:30720\n"
    "v_smfmac_f32_16x16x64_bf16 v[108:111], v[28:31], a[184:191], v2\n"
    "ds_read_b128 v[132:135], v7 offset:31744\n"
    "s_waitcnt lgkmcnt(3)\n"
    "v_smfmac_f32_16x16x64_bf16 v[80:83], v[32:35], a[192:199], v2\n"
    "v_smfmac_f32_16x16x64_bf16 v[84:87], v[32:35], a[200:207], v2\n"
    "s_waitcnt lgkmcnt(2)\n"
    "s_barrier\n"
    "ds_read_b64_tr_b16 v[36:37], v9 offset:2048\n"
    "ds_read_b64_tr_b16 v[38:39], v9 offset:2560\n"
    "v_smfmac_f32_16x16x64_bf16 v[88:91], v[32:35], a[208:215], v2\n"
    "v_smfmac_f32_16x16x64_bf16 v[92:95], v[32:35], a[216:223], v2\n"
    "ds_read_b128 v[136:139], v7 offset:32768\n"
    "v_smfmac_f32_16x16x64_bf16 v[96:99], v[32:35], a[224:231], v2\n"
    "ds_read_b128 v[140:143], v7 offset:33792\n"
    "v_smfmac_f32_16x16x64_bf16 v[100:103], v[32:35], a[232:239], v2\n"
    "ds_read_b128 v[144:147], v7 offset:34816\n"
    "s_waitcnt lgkmcnt(3)\n"
    "v_smfmac_f32_16x16x64_bf16 v[80:83], v[36:39], a[240:247], v2\n"
    "ds_read_b128 v[148:151], v7 offset:35840\n"
    "v_smfmac_f32_16x16x64_bf16 v[84:87], v[36:39], a[248:255], v2\n"
    "s_nop 0\n"
    "v_smfmac_f32_16x16x64_bf16 v[88:91], v[36:39], v[176:183], v2\n"
    "s_nop 0\n"
    "v_smfmac_f32_16x16x64_bf16 v[92:95], v[36:39], v[184:191], v2\n"
    "s_nop 0\n"
    "v_smfmac_f32_16x16x64_bf16 v[96:99], v[36:39], v[192:199], v2\n"
    "v_add_f32 v80, v80, v82\n"
    "v_add_f32 v81, v81, v83\n"
    "v_smfmac_f32_16x16x64_bf16 v[100:103], v[36:39], v[200:207], v2\n"
    "v_cvt_pk_bf16_f32 v40, v80, v81\n"
    "v_add_f32 v84, v84, v86\n"
    "v_smfmac_f32_16x16x64_bf16 v[104:107], v[32:35], v[120:127], v2\n"
    "v_add_f32 v85, v85, v87\n"
    "v_cvt_pk_bf16_f32 v41, v84, v85\n"
    "v_smfmac_f32_16x16x64_bf16 v[104:107], v[36:39], v[128:135], v2\n"
    "v_add_f32 v88, v88, v90\n"
    "v_add_f32 v89, v89, v91\n"
    "s_waitcnt lgkmcnt(2)\n"
    "v_smfmac_f32_16x16x64_bf16 v[108:111], v[32:35], v[136:143], v2\n"
    "v_cvt_pk_bf16_f32 v42, v88, v89\n"
    "v_add_f32 v92, v92, v94\n"
    "s_waitcnt lgkmcnt(0)\n"
    "v_smfmac_f32_16x16x64_bf16 v[108:111], v[36:39], v[144:151], v2\n"
    "v_add_f32 v93, v93, v95\n"
    "v_cvt_pk_bf16_f32 v43, v92, v93\n"
    "v_smfmac_f32_16x16x64_bf16 v[112:115], v[28:31], v[208:215], v2\n"
    "v_permlane16_swap_b32 v40, v41\n"
    "v_smfmac_f32_16x16x64_bf16 v[112:115], v[32:35], v[216:223], v2\n"
    "v_permlane16_swap_b32 v42, v43\n"
    "v_smfmac_f32_16x16x64_bf16 v[112:115], v[36:39], v[224:231], v2\n"
    "v_add_f32 v96, v96, v98\n"
    "v_add_f32 v97, v97, v99\n"
    "v_smfmac_f32_16x16x64_bf16 v[116:119], v[28:31], v[232:239], v2\n"
    "v_permlane32_swap_b32 v40, v42\n"
    "v_smfmac_f32_16x16x64_bf16 v[116:119], v[32:35], v[240:247], v2\n"
    "v_permlane32_swap_b32 v41, v43\n"
    "v_smfmac_f32_16x16x64_bf16 v[116:119], v[36:39], v[248:255], v2\n"
    "buffer_store_dwordx4 v[40:43], v10, s[8:11], s31 offen sc1\n"
    "v_cvt_pk_bf16_f32 v44, v96, v97\n"
    "v_pk_add_f32 v[100:101], v[100:101], v[102:103]\n"
    "v_cvt_pk_bf16_f32 v45, v100, v101\n"
    "v_pk_add_f32 v[104:105], v[104:105], v[106:107]\n"
    "v_cvt_pk_bf16_f32 v46, v104, v105\n"
    "v_pk_add_f32 v[108:109], v[108:109], v[110:111]\n"
    "v_cvt_pk_bf16_f32 v47, v108, v109\n"
    "v_permlane16_swap_b32 v44, v45\n"
    "v_pk_add_f32 v[112:113], v[112:113], v[114:115]\n"
    "v_permlane16_swap_b32 v46, v47\n"
    "v_pk_add_f32 v[116:117], v[116:117], v[118:119]\n"
    "v_cvt_pk_bf16_f32 v48, v112, v113\n"
    "v_permlane32_swap_b32 v44, v46\n"
    "v_cvt_pk_bf16_f32 v49, v116, v117\n"
    "v_permlane32_swap_b32 v45, v47\n"
    "buffer_store_dwordx4 v[44:47], v10, s[8:11], s50 offen sc1\n"
    "v_permlane16_swap_b32 v48, v49\n"
    "buffer_store_dwordx2 v[48:49], v12, s[8:11], s51 offen sc1\n"
    "s_cmp_eq_u32 s24, 63\n"
    "s_cbranch_scc1 .LW_last\n"
    "s_cmp_lg_u32 s21, 0\n"
    "s_cbranch_scc1 .LWt_84\n"
    "s_mov_b32 exec_hi, 0\n"
    "buffer_load_dwordx2 v[52:53], v15, s[8:11], s33 offen sc1\n"
    "s_mov_b32 exec_hi, -1\n"
    "s_add_u32 s41, s24, s4\n"
    "s_memrealtime s[42:43]\n"
    "s_waitcnt vmcnt(0)\n"
    ".Lhp_85:\n"
    "v_cmp_ne_u32 vcc, s41, v53\n"
    "s_nop 1\n"
    "s_cmp_eq_u32 vcc_lo, 0\n"
    "s_cbranch_scc1 .Lhd_86\n"
    "s_mov_b32 exec_hi, 0\n"
    "buffer_load_dwordx2 v[52:53], v15, s[8:11], s33 offen sc1\n"
    "s_mov_b32 exec_hi, -1\n"
    "s_waitcnt lgkmcnt(0)\n"
    "s_memrealtime s[42:43]\n"
    "s_waitcnt vmcnt(0)\n"
    "s_sub_u32 s25, s25, 1\n"
    "s_cbranch_scc0 .Lhp_85\n"
    "s_branch .Lfail\n"
    ".Lhd_86:\n"
    "s_waitcnt lgkmcnt(0)\n"
    "s_mul_i32 s42, s42, 6\n"
    "v_subrev_u32 v52, s42, v52\n"
    "s_nop 1\n"
    "v_max_i32_dpp v52, v52, v52 row_ror:8 row_mask:0xf bank_mask:0xf\n"
    "s_nop 1\n"
    "v_max_i32_dpp v52, v52, v52 row_ror:4 row_mask:0xf bank_mask:0xf\n"
    "s_nop 1\n"
    "v_max_i32_dpp v52, v52, v52 row_ror:2 row_mask:0xf bank_mask:0xf\n"
    "s_nop 1\n"
    "v_max_i32_dpp v52, v52, v52 row_ror:1 row_mask:0xf bank_mask:0xf\n"
    "s_nop 0\n"
    "v_readlane_b32 s40, v52, 0\n"
    "v_readlane_b32 s41, v52, 16\n"
    "s_nop 0\n"
    "s_max_i32 s40, s40, s41\n"
    "s_min_i32 s40, s40, 2000\n"
    "s_add_u32 s40, s40, s42\n"
    "s_sub_u32 s41, s40, s59\n"
    "s_mov_b32 s59, s40\n"
    "s_sub_u32 s41, s41, s82\n"
    "s_add_u32 s83, s83, 4\n"
    "s_min_u32 s83, s83, s41\n"
    "s_sub_u32 s41, s41, s83\n"
    "s_cmp_gt_u32 s41, 90\n"
    "s_cselect_b32 s41, 12, 0\n"
    "s_add_u32 s82, s82, s41\n"
    "s_cmp_lt_u32 s24, 46\n"
    "s_cselect_b32 s41, 1, 4\n"
    "s_sub_u32 s82, s82, s41\n"
    "s_cmp_lt_u32 s24, 46\n"
    "s_cselect_b32 s41, 40, 0\n"
    "s_max_i32 s82, s82, s41\n"
    "s_min_i32 s82, s82, 100\n"
    ".LWt_84:\n"
    "s_add_u32 s24, s24, 1\n"
    "s_add_u32 s30, s30, 32768\n"
    "s_cmp_eq_u32 s30, 98304\n"
    "s_cselect_b32 s30, 0, s30\n"
    "s_cmp_lt_u32 s24, 63\n"
    "s_cbranch_scc1 .LW_idle\n"
    "s_or_b32 s40, s20, s21\n"
    "s_cbranch_scc1 .LW_idle\n"
    "v_mov_b32 v0, 1\n"
    "buffer_store_dword v0, v14, s[8:11], 0 offen sc1\n"
    "s_nop 0\n"
    "s_branch .LW_idle\n"
    ".LW_last:\n"
    "s_endpgm\n"
    ".LW_again:\n"
    "s_nop 7\n"
    "v_mov_b64 v[16:17], 0\n"
    "v_mov_b64 v[18:19], 0\n"
    "v_mov_b64 v[20:21], 0\n"
    "v_mov_b64 v[22:23], 0\n"
    "v_mov_b64 v[24:25], 0\n"
    "v_mov_b64 v[26:27], 0\n"
    ".LW_poll:\n"
    "buffer_load_dwordx4 v[40:43], v3, s[8:11], s30 offen offset:0 sc1\n"
    "buffer_load_dwordx4 v[44:47], v3, s[8:11], s30 offen offset:1024 sc1\n"
    "buffer_load_dwordx4 v[48:51], v3, s[8:11], s30 offen offset:2048 sc1\n"
    "s_waitcnt vmcnt(2)\n"
    "v_max3_u32 v0, v40, v41, v42\n"
    "v_max_u32 v0, v0, v43\n"
    "s_waitcnt vmcnt(1)\n"
    "v_max3_u32 v0, v0, v44, v45\n"
    "v_max3_u32 v0, v0, v46, v47\n"
    "s_waitcnt vmcnt(0)\n"
    "v_max3_u32 v0, v0, v48, v49\n"
    "v_max3_u32 v0, v0, v50, v51\n"
    "v_cmp_eq_u32 vcc, -1, v0\n"
    "s_cbranch_vccz .LW_have\n"
    "s_sub_u32 s25, s25, 1\n"
    "s_nop 2\n"
    "s_cbranch_scc0 .LW_poll\n"
    "s_branch .Lfail\n"
    ".Lrole_C:\n"
    "s_waitcnt lgkmcnt(0)\n"
    "s_add_u32 s4, s3, 1\n"
    "v_mov_b32 v2, s3\n"
    "s_or_b32 s41, s21, s6\n"
    "s_cbranch_scc1 .Lcl_87\n"
    "s_lshl_b32 s41, s5, 2\n"
    "s_add_u32 s41, s41, 4178944\n"
    "v_mov_b32 v3, s41\n"
    "v_mov_b32 v2, s4\n"
    "s_mov_b64 exec, 1\n"
    "buffer_atomic_swap v2, v3, s[8:11], 0 offen sc0 sc1\n"
    "s_mov_b64 exec, -1\n"
    ".Lcl_87:\n"
    "v_min_u32 v6, 54, v1\n"
    "v_lshlrev_b32 v6, 7, v6\n"
    "v_add_u32 v6, 4182016, v6\n"
    "buffer_load_dwordx2 v[4:5], v6, s[8:11], 0 offen sc1\n"
    "s_waitcnt vmcnt(0)\n"
    "v_cmp_ne_u32 vcc, s4, v4\n"
    "s_nop 1\n"
    "s_cmp_eq_u32 vcc_lo, 0\n"
    "s_cbranch_scc1 .Lrx_90\n"
    ".Lrp_88:\n"
    "v_min_u32 v6, 54, v1\n"
    "v_lshlrev_b32 v6, 7, v6\n"
    "v_add_u32 v6, 4182016, v6\n"
    "buffer_load_dwordx2 v[4:5], v6, s[8:11], 0 offen sc1\n"
    "s_waitcnt vmcnt(0)\n"
    "v_cmp_ne_u32 vcc, s4, v4\n"
    "s_nop 1\n"
    "s_cmp_eq_u32 vcc_lo, 0\n"
    "s_cbranch_scc1 .Lro_89\n"
    "s_sleep 2\n"
    "s_sub_u32 s25, s25, 1\n"
    "s_cbranch_scc0 .Lrp_88\n"
    "s_branch .Lfail\n"
    ".Lro_89:\n"
    ".Lrx_90:\n"
    "v_cmp_ne_u32 vcc, s4, v4\n"
    "s_nop 1\n"
    "s_cmp_eq_u32 vcc_hi, 0\n"
    "s_cbranch_scc1 .Lrx_93\n"
    ".Lrp_91:\n"
    "v_min_u32 v6, 54, v1\n"
    "v_lshlrev_b32 v6, 7, v6\n"
    "v_add_u32 v6, 4182016, v6\n"
    "buffer_load_dwordx2 v[4:5], v6, s[8:11], 0 offen sc1\n"
    "s_waitcnt vmcnt(0)\n"
    "v_cmp_ne_u32 vcc, s4, v4\n"
    "s_nop 1\n"
    "s_cmp_eq_u32 vcc_hi, 0\n"
    "s_cbranch_scc1 .Lro_92\n"
    "s_sleep 2\n"
    "s_sub_u32 s25, s25, 1\n"
    "s_cbranch_scc0 .Lrp_91\n"
    "s_branch .Lfail\n"
    ".Lro_92:\n"
    ".Lrx_93:\n"
    "v_cmp_ne_u32 vcc, 0, v5\n"
    "s_nop 1\n"
    "s_and_b32 s22, vcc_hi, 1\n"
    "s_lshl_b32 s22, s22, 13\n"
    "s_lshr_b32 s23, vcc_hi, 1\n"
    "s_cmp_lg_u32 s21, 0\n"
    "s_cbranch_scc1 .Lgt_94\n"
    "v_mov_b32 v3, 0\n"
    "v_readfirstlane_b32 s40, v2\n"
    "s_nop 1\n"
    "v_mov_b32 v2, s40\n"
    "ds_write_b32 v3, v2\n"
    "s_waitcnt lgkmcnt(0)\n"
    ".Lgt_94:\n"
    "s_barrier\n"
    "v_mov_b32 v3, 0\n"
    "ds_read_b32 v2, v3\n"
    "s_waitcnt lgkmcnt(0)\n"
    "v_readfirstlane_b32 s40, v2\n"
    "s_barrier\n"
    "s_cmp_eq_u32 s40, s4\n"
    "s_cbranch_scc1 .Llost\n"
    "s_cmp_lg_u32 s21, 0\n"
    "s_cbranch_scc1 .Lend\n"
    "v_and_b32 v0, 2, v1\n"
    "v_cmp_ne_u32 vcc, 0, v0\n"
    "v_mov_b32 v2, 0x4444\n"
    "v_mov_b32 v0, 0xeeee\n"
    "v_cndmask_b32 v2, v2, v0, vcc\n"
    "v_and_b32 v0, 1, v1\n"
    "v_cmp_ne_u32 vcc, 0, v0\n"
    "v_mov_b32 v9, 0x01000504\n"
    "v_mov_b32 v0, 0x07060302\n"
    "v_cndmask_b32 v9, v9, v0, vcc\n"
    "v_lshlrev_b32 v3, 4, v1\n"
    "v_bfe_u32 v4, v1, 5, 1\n"
    "v_lshlrev_b32 v4, 9, v4\n"
    "v_bfe_u32 v0, v1, 2, 2\n"
    "v_lshl_add_u32 v4, v0, 7, v4\n"
    "v_bfe_u32 v0, v1, 4, 1\n"
    "v_lshl_add_u32 v4, v0, 6, v4\n"
    "v_bfe_u32 v0, v1, 0, 1\n"
    "v_lshl_add_u32 v4, v0, 5, v4\n"
    "v_bfe_u32 v0, v1, 1, 1\n"
    "v_lshl_add_u32 v4, v0, 4, v4\n"
    "v_add_u32 v4, s22, v4\n"
    "v_bfe_u32 v5, v1, 3, 1\n"
    "v_lshlrev_b32 v5, 9, v5\n"
    "v_bfe_u32 v0, v1, 4, 2\n"
    "v_lshl_add_u32 v5, v0, 7, v5\n"
    "v_bfe_u32 v0, v1, 2, 1\n"
    "v_lshl_add_u32 v5, v0, 6, v5\n"
    "v_bfe_u32 v0, v1, 0, 1\n"
    "v_lshl_add_u32 v5, v0, 5, v5\n"
    "v_bfe_u32 v0, v1, 1, 1\n"
    "v_lshl_add_u32 v5, v0, 4, v5\n"
    "v_add_u32 v5, s22, v5\n"
    "s_mov_b32 s34, 0\n"
    "s_mov_b32 s35, -1\n"
    "s_lshl_b32 s40, s20, 14\n"
    "v_add_u32 v6, s40, v3\n"
    "s_lshl_b32 s40, s20, 8\n"
    "s_cmp_ge_u32 s20, 20\n"
    "s_cselect_b32 s41, 11264, 0\n"
    "s_add_u32 s40, s40, s41\n"
    "s_add_u32 s40, s40, s22\n"
    "v_bfe_u32 v7, v1, 4, 1\n"
    "v_lshlrev_b32 v7, 7, v7\n"
    "v_bfe_u32 v0, v1, 0, 1\n"
    "v_lshl_add_u32 v7, v0, 6, v7\n"
    "v_bfe_u32 v0, v1, 1, 1\n"
    "v_lshl_add_u32 v7, v0, 5, v7\n"
    "v_bfe_u32 v0, v1, 5, 1\n"
    "v_lshl_add_u32 v7, v0, 4, v7\n"
    "v_bfe_u32 v0, v1, 2, 2\n"
    "v_lshl_add_u32 v7, v0, 2, v7\n"
    "v_add_u32 v7, s40, v7\n"
    "s_mul_i32 s40, s20, 26\n"
    "s_lshr_b32 s40, s40, 8\n"
    "s_lshr_b32 s41, s5, 5\n"
    "s_add_u32 s72, s41, 0\n"
    "s_and_b32 s72, s72, 3\n"
    "s_lshl_b32 s72, s72, 11\n"
    "s_add_u32 s73, s41, 1\n"
    "s_and_b32 s73, s73, 3\n"
    "s_lshl_b32 s73, s73, 11\n"
    "s_add_u32 s74, s41, 2\n"
    "s_and_b32 s74, s74, 3\n"
    "s_lshl_b32 s74, s74, 11\n"
    "s_add_u32 s75, s41, 3\n"
    "s_and_b32 s75, s75, 3\n"
    "s_lshl_b32 s75, s75, 11\n"
    "s_lshl_b32 s40, s40, 12\n"
    "s_add_u32 s40, s40, 4227072\n"
    "v_and_b32 v0, 31, v1\n"
    "v_lshl_add_u32 v10, v0, 7, s40\n"
    "v_bfe_u32 v0, v1, 5, 1\n"
    "v_lshl_add_u32 v10, v0, 14, v10\n"
    "s_add_u32 s40, s22, 294912\n"
    "v_and_b32 v0, 31, v1\n"
    "v_lshl_add_u32 v8, v0, 7, s40\n"
    "s_mov_b32 s82, 40\n"
    "s_mov_b32 s83, 0x100000\n"
    "s_memrealtime s[42:43]\n"
    "s_waitcnt lgkmcnt(0)\n"
    "s_mul_i32 s59, s42, 6\n"
    "s_sub_u32 s59, s59, 0x10000\n"
    "s_mov_b32 s78, -1\n"
    "s_mov_b32 s79, 0\n"
    "s_mov_b32 s24, 0\n"
    "s_mov_b32 s26, 98304\n"
    "s_mov_b32 s28, 32768\n"
    "s_mov_b32 s29, 0\n"
    "s_lshl_b32 s30, s20, 15\n"
    "s_add_u32 s30, s30, 59768832\n"
    "v_lshlrev_b32 v152, 12, v1\n"
    "v_mov_b64 v[148:149], -1\n"
    "v_mov_b64 v[150:151], -1\n"
    "v_mov_b32 v11, 0\n"
    ".LC_idle:\n"
    "s_add_u32 s41, s24, 1\n"
    "s_mov_b64 exec, s[78:79]\n"
    "v_cmp_gt_u32 vcc, s41, v11\n"
    "s_cbranch_vccz .LC_ugo\n"
    "s_nop 3\n"
    ".Lpl_95:\n"
    "buffer_load_dword v11, v10, s[8:11], 0 offen sc1\n"
    "s_waitcnt vmcnt(0)\n"
    "v_cmp_gt_u32 vcc, s41, v11\n"
    "s_nop 1\n"
    "s_cbranch_vccz .Lpd_96\n"
    "s_sub_u32 s25, s25, 1\n"
    "s_nop 0\n"
    "s_cbranch_scc0 .Lpl_95\n"
    "s_branch .Lfail\n"
    ".Lpd_96:\n"
    ".LC_ugo:\n"
    "s_mov_b64 exec, -1\n"
    "s_lshr_b32 s41, s82, 0\n"
    "s_add_u32 s41, s41, s59\n"
    "s_add_u32 s41, s41, 434\n"
    ".Lwt_97:\n"
    "s_memrealtime s[42:43]\n"
    "s_waitcnt lgkmcnt(0)\n"
    "s_mul_i32 s42, s42, 6\n"
    "s_sub_u32 s40, s41, s42\n"
    "s_cmp_gt_i32 s40, 30\n"
    "s_cbranch_scc1 .Lwt_97\n"
    "s_lshl_b32 s40, s72, 2\n"
    "s_add_u32 s40, s40, s30\n"
    "buffer_load_dwordx4 a[0:3], v3, s[8:11], s40 offen offset:0 sc1\n"
    "buffer_load_dwordx4 a[4:7], v3, s[8:11], s40 offen offset:1024 sc1\n"
    "buffer_load_dwordx4 a[8:11], v3, s[8:11], s40 offen offset:2048 sc1\n"
    "buffer_load_dwordx4 a[12:15], v3, s[8:11], s40 offen offset:3072 sc1\n"
    "s_lshl_b32 s40, s72, 2\n"
    "s_add_u32 s40, s40, s30\n"
    "s_add_u32 s40, s40, 4096\n"
    "buffer_load_dwordx4 a[16:19], v3, s[8:11], s40 offen offset:0 sc1\n"
    "buffer_load_dwordx4 a[20:23], v3, s[8:11], s40 offen offset:1024 sc1\n"
    "buffer_load_dwordx4 a[24:27], v3, s[8:11], s40 offen offset:2048 sc1\n"
    "buffer_load_dwordx4 a[28:31], v3, s[8:11], s40 offen offset:3072 sc1\n"
    "s_lshl_b32 s40, s73, 2\n"
    "s_add_u32 s40, s40, s30\n"
    "buffer_load_dwordx4 a[32:35], v3, s[8:11], s40 offen offset:0 sc1\n"
    "buffer_load_dwordx4 a[36:39], v3, s[8:11], s40 offen offset:1024 sc1\n"
    "buffer_load_dwordx4 a[40:43], v3, s[8:11], s40 offen offset:2048 sc1\n"
    "buffer_load_dwordx4 a[44:47], v3, s[8:11], s40 offen offset:3072 sc1\n"
    "s_lshl_b32 s40, s73, 2\n"
    "s_add_u32 s40, s40, s30\n"
    "s_add_u32 s40, s40, 4096\n"
    "buffer_load_dwordx4 a[48:51], v3, s[8:11], s40 offen offset:0 sc1\n"
    "buffer_load_dwordx4 a[52:55], v3, s[8:11], s40 offen offset:1024 sc1\n"
    "buffer_load_dwordx4 a[56:59], v3, s[8:11], s40 offen offset:2048 sc1\n"
    "buffer_load_dwordx4 a[60:63], v3, s[8:11], s40 offen offset:3072 sc1\n"
    "s_lshl_b32 s40, s74, 2\n"
    "s_add_u32 s40, s40, s30\n"
    "buffer_load_dwordx4 a[64:67], v3, s[8:11], s40 offen offset:0 sc1\n"
    "buffer_load_dwordx4 a[68:71], v3, s[8:11], s40 offen offset:1024 sc1\n"
    "buffer_load_dwordx4 a[72:75], v3, s[8:11], s40 offen offset:2048 sc1\n"
    "buffer_load_dwordx4 a[76:79], v3, s[8:11], s40 offen offset:3072 sc1\n"
    "s_lshl_b32 s40, s74, 2\n"
    "s_add_u32 s40, s40, s30\n"
    "s_add_u32 s40, s40, 4096\n"
    "buffer_load_dwordx4 a[80:83], v3, s[8:11], s40 offen offset:0 sc1\n"
    "buffer_load_dwordx4 a[84:87], v3, s[8:11], s40 offen offset:1024 sc1\n"
    "buffer_load_dwordx4 a[88:91], v3, s[8:11], s40 offen offset:2048 sc1\n"
    "buffer_load_dwordx4 a[92:95], v3, s[8:11], s40 offen offset:3072 sc1\n"
    "s_lshl_b32 s40, s75, 2\n"
    "s_add_u32 s40, s40, s30\n"
    "buffer_load_dwordx4 a[96:99], v3, s[8:11], s40 offen offset:0 sc1\n"
    "buffer_load_dwordx4 a[100:103], v3, s[8:11], s40 offen offset:1024 sc1\n"
    "buffer_load_dwordx4 a[104:107], v3, s[8:11], s40 offen offset:2048 sc1\n"
    "buffer_load_dwordx4 a[108:111], v3, s[8:11], s40 offen offset:3072 sc1\n"
    "s_lshl_b32 s40, s75, 2\n"
    "s_add_u32 s40, s40, s30\n"
    "s_add_u32 s40, s40, 4096\n"
    "buffer_load_dwordx4 a[112:115], v3, s[8:11], s40 offen offset:0 sc1\n"
    "buffer_load_dwordx4 a[116:119], v3, s[8:11], s40 offen offset:1024 sc1\n"
    "buffer_load_dwordx4 a[120:123], v3, s[8:11], s40 offen offset:2048 sc1\n"
    "buffer_load_dwordx4 a[124:127], v3, s[8:11], s40 offen offset:3072 sc1\n"
    "s_cmp_eq_u32 s24, 0\n"
    "s_cbranch_scc1 .LC_noarch\n"
    "s_add_u32 s40, s29, 0\n"
    "buffer_store_dword v148, v7, s[8:11], s40 offen sc1\n"
    "s_add_u32 s40, s29, 524288\n"
    "buffer_store_dword v148, v7, s[8:11], s40 offen sc1\n"
    "s_add_u32 s29, s29, 32768\n"
    "s_cmp_eq_u32 s29, 98304\n"
    "s_cselect_b32 s29, 0, s29\n"
    "s_cmp_eq_u32 s37, 1\n"
    "s_cbranch_scc1 .LC_noarch\n"
    "s_sub_u32 s24, s24, 1\n"
    "s_sub_u32 s36, s36, s82\n"
    "s_add_u32 s83, s83, 4\n"
    "s_min_u32 s83, s83, s36\n"
    "s_sub_u32 s36, s36, s83\n"
    "s_cmp_gt_u32 s36, 90\n"
    "s_cselect_b32 s36, 12, 0\n"
    "s_add_u32 s82, s82, s36\n"
    "s_cmp_lt_u32 s24, 46\n"
    "s_cselect_b32 s36, 1, 4\n"
    "s_sub_u32 s82, s82, s36\n"
    "s_cmp_lt_u32 s24, 46\n"
    "s_cselect_b32 s36, 40, 0\n"
    "s_max_i32 s82, s82, s36\n"
    "s_min_i32 s82, s82, 100\n"
    "s_add_u32 s24, s24, 1\n"
    ".LC_noarch:\n"
    "s_and_b32 s33, s24, 2\n"
    "s_lshl_b32 s33, s33, 13\n"
    "s_and_b32 s40, s24, 1\n"
    "s_lshl_b32 s40, s40, 12\n"
    "s_add_u32 s33, s33, s40\n"
    "s_mul_i32 s40, s24, 43\n"
    "s_lshr_b32 s40, s40, 7\n"
    "s_mul_i32 s27, s24, 655360\n"
    "s_lshl_b32 s41, s40, 17\n"
    "s_add_u32 s27, s27, s41\n"
    "s_lshr_b32 s40, s23, s40\n"
    "s_and_b32 s40, s40, 1\n"
    "s_lshl_b32 s40, s40, 13\n"
    "s_add_u32 s27, s27, s40\n"
    "s_add_u32 s27, s27, 161480704\n"
    "s_not_b64 s[78:79], s[78:79]\n"
    "s_and_b32 s40, s72, 4096\n"
    "s_add_u32 s40, s40, s72\n"
    "s_lshl_b32 s40, s40, 1\n"
    "s_add_u32 s48, s26, s40\n"
    "s_lshr_b32 s40, s72, 1\n"
    "s_add_u32 s40, s40, 32768\n"
    "s_add_u32 s52, s26, s40\n"
    "s_add_u32 s64, s27, s72\n"
    "s_add_u32 s68, s28, 0\n"
    "s_and_b32 s40, s73, 4096\n"
    "s_add_u32 s40, s40, s73\n"
    "s_lshl_b32 s40, s40, 1\n"
    "s_add_u32 s49, s26, s40\n"
    "s_lshr_b32 s40, s73, 1\n"
    "s_add_u32 s40, s40, 32768\n"
    "s_add_u32 s53, s26, s40\n"
    "s_add_u32 s65, s27, s73\n"
    "s_add_u32 s69, s28, 524288\n"
    "s_and_b32 s40, s74, 4096\n"
    "s_add_u32 s40, s40, s74\n"
    "s_lshl_b32 s40, s40, 1\n"
    "s_add_u32 s50, s26, s40\n"
    "s_lshr_b32 s40, s74, 1\n"
    "s_add_u32 s40, s40, 32768\n"
    "s_add_u32 s54, s26, s40\n"
    "s_add_u32 s66, s27, s74\n"
    "s_add_u32 s70, s28, 1048576\n"
    "s_and_b32 s40, s75, 4096\n"
    "s_add_u32 s40, s40, s75\n"
    "s_lshl_b32 s40, s40, 1\n"
    "s_add_u32 s51, s26, s40\n"
    "s_lshr_b32 s40, s75, 1\n"
    "s_add_u32 s40, s40, 32768\n"
    "s_add_u32 s55, s26, s40\n"
    "s_add_u32 s67, s27, s75\n"
    "s_add_u32 s71, s28, 1572864\n"
    "s_mov_b32 s38, 2\n"
    "s_lshr_b32 s41, s82, 0\n"
    "s_add_u32 s41, s41, s59\n"
    "s_add_u32 s41, s41, 729\n"
    ".Lwt_100:\n"
    "s_memrealtime s[42:43]\n"
    "s_waitcnt lgkmcnt(0)\n"
    "s_mul_i32 s42, s42, 6\n"
    "s_sub_u32 s40, s41, s42\n"
    "s_cmp_gt_i32 s40, 30\n"
    "s_cbranch_scc1 .Lwt_100\n"
    "s_mov_b32 exec_hi, 0\n"
    "buffer_load_dwordx2 v[146:147], v8, s[8:11], s33 offen sc1\n"
    "s_mov_b32 exec_hi, -1\n"
    "s_add_u32 s40, s24, 2\n"
    "s_mov_b64 exec, s[78:79]\n"
    "v_cmp_gt_u32 vcc, s40, v11\n"
    "s_nop 1\n"
    "s_cmp_eq_u64 vcc, 0\n"
    "s_cselect_b64 exec, 1, s[78:79]\n"
    "buffer_load_dword v11, v10, s[8:11], 0 offen sc1\n"
    "s_mov_b64 exec, -1\n"
    "s_add_u32 s30, s30, 1310720\n"
    "s_cmp_eq_u32 s24, 0\n"
    "s_cbranch_scc1 .Lnt_103\n"
    "s_mov_b64 exec, 0xff\n"
    "buffer_load_dword v150, v152, s[8:11], s30 offen\n"
    "s_add_u32 s41, s27, 786432\n"
    "s_or_b32 s40, s41, 0x1fffff\n"
    "s_sub_u32 s40, s40, 131071\n"
    "s_mov_b64 exec, 1\n"
    "buffer_load_dword v151, v1, s[8:11], s40 offen\n"
    "s_mov_b64 exec, -1\n"
    ".Lnt_103:\n"
    "s_cmp_eq_u32 s24, 0\n"
    "s_cselect_b32 s37, 1, 0\n"
    "s_cbranch_scc1 .LC_retry\n"
    "s_add_u32 s41, s24, s4\n"
    "s_lshr_b32 s42, s82, 3\n"
    "s_add_u32 s42, s42, 14\n"
    "s_getpc_b64 s[76:77]\n"
    ".Lfp_104h:\n"
    "s_add_u32 s76, s76, .Lfd_107-.Lfp_104h\n"
    "s_addc_u32 s77, s77, 0\n"
    "s_mov_b32 exec_hi, 0\n"
    "s_waitcnt vmcnt(3)\n"
    ".Lfp_104:\n"
    "s_memrealtime s[12:13]\n"
    "v_cmp_ne_u32 vcc, s41, v147\n"
    "v_subrev_u32 v147, s59, v146\n"
    "s_nop 0\n"
    "s_memrealtime s[14:15]\n"
    "v_max_i32_dpp v147, v147, v147 row_ror:8 row_mask:0xf bank_mask:0xf\n"
    "s_nop 0\n"
    "s_memrealtime s[16:17]\n"
    "v_max_i32_dpp v147, v147, v147 row_ror:4 row_mask:0xf bank_mask:0xf\n"
    "s_cbranch_vccnz .Lfa_105\n"
    "s_memrealtime s[18:19]\n"
    "v_max_i32_dpp v147, v147, v147 row_ror:2 row_mask:0xf bank_mask:0xf\n"
    "s_nop 0\n"
    "s_memrealtime s[44:45]\n"
    "v_max_i32_dpp v147, v147, v147 row_ror:1 row_mask:0xf bank_mask:0xf\n"
    "s_nop 0\n"
    "s_memrealtime s[46:47]\n"
    "v_readlane_b32 s36, v147, 0\n"
    "v_readlane_b32 s40, v147, 16\n"
    "s_mov_b32 exec_hi, -1\n"
    "s_max_i32 s36, s36, s40\n"
    "s_add_u32 s59, s59, s36\n"
    "s_add_u32 s41, s59, s42\n"
    "s_sub_u32 s40, s41, 52\n"
    "s_waitcnt lgkmcnt(0)\n"
    "s_sub_u32 s40, s40, s12\n"
    "s_sub_u32 s40, s40, s14\n"
    "s_sub_u32 s40, s40, s16\n"
    "s_sub_u32 s40, s40, s18\n"
    "s_sub_u32 s40, s40, s44\n"
    "s_sub_u32 s40, s40, s46\n"
    "s_cmp_gt_i32 s40, 88\n"
    "s_cbranch_scc1 .Lfs_106\n"
    "s_max_i32 s40, s40, 0\n"
    "s_lshl_b32 s40, s40, 2\n"
    "s_sub_u32 s76, s76, s40\n"
    "s_subb_u32 s77, s77, 0\n"
    "s_setpc_b64 s[76:77]\n"
    ".Lfa_105:\n"
    "buffer_load_dwordx2 v[146:147], v8, s[8:11], s33 offen sc1\n"
    "s_waitcnt vmcnt(0) lgkmcnt(0)\n"
    "s_sub_u32 s25, s25, 1\n"
    "s_cbranch_scc0 .Lfp_104\n"
    "s_branch .Lfail\n"
    ".Lfs_106:\n"
    "s_cmp_gt_i32 s40, 2000\n"
    "s_cselect_b32 s40, s40, 0\n"
    "s_sub_u32 s59, s59, s40\n"
    "s_sub_u32 s41, s41, s40\n"
    ".Lfs_106w:\n"
    ".Lwt_108:\n"
    "s_memrealtime s[42:43]\n"
    "s_waitcnt lgkmcnt(0)\n"
    "s_mul_i32 s42, s42, 6\n"
    "s_sub_u32 s40, s41, s42\n"
    "s_cmp_gt_i32 s40, 120\n"
    "s_cbranch_scc1 .Lwt_108\n"
    "s_cmp_lt_i32 s40, 50\n"
    "s_cbranch_scc1 .Lfd_107\n"
    "s_memrealtime s[12:13]\n"
    "s_nop 2\n"
    "s_memrealtime s[14:15]\n"
    "s_nop 2\n"
    "s_memrealtime s[16:17]\n"
    "s_nop 2\n"
    "s_memrealtime s[18:19]\n"
    "s_nop 2\n"
    "s_memrealtime s[44:45]\n"
    "s_nop 2\n"
    "s_memrealtime s[46:47]\n"
    "s_waitcnt lgkmcnt(0)\n"
    "s_add_u32 s40, s12, s14\n"
    "s_add_u32 s40, s40, s16\n"
    "s_add_u32 s40, s40, s18\n"
    "s_add_u32 s40, s40, s44\n"
    "s_add_u32 s40, s40, s46\n"
    "s_sub_u32 s40, s41, s40\n"
    "s_sub_u32 s40, s40, 56\n"
    "s_max_i32 s40, s40, 0\n"
    "s_min_i32 s40, s40, 88\n"
    "s_lshl_b32 s40, s40, 2\n"
    "s_getpc_b64 s[42:43]\n"
    ".Lwh_109:\n"
    "s_sub_u32 s40, .Lfd_107-.Lwh_109, s40\n"
    "s_add_u32 s42, s42, s40\n"
    "s_addc_u32 s43, s43, 0\n"
    "s_setpc_b64 s[42:43]\n"
    "s_nop 0\n"
    "s_nop 0\n"
    "s_nop 0\n"
    "s_nop 0\n"
    "s_nop 0\n"
    "s_nop 0\n"
    "s_nop 0\n"
    "s_nop 0\n"
    "s_nop 0\n"
    "s_nop 0\n"
    "s_nop 0\n"
    "s_nop 0\n"
    "s_nop 0\n"
    "s_nop 0\n"
    "s_nop 0\n"
    "s_nop 0\n"
    "s_nop 0\n"
    "s_nop 0\n"
    "s_nop 0\n"
    "s_nop 0\n"
    "s_nop 0\n"
    "s_nop 0\n"
    "s_nop 0\n"
    "s_nop 0\n"
    "s_nop 0\n"
    "s_nop 0\n"
    "s_nop 0\n"
    "s_nop 0\n"
    "s_nop 0\n"
    "s_nop 0\n"
    "s_nop 0\n"
    "s_nop 0\n"
    "s_nop 0\n"
    "s_nop 0\n"
    "s_nop 0\n"
    "s_nop 0\n"
    "s_nop 0\n"
    "s_nop 0\n"
    "s_nop 0\n"
    "s_nop 0\n"
    "s_nop 0\n"
    "s_nop 0\n"
    "s_nop 0\n"
    "s_nop 0\n"
    "s_nop 0\n"
    "s_nop 0\n"
    "s_nop 0\n"
    "s_nop 0\n"
    "s_nop 0\n"
    "s_nop 0\n"
    "s_nop 0\n"
    "s_nop 0\n"
    "s_nop 0\n"
    "s_nop 0\n"
    "s_nop 0\n"
    "s_nop 0\n"
    "s_nop 0\n"
    "s_nop 0\n"
    "s_nop 0\n"
    "s_nop 0\n"
    "s_nop 0\n"
    "s_nop 0\n"
    "s_nop 0\n"
    "s_nop 0\n"
    "s_nop 0\n"
    "s_nop 0\n"
    "s_nop 0\n"
    "s_nop 0\n"
    "s_nop 0\n"
    "s_nop 0\n"
    "s_nop 0\n"
    "s_nop 0\n"
    "s_nop 0\n"
    "s_nop 0\n"
    "s_nop 0\n"
    "s_nop 0\n"
    "s_nop 0\n"
    "s_nop 0\n"
    "s_nop 0\n"
    "s_nop 0\n"
    "s_nop 0\n"
    "s_nop 0\n"
    "s_nop 0\n"
    "s_nop 0\n"
    "s_nop 0\n"
    "s_nop 0\n"
    "s_nop 0\n"
    "s_nop 0\n"
    ".Lfd_107:\n"
    ".LC_retry:\n"
    "buffer_load_dwordx4 v[32:35], v6, s[8:11], s64 offen offset:0 sc1\n"
    "buffer_load_dwordx4 v[36:39], v6, s[8:11], s64 offen offset:1024 sc1\n"
    "buffer_load_dwordx4 v[40:43], v6, s[8:11], s65 offen offset:0 sc1\n"
    "buffer_load_dwordx4 v[44:47], v6, s[8:11], s65 offen offset:1024 sc1\n"
    "buffer_load_dwordx4 v[48:51], v6, s[8:11], s66 offen offset:0 sc1\n"
    "buffer_load_dwordx4 v[52:55], v6, s[8:11], s66 offen offset:1024 sc1\n"
    "buffer_load_dwordx4 v[56:59], v6, s[8:11], s67 offen offset:0 sc1\n"
    "buffer_load_dwordx4 v[60:63], v6, s[8:11], s67 offen offset:1024 sc1\n"
    "buffer_load_dwordx4 v[64:67], v5, s[8:11], s52 offen sc1\n"
    "buffer_load_dwordx4 v[68:71], v5, s[8:11], s53 offen sc1\n"
    "buffer_load_dwordx4 v[72:75], v5, s[8:11], s54 offen sc1\n"
    "buffer_load_dwordx4 v[76:79], v5, s[8:11], s55 offen sc1\n"
    "buffer_load_dwordx4 v[80:83], v4, s[8:11], s48 offen offset:0 sc1\n"
    "buffer_load_dwordx4 v[84:87], v4, s[8:11], s48 offen offset:1024 sc1\n"
    "buffer_load_dwordx4 v[88:91], v4, s[8:11], s48 offen offset:2048 sc1\n"
    "buffer_load_dwordx4 v[92:95], v4, s[8:11], s48 offen offset:3072 sc1\n"
    "buffer_load_dwordx4 v[96:99], v4, s[8:11], s49 offen offset:0 sc1\n"
    "buffer_load_dwordx4 v[100:103], v4, s[8:11], s49 offen offset:1024 sc1\n"
    "buffer_load_dwordx4 v[104:107], v4, s[8:11], s49 offen offset:2048 sc1\n"
    "buffer_load_dwordx4 v[108:111], v4, s[8:11], s49 offen offset:3072 sc1\n"
    "buffer_load_dwordx4 v[112:115], v4, s[8:11], s50 offen offset:0 sc1\n"
    "buffer_load_dwordx4 v[116:119], v4, s[8:11], s50 offen offset:1024 sc1\n"
    "buffer_load_dwordx4 v[120:123], v4, s[8:11], s50 offen offset:2048 sc1\n"
    "buffer_load_dwordx4 v[124:127], v4, s[8:11], s50 offen offset:3072 sc1\n"
    "buffer_load_dwordx4 v[128:131], v4, s[8:11], s51 offen offset:0 sc1\n"
    "buffer_load_dwordx4 v[132:135], v4, s[8:11], s51 offen offset:1024 sc1\n"
    "buffer_load_dwordx4 v[136:139], v4, s[8:11], s51 offen offset:2048 sc1\n"
    "buffer_load_dwordx4 v[140:143], v4, s[8:11], s51 offen offset:3072 sc1\n"
    "v_mov_b64 v[12:13], 0\n"
    "v_mov_b64 v[14:15], 0\n"
    "s_waitcnt vmcnt(19)\n"
    "v_mfma_f32_16x16x32_bf16 v[16:19], v[64:67], v[32:35], 0\n"
    "v_mfma_f32_16x16x32_bf16 v[20:23], v[64:67], v[36:39], 0\n"
    "s_waitcnt vmcnt(18)\n"
    "v_mfma_f32_16x16x32_bf16 v[16:19], v[68:71], v[40:43], v[16:19]\n"
    "v_mfma_f32_16x16x32_bf16 v[20:23], v[68:71], v[44:47], v[20:23]\n"
    "s_waitcnt vmcnt(17)\n"
    "v_mfma_f32_16x16x32_bf16 v[16:19], v[72:75], v[48:51], v[16:19]\n"
    "v_mfma_f32_16x16x32_bf16 v[20:23], v[72:75], v[52:55], v[20:23]\n"
    "s_waitcnt vmcnt(16)\n"
    "v_mfma_f32_16x16x32_bf16 v[16:19], v[76:79], v[56:59], v[16:19]\n"
    "v_mfma_f32_16x16x32_bf16 v[20:23], v[76:79], v[60:63], v[20:23]\n"
    "s_waitcnt vmcnt(15)\n"
    "v_smfmac_f32_16x16x64_bf16 v[12:15], v[80:83], a[0:7], v2\n"
    "s_waitcnt vmcnt(14)\n"
    "v_smfmac_f32_16x16x64_bf16 v[12:15], v[84:87], a[8:15], v2\n"
    "s_waitcnt vmcnt(13)\n"
    "v_smfmac_f32_16x16x64_bf16 v[12:15], v[88:91], a[16:23], v2\n"
    "s_waitcnt vmcnt(12)\n"
    "v_smfmac_f32_16x16x64_bf16 v[12:15], v[92:95], a[24:31], v2\n"
    "s_waitcnt vmcnt(11)\n"
    "v_smfmac_f32_16x16x64_bf16 v[12:15], v[96:99], a[32:39], v2\n"
    "s_waitcnt vmcnt(10)\n"
    "v_smfmac_f32_16x16x64_bf16 v[12:15], v[100:103], a[40:47], v2\n"
    "v_permlane32_swap_b32 v20, v18\n"
    "s_waitcnt vmcnt(9)\n"
    "v_smfmac_f32_16x16x64_bf16 v[12:15], v[104:107], a[48:55], v2\n"
    "v_permlane32_swap_b32 v21, v19\n"
    "s_waitcnt vmcnt(8)\n"
    "v_smfmac_f32_16x16x64_bf16 v[12:15], v[108:111], a[56:63], v2\n"
    "v_add_f32 v16, v16, v18\n"
    "s_waitcnt vmcnt(7)\n"
    "v_smfmac_f32_16x16x64_bf16 v[12:15], v[112:115], a[64:71], v2\n"
    "v_add_f32 v17, v17, v19\n"
    "s_waitcnt vmcnt(6)\n"
    "v_smfmac_f32_16x16x64_bf16 v[12:15], v[116:119], a[72:79], v2\n"
    "v_add_f32 v20, v20, v22\n"
    "s_waitcnt vmcnt(5)\n"
    "v_smfmac_f32_16x16x64_bf16 v[12:15], v[120:123], a[80:87], v2\n"
    "v_add_f32 v21, v21, v23\n"
    "s_waitcnt vmcnt(4)\n"
    "v_smfmac_f32_16x16x64_bf16 v[12:15], v[124:127], a[88:95], v2\n"
    "v_cndmask_b32 v16, v16, v20, s[34:35]\n"
    "s_waitcnt vmcnt(3)\n"
    "v_smfmac_f32_16x16x64_bf16 v[12:15], v[128:131], a[96:103], v2\n"
    "v_cndmask_b32 v17, v17, v21, s[34:35]\n"
    "s_waitcnt vmcnt(2)\n"
    "v_smfmac_f32_16x16x64_bf16 v[12:15], v[132:135], a[104:111], v2\n"
    "s_waitcnt vmcnt(1)\n"
    "v_smfmac_f32_16x16x64_bf16 v[12:15], v[136:139], a[112:119], v2\n"
    "s_waitcnt vmcnt(0)\n"
    "v_smfmac_f32_16x16x64_bf16 v[12:15], v[140:143], a[120:127], v2\n"
    "s_nop 7\n"
    "v_pk_add_f32 v[16:17], v[16:17], v[12:13]\n"
    "v_pk_add_f32 v[16:17], v[16:17], v[14:15]\n"
    "v_cmp_u_f32 vcc, v16, v17\n"
    "v_cvt_pk_bf16_f32 v24, v16, v17\n"
    "s_nop 1\n"
    "v_mov_b32_dpp v25, v24 quad_perm:[1,0,3,2] row_mask:0xf bank_mask:0xf\n"
    "v_perm_b32 v24, v24, v25, v9\n"
    "s_cbranch_vccnz .LC_again\n"
    ".LC_store:\n"
    "buffer_store_dword v24, v7, s[8:11], s68 offen sc1\n"
    "buffer_store_dword v24, v7, s[8:11], s69 offen sc1\n"
    "s_cmp_eq_u32 s37, 0\n"
    "s_cbranch_scc1 .LC_paced\n"
    "s_add_u32 s41, s24, s4\n"
    "s_memrealtime s[42:43]\n"
    "s_waitcnt vmcnt(2)\n"
    ".Lhp_110:\n"
    "v_cmp_ne_u32 vcc, s41, v147\n"
    "s_nop 1\n"
    "s_cmp_eq_u32 vcc_lo, 0\n"
    "s_cbranch_scc1 .Lhd_111\n"
    "s_mov_b32 exec_hi, 0\n"
    "buffer_load_dwordx2 v[146:147], v8, s[8:11], s33 offen sc1\n"
    "s_mov_b32 exec_hi, -1\n"
    "s_waitcnt lgkmcnt(0)\n"
    "s_memrealtime s[42:43]\n"
    "s_waitcnt vmcnt(0)\n"
    "s_sub_u32 s25, s25, 1\n"
    "s_cbranch_scc0 .Lhp_110\n"
    "s_branch .Lfail\n"
    ".Lhd_111:\n"
    "s_waitcnt lgkmcnt(0)\n"
    "s_mul_i32 s42, s42, 6\n"
    "v_subrev_u32 v146, s42, v146\n"
    "s_nop 1\n"
    "v_max_i32_dpp v146, v146, v146 row_ror:8 row_mask:0xf bank_mask:0xf\n"
    "s_nop 1\n"
    "v_max_i32_dpp v146, v146, v146 row_ror:4 row_mask:0xf bank_mask:0xf\n"
    "s_nop 1\n"
    "v_max_i32_dpp v146, v146, v146 row_ror:2 row_mask:0xf bank_mask:0xf\n"
    "s_nop 1\n"
    "v_max_i32_dpp v146, v146, v146 row_ror:1 row_mask:0xf bank_mask:0xf\n"
    "s_nop 0\n"
    "v_readlane_b32 s40, v146, 0\n"
    "v_readlane_b32 s41, v146, 16\n"
    "s_nop 0\n"
    "s_max_i32 s40, s40, s41\n"
    "s_min_i32 s40, s40, 2000\n"
    "s_add_u32 s40, s40, s42\n"
    "s_sub_u32 s41, s40, s59\n"
    "s_mov_b32 s59, s40\n"
    "s_sub_u32 s41, s41, s82\n"
    "s_add_u32 s83, s83, 4\n"
    "s_min_u32 s83, s83, s41\n"
    "s_sub_u32 s41, s41, s83\n"
    "s_cmp_gt_u32 s41, 90\n"
    "s_cselect_b32 s41, 12, 0\n"
    "s_add_u32 s82, s82, s41\n"
    "s_cmp_lt_u32 s24, 46\n"
    "s_cselect_b32 s41, 1, 4\n"
    "s_sub_u32 s82, s82, s41\n"
    "s_cmp_lt_u32 s24, 46\n"
    "s_cselect_b32 s41, 40, 0\n"
    "s_max_i32 s82, s82, s41\n"
    "s_min_i32 s82, s82, 100\n"
    ".LC_paced:\n"
    "s_add_u32 s24, s24, 1\n"
    "s_add_u32 s26, s26, 65536\n"
    "s_cmp_eq_u32 s26, 294912\n"
    "s_cselect_b32 s26, 98304, s26\n"
    "s_add_u32 s28, s28, 32768\n"
    "s_cmp_eq_u32 s28, 98304\n"
    "s_cselect_b32 s28, 0, s28\n"
    "s_cmp_lt_u32 s24, 63\n"
    "s_cbranch_scc1 .LC_idle\n"
    "s_endpgm\n"
    ".LC_again:\n"
    "s_sub_u32 s38, s38, 1\n"
    "s_cbranch_scc0 .LC_retry\n"
    "s_mov_b32 s38, 0\n"
    "v_mov_b32 v28, 0\n"
    "v_mov_b32 v29, 0\n"
    "v_mov_b32 v30, 0\n"
    "v_mov_b32 v31, 0\n"
    "v_max3_u32 v28, v28, v32, v33\n"
    "v_max3_u32 v29, v29, v34, v35\n"
    "v_max3_u32 v30, v30, v36, v37\n"
    "v_max3_u32 v31, v31, v38, v39\n"
    "v_max3_u32 v28, v28, v40, v41\n"
    "v_max3_u32 v29, v29, v42, v43\n"
    "v_max3_u32 v30, v30, v44, v45\n"
    "v_max3_u32 v31, v31, v46, v47\n"
    "v_max3_u32 v28, v28, v48, v49\n"
    "v_max3_u32 v29, v29, v50, v51\n"
    "v_max3_u32 v30, v30, v52, v53\n"
    "v_max3_u32 v31, v31, v54, v55\n"
    "v_max3_u32 v28, v28, v56, v57\n"
    "v_max3_u32 v29, v29, v58, v59\n"
    "v_max3_u32 v30, v30, v60, v61\n"
    "v_max3_u32 v31, v31, v62, v63\n"
    "v_max3_u32 v28, v28, v64, v65\n"
    "v_max3_u32 v29, v29, v66, v67\n"
    "v_max3_u32 v30, v30, v68, v69\n"
    "v_max3_u32 v31, v31, v70, v71\n"
    "v_max3_u32 v28, v28, v72, v73\n"
    "v_max3_u32 v29, v29, v74, v75\n"
    "v_max3_u32 v30, v30, v76, v77\n"
    "v_max3_u32 v31, v31, v78, v79\n"
    "v_max3_u32 v28, v28, v80, v81\n"
    "v_max3_u32 v29, v29, v82, v83\n"
    "v_max3_u32 v30, v30, v84, v85\n"
    "v_max3_u32 v31, v31, v86, v87\n"
    "v_max3_u32 v28, v28, v88, v89\n"
    "v_max3_u32 v29, v29, v90, v91\n"
    "v_max3_u32 v30, v30, v92, v93\n"
    "v_max3_u32 v31, v31, v94, v95\n"
    "v_max3_u32 v28, v28, v96, v97\n"
    "v_max3_u32 v29, v29, v98, v99\n"
    "v_max3_u32 v30, v30, v100, v101\n"
    "v_max3_u32 v31, v31, v102, v103\n"
    "v_max3_u32 v28, v28, v104, v105\n"
    "v_max3_u32 v29, v29, v106, v107\n"
    "v_max3_u32 v30, v30, v108, v109\n"
    "v_max3_u32 v31, v31, v110, v111\n"
    "v_max3_u32 v28, v28, v112, v113\n"
    "v_max3_u32 v29, v29, v114, v115\n"
    "v_max3_u32 v30, v30, v116, v117\n"
    "v_max3_u32 v31, v31, v118, v119\n"
    "v_max3_u32 v28, v28, v120, v121\n"
    "v_max3_u32 v29, v29, v122, v123\n"
    "v_max3_u32 v30, v30, v124, v125\n"
    "v_max3_u32 v31, v31, v126, v127\n"
    "v_max3_u32 v28, v28, v128, v129\n"
    "v_max3_u32 v29, v29, v130, v131\n"
    "v_max3_u32 v30, v30, v132, v133\n"
    "v_max3_u32 v31, v31, v134, v135\n"
    "v_max3_u32 v28, v28, v136, v137\n"
    "v_max3_u32 v29, v29, v138, v139\n"
    "v_max3_u32 v30, v30, v140, v141\n"
    "v_max3_u32 v31, v31, v142, v143\n"
    "v_max3_u32 v28, v28, v29, v30\n"
    "v_max_u32 v28, v28, v31\n"
    "v_cmp_eq_u32 vcc, -1, v28\n"
    "s_nop 1\n"
    "s_cbranch_vccz .LC_store\n"
    "s_sub_u32 s25, s25, 1\n"
    "s_nop 0\n"
    "s_cbranch_scc0 .LC_retry\n"
    "s_branch .Lfail\n"
    ".Lrole_D:\n"
    "s_mov_b32 s12, s78\n"
    "s_and_b32 s13, s79, 0xffff\n"
    "s_mov_b32 s14, 1966080\n"
    "s_mov_b32 s15, 0x20000\n"
    "s_and_b32 s34, s20, 1\n"
    "s_lshr_b32 s35, s21, 1\n"
    "s_lshl_b32 s40, s34, 1\n"
    "s_add_u32 s40, s40, s35\n"
    "s_mul_i32 s48, s40, 491520\n"
    "s_lshr_b32 s40, s20, 1\n"
    "s_mul_i32 s41, s40, 320\n"
    "s_add_u32 s48, s48, s41\n"
    "s_and_b32 s41, s21, 1\n"
    "s_lshl_b32 s40, s40, 1\n"
    "s_add_u32 s36, s40, s41\n"
    "s_mul_i32 s26, s21, 5120\n"
    "s_add_u32 s26, s26, 0\n"
    "s_and_b32 s40, s21, 1\n"
    "s_mul_i32 s41, s40, 320\n"
    "v_add_u32 v10, s41, v1\n"
    "s_add_u32 s41, s41, 64\n"
    "v_mul_u32_u24 v0, 3277, v10\n"
    "v_lshrrev_b32 v0, 16, v0\n"
    "v_mul_u32_u24 v15, 20, v0\n"
    "v_sub_u32 v10, v10, v15\n"
    "v_mul_u32_u24 v0, 1280, v0\n"
    "v_lshl_add_u32 v10, v10, 4, v0\n"
    "v_add_u32 v11, s41, v1\n"
    "s_add_u32 s41, s41, 64\n"
    "v_mul_u32_u24 v0, 3277, v11\n"
    "v_lshrrev_b32 v0, 16, v0\n"
    "v_mul_u32_u24 v15, 20, v0\n"
    "v_sub_u32 v11, v11, v15\n"
    "v_mul_u32_u24 v0, 1280, v0\n"
    "v_lshl_add_u32 v11, v11, 4, v0\n"
    "v_add_u32 v12, s41, v1\n"
    "s_add_u32 s41, s41, 64\n"
    "v_mul_u32_u24 v0, 3277, v12\n"
    "v_lshrrev_b32 v0, 16, v0\n"
    "v_mul_u32_u24 v15, 20, v0\n"
    "v_sub_u32 v12, v12, v15\n"
    "v_mul_u32_u24 v0, 1280, v0\n"
    "v_lshl_add_u32 v12, v12, 4, v0\n"
    "v_add_u32 v13, s41, v1\n"
    "s_add_u32 s41, s41, 64\n"
    "v_mul_u32_u24 v0, 3277, v13\n"
    "v_lshrrev_b32 v0, 16, v0\n"
    "v_mul_u32_u24 v15, 20, v0\n"
    "v_sub_u32 v13, v13, v15\n"
    "v_mul_u32_u24 v0, 1280, v0\n"
    "v_lshl_add_u32 v13, v13, 4, v0\n"
    "v_add_u32 v14, s41, v1\n"
    "s_add_u32 s41, s41, 64\n"
    "v_mul_u32_u24 v0, 3277, v14\n"
    "v_lshrrev_b32 v0, 16, v0\n"
    "v_mul_u32_u24 v15, 20, v0\n"
    "v_sub_u32 v14, v14, v15\n"
    "v_mul_u32_u24 v0, 1280, v0\n"
    "v_lshl_add_u32 v14, v14, 4, v0\n"
    "s_mul_i32 s40, s40, 4960\n"
    "s_sub_u32 s40, s26, s40\n"
    "v_bfe_u32 v15, v1, 4, 2\n"
    "v_lshlrev_b32 v15, 3, v15\n"
    "v_bfe_u32 v0, v1, 2, 2\n"
    "v_lshl_add_u32 v15, v0, 0, v15\n"
    "v_mul_u32_u24 v15, 320, v15\n"
    "v_and_b32 v0, 3, v1\n"
    "v_mul_u32_u24 v0, 8, v0\n"
    "v_add3_u32 v15, v15, v0, s40\n"
    "v_add_u32 v16, 57344, v15\n"
    "v_add_u32 v17, 114688, v15\n"
    "s_add_u32 s40, s48, 0\n"
    "s_add_u32 m0, s26, 0\n"
    "s_nop 0\n"
    "buffer_load_dwordx4 v10, s[12:15], s40 offen nt lds\n"
    "s_add_u32 m0, s26, 1024\n"
    "s_nop 0\n"
    "buffer_load_dwordx4 v11, s[12:15], s40 offen nt lds\n"
    "s_add_u32 m0, s26, 2048\n"
    "s_nop 0\n"
    "buffer_load_dwordx4 v12, s[12:15], s40 offen nt lds\n"
    "s_add_u32 m0, s26, 3072\n"
    "s_nop 0\n"
    "buffer_load_dwordx4 v13, s[12:15], s40 offen nt lds\n"
    "s_add_u32 m0, s26, 4096\n"
    "s_nop 0\n"
    "buffer_load_dwordx4 v14, s[12:15], s40 offen nt lds\n"
    "s_add_u32 s40, s48, 40960\n"
    "s_add_u32 m0, s26, 20480\n"
    "s_nop 0\n"
    "buffer_load_dwordx4 v10, s[12:15], s40 offen nt lds\n"
    "s_add_u32 m0, s26, 21504\n"
    "s_nop 0\n"
    "buffer_load_dwordx4 v11, s[12:15], s40 offen nt lds\n"
    "s_add_u32 m0, s26, 22528\n"
    "s_nop 0\n"
    "buffer_load_dwordx4 v12, s[12:15], s40 offen nt lds\n"
    "s_add_u32 m0, s26, 23552\n"
    "s_nop 0\n"
    "buffer_load_dwordx4 v13, s[12:15], s40 offen nt lds\n"
    "s_add_u32 m0, s26, 24576\n"
    "s_nop 0\n"
    "buffer_load_dwordx4 v14, s[12:15], s40 offen nt lds\n"
    "s_add_u32 s40, s48, 81920\n"
    "s_add_u32 m0, s26, 40960\n"
    "s_nop 0\n"
    "buffer_load_dwordx4 v10, s[12:15], s40 offen nt lds\n"
    "s_add_u32 m0, s26, 41984\n"
    "s_nop 0\n"
    "buffer_load_dwordx4 v11, s[12:15], s40 offen nt lds\n"
    "s_add_u32 m0, s26, 43008\n"
    "s_nop 0\n"
    "buffer_load_dwordx4 v12, s[12:15], s40 offen nt lds\n"
    "s_add_u32 m0, s26, 44032\n"
    "s_nop 0\n"
    "buffer_load_dwordx4 v13, s[12:15], s40 offen nt lds\n"
    "s_add_u32 m0, s26, 45056\n"
    "s_nop 0\n"
    "buffer_load_dwordx4 v14, s[12:15], s40 offen nt lds\n"
    "s_add_u32 s40, s48, 122880\n"
    "s_add_u32 m0, s26, 61440\n"
    "s_nop 0\n"
    "buffer_load_dwordx4 v10, s[12:15], s40 offen nt lds\n"
    "s_add_u32 m0, s26, 62464\n"
    "s_nop 0\n"
    "buffer_load_dwordx4 v11, s[12:15], s40 offen nt lds\n"
    "s_add_u32 m0, s26, 63488\n"
    "s_nop 0\n"
    "buffer_load_dwordx4 v12, s[12:15], s40 offen nt lds\n"
    "s_add_u32 m0, s26, 64512\n"
    "s_nop 0\n"
    "buffer_load_dwordx4 v13, s[12:15], s40 offen nt lds\n"
    "s_add_u32 m0, s26, 65536\n"
    "s_nop 0\n"
    "buffer_load_dwordx4 v14, s[12:15], s40 offen nt lds\n"
    "s_add_u32 s40, s48, 163840\n"
    "s_add_u32 m0, s26, 81920\n"
    "s_nop 0\n"
    "buffer_load_dwordx4 v10, s[12:15], s40 offen nt lds\n"
    "s_add_u32 m0, s26, 82944\n"
    "s_nop 0\n"
    "buffer_load_dwordx4 v11, s[12:15], s40 offen nt lds\n"
    "s_add_u32 m0, s26, 83968\n"
    "s_nop 0\n"
    "buffer_load_dwordx4 v12, s[12:15], s40 offen nt lds\n"
    "s_add_u32 m0, s26, 84992\n"
    "s_nop 0\n"
    "buffer_load_dwordx4 v13, s[12:15], s40 offen nt lds\n"
    "s_add_u32 m0, s26, 86016\n"
    "s_nop 0\n"
    "buffer_load_dwordx4 v14, s[12:15], s40 offen nt lds\n"
    "s_add_u32 s40, s48, 204800\n"
    "s_add_u32 m0, s26, 102400\n"
    "s_nop 0\n"
    "buffer_load_dwordx4 v10, s[12:15], s40 offen nt lds\n"
    "s_add_u32 m0, s26, 103424\n"
    "s_nop 0\n"
    "buffer_load_dwordx4 v11, s[12:15], s40 offen nt lds\n"
    "s_add_u32 m0, s26, 104448\n"
    "s_nop 0\n"
    "buffer_load_dwordx4 v12, s[12:15], s40 offen nt lds\n"
    "s_add_u32 m0, s26, 105472\n"
    "s_nop 0\n"
    "buffer_load_dwordx4 v13, s[12:15], s40 offen nt lds\n"
    "s_add_u32 m0, s26, 106496\n"
    "s_nop 0\n"
    "buffer_load_dwordx4 v14, s[12:15], s40 offen nt lds\n"
    "s_add_u32 s40, s48, 245760\n"
    "s_add_u32 m0, s26, 122880\n"
    "s_nop 0\n"
    "buffer_load_dwordx4 v10, s[12:15], s40 offen nt lds\n"
    "s_add_u32 m0, s26, 123904\n"
    "s_nop 0\n"
    "buffer_load_dwordx4 v11, s[12:15], s40 offen nt lds\n"
    "s_add_u32 m0, s26, 124928\n"
    "s_nop 0\n"
    "buffer_load_dwordx4 v12, s[12:15], s40 offen nt lds\n"
    "s_add_u32 m0, s26, 125952\n"
    "s_nop 0\n"
    "buffer_load_dwordx4 v13, s[12:15], s40 offen nt lds\n"
    "s_add_u32 m0, s26, 126976\n"
    "s_nop 0\n"
    "buffer_load_dwordx4 v14, s[12:15], s40 offen nt lds\n"
    "s_waitcnt vmcnt(30)\n"
    "s_barrier\n"
    "ds_read_b64_tr_b16 a[0:1], v15 offset:0\n"
    "ds_read_b64_tr_b16 a[2:3], v15 offset:1280\n"
    "ds_read_b64_tr_b16 a[4:5], v15 offset:32\n"
    "ds_read_b64_tr_b16 a[6:7], v15 offset:1312\n"
    "ds_read_b64_tr_b16 a[8:9], v15 offset:64\n"
    "ds_read_b64_tr_b16 a[10:11], v15 offset:1344\n"
    "ds_read_b64_tr_b16 a[12:13], v15 offset:96\n"
    "ds_read_b64_tr_b16 a[14:15], v15 offset:1376\n"
    "ds_read_b64_tr_b16 a[16:17], v15 offset:128\n"
    "ds_read_b64_tr_b16 a[18:19], v15 offset:1408\n"
    "s_waitcnt lgkmcnt(0)\n"
    "s_waitcnt vmcnt(25)\n"
    "s_barrier\n"
    "ds_read_b64_tr_b16 a[20:21], v15 offset:20480\n"
    "ds_read_b64_tr_b16 a[22:23], v15 offset:21760\n"
    "ds_read_b64_tr_b16 a[24:25], v15 offset:20512\n"
    "ds_read_b64_tr_b16 a[26:27], v15 offset:21792\n"
    "ds_read_b64_tr_b16 a[28:29], v15 offset:20544\n"
    "ds_read_b64_tr_b16 a[30:31], v15 offset:21824\n"
    "ds_read_b64_tr_b16 a[32:33], v15 offset:20576\n"
    "ds_read_b64_tr_b16 a[34:35], v15 offset:21856\n"
    "ds_read_b64_tr_b16 a[36:37], v15 offset:20608\n"
    "ds_read_b64_tr_b16 a[38:39], v15 offset:21888\n"
    "s_add_u32 s40, s48, 286720\n"
    "s_add_u32 m0, s26, 0\n"
    "s_nop 0\n"
    "buffer_load_dwordx4 v10, s[12:15], s40 offen nt lds\n"
    "s_add_u32 m0, s26, 1024\n"
    "s_nop 0\n"
    "buffer_load_dwordx4 v11, s[12:15], s40 offen nt lds\n"
    "s_add_u32 m0, s26, 2048\n"
    "s_nop 0\n"
    "buffer_load_dwordx4 v12, s[12:15], s40 offen nt lds\n"
    "s_add_u32 m0, s26, 3072\n"
    "s_nop 0\n"
    "buffer_load_dwordx4 v13, s[12:15], s40 offen nt lds\n"
    "s_add_u32 m0, s26, 4096\n"
    "s_nop 0\n"
    "buffer_load_dwordx4 v14, s[12:15], s40 offen nt lds\n"
    "s_waitcnt lgkmcnt(0)\n"
    "s_waitcnt vmcnt(25)\n"
    "s_barrier\n"
    "ds_read_b64_tr_b16 a[40:41], v15 offset:40960\n"
    "ds_read_b64_tr_b16 a[42:43], v15 offset:42240\n"
    "ds_read_b64_tr_b16 a[44:45], v15 offset:40992\n"
    "ds_read_b64_tr_b16 a[46:47], v15 offset:42272\n"
    "ds_read_b64_tr_b16 a[48:49], v15 offset:41024\n"
    "ds_read_b64_tr_b16 a[50:51], v15 offset:42304\n"
    "ds_read_b64_tr_b16 a[52:53], v15 offset:41056\n"
    "ds_read_b64_tr_b16 a[54:55], v15 offset:42336\n"
    "ds_read_b64_tr_b16 a[56:57], v15 offset:41088\n"
    "ds_read_b64_tr_b16 a[58:59], v15 offset:42368\n"
    "s_add_u32 s40, s48, 327680\n"
    "s_add_u32 m0, s26, 20480\n"
    "s_nop 0\n"
    "buffer_load_dwordx4 v10, s[12:15], s40 offen nt lds\n"
    "s_add_u32 m0, s26, 21504\n"
    "s_nop 0\n"
    "buffer_load_dwordx4 v11, s[12:15], s40 offen nt lds\n"
    "s_add_u32 m0, s26, 22528\n"
    "s_nop 0\n"
    "buffer_load_dwordx4 v12, s[12:15], s40 offen nt lds\n"
    "s_add_u32 m0, s26, 23552\n"
    "s_nop 0\n"
    "buffer_load_dwordx4 v13, s[12:15], s40 offen nt lds\n"
    "s_add_u32 m0, s26, 24576\n"
    "s_nop 0\n"
    "buffer_load_dwordx4 v14, s[12:15], s40 offen nt lds\n"
    "s_waitcnt lgkmcnt(0)\n"
    "s_waitcnt vmcnt(25)\n"
    "s_barrier\n"
    "ds_read_b64_tr_b16 a[60:61], v16 offset:4096\n"
    "ds_read_b64_tr_b16 a[62:63], v16 offset:5376\n"
    "ds_read_b64_tr_b16 a[64:65], v16 offset:4128\n"
    "ds_read_b64_tr_b16 a[66:67], v16 offset:5408\n"
    "ds_read_b64_tr_b16 a[68:69], v16 offset:4160\n"
    "ds_read_b64_tr_b16 a[70:71], v16 offset:5440\n"
    "ds_read_b64_tr_b16 a[72:73], v16 offset:4192\n"
    "ds_read_b64_tr_b16 a[74:75], v16 offset:5472\n"
    "ds_read_b64_tr_b16 a[76:77], v16 offset:4224\n"
    "ds_read_b64_tr_b16 a[78:79], v16 offset:5504\n"
    "s_add_u32 s40, s48, 368640\n"
    "s_add_u32 m0, s26, 40960\n"
    "s_nop 0\n"
    "buffer_load_dwordx4 v10, s[12:15], s40 offen nt lds\n"
    "s_add_u32 m0, s26, 41984\n"
    "s_nop 0\n"
    "buffer_load_dwordx4 v11, s[12:15], s40 offen nt lds\n"
    "s_add_u32 m0, s26, 43008\n"
    "s_nop 0\n"
    "buffer_load_dwordx4 v12, s[12:15], s40 offen nt lds\n"
    "s_add_u32 m0, s26, 44032\n"
    "s_nop 0\n"
    "buffer_load_dwordx4 v13, s[12:15], s40 offen nt lds\n"
    "s_add_u32 m0, s26, 45056\n"
    "s_nop 0\n"
    "buffer_load_dwordx4 v14, s[12:15], s40 offen nt lds\n"
    "s_waitcnt lgkmcnt(0)\n"
    "s_add_u32 s4, s3, 1\n"
    "v_mov_b32 v2, s3\n"
    "s_or_b32 s41, s21, s6\n"
    "s_cbranch_scc1 .Lcl_112\n"
    "s_lshl_b32 s41, s5, 2\n"
    "s_add_u32 s41, s41, 4178944\n"
    "v_mov_b32 v3, s41\n"
    "v_mov_b32 v2, s4\n"
    "s_mov_b64 exec, 1\n"
    "buffer_atomic_swap v2, v3, s[8:11], 0 offen sc0 sc1\n"
    "s_mov_b64 exec, -1\n"
    ".Lcl_112:\n"
    "v_min_u32 v6, 31, v1\n"
    "v_lshlrev_b32 v6, 7, v6\n"
    "v_add_u32 v6, 4182016, v6\n"
    "buffer_load_dwordx2 v[4:5], v6, s[8:11], 0 offen sc1\n"
    "s_waitcnt lgkmcnt(0)\n"
    "s_waitcnt vmcnt(26)\n"
    "s_barrier\n"
    "ds_read_b64_tr_b16 a[80:81], v16 offset:24576\n"
    "ds_read_b64_tr_b16 a[82:83], v16 offset:25856\n"
    "ds_read_b64_tr_b16 a[84:85], v16 offset:24608\n"
    "ds_read_b64_tr_b16 a[86:87], v16 offset:25888\n"
    "ds_read_b64_tr_b16 a[88:89], v16 offset:24640\n"
    "ds_read_b64_tr_b16 a[90:91], v16 offset:25920\n"
    "ds_read_b64_tr_b16 a[92:93], v16 offset:24672\n"
    "ds_read_b64_tr_b16 a[94:95], v16 offset:25952\n"
    "ds_read_b64_tr_b16 a[96:97], v16 offset:24704\n"
    "ds_read_b64_tr_b16 a[98:99], v16 offset:25984\n"
    "s_add_u32 s40, s48, 409600\n"
    "s_add_u32 m0, s26, 61440\n"
    "s_nop 0\n"
    "buffer_load_dwordx4 v10, s[12:15], s40 offen nt lds\n"
    "s_add_u32 m0, s26, 62464\n"
    "s_nop 0\n"
    "buffer_load_dwordx4 v11, s[12:15], s40 offen nt lds\n"
    "s_add_u32 m0, s26, 63488\n"
    "s_nop 0\n"
    "buffer_load_dwordx4 v12, s[12:15], s40 offen nt lds\n"
    "s_add_u32 m0, s26, 64512\n"
    "s_nop 0\n"
    "buffer_load_dwordx4 v13, s[12:15], s40 offen nt lds\n"
    "s_add_u32 m0, s26, 65536\n"
    "s_nop 0\n"
    "buffer_load_dwordx4 v14, s[12:15], s40 offen nt lds\n"
    "s_waitcnt lgkmcnt(0)\n"
    "s_waitcnt vmcnt(26)\n"
    "s_barrier\n"
    "ds_read_b64_tr_b16 a[100:101], v16 offset:45056\n"
    "ds_read_b64_tr_b16 a[102:103], v16 offset:46336\n"
    "ds_read_b64_tr_b16 a[104:105], v16 offset:45088\n"
    "ds_read_b64_tr_b16 a[106:107], v16 offset:46368\n"
    "ds_read_b64_tr_b16 a[108:109], v16 offset:45120\n"
    "ds_read_b64_tr_b16 a[110:111], v16 offset:46400\n"
    "ds_read_b64_tr_b16 a[112:113], v16 offset:45152\n"
    "ds_read_b64_tr_b16 a[114:115], v16 offset:46432\n"
    "ds_read_b64_tr_b16 a[116:117], v16 offset:45184\n"
    "ds_read_b64_tr_b16 a[118:119], v16 offset:46464\n"
    "s_add_u32 s40, s48, 450560\n"
    "s_add_u32 m0, s26, 81920\n"
    "s_nop 0\n"
    "buffer_load_dwordx4 v10, s[12:15], s40 offen nt lds\n"
    "s_add_u32 m0, s26, 82944\n"
    "s_nop 0\n"
    "buffer_load_dwordx4 v11, s[12:15], s40 offen nt lds\n"
    "s_add_u32 m0, s26, 83968\n"
    "s_nop 0\n"
    "buffer_load_dwordx4 v12, s[12:15], s40 offen nt lds\n"
    "s_add_u32 m0, s26, 84992\n"
    "s_nop 0\n"
    "buffer_load_dwordx4 v13, s[12:15], s40 offen nt lds\n"
    "s_add_u32 m0, s26, 86016\n"
    "s_nop 0\n"
    "buffer_load_dwordx4 v14, s[12:15], s40 offen nt lds\n"
    "s_waitcnt lgkmcnt(0)\n"
    "s_waitcnt vmcnt(26)\n"
    "s_barrier\n"
    "ds_read_b64_tr_b16 a[120:121], v17 offset:8192\n"
    "ds_read_b64_tr_b16 a[122:123], v17 offset:9472\n"
    "ds_read_b64_tr_b16 a[124:125], v17 offset:8224\n"
    "ds_read_b64_tr_b16 a[126:127], v17 offset:9504\n"
    "ds_read_b64_tr_b16 a[128:129], v17 offset:8256\n"
    "ds_read_b64_tr_b16 a[130:131], v17 offset:9536\n"
    "ds_read_b64_tr_b16 a[132:133], v17 offset:8288\n"
    "ds_read_b64_tr_b16 a[134:135], v17 offset:9568\n"
    "ds_read_b64_tr_b16 a[136:137], v17 offset:8320\n"
    "ds_read_b64_tr_b16 a[138:139], v17 offset:9600\n"
    "s_waitcnt lgkmcnt(0)\n"
    "s_waitcnt vmcnt(21)\n"
    "s_barrier\n"
    "ds_read_b64_tr_b16 a[140:141], v15 offset:0\n"
    "ds_read_b64_tr_b16 a[142:143], v15 offset:1280\n"
    "ds_read_b64_tr_b16 a[144:145], v15 offset:32\n"
    "ds_read_b64_tr_b16 a[146:147], v15 offset:1312\n"
    "ds_read_b64_tr_b16 a[148:149], v15 offset:64\n"
    "ds_read_b64_tr_b16 a[150:151], v15 offset:1344\n"
    "ds_read_b64_tr_b16 a[152:153], v15 offset:96\n"
    "ds_read_b64_tr_b16 a[154:155], v15 offset:1376\n"
    "ds_read_b64_tr_b16 a[156:157], v15 offset:128\n"
    "ds_read_b64_tr_b16 a[158:159], v15 offset:1408\n"
    "s_waitcnt lgkmcnt(0)\n"
    "s_waitcnt vmcnt(16)\n"
    "s_barrier\n"
    "ds_read_b64_tr_b16 a[160:161], v15 offset:20480\n"
    "ds_read_b64_tr_b16 a[162:163], v15 offset:21760\n"
    "ds_read_b64_tr_b16 a[164:165], v15 offset:20512\n"
    "ds_read_b64_tr_b16 a[166:167], v15 offset:21792\n"
    "ds_read_b64_tr_b16 a[168:169], v15 offset:20544\n"
    "ds_read_b64_tr_b16 a[170:171], v15 offset:21824\n"
    "ds_read_b64_tr_b16 a[172:173], v15 offset:20576\n"
    "ds_read_b64_tr_b16 a[174:175], v15 offset:21856\n"
    "ds_read_b64_tr_b16 a[176:177], v15 offset:20608\n"
    "ds_read_b64_tr_b16 a[178:179], v15 offset:21888\n"
    "s_waitcnt lgkmcnt(0)\n"
    "s_waitcnt vmcnt(11)\n"
    "s_barrier\n"
    "ds_read_b64_tr_b16 a[180:181], v15 offset:40960\n"
    "ds_read_b64_tr_b16 a[182:183], v15 offset:42240\n"
    "ds_read_b64_tr_b16 a[184:185], v15 offset:40992\n"
    "ds_read_b64_tr_b16 a[186:187], v15 offset:42272\n"
    "ds_read_b64_tr_b16 a[188:189], v15 offset:41024\n"
    "ds_read_b64_tr_b16 a[190:191], v15 offset:42304\n"
    "ds_read_b64_tr_b16 a[192:193], v15 offset:41056\n"
    "ds_read_b64_tr_b16 a[194:195], v15 offset:42336\n"
    "ds_read_b64_tr_b16 a[196:197], v15 offset:41088\n"
    "ds_read_b64_tr_b16 a[198:199], v15 offset:42368\n"
    "s_waitcnt lgkmcnt(0)\n"
    "s_waitcnt vmcnt(5)\n"
    "s_barrier\n"
    "ds_read_b64_tr_b16 a[200:201], v16 offset:4096\n"
    "ds_read_b64_tr_b16 a[202:203], v16 offset:5376\n"
    "ds_read_b64_tr_b16 a[204:205], v16 offset:4128\n"
    "ds_read_b64_tr_b16 a[206:207], v16 offset:5408\n"
    "ds_read_b64_tr_b16 a[208:209], v16 offset:4160\n"
    "ds_read_b64_tr_b16 a[210:211], v16 offset:5440\n"
    "ds_read_b64_tr_b16 a[212:213], v16 offset:4192\n"
    "ds_read_b64_tr_b16 a[214:215], v16 offset:5472\n"
    "ds_read_b64_tr_b16 a[216:217], v16 offset:4224\n"
    "ds_read_b64_tr_b16 a[218:219], v16 offset:5504\n"
    "s_waitcnt lgkmcnt(0)\n"
    "s_waitcnt vmcnt(0)\n"
    "s_barrier\n"
    "ds_read_b64_tr_b16 a[220:221], v16 offset:24576\n"
    "ds_read_b64_tr_b16 a[222:223], v16 offset:25856\n"
    "ds_read_b64_tr_b16 a[224:225], v16 offset:24608\n"
    "ds_read_b64_tr_b16 a[226:227], v16 offset:25888\n"
    "ds_read_b64_tr_b16 a[228:229], v16 offset:24640\n"
    "ds_read_b64_tr_b16 a[230:231], v16 offset:25920\n"
    "ds_read_b64_tr_b16 a[232:233], v16 offset:24672\n"
    "ds_read_b64_tr_b16 a[234:235], v16 offset:25952\n"
    "ds_read_b64_tr_b16 a[236:237], v16 offset:24704\n"
    "ds_read_b64_tr_b16 a[238:239], v16 offset:25984\n"
    "s_waitcnt lgkmcnt(0)\n"
    "v_cmp_ne_u32 vcc, s4, v4\n"
    "s_nop 1\n"
    "s_cmp_eq_u32 vcc_lo, 0\n"
    "s_cbranch_scc1 .Lrx_115\n"
    ".Lrp_113:\n"
    "v_min_u32 v6, 31, v1\n"
    "v_lshlrev_b32 v6, 7, v6\n"
    "v_add_u32 v6, 4182016, v6\n"
    "buffer_load_dwordx2 v[4:5], v6, s[8:11], 0 offen sc1\n"
    "s_waitcnt vmcnt(0)\n"
    "v_cmp_ne_u32 vcc, s4, v4\n"
    "s_nop 1\n"
    "s_cmp_eq_u32 vcc_lo, 0\n"
    "s_cbranch_scc1 .Lro_114\n"
    "s_sleep 2\n"
    "s_sub_u32 s25, s25, 1\n"
    "s_cbranch_scc0 .Lrp_113\n"
    "s_branch .Lfail\n"
    ".Lro_114:\n"
    ".Lrx_115:\n"
    "s_cmp_lg_u32 s21, 0\n"
    "s_cbranch_scc1 .Lgt_116\n"
    "v_mov_b32 v3, 159744\n"
    "v_readfirstlane_b32 s40, v2\n"
    "s_nop 1\n"
    "v_mov_b32 v2, s40\n"
    "ds_write_b32 v3, v2\n"
    "s_waitcnt lgkmcnt(0)\n"
    ".Lgt_116:\n"
    "s_barrier\n"
    "v_mov_b32 v3, 159744\n"
    "ds_read_b32 v2, v3\n"
    "s_waitcnt lgkmcnt(0)\n"
    "v_readfirstlane_b32 s40, v2\n"
    "s_barrier\n"
    "s_cmp_eq_u32 s40, s4\n"
    "s_cbranch_scc1 .Llost\n"
    "v_lshlrev_b32 v2, 4, v1\n"
    "v_lshlrev_b32 v11, 3, v1\n"
    "s_mul_i32 s40, s35, 25600\n"
    "v_add_u32 v10, s40, v2\n"
    "v_xor_b32 v9, 1024, v10\n"
    "s_mul_i32 s40, s21, 5120\n"
    "s_add_u32 s40, s40, 98304\n"
    "v_add_u32 v3, s40, v2\n"
    "s_xor_b32 s40, s21, 2\n"
    "s_mul_i32 s40, s40, 5120\n"
    "s_add_u32 s40, s40, 98304\n"
    "v_add_u32 v4, s40, v2\n"
    "s_mul_i32 s29, s36, 5120\n"
    "s_mul_i32 s40, s34, 40960\n"
    "s_add_u32 s29, s29, s40\n"
    "s_lshl_b32 s40, s35, 9\n"
    "s_add_u32 s29, s29, s40\n"
    "s_add_u32 s29, s29, 155189248\n"
    "s_lshl_b32 s40, s20, 2\n"
    "s_add_u32 s40, s40, s21\n"
    "s_lshl_b32 s40, s40, 7\n"
    "s_add_u32 s40, s40, 4284416\n"
    "v_mov_b32 v5, s40\n"
    "v_cmp_ne_u32 vcc, 0, v1\n"
    "v_mov_b32 v0, 0x7ff00000\n"
    "s_nop 0\n"
    "v_cndmask_b32 v5, v5, v0, vcc\n"
    "s_lshl_b32 s40, s34, 2\n"
    "s_add_u32 s40, s40, s21\n"
    "s_mul_i32 s27, s40, 12288\n"
    "s_add_u32 s27, s27, 148897792\n"
    "s_mul_i32 s40, s40, 768\n"
    "s_add_u32 s40, s40, 4220928\n"
    "v_min_u32 v0, 5, v1\n"
    "v_lshl_add_u32 v6, v0, 7, s40\n"
    "s_mul_i32 s28, s21, 12288\n"
    "s_mov_b32 s24, 0\n"
    "v_mov_b32 v7, 0\n"
    "s_mov_b32 s30, 49152\n"
    "s_mov_b32 s31, 20480\n"
    "s_mov_b32 s41, 1\n"
    ".Lpl_117:\n"
    "buffer_load_dword v8, v6, s[8:11], 0 offen sc1\n"
    "s_waitcnt vmcnt(0)\n"
    "v_cmp_gt_u32 vcc, s41, v8\n"
    "s_nop 1\n"
    "s_cbranch_vccz .Lpd_118\n"
    "s_sleep 8\n"
    "s_sub_u32 s25, s25, 1\n"
    "s_cbranch_scc0 .Lpl_117\n"
    "s_branch .Lfail\n"
    ".Lpd_118:\n"
    "s_add_u32 m0, s28, 0\n"
    "s_add_u32 s40, s27, 0\n"
    "buffer_load_dwordx4 v2, s[8:11], s40 offen offset:0 sc1 lds\n"
    "buffer_load_dwordx4 v2, s[8:11], s40 offen offset:1024 sc1 lds\n"
    "buffer_load_dwordx4 v2, s[8:11], s40 offen offset:2048 sc1 lds\n"
    "buffer_load_dwordx4 v2, s[8:11], s40 offen offset:3072 sc1 lds\n"
    "s_add_u32 m0, s28, 4096\n"
    "s_add_u32 s40, s27, 4096\n"
    "buffer_load_dwordx4 v2, s[8:11], s40 offen offset:0 sc1 lds\n"
    "buffer_load_dwordx4 v2, s[8:11], s40 offen offset:1024 sc1 lds\n"
    "buffer_load_dwordx4 v2, s[8:11], s40 offen offset:2048 sc1 lds\n"
    "buffer_load_dwordx4 v2, s[8:11], s40 offen offset:3072 sc1 lds\n"
    "s_add_u32 m0, s28, 8192\n"
    "s_add_u32 s40, s27, 8192\n"
    "buffer_load_dwordx4 v2, s[8:11], s40 offen offset:0 sc1 lds\n"
    "buffer_load_dwordx4 v2, s[8:11], s40 offen offset:1024 sc1 lds\n"
    "buffer_load_dwordx4 v2, s[8:11], s40 offen offset:2048 sc1 lds\n"
    "buffer_load_dwordx4 v2, s[8:11], s40 offen offset:3072 sc1 lds\n"
    "s_waitcnt vmcnt(0)\n"
    "s_barrier\n"
    "ds_read_b128 v[52:55], v9 offset:0\n"
    "ds_read_b128 v[56:59], v9 offset:2048\n"
    "ds_read_b128 v[60:63], v9 offset:4096\n"
    "s_waitcnt lgkmcnt(0)\n"
    ".p2align 4\n"
    ".LD_loop:\n"
    "v_mfma_f32_16x16x32_bf16 v[12:15], a[0:3], v[52:55], 0\n"
    "ds_read_b128 v[64:67], v9 offset:6144\n"
    "v_mfma_f32_16x16x32_bf16 v[16:19], a[4:7], v[52:55], 0\n"
    "s_add_u32 s41, s24, 2\n"
    "s_cmp_le_u32 s41, 64\n"
    "s_cselect_b32 s40, 98304, 0\n"
    "v_mfma_f32_16x16x32_bf16 v[20:23], a[8:11], v[52:55], 0\n"
    "s_add_u32 s27, s27, s40\n"
    "s_min_u32 s41, s41, 64\n"
    "s_add_u32 s28, s28, s30\n"
    "v_mfma_f32_16x16x32_bf16 v[24:27], a[12:15], v[52:55], 0\n"
    "v_cmp_gt_u32 vcc, s41, v8\n"
    "s_cbranch_vccz .LDg_119\n"
    "s_waitcnt vmcnt(0)\n"
    "buffer_store_dword v7, v5, s[8:11], 0 offen sc1\n"
    "s_nop 3\n"
    ".Lpl_120:\n"
    "buffer_load_dword v8, v6, s[8:11], 0 offen sc1\n"
    "s_waitcnt vmcnt(0)\n"
    "v_cmp_gt_u32 vcc, s41, v8\n"
    "s_nop 1\n"
    "s_cbranch_vccz .Lpd_121\n"
    "s_sleep 4\n"
    "s_sub_u32 s25, s25, 1\n"
    "s_cbranch_scc0 .Lpl_120\n"
    "s_branch .Lfail\n"
    ".Lpd_121:\n"
    ".LDg_119:\n"
    "v_mfma_f32_16x16x32_bf16 v[28:31], a[16:19], v[52:55], 0\n"
    "s_add_u32 m0, s28, 0\n"
    "s_add_u32 s40, s27, 0\n"
    "buffer_load_dwordx4 v2, s[8:11], s40 offen offset:0 sc1 lds\n"
    "v_mfma_f32_16x16x32_bf16 v[12:15], a[20:23], v[56:59], v[12:15]\n"
    "ds_read_b128 v[52:55], v9 offset:8192\n"
    "v_mfma_f32_16x16x32_bf16 v[16:19], a[24:27], v[56:59], v[16:19]\n"
    "v_mfma_f32_16x16x32_bf16 v[20:23], a[28:31], v[56:59], v[20:23]\n"
    "v_add_f32 v68, v68, v32\n"
    "v_add_f32 v69, v69, v33\n"
    "buffer_load_dwordx4 v2, s[8:11], s40 offen offset:1024 sc1 lds\n"
    "v_mfma_f32_16x16x32_bf16 v[24:27], a[32:35], v[56:59], v[24:27]\n"
    "v_add_f32 v70, v70, v34\n"
    "v_add_f32 v71, v71, v35\n"
    "v_mfma_f32_16x16x32_bf16 v[28:31], a[36:39], v[56:59], v[28:31]\n"
    "v_add_f32 v72, v72, v36\n"
    "v_add_f32 v73, v73, v37\n"
    "v_mfma_f32_16x16x32_bf16 v[12:15], a[40:43], v[60:63], v[12:15]\n"
    "ds_read_b128 v[56:59], v9 offset:10240\n"
    "v_add_f32 v74, v74, v38\n"
    "v_add_f32 v75, v75, v39\n"
    "buffer_load_dwordx4 v2, s[8:11], s40 offen offset:2048 sc1 lds\n"
    "v_mfma_f32_16x16x32_bf16 v[16:19], a[44:47], v[60:63], v[16:19]\n"
    "v_add_f32 v76, v76, v40\n"
    "v_add_f32 v77, v77, v41\n"
    "v_mfma_f32_16x16x32_bf16 v[20:23], a[48:51], v[60:63], v[20:23]\n"
    "v_add_f32 v78, v78, v42\n"
    "v_add_f32 v79, v79, v43\n"
    "v_mfma_f32_16x16x32_bf16 v[24:27], a[52:55], v[60:63], v[24:27]\n"
    "v_add_f32 v80, v80, v44\n"
    "v_add_f32 v81, v81, v45\n"
    "buffer_load_dwordx4 v2, s[8:11], s40 offen offset:3072 sc1 lds\n"
    "v_mfma_f32_16x16x32_bf16 v[28:31], a[56:59], v[60:63], v[28:31]\n"
    "v_add_f32 v82, v82, v46\n"
    "v_add_f32 v83, v83, v47\n"
    "s_waitcnt lgkmcnt(2)\n"
    "v_mfma_f32_16x16x32_bf16 v[12:15], a[60:63], v[64:67], v[12:15]\n"
    "ds_read_b128 v[60:63], v9 offset:12288\n"
    "v_add_f32 v84, v84, v48\n"
    "v_add_f32 v85, v85, v49\n"
    "v_mfma_f32_16x16x32_bf16 v[16:19], a[64:67], v[64:67], v[16:19]\n"
    "v_add_f32 v86, v86, v50\n"
    "v_add_f32 v87, v87, v51\n"
    "s_add_u32 m0, s28, 4096\n"
    "s_add_u32 s40, s27, 4096\n"
    "buffer_load_dwordx4 v2, s[8:11], s40 offen offset:0 sc1 lds\n"
    "v_mfma_f32_16x16x32_bf16 v[20:23], a[68:71], v[64:67], v[20:23]\n"
    "v_cvt_pk_bf16_f32 v68, v68, v69\n"
    "v_cvt_pk_bf16_f32 v69, v70, v71\n"
    "v_mfma_f32_16x16x32_bf16 v[24:27], a[72:75], v[64:67], v[24:27]\n"
    "v_cvt_pk_bf16_f32 v70, v72, v73\n"
    "v_cvt_pk_bf16_f32 v71, v74, v75\n"
    "v_mfma_f32_16x16x32_bf16 v[28:31], a[76:79], v[64:67], v[28:31]\n"
    "v_cvt_pk_bf16_f32 v72, v76, v77\n"
    "v_cvt_pk_bf16_f32 v73, v78, v79\n"
    "buffer_load_dwordx4 v2, s[8:11], s40 offen offset:1024 sc1 lds\n"
    "s_waitcnt lgkmcnt(2)\n"
    "v_mfma_f32_16x16x32_bf16 v[12:15], a[80:83], v[52:55], v[12:15]\n"
    "ds_read_b128 v[64:67], v9 offset:14336\n"
    "v_cvt_pk_bf16_f32 v74, v80, v81\n"
    "v_cvt_pk_bf16_f32 v75, v82, v83\n"
    "v_mfma_f32_16x16x32_bf16 v[16:19], a[84:87], v[52:55], v[16:19]\n"
    "v_cvt_pk_bf16_f32 v76, v84, v85\n"
    "v_cvt_pk_bf16_f32 v77, v86, v87\n"
    "v_mfma_f32_16x16x32_bf16 v[20:23], a[88:91], v[52:55], v[20:23]\n"
    "buffer_load_dwordx4 v2, s[8:11], s40 offen offset:2048 sc1 lds\n"
    "v_mfma_f32_16x16x32_bf16 v[24:27], a[92:95], v[52:55], v[24:27]\n"
    "v_mfma_f32_16x16x32_bf16 v[28:31], a[96:99], v[52:55], v[28:31]\n"
    "s_waitcnt lgkmcnt(2)\n"
    "v_mfma_f32_16x16x32_bf16 v[12:15], a[100:103], v[56:59], v[12:15]\n"
    "ds_read_b128 v[52:55], v9 offset:16384\n"
    "buffer_load_dwordx4 v2, s[8:11], s40 offen offset:3072 sc1 lds\n"
    "v_mfma_f32_16x16x32_bf16 v[16:19], a[104:107], v[56:59], v[16:19]\n"
    "v_mfma_f32_16x16x32_bf16 v[20:23], a[108:111], v[56:59], v[20:23]\n"
    "v_mfma_f32_16x16x32_bf16 v[24:27], a[112:115], v[56:59], v[24:27]\n"
    "s_add_u32 m0, s28, 8192\n"
    "s_add_u32 s40, s27, 8192\n"
    "buffer_load_dwordx4 v2, s[8:11], s40 offen offset:0 sc1 lds\n"
    "v_mfma_f32_16x16x32_bf16 v[28:31], a[116:119], v[56:59], v[28:31]\n"
    "s_waitcnt lgkmcnt(2)\n"
    "v_mfma_f32_16x16x32_bf16 v[12:15], a[120:123], v[60:63], v[12:15]\n"
    "ds_read_b128 v[56:59], v9 offset:18432\n"
    "v_mfma_f32_16x16x32_bf16 v[16:19], a[124:127], v[60:63], v[16:19]\n"
    "buffer_load_dwordx4 v2, s[8:11], s40 offen offset:1024 sc1 lds\n"
    "v_mfma_f32_16x16x32_bf16 v[20:23], a[128:131], v[60:63], v[20:23]\n"
    "v_mfma_f32_16x16x32_bf16 v[24:27], a[132:135], v[60:63], v[24:27]\n"
    "v_mfma_f32_16x16x32_bf16 v[28:31], a[136:139], v[60:63], v[28:31]\n"
    "buffer_load_dwordx4 v2, s[8:11], s40 offen offset:2048 sc1 lds\n"
    "s_waitcnt lgkmcnt(2)\n"
    "v_mfma_f32_16x16x32_bf16 v[12:15], a[140:143], v[64:67], v[12:15]\n"
    "ds_read_b128 v[60:63], v9 offset:20480\n"
    "v_mfma_f32_16x16x32_bf16 v[16:19], a[144:147], v[64:67], v[16:19]\n"
    "v_mfma_f32_16x16x32_bf16 v[20:23], a[148:151], v[64:67], v[20:23]\n"
    "buffer_load_dwordx4 v2, s[8:11], s40 offen offset:3072 sc1 lds\n"
    "v_mfma_f32_16x16x32_bf16 v[24:27], a[152:155], v[64:67], v[24:27]\n"
    "v_mfma_f32_16x16x32_bf16 v[28:31], a[156:159], v[64:67], v[28:31]\n"
    "s_waitcnt lgkmcnt(2)\n"
    "v_mfma_f32_16x16x32_bf16 v[12:15], a[160:163], v[52:55], v[12:15]\n"
    "ds_read_b128 v[64:67], v9 offset:22528\n"
    "buffer_load_dword v8, v6, s[8:11], 0 offen sc1\n"
    "v_mfma_f32_16x16x32_bf16 v[16:19], a[164:167], v[52:55], v[16:19]\n"
    "v_mfma_f32_16x16x32_bf16 v[20:23], a[168:171], v[52:55], v[20:23]\n"
    "v_mfma_f32_16x16x32_bf16 v[24:27], a[172:175], v[52:55], v[24:27]\n"
    "s_waitcnt vmcnt(13)\n"
    "buffer_store_dword v7, v5, s[8:11], 0 offen sc1\n"
    "v_mfma_f32_16x16x32_bf16 v[28:31], a[176:179], v[52:55], v[28:31]\n"
    "s_waitcnt lgkmcnt(2)\n"
    "v_mfma_f32_16x16x32_bf16 v[12:15], a[180:183], v[56:59], v[12:15]\n"
    "ds_read_b128 v[52:55], v10 offset:0\n"
    "v_mfma_f32_16x16x32_bf16 v[16:19], a[184:187], v[56:59], v[16:19]\n"
    "s_add_u32 s42, s29, 0\n"
    "buffer_store_dwordx2 v[68:69], v11, s[8:11], s42 offen offset:0 sc1\n"
    "v_mfma_f32_16x16x32_bf16 v[20:23], a[188:191], v[56:59], v[20:23]\n"
    "v_mfma_f32_16x16x32_bf16 v[24:27], a[192:195], v[56:59], v[24:27]\n"
    "v_mfma_f32_16x16x32_bf16 v[28:31], a[196:199], v[56:59], v[28:31]\n"
    "buffer_store_dwordx2 v[70:71], v11, s[8:11], s42 offen offset:1024 sc1\n"
    "s_waitcnt lgkmcnt(2)\n"
    "v_mfma_f32_16x16x32_bf16 v[12:15], a[200:203], v[60:63], v[12:15]\n"
    "ds_read_b128 v[56:59], v10 offset:2048\n"
    "v_mfma_f32_16x16x32_bf16 v[16:19], a[204:207], v[60:63], v[16:19]\n"
    "v_mfma_f32_16x16x32_bf16 v[20:23], a[208:211], v[60:63], v[20:23]\n"
    "buffer_store_dwordx2 v[72:73], v11, s[8:11], s42 offen offset:2048 sc1\n"
    "v_mfma_f32_16x16x32_bf16 v[24:27], a[212:215], v[60:63], v[24:27]\n"
    "v_mfma_f32_16x16x32_bf16 v[28:31], a[216:219], v[60:63], v[28:31]\n"
    "s_waitcnt lgkmcnt(2)\n"
    "v_mfma_f32_16x16x32_bf16 v[12:15], a[220:223], v[64:67], v[12:15]\n"
    "ds_read_b128 v[60:63], v10 offset:4096\n"
    "buffer_store_dwordx2 v[74:75], v11, s[8:11], s42 offen offset:3072 sc1\n"
    "v_mfma_f32_16x16x32_bf16 v[16:19], a[224:227], v[64:67], v[16:19]\n"
    "v_mfma_f32_16x16x32_bf16 v[20:23], a[228:231], v[64:67], v[20:23]\n"
    "v_mfma_f32_16x16x32_bf16 v[24:27], a[232:235], v[64:67], v[24:27]\n"
    "s_add_u32 s42, s29, 4096\n"
    "buffer_store_dwordx2 v[76:77], v11, s[8:11], s42 offen offset:0 sc1\n"
    "v_mfma_f32_16x16x32_bf16 v[28:31], a[236:239], v[64:67], v[28:31]\n"
    "s_waitcnt lgkmcnt(2)\n"
    "v_mfma_f32_16x16x32_bf16 v[32:35], a[0:3], v[52:55], 0\n"
    "ds_read_b128 v[64:67], v10 offset:6144\n"
    "v_mfma_f32_16x16x32_bf16 v[36:39], a[4:7], v[52:55], 0\n"
    "v_mfma_f32_16x16x32_bf16 v[40:43], a[8:11], v[52:55], 0\n"
    "v_mov_b32 v7, s24\n"
    "v_mfma_f32_16x16x32_bf16 v[44:47], a[12:15], v[52:55], 0\n"
    "v_mfma_f32_16x16x32_bf16 v[48:51], a[16:19], v[52:55], 0\n"
    "s_waitcnt lgkmcnt(2)\n"
    "v_mfma_f32_16x16x32_bf16 v[32:35], a[20:23], v[56:59], v[32:35]\n"
    "ds_read_b128 v[52:55], v10 offset:8192\n"
    "v_mfma_f32_16x16x32_bf16 v[36:39], a[24:27], v[56:59], v[36:39]\n"
    "v_mfma_f32_16x16x32_bf16 v[40:43], a[28:31], v[56:59], v[40:43]\n"
    "v_mfma_f32_16x16x32_bf16 v[44:47], a[32:35], v[56:59], v[44:47]\n"
    "ds_write_b128 v3, v[12:15] offset:0\n"
    "v_mfma_f32_16x16x32_bf16 v[48:51], a[36:39], v[56:59], v[48:51]\n"
    "s_waitcnt lgkmcnt(3)\n"
    "v_mfma_f32_16x16x32_bf16 v[32:35], a[40:43], v[60:63], v[32:35]\n"
    "ds_read_b128 v[56:59], v10 offset:10240\n"
    "v_mfma_f32_16x16x32_bf16 v[36:39], a[44:47], v[60:63], v[36:39]\n"
    "v_mfma_f32_16x16x32_bf16 v[40:43], a[48:51], v[60:63], v[40:43]\n"
    "ds_write_b128 v3, v[16:19] offset:1024\n"
    "v_mfma_f32_16x16x32_bf16 v[44:47], a[52:55], v[60:63], v[44:47]\n"
    "v_mfma_f32_16x16x32_bf16 v[48:51], a[56:59], v[60:63], v[48:51]\n"
    "s_waitcnt lgkmcnt(4)\n"
    "v_mfma_f32_16x16x32_bf16 v[32:35], a[60:63], v[64:67], v[32:35]\n"
    "ds_read_b128 v[60:63], v10 offset:12288\n"
    "v_mfma_f32_16x16x32_bf16 v[36:39], a[64:67], v[64:67], v[36:39]\n"
    "ds_write_b128 v3, v[20:23] offset:2048\n"
    "v_mfma_f32_16x16x32_bf16 v[40:43], a[68:71], v[64:67], v[40:43]\n"
    "v_mfma_f32_16x16x32_bf16 v[44:47], a[72:75], v[64:67], v[44:47]\n"
    "v_mfma_f32_16x16x32_bf16 v[48:51], a[76:79], v[64:67], v[48:51]\n"
    "s_waitcnt lgkmcnt(5)\n"
    "v_mfma_f32_16x16x32_bf16 v[32:35], a[80:83], v[52:55], v[32:35]\n"
    "ds_read_b128 v[64:67], v10 offset:14336\n"
    "ds_write_b128 v3, v[24:27] offset:3072\n"
    "v_mfma_f32_16x16x32_bf16 v[36:39], a[84:87], v[52:55], v[36:39]\n"
    "v_mfma_f32_16x16x32_bf16 v[40:43], a[88:91], v[52:55], v[40:43]\n"
    "v_mfma_f32_16x16x32_bf16 v[44:47], a[92:95], v[52:55], v[44:47]\n"
    "v_mfma_f32_16x16x32_bf16 v[48:51], a[96:99], v[52:55], v[48:51]\n"
    "ds_write_b128 v3, v[28:31] offset:4096\n"
    "s_waitcnt lgkmcnt(6)\n"
    "v_mfma_f32_16x16x32_bf16 v[32:35], a[100:103], v[56:59], v[32:35]\n"
    "ds_read_b128 v[52:55], v10 offset:16384\n"
    "v_mfma_f32_16x16x32_bf16 v[36:39], a[104:107], v[56:59], v[36:39]\n"
    "v_mfma_f32_16x16x32_bf16 v[40:43], a[108:111], v[56:59], v[40:43]\n"
    "v_mfma_f32_16x16x32_bf16 v[44:47], a[112:115], v[56:59], v[44:47]\n"
    "v_mfma_f32_16x16x32_bf16 v[48:51], a[116:119], v[56:59], v[48:51]\n"
    "s_waitcnt lgkmcnt(5)\n"
    "v_mfma_f32_16x16x32_bf16 v[32:35], a[120:123], v[60:63], v[32:35]\n"
    "ds_read_b128 v[56:59], v10 offset:18432\n"
    "v_mfma_f32_16x16x32_bf16 v[36:39], a[124:127], v[60:63], v[36:39]\n"
    "s_cmp_lg_u32 s24, 0\n"
    "s_cselect_b32 s40, 81920, 0\n"
    "s_add_u32 s29, s29, s40\n"
    "v_mfma_f32_16x16x32_bf16 v[40:43], a[128:131], v[60:63], v[40:43]\n"
    "v_add_u32 v3, s31, v3\n"
    "v_mfma_f32_16x16x32_bf16 v[44:47], a[132:135], v[60:63], v[44:47]\n"
    "v_mfma_f32_16x16x32_bf16 v[48:51], a[136:139], v[60:63], v[48:51]\n"
    "s_waitcnt lgkmcnt(4)\n"
    "v_mfma_f32_16x16x32_bf16 v[32:35], a[140:143], v[64:67], v[32:35]\n"
    "ds_read_b128 v[60:63], v10 offset:20480\n"
    "v_mfma_f32_16x16x32_bf16 v[36:39], a[144:147], v[64:67], v[36:39]\n"
    "v_mfma_f32_16x16x32_bf16 v[40:43], a[148:151], v[64:67], v[40:43]\n"
    "v_mfma_f32_16x16x32_bf16 v[44:47], a[152:155], v[64:67], v[44:47]\n"
    "v_mfma_f32_16x16x32_bf16 v[48:51], a[156:159], v[64:67], v[48:51]\n"
    "s_waitcnt lgkmcnt(2)\n"
    "v_mfma_f32_16x16x32_bf16 v[32:35], a[160:163], v[52:55], v[32:35]\n"
    "ds_read_b128 v[64:67], v10 offset:22528\n"
    "v_mfma_f32_16x16x32_bf16 v[36:39], a[164:167], v[52:55], v[36:39]\n"
    "v_mfma_f32_16x16x32_bf16 v[40:43], a[168:171], v[52:55], v[40:43]\n"
    "v_add_u32 v9, s30, v9\n"
    "v_add_u32 v10, s30, v10\n"
    "v_mfma_f32_16x16x32_bf16 v[44:47], a[172:175], v[52:55], v[44:47]\n"
    "s_sub_u32 s30, 0, s30\n"
    "s_add_u32 s24, s24, 1\n"
    "v_mfma_f32_16x16x32_bf16 v[48:51], a[176:179], v[52:55], v[48:51]\n"
    "s_waitcnt lgkmcnt(2)\n"
    "v_mfma_f32_16x16x32_bf16 v[32:35], a[180:183], v[56:59], v[32:35]\n"
    "v_mfma_f32_16x16x32_bf16 v[36:39], a[184:187], v[56:59], v[36:39]\n"
    "s_waitcnt vmcnt(6) lgkmcnt(0)\n"
    "s_barrier\n"
    "ds_read_b128 v[52:55], v9 offset:0\n"
    "v_mfma_f32_16x16x32_bf16 v[40:43], a[188:191], v[56:59], v[40:43]\n"
    "ds_read_b128 v[68:71], v4 offset:0\n"
    "v_mfma_f32_16x16x32_bf16 v[44:47], a[192:195], v[56:59], v[44:47]\n"
    "ds_read_b128 v[72:75], v4 offset:1024\n"
    "v_mfma_f32_16x16x32_bf16 v[48:51], a[196:199], v[56:59], v[48:51]\n"
    "ds_read_b128 v[76:79], v4 offset:2048\n"
    "v_mfma_f32_16x16x32_bf16 v[32:35], a[200:203], v[60:63], v[32:35]\n"
    "ds_read_b128 v[56:59], v9 offset:2048\n"
    "v_mfma_f32_16x16x32_bf16 v[36:39], a[204:207], v[60:63], v[36:39]\n"
    "ds_read_b128 v[80:83], v4 offset:3072\n"
    "v_mfma_f32_16x16x32_bf16 v[40:43], a[208:211], v[60:63], v[40:43]\n"
    "ds_read_b128 v[84:87], v4 offset:4096\n"
    "v_mfma_f32_16x16x32_bf16 v[44:47], a[212:215], v[60:63], v[44:47]\n"
    "v_add_u32 v4, s31, v4\n"
    "s_sub_u32 s31, 0, s31\n"
    "v_mfma_f32_16x16x32_bf16 v[48:51], a[216:219], v[60:63], v[48:51]\n"
    "v_mfma_f32_16x16x32_bf16 v[32:35], a[220:223], v[64:67], v[32:35]\n"
    "ds_read_b128 v[60:63], v9 offset:4096\n"
    "v_mfma_f32_16x16x32_bf16 v[36:39], a[224:227], v[64:67], v[36:39]\n"
    "v_mfma_f32_16x16x32_bf16 v[40:43], a[228:231], v[64:67], v[40:43]\n"
    "v_mfma_f32_16x16x32_bf16 v[44:47], a[232:235], v[64:67], v[44:47]\n"
    "s_cmp_lt_u32 s24, 64\n"
    "v_mfma_f32_16x16x32_bf16 v[48:51], a[236:239], v[64:67], v[48:51]\n"
    "s_waitcnt lgkmcnt(0)\n"
    "s_cbranch_scc1 .LD_loop\n"
    "v_add_f32 v68, v68, v32\n"
    "v_add_f32 v69, v69, v33\n"
    "v_add_f32 v70, v70, v34\n"
    "v_add_f32 v71, v71, v35\n"
    "v_add_f32 v72, v72, v36\n"
    "v_add_f32 v73, v73, v37\n"
    "v_add_f32 v74, v74, v38\n"
    "v_add_f32 v75, v75, v39\n"
    "v_add_f32 v76, v76, v40\n"
    "v_add_f32 v77, v77, v41\n"
    "v_add_f32 v78, v78, v42\n"
    "v_add_f32 v79, v79, v43\n"
    "v_add_f32 v80, v80, v44\n"
    "v_add_f32 v81, v81, v45\n"
    "v_add_f32 v82, v82, v46\n"
    "v_add_f32 v83, v83, v47\n"
    "v_add_f32 v84, v84, v48\n"
    "v_add_f32 v85, v85, v49\n"
    "v_add_f32 v86, v86, v50\n"
    "v_add_f32 v87, v87, v51\n"
    "v_cvt_pk_bf16_f32 v68, v68, v69\n"
    "v_cvt_pk_bf16_f32 v69, v70, v71\n"
    "v_cvt_pk_bf16_f32 v70, v72, v73\n"
    "v_cvt_pk_bf16_f32 v71, v74, v75\n"
    "v_cvt_pk_bf16_f32 v72, v76, v77\n"
    "v_cvt_pk_bf16_f32 v73, v78, v79\n"
    "v_cvt_pk_bf16_f32 v74, v80, v81\n"
    "v_cvt_pk_bf16_f32 v75, v82, v83\n"
    "v_cvt_pk_bf16_f32 v76, v84, v85\n"
    "v_cvt_pk_bf16_f32 v77, v86, v87\n"
    "s_add_u32 s42, s29, 0\n"
    "buffer_store_dwordx2 v[68:69], v11, s[8:11], s42 offen offset:0 sc1\n"
    "buffer_store_dwordx2 v[70:71], v11, s[8:11], s42 offen offset:1024 sc1\n"
    "buffer_store_dwordx2 v[72:73], v11, s[8:11], s42 offen offset:2048 sc1\n"
    "buffer_store_dwordx2 v[74:75], v11, s[8:11], s42 offen offset:3072 sc1\n"
    "s_add_u32 s42, s29, 4096\n"
    "buffer_store_dwordx2 v[76:77], v11, s[8:11], s42 offen offset:0 sc1\n"
    "v_mov_b32 v7, s24\n"
    "s_waitcnt vmcnt(0)\n"
    "buffer_store_dword v7, v5, s[8:11], 0 offen sc1\n"
    "s_endpgm\n"
    ".Lrole_O:\n"
    "s_waitcnt lgkmcnt(0)\n"
    "s_add_u32 s4, s3, 1\n"
    "v_mov_b32 v2, s3\n"
    "s_or_b32 s41, s21, s6\n"
    "s_cbranch_scc1 .Lcl_122\n"
    "s_lshl_b32 s41, s5, 2\n"
    "s_add_u32 s41, s41, 4178944\n"
    "v_mov_b32 v3, s41\n"
    "v_mov_b32 v2, s4\n"
    "s_mov_b64 exec, 1\n"
    "buffer_atomic_swap v2, v3, s[8:11], 0 offen sc0 sc1\n"
    "s_mov_b64 exec, -1\n"
    ".Lcl_122:\n"
    "v_min_u32 v6, 54, v1\n"
    "v_lshlrev_b32 v6, 7, v6\n"
    "v_add_u32 v6, 4182016, v6\n"
    "buffer_load_dwordx2 v[4:5], v6, s[8:11], 0 offen sc1\n"
    "s_waitcnt vmcnt(0)\n"
    "v_cmp_ne_u32 vcc, s4, v4\n"
    "s_nop 1\n"
    "s_cmp_eq_u32 vcc_lo, 0\n"
    "s_cbranch_scc1 .Lrx_125\n"
    ".Lrp_123:\n"
    "v_min_u32 v6, 54, v1\n"
    "v_lshlrev_b32 v6, 7, v6\n"
    "v_add_u32 v6, 4182016, v6\n"
    "buffer_load_dwordx2 v[4:5], v6, s[8:11], 0 offen sc1\n"
    "s_waitcnt vmcnt(0)\n"
    "v_cmp_ne_u32 vcc, s4, v4\n"
    "s_nop 1\n"
    "s_cmp_eq_u32 vcc_lo, 0\n"
    "s_cbranch_scc1 .Lro_124\n"
    "s_sleep 2\n"
    "s_sub_u32 s25, s25, 1\n"
    "s_cbranch_scc0 .Lrp_123\n"
    "s_branch .Lfail\n"
    ".Lro_124:\n"
    ".Lrx_125:\n"
    "v_cmp_ne_u32 vcc, s4, v4\n"
    "s_nop 1\n"
    "s_cmp_eq_u32 vcc_hi, 0\n"
    "s_cbranch_scc1 .Lrx_128\n"
    ".Lrp_126:\n"
    "v_min_u32 v6, 54, v1\n"
    "v_lshlrev_b32 v6, 7, v6\n"
    "v_add_u32 v6, 4182016, v6\n"
    "buffer_load_dwordx2 v[4:5], v6, s[8:11], 0 offen sc1\n"
    "s_waitcnt vmcnt(0)\n"
    "v_cmp_ne_u32 vcc, s4, v4\n"
    "s_nop 1\n"
    "s_cmp_eq_u32 vcc_hi, 0\n"
    "s_cbranch_scc1 .Lro_127\n"
    "s_sleep 2\n"
    "s_sub_u32 s25, s25, 1\n"
    "s_cbranch_scc0 .Lrp_126\n"
    "s_branch .Lfail\n"
    ".Lro_127:\n"
    ".Lrx_128:\n"
    "v_cmp_ne_u32 vcc, 0, v5\n"
    "s_nop 1\n"
    "s_and_b32 s22, vcc_hi, 1\n"
    "s_lshl_b32 s22, s22, 13\n"
    "s_lshr_b32 s23, vcc_hi, 1\n"
    "s_cmp_lg_u32 s21, 0\n"
    "s_cbranch_scc1 .Lgt_129\n"
    "v_mov_b32 v3, 0\n"
    "v_readfirstlane_b32 s40, v2\n"
    "s_nop 1\n"
    "v_mov_b32 v2, s40\n"
    "ds_write_b32 v3, v2\n"
    "s_waitcnt lgkmcnt(0)\n"
    ".Lgt_129:\n"
    "s_barrier\n"
    "v_mov_b32 v3, 0\n"
    "ds_read_b32 v2, v3\n"
    "s_waitcnt lgkmcnt(0)\n"
    "v_readfirstlane_b32 s40, v2\n"
    "s_barrier\n"
    "s_cmp_eq_u32 s40, s4\n"
    "s_cbranch_scc1 .Llost\n"
    "s_mov_b32 s24, 0\n"
    "s_mov_b32 s37, 1\n"
    "s_mov_b32 s38, 35\n"
    ".LO_enter:\n"
    "s_mov_b32 s12, s80\n"
    "s_and_b32 s13, s81, 0xffff\n"
    "s_mov_b32 s14, 2621440\n"
    "s_mov_b32 s15, 0x20000\n"
    "s_mov_b32 s16, s64\n"
    "s_and_b32 s17, s65, 0xffff\n"
    "s_mov_b32 s18, 2621440\n"
    "s_mov_b32 s19, 0x20000\n"
    "v_lshlrev_b32 v2, 4, v1\n"
    "v_and_b32 v0, 15, v1\n"
    "v_mul_u32_u24 v3, 1280, v0\n"
    "v_bfe_u32 v0, v1, 4, 2\n"
    "v_lshl_add_u32 v3, v0, 3, v3\n"
    "s_and_b32 s40, s21, 1\n"
    "s_mul_i32 s31, s40, 20480\n"
    "s_lshr_b32 s40, s21, 1\n"
    "s_lshl_b32 s40, s40, 5\n"
    "s_add_u32 s31, s31, s40\n"
    "s_lshl_b32 s40, s20, 6\n"
    "s_add_u32 s31, s31, s40\n"
    "s_lshl_b32 s40, s21, 12\n"
    "v_add_u32 v4, s40, v2\n"
    "s_lshl_b32 s40, s21, 10\n"
    "v_add_u32 v5, s40, v2\n"
    "s_lshl_b32 s41, s20, 12\n"
    "s_add_u32 s30, s40, s41\n"
    "s_lshr_b32 s30, s30, 1\n"
    "v_lshlrev_b32 v11, 3, v1\n"
    "s_add_u32 s30, s30, 155189248\n"
    "s_lshl_b32 s40, s21, 13\n"
    "s_add_u32 s26, s40, 143654912\n"
    "s_lshl_b32 s41, s20, 16\n"
    "s_add_u32 s29, s40, s41\n"
    "s_add_u32 s29, s29, 59768832\n"
    "s_lshl_b32 s40, s21, 11\n"
    "s_add_u32 s27, s40, 147849216\n"
    "s_lshl_b32 s41, s20, 15\n"
    "s_add_u32 s28, s40, s41\n"
    "s_mul_i32 s42, s20, 52\n"
    "s_lshr_b32 s42, s42, 8\n"
    "s_lshl_b32 s42, s42, 12\n"
    "s_lshl_b32 s43, s21, 10\n"
    "s_add_u32 s42, s42, s43\n"
    "s_lshl_b32 s40, s20, 1\n"
    "s_lshr_b32 s41, s21, 1\n"
    "s_add_u32 s40, s40, s41\n"
    "s_mul_i32 s40, s40, 52\n"
    "s_lshr_b32 s40, s40, 8\n"
    "s_and_b32 s41, s40, 6\n"
    "s_lshl_b32 s41, s41, 2\n"
    "s_and_b32 s40, s40, 1\n"
    "s_add_u32 s40, s40, s41\n"
    "s_and_b32 s41, s21, 1\n"
    "s_lshl_b32 s41, s41, 1\n"
    "s_add_u32 s40, s40, s41\n"
    "s_lshl_b32 s40, s40, 7\n"
    "s_add_u32 s40, s40, 4284416\n"
    "v_bfe_u32 v6, v1, 0, 1\n"
    "v_lshlrev_b32 v6, 9, v6\n"
    "v_add_u32 v6, s40, v6\n"
    "v_bfe_u32 v10, v1, 0, 3\n"
    "v_lshlrev_b32 v10, 7, v10\n"
    "s_add_u32 s40, s42, 4227072\n"
    "v_bfe_u32 v0, v1, 3, 1\n"
    "v_lshl_add_u32 v36, v0, 14, v10\n"
    "v_add_u32 v36, s40, v36\n"
    "v_cmp_gt_u32 vcc, 16, v1\n"
    "s_add_u32 s40, s43, 4259840\n"
    "v_bfe_u32 v0, v1, 3, 1\n"
    "v_lshl_add_u32 v10, v0, 12, v10\n"
    "v_add_u32 v0, s40, v10\n"
    "v_cndmask_b32 v6, v6, v0, vcc\n"
    "s_mov_b32 s34, 16384\n"
    "s_mov_b32 s35, 0\n"
    "s_mul_i32 s40, s24, 65536\n"
    "s_add_u32 s26, s26, s40\n"
    "s_mul_i32 s48, s37, 65536\n"
    "s_mul_i32 s40, s24, 16384\n"
    "s_add_u32 s27, s27, s40\n"
    "s_mul_i32 s49, s37, 16384\n"
    "s_mul_i32 s40, s24, 0\n"
    "s_add_u32 s28, s28, s40\n"
    "s_mul_i32 s50, s37, 0\n"
    "s_mul_i32 s40, s24, 81920\n"
    "s_add_u32 s30, s30, s40\n"
    "s_mul_i32 s51, s37, 81920\n"
    "s_mul_i32 s40, s24, 1310720\n"
    "s_add_u32 s29, s29, s40\n"
    "s_mul_i32 s52, s37, 1310720\n"
    "s_mul_i32 s40, s24, 40960\n"
    "s_add_u32 s31, s31, s40\n"
    "s_mul_i32 s53, s37, 40960\n"
    "s_mov_b32 s39, s31\n"
    "s_and_b32 s55, s20, 1\n"
    "s_lshl_b32 s55, s55, 12\n"
    "s_xor_b32 s56, s55, 4096\n"
    "s_add_u32 s54, s24, 1\n"
    "v_mov_b32 v9, v36\n"
    "s_cmp_le_u32 s37, 2\n"
    "s_cselect_b32 s41, s54, 63\n"
    ".Lpl_130:\n"
    "buffer_load_dword v8, v9, s[8:11], 0 offen sc1\n"
    "s_waitcnt vmcnt(0)\n"
    "v_cmp_gt_u32 vcc, s41, v8\n"
    "s_nop 1\n"
    "s_cbranch_vccz .Lpd_131\n"
    "s_sleep 8\n"
    "s_sub_u32 s25, s25, 1\n"
    "s_cbranch_scc0 .Lpl_130\n"
    "s_branch .Lfail\n"
    ".Lpd_131:\n"
    "s_cmp_eq_u32 s37, 1\n"
    "s_cselect_b32 s41, 0, s20\n"
    ".Loj_132:\n"
    "s_sleep 3\n"
    "s_sub_u32 s41, s41, 1\n"
    "s_cbranch_scc0 .Loj_132\n"
    "s_add_u32 s40, s29, s55\n"
    "s_add_u32 s40, s40, 0\n"
    "buffer_load_dwordx4 a[0:3], v2, s[8:11], s40 offen offset:0 sc1\n"
    "buffer_load_dwordx4 a[4:7], v2, s[8:11], s40 offen offset:1024 sc1\n"
    "buffer_load_dwordx4 a[8:11], v2, s[8:11], s40 offen offset:2048 sc1\n"
    "buffer_load_dwordx4 a[12:15], v2, s[8:11], s40 offen offset:3072 sc1\n"
    "s_add_u32 s40, s29, s56\n"
    "s_add_u32 s40, s40, 0\n"
    "buffer_load_dwordx4 a[16:19], v2, s[8:11], s40 offen offset:0 sc1\n"
    "buffer_load_dwordx4 a[20:23], v2, s[8:11], s40 offen offset:1024 sc1\n"
    "buffer_load_dwordx4 a[24:27], v2, s[8:11], s40 offen offset:2048 sc1\n"
    "buffer_load_dwordx4 a[28:31], v2, s[8:11], s40 offen offset:3072 sc1\n"
    "s_add_u32 s40, s29, s55\n"
    "s_add_u32 s40, s40, 32768\n"
    "buffer_load_dwordx4 a[32:35], v2, s[8:11], s40 offen offset:0 sc1\n"
    "buffer_load_dwordx4 a[36:39], v2, s[8:11], s40 offen offset:1024 sc1\n"
    "buffer_load_dwordx4 a[40:43], v2, s[8:11], s40 offen offset:2048 sc1\n"
    "buffer_load_dwordx4 a[44:47], v2, s[8:11], s40 offen offset:3072 sc1\n"
    "s_add_u32 s40, s29, s56\n"
    "s_add_u32 s40, s40, 32768\n"
    "buffer_load_dwordx4 a[48:51], v2, s[8:11], s40 offen offset:0 sc1\n"
    "buffer_load_dwordx4 a[52:55], v2, s[8:11], s40 offen offset:1024 sc1\n"
    "buffer_load_dwordx4 a[56:59], v2, s[8:11], s40 offen offset:2048 sc1\n"
    "buffer_load_dwordx4 a[60:63], v2, s[8:11], s40 offen offset:3072 sc1\n"
    "s_add_u32 s29, s29, s52\n"
    ".p2align 4\n"
    ".LO_loop:\n"
    "s_cmp_lg_u32 s35, 0\n"
    "s_cbranch_scc1 .LOh_133\n"
    ".LOt_134:\n"
    "buffer_load_dword v10, v6, s[8:11], 0 offen sc1\n"
    "s_waitcnt vmcnt(0)\n"
    "v_cmp_gt_u32 vcc, s54, v10\n"
    "s_cbranch_vccz .LOg_135\n"
    "s_sleep 12\n"
    "s_sub_u32 s25, s25, 1\n"
    "s_nop 1\n"
    "s_cbranch_scc0 .LOt_134\n"
    "s_branch .Lfail\n"
    ".LOg_135:\n"
    "s_sub_u32 s40, s54, 1\n"
    "s_mul_i32 s42, s40, 43\n"
    "s_lshr_b32 s42, s42, 7\n"
    "s_mul_i32 s36, s40, 655360\n"
    "s_lshl_b32 s43, s42, 17\n"
    "s_add_u32 s36, s36, s43\n"
    "s_lshr_b32 s42, s23, s42\n"
    "s_and_b32 s42, s42, 1\n"
    "s_lshl_b32 s42, s42, 13\n"
    "s_add_u32 s36, s36, s42\n"
    "s_add_u32 s36, s36, 161480704\n"
    "s_add_u32 s36, s36, s28\n"
    "s_add_u32 s40, s26, s55\n"
    "s_add_u32 s40, s40, 0\n"
    "buffer_load_dwordx4 v[52:55], v2, s[8:11], s40 offen offset:0 sc1\n"
    "buffer_load_dwordx4 v[60:63], v2, s[8:11], s40 offen offset:1024 sc1\n"
    "buffer_load_dwordx4 v[68:71], v2, s[8:11], s40 offen offset:2048 sc1\n"
    "buffer_load_dwordx4 v[76:79], v2, s[8:11], s40 offen offset:3072 sc1\n"
    "s_add_u32 s40, s26, s55\n"
    "s_add_u32 s40, s40, 32768\n"
    "buffer_load_dwordx4 v[56:59], v2, s[8:11], s40 offen offset:0 sc1\n"
    "buffer_load_dwordx4 v[64:67], v2, s[8:11], s40 offen offset:1024 sc1\n"
    "buffer_load_dwordx4 v[72:75], v2, s[8:11], s40 offen offset:2048 sc1\n"
    "buffer_load_dwordx4 v[80:83], v2, s[8:11], s40 offen offset:3072 sc1\n"
    "s_add_u32 s40, s27, 0\n"
    "buffer_load_dwordx4 v[116:119], v2, s[8:11], s40 offen offset:0 sc1\n"
    "s_add_u32 s40, s36, 0\n"
    "buffer_load_dwordx4 v[132:135], v2, s[8:11], s40 offen offset:0 sc1\n"
    "s_add_u32 s40, s27, 8192\n"
    "buffer_load_dwordx4 v[120:123], v2, s[8:11], s40 offen offset:0 sc1\n"
    "s_add_u32 s40, s36, 16384\n"
    "buffer_load_dwordx4 v[136:139], v2, s[8:11], s40 offen offset:0 sc1\n"
    "s_add_u32 s40, s27, 0\n"
    "buffer_load_dwordx4 v[124:127], v2, s[8:11], s40 offen offset:1024 sc1\n"
    "s_add_u32 s40, s36, 0\n"
    "buffer_load_dwordx4 v[140:143], v2, s[8:11], s40 offen offset:1024 sc1\n"
    "s_add_u32 s40, s27, 8192\n"
    "buffer_load_dwordx4 v[128:131], v2, s[8:11], s40 offen offset:1024 sc1\n"
    "s_add_u32 s40, s36, 16384\n"
    "buffer_load_dwordx4 v[144:147], v2, s[8:11], s40 offen offset:1024 sc1\n"
    "buffer_load_dwordx2 v[28:29], v11, s[8:11], s30 offen sc1\n"
    "s_add_u32 s40, s30, 40960\n"
    "buffer_load_dwordx2 v[30:31], v11, s[8:11], s40 offen sc1\n"
    "buffer_load_dwordx2 v[244:245], v3, s[16:19], s39 offen\n"
    "s_add_u32 s40, s26, s56\n"
    "s_add_u32 s40, s40, 0\n"
    "buffer_load_dwordx4 v[84:87], v2, s[8:11], s40 offen offset:0 sc1\n"
    "buffer_load_dwordx4 v[92:95], v2, s[8:11], s40 offen offset:1024 sc1\n"
    "buffer_load_dwordx4 v[100:103], v2, s[8:11], s40 offen offset:2048 sc1\n"
    "buffer_load_dwordx4 v[108:111], v2, s[8:11], s40 offen offset:3072 sc1\n"
    "s_add_u32 s40, s26, s56\n"
    "s_add_u32 s40, s40, 32768\n"
    "buffer_load_dwordx4 v[88:91], v2, s[8:11], s40 offen offset:0 sc1\n"
    "buffer_load_dwordx4 v[96:99], v2, s[8:11], s40 offen offset:1024 sc1\n"
    "buffer_load_dwordx4 v[104:107], v2, s[8:11], s40 offen offset:2048 sc1\n"
    "buffer_load_dwordx4 v[112:115], v2, s[8:11], s40 offen offset:3072 sc1\n"
    "s_add_u32 s39, s39, s53\n"
    "s_add_u32 s26, s26, s48\n"
    "s_add_u32 s27, s27, s49\n"
    "s_add_u32 s28, s28, s50\n"
    "s_add_u32 s30, s30, s51\n"
    "s_add_u32 s54, s54, s37\n"
    ".LOh_133:\n"
    "s_add_u32 s41, s24, s37\n"
    "s_add_u32 s41, s41, 1\n"
    "s_min_u32 s41, s41, 63\n"
    "v_cmp_gt_u32 vcc, s41, v8\n"
    "s_cbranch_vccz .LOw_139\n"
    "s_nop 3\n"
    ".Lpl_140:\n"
    "buffer_load_dword v8, v9, s[8:11], 0 offen sc1\n"
    "s_waitcnt vmcnt(0)\n"
    "v_cmp_gt_u32 vcc, s41, v8\n"
    "s_nop 1\n"
    "s_cbranch_vccz .Lpd_141\n"
    "s_sleep 1\n"
    "s_sub_u32 s25, s25, 1\n"
    "s_cbranch_scc0 .Lpl_140\n"
    "s_branch .Lfail\n"
    ".Lpd_141:\n"
    ".LOw_139:\n"
    "buffer_load_dword v10, v6, s[8:11], 0 offen sc1\n"
    "s_add_u32 s40, s24, s37\n"
    "s_cmp_lt_u32 s40, s38\n"
    "s_cbranch_scc1 .LOu_138\n"
    "s_mov_b32 s35, 0\n"
    "s_waitcnt vmcnt(0)\n"
    "s_branch .LOl_136\n"
    ".LOu_138:\n"
    "s_add_u32 s40, s29, s55\n"
    "s_add_u32 s40, s40, 0\n"
    "buffer_load_dwordx4 a[64:67], v2, s[8:11], s40 offen offset:0 sc1\n"
    "buffer_load_dwordx4 a[68:71], v2, s[8:11], s40 offen offset:1024 sc1\n"
    "buffer_load_dwordx4 a[72:75], v2, s[8:11], s40 offen offset:2048 sc1\n"
    "buffer_load_dwordx4 a[76:79], v2, s[8:11], s40 offen offset:3072 sc1\n"
    "s_add_u32 s40, s29, s56\n"
    "s_add_u32 s40, s40, 0\n"
    "buffer_load_dwordx4 a[80:83], v2, s[8:11], s40 offen offset:0 sc1\n"
    "buffer_load_dwordx4 a[84:87], v2, s[8:11], s40 offen offset:1024 sc1\n"
    "buffer_load_dwordx4 a[88:91], v2, s[8:11], s40 offen offset:2048 sc1\n"
    "buffer_load_dwordx4 a[92:95], v2, s[8:11], s40 offen offset:3072 sc1\n"
    "s_add_u32 s40, s29, s55\n"
    "s_add_u32 s40, s40, 32768\n"
    "buffer_load_dwordx4 a[96:99], v2, s[8:11], s40 offen offset:0 sc1\n"
    "buffer_load_dwordx4 a[100:103], v2, s[8:11], s40 offen offset:1024 sc1\n"
    "buffer_load_dwordx4 a[104:107], v2, s[8:11], s40 offen offset:2048 sc1\n"
    "buffer_load_dwordx4 a[108:111], v2, s[8:11], s40 offen offset:3072 sc1\n"
    "s_add_u32 s40, s29, s56\n"
    "s_add_u32 s40, s40, 32768\n"
    "buffer_load_dwordx4 a[112:115], v2, s[8:11], s40 offen offset:0 sc1\n"
    "buffer_load_dwordx4 a[116:119], v2, s[8:11], s40 offen offset:1024 sc1\n"
    "buffer_load_dwordx4 a[120:123], v2, s[8:11], s40 offen offset:2048 sc1\n"
    "buffer_load_dwordx4 a[124:127], v2, s[8:11], s40 offen offset:3072 sc1\n"
    "s_add_u32 s29, s29, s52\n"
    "s_waitcnt vmcnt(16)\n"
    "v_cmp_gt_u32 vcc, s54, v10\n"
    "s_mov_b32 s35, 0\n"
    "s_cbranch_vccnz .LOl_136\n"
    "s_mov_b32 s35, 1\n"
    "s_sub_u32 s40, s54, 1\n"
    "s_mul_i32 s42, s40, 43\n"
    "s_lshr_b32 s42, s42, 7\n"
    "s_mul_i32 s36, s40, 655360\n"
    "s_lshl_b32 s43, s42, 17\n"
    "s_add_u32 s36, s36, s43\n"
    "s_lshr_b32 s42, s23, s42\n"
    "s_and_b32 s42, s42, 1\n"
    "s_lshl_b32 s42, s42, 13\n"
    "s_add_u32 s36, s36, s42\n"
    "s_add_u32 s36, s36, 161480704\n"
    "s_add_u32 s36, s36, s28\n"
    "s_add_u32 s40, s26, s55\n"
    "s_add_u32 s40, s40, 0\n"
    "buffer_load_dwordx4 v[148:151], v2, s[8:11], s40 offen offset:0 sc1\n"
    "buffer_load_dwordx4 v[156:159], v2, s[8:11], s40 offen offset:1024 sc1\n"
    "buffer_load_dwordx4 v[164:167], v2, s[8:11], s40 offen offset:2048 sc1\n"
    "buffer_load_dwordx4 v[172:175], v2, s[8:11], s40 offen offset:3072 sc1\n"
    "s_add_u32 s40, s26, s55\n"
    "s_add_u32 s40, s40, 32768\n"
    "buffer_load_dwordx4 v[152:155], v2, s[8:11], s40 offen offset:0 sc1\n"
    "buffer_load_dwordx4 v[160:163], v2, s[8:11], s40 offen offset:1024 sc1\n"
    "buffer_load_dwordx4 v[168:171], v2, s[8:11], s40 offen offset:2048 sc1\n"
    "buffer_load_dwordx4 v[176:179], v2, s[8:11], s40 offen offset:3072 sc1\n"
    "s_add_u32 s40, s27, 0\n"
    "buffer_load_dwordx4 v[212:215], v2, s[8:11], s40 offen offset:0 sc1\n"
    "s_add_u32 s40, s36, 0\n"
    "buffer_load_dwordx4 v[228:231], v2, s[8:11], s40 offen offset:0 sc1\n"
    "s_add_u32 s40, s27, 8192\n"
    "buffer_load_dwordx4 v[216:219], v2, s[8:11], s40 offen offset:0 sc1\n"
    "s_add_u32 s40, s36, 16384\n"
    "buffer_load_dwordx4 v[232:235], v2, s[8:11], s40 offen offset:0 sc1\n"
    "s_add_u32 s40, s27, 0\n"
    "buffer_load_dwordx4 v[220:223], v2, s[8:11], s40 offen offset:1024 sc1\n"
    "s_add_u32 s40, s36, 0\n"
    "buffer_load_dwordx4 v[236:239], v2, s[8:11], s40 offen offset:1024 sc1\n"
    "s_add_u32 s40, s27, 8192\n"
    "buffer_load_dwordx4 v[224:227], v2, s[8:11], s40 offen offset:1024 sc1\n"
    "s_add_u32 s40, s36, 16384\n"
    "buffer_load_dwordx4 v[240:243], v2, s[8:11], s40 offen offset:1024 sc1\n"
    "buffer_load_dwordx2 v[32:33], v11, s[8:11], s30 offen sc1\n"
    "s_add_u32 s40, s30, 40960\n"
    "buffer_load_dwordx2 v[34:35], v11, s[8:11], s40 offen sc1\n"
    "buffer_load_dwordx2 v[246:247], v3, s[16:19], s39 offen\n"
    "s_add_u32 s40, s26, s56\n"
    "s_add_u32 s40, s40, 0\n"
    "buffer_load_dwordx4 v[180:183], v2, s[8:11], s40 offen offset:0 sc1\n"
    "buffer_load_dwordx4 v[188:191], v2, s[8:11], s40 offen offset:1024 sc1\n"
    "buffer_load_dwordx4 v[196:199], v2, s[8:11], s40 offen offset:2048 sc1\n"
    "buffer_load_dwordx4 v[204:207], v2, s[8:11], s40 offen offset:3072 sc1\n"
    "s_add_u32 s40, s26, s56\n"
    "s_add_u32 s40, s40, 32768\n"
    "buffer_load_dwordx4 v[184:187], v2, s[8:11], s40 offen offset:0 sc1\n"
    "buffer_load_dwordx4 v[192:195], v2, s[8:11], s40 offen offset:1024 sc1\n"
    "buffer_load_dwordx4 v[200:203], v2, s[8:11], s40 offen offset:2048 sc1\n"
    "buffer_load_dwordx4 v[208:211], v2, s[8:11], s40 offen offset:3072 sc1\n"
    "s_add_u32 s39, s39, s53\n"
    "s_add_u32 s26, s26, s48\n"
    "s_add_u32 s27, s27, s49\n"
    "s_add_u32 s28, s28, s50\n"
    "s_add_u32 s30, s30, s51\n"
    "s_add_u32 s54, s54, s37\n"
    ".LOl_136:\n"
    "v_mfma_f32_16x16x32_bf16 v[12:15], v[132:135], v[116:119], 0\n"
    "v_mfma_f32_16x16x32_bf16 v[16:19], v[132:135], v[120:123], 0\n"
    "v_mfma_f32_16x16x32_bf16 v[20:23], v[136:139], v[116:119], 0\n"
    "v_mfma_f32_16x16x32_bf16 v[24:27], v[136:139], v[120:123], 0\n"
    "v_mfma_f32_16x16x32_bf16 v[12:15], v[140:143], v[124:127], v[12:15]\n"
    "v_mfma_f32_16x16x32_bf16 v[16:19], v[140:143], v[128:131], v[16:19]\n"
    "v_mfma_f32_16x16x32_bf16 v[20:23], v[144:147], v[124:127], v[20:23]\n"
    "v_mfma_f32_16x16x32_bf16 v[24:27], v[144:147], v[128:131], v[24:27]\n"
    "v_mfma_f32_16x16x32_bf16 v[12:15], a[0:3], v[52:55], v[12:15]\n"
    "v_mfma_f32_16x16x32_bf16 v[16:19], a[0:3], v[56:59], v[16:19]\n"
    "v_mfma_f32_16x16x32_bf16 v[20:23], a[32:35], v[52:55], v[20:23]\n"
    "v_mfma_f32_16x16x32_bf16 v[24:27], a[32:35], v[56:59], v[24:27]\n"
    "v_mfma_f32_16x16x32_bf16 v[12:15], a[4:7], v[60:63], v[12:15]\n"
    "v_mfma_f32_16x16x32_bf16 v[16:19], a[4:7], v[64:67], v[16:19]\n"
    "v_mfma_f32_16x16x32_bf16 v[20:23], a[36:39], v[60:63], v[20:23]\n"
    "v_mfma_f32_16x16x32_bf16 v[24:27], a[36:39], v[64:67], v[24:27]\n"
    "v_mfma_f32_16x16x32_bf16 v[12:15], a[8:11], v[68:71], v[12:15]\n"
    "v_mfma_f32_16x16x32_bf16 v[16:19], a[8:11], v[72:75], v[16:19]\n"
    "v_mfma_f32_16x16x32_bf16 v[20:23], a[40:43], v[68:71], v[20:23]\n"
    "v_mfma_f32_16x16x32_bf16 v[24:27], a[40:43], v[72:75], v[24:27]\n"
    "v_mfma_f32_16x16x32_bf16 v[12:15], a[12:15], v[76:79], v[12:15]\n"
    "v_mfma_f32_16x16x32_bf16 v[16:19], a[12:15], v[80:83], v[16:19]\n"
    "v_mfma_f32_16x16x32_bf16 v[20:23], a[44:47], v[76:79], v[20:23]\n"
    "v_mfma_f32_16x16x32_bf16 v[24:27], a[44:47], v[80:83], v[24:27]\n"
    "v_mfma_f32_16x16x32_bf16 v[12:15], a[16:19], v[84:87], v[12:15]\n"
    "v_mfma_f32_16x16x32_bf16 v[16:19], a[16:19], v[88:91], v[16:19]\n"
    "v_mfma_f32_16x16x32_bf16 v[20:23], a[48:51], v[84:87], v[20:23]\n"
    "v_mfma_f32_16x16x32_bf16 v[24:27], a[48:51], v[88:91], v[24:27]\n"
    "v_mfma_f32_16x16x32_bf16 v[12:15], a[20:23], v[92:95], v[12:15]\n"
    "v_mfma_f32_16x16x32_bf16 v[16:19], a[20:23], v[96:99], v[16:19]\n"
    "v_mfma_f32_16x16x32_bf16 v[20:23], a[52:55], v[92:95], v[20:23]\n"
    "v_mfma_f32_16x16x32_bf16 v[24:27], a[52:55], v[96:99], v[24:27]\n"
    "v_mfma_f32_16x16x32_bf16 v[12:15], a[24:27], v[100:103], v[12:15]\n"
    "v_mfma_f32_16x16x32_bf16 v[16:19], a[24:27], v[104:107], v[16:19]\n"
    "v_mfma_f32_16x16x32_bf16 v[20:23], a[56:59], v[100:103], v[20:23]\n"
    "v_mfma_f32_16x16x32_bf16 v[24:27], a[56:59], v[104:107], v[24:27]\n"
    "v_mfma_f32_16x16x32_bf16 v[12:15], a[28:31], v[108:111], v[12:15]\n"
    "v_mfma_f32_16x16x32_bf16 v[16:19], a[28:31], v[112:115], v[16:19]\n"
    "v_mfma_f32_16x16x32_bf16 v[20:23], a[60:63], v[108:111], v[20:23]\n"
    "v_mfma_f32_16x16x32_bf16 v[24:27], a[60:63], v[112:115], v[24:27]\n"
    "s_nop 4\n"
    "ds_write_b128 v4, v[12:15] offset:0\n"
    "ds_write_b128 v4, v[16:19] offset:1024\n"
    "ds_write_b128 v4, v[20:23] offset:2048\n"
    "ds_write_b128 v4, v[24:27] offset:3072\n"
    "s_waitcnt lgkmcnt(0)\n"
    "s_barrier\n"
    "ds_read_b128 v[36:39], v5 offset:0\n"
    "ds_read_b128 v[40:43], v5 offset:4096\n"
    "ds_read_b128 v[44:47], v5 offset:8192\n"
    "ds_read_b128 v[48:51], v5 offset:12288\n"
    "v_add_u32 v4, s34, v4\n"
    "v_add_u32 v5, s34, v5\n"
    "s_sub_u32 s34, 0, s34\n"
    "v_lshlrev_b32 v12, 16, v244\n"
    "v_and_b32 v13, 0xffff0000, v244\n"
    "v_lshlrev_b32 v14, 16, v245\n"
    "v_and_b32 v15, 0xffff0000, v245\n"
    "v_lshlrev_b32 v16, 16, v28\n"
    "v_and_b32 v17, 0xffff0000, v28\n"
    "v_pk_add_f32 v[12:13], v[12:13], v[16:17]\n"
    "v_lshlrev_b32 v18, 16, v29\n"
    "v_and_b32 v19, 0xffff0000, v29\n"
    "v_pk_add_f32 v[14:15], v[14:15], v[18:19]\n"
    "v_lshlrev_b32 v20, 16, v30\n"
    "v_and_b32 v21, 0xffff0000, v30\n"
    "v_pk_add_f32 v[12:13], v[12:13], v[20:21]\n"
    "v_lshlrev_b32 v22, 16, v31\n"
    "v_and_b32 v23, 0xffff0000, v31\n"
    "v_pk_add_f32 v[14:15], v[14:15], v[22:23]\n"
    "s_waitcnt lgkmcnt(3)\n"
    "v_pk_add_f32 v[12:13], v[12:13], v[36:37]\n"
    "v_pk_add_f32 v[14:15], v[14:15], v[38:39]\n"
    "s_waitcnt lgkmcnt(2)\n"
    "v_pk_add_f32 v[12:13], v[12:13], v[40:41]\n"
    "v_pk_add_f32 v[14:15], v[14:15], v[42:43]\n"
    "s_waitcnt lgkmcnt(1)\n"
    "v_pk_add_f32 v[12:13], v[12:13], v[44:45]\n"
    "v_pk_add_f32 v[14:15], v[14:15], v[46:47]\n"
    "s_waitcnt lgkmcnt(0)\n"
    "v_pk_add_f32 v[12:13], v[12:13], v[48:49]\n"
    "v_pk_add_f32 v[14:15], v[14:15], v[50:51]\n"
    "v_cvt_pk_bf16_f32 v36, v12, v13\n"
    "v_cvt_pk_bf16_f32 v37, v14, v15\n"
    "buffer_store_dwordx2 v[36:37], v3, s[12:15], s31 offen\n"
    "s_add_u32 s31, s31, s53\n"
    "s_add_u32 s24, s24, s37\n"
    "s_mul_i32 s40, s35, s37\n"
    "s_cmp_lt_u32 s40, 2\n"
    "s_cbranch_scc1 .Lop_142\n"
    "v_readfirstlane_b32 s40, v10\n"
    "s_nop 0\n"
    "s_sub_u32 s40, 61, s40\n"
    "s_mul_i32 s40, s40, 16\n"
    "s_sub_u32 s41, 64, s24\n"
    "s_mul_i32 s41, s41, 7\n"
    "s_cmp_lt_i32 s40, s41\n"
    "s_cbranch_scc1 .Lop_142\n"
    "s_sleep 60\n"
    ".Lop_142:\n"
    "s_cmp_lt_u32 s24, s38\n"
    "s_cbranch_scc0 .LO_done\n"
    "s_cmp_lg_u32 s35, 0\n"
    "s_cbranch_scc1 .LOh_143\n"
    ".LOt_144:\n"
    "buffer_load_dword v10, v6, s[8:11], 0 offen sc1\n"
    "s_waitcnt vmcnt(0)\n"
    "v_cmp_gt_u32 vcc, s54, v10\n"
    "s_cbranch_vccz .LOg_145\n"
    "s_sleep 12\n"
    "s_sub_u32 s25, s25, 1\n"
    "s_nop 1\n"
    "s_cbranch_scc0 .LOt_144\n"
    "s_branch .Lfail\n"
    ".LOg_145:\n"
    "s_sub_u32 s40, s54, 1\n"
    "s_mul_i32 s42, s40, 43\n"
    "s_lshr_b32 s42, s42, 7\n"
    "s_mul_i32 s36, s40, 655360\n"
    "s_lshl_b32 s43, s42, 17\n"
    "s_add_u32 s36, s36, s43\n"
    "s_lshr_b32 s42, s23, s42\n"
    "s_and_b32 s42, s42, 1\n"
    "s_lshl_b32 s42, s42, 13\n"
    "s_add_u32 s36, s36, s42\n"
    "s_add_u32 s36, s36, 161480704\n"
    "s_add_u32 s36, s36, s28\n"
    "s_add_u32 s40, s26, s55\n"
    "s_add_u32 s40, s40, 0\n"
    "buffer_load_dwordx4 v[148:151], v2, s[8:11], s40 offen offset:0 sc1\n"
    "buffer_load_dwordx4 v[156:159], v2, s[8:11], s40 offen offset:1024 sc1\n"
    "buffer_load_dwordx4 v[164:167], v2, s[8:11], s40 offen offset:2048 sc1\n"
    "buffer_load_dwordx4 v[172:175], v2, s[8:11], s40 offen offset:3072 sc1\n"
    "s_add_u32 s40, s26, s55\n"
    "s_add_u32 s40, s40, 32768\n"
    "buffer_load_dwordx4 v[152:155], v2, s[8:11], s40 offen offset:0 sc1\n"
    "buffer_load_dwordx4 v[160:163], v2, s[8:11], s40 offen offset:1024 sc1\n"
    "buffer_load_dwordx4 v[168:171], v2, s[8:11], s40 offen offset:2048 sc1\n"
    "buffer_load_dwordx4 v[176:179], v2, s[8:11], s40 offen offset:3072 sc1\n"
    "s_add_u32 s40, s27, 0\n"
    "buffer_load_dwordx4 v[212:215], v2, s[8:11], s40 offen offset:0 sc1\n"
    "s_add_u32 s40, s36, 0\n"
    "buffer_load_dwordx4 v[228:231], v2, s[8:11], s40 offen offset:0 sc1\n"
    "s_add_u32 s40, s27, 8192\n"
    "buffer_load_dwordx4 v[216:219], v2, s[8:11], s40 offen offset:0 sc1\n"
    "s_add_u32 s40, s36, 16384\n"
    "buffer_load_dwordx4 v[232:235], v2, s[8:11], s40 offen offset:0 sc1\n"
    "s_add_u32 s40, s27, 0\n"
    "buffer_load_dwordx4 v[220:223], v2, s[8:11], s40 offen offset:1024 sc1\n"
    "s_add_u32 s40, s36, 0\n"
    "buffer_load_dwordx4 v[236:239], v2, s[8:11], s40 offen offset:1024 sc1\n"
    "s_add_u32 s40, s27, 8192\n"
    "buffer_load_dwordx4 v[224:227], v2, s[8:11], s40 offen offset:1024 sc1\n"
    "s_add_u32 s40, s36, 16384\n"
    "buffer_load_dwordx4 v[240:243], v2, s[8:11], s40 offen offset:1024 sc1\n"
    "buffer_load_dwordx2 v[32:33], v11, s[8:11], s30 offen sc1\n"
    "s_add_u32 s40, s30, 40960\n"
    "buffer_load_dwordx2 v[34:35], v11, s[8:11], s40 offen sc1\n"
    "buffer_load_dwordx2 v[246:247], v3, s[16:19], s39 offen\n"
    "s_add_u32 s40, s26, s56\n"
    "s_add_u32 s40, s40, 0\n"
    "buffer_load_dwordx4 v[180:183], v2, s[8:11], s40 offen offset:0 sc1\n"
    "buffer_load_dwordx4 v[188:191], v2, s[8:11], s40 offen offset:1024 sc1\n"
    "buffer_load_dwordx4 v[196:199], v2, s[8:11], s40 offen offset:2048 sc1\n"
    "buffer_load_dwordx4 v[204:207], v2, s[8:11], s40 offen offset:3072 sc1\n"
    "s_add_u32 s40, s26, s56\n"
    "s_add_u32 s40, s40, 32768\n"
    "buffer_load_dwordx4 v[184:187], v2, s[8:11], s40 offen offset:0 sc1\n"
    "buffer_load_dwordx4 v[192:195], v2, s[8:11], s40 offen offset:1024 sc1\n"
    "buffer_load_dwordx4 v[200:203], v2, s[8:11], s40 offen offset:2048 sc1\n"
    "buffer_load_dwordx4 v[208:211], v2, s[8:11], s40 offen offset:3072 sc1\n"
    "s_add_u32 s39, s39, s53\n"
    "s_add_u32 s26, s26, s48\n"
    "s_add_u32 s27, s27, s49\n"
    "s_add_u32 s28, s28, s50\n"
    "s_add_u32 s30, s30, s51\n"
    "s_add_u32 s54, s54, s37\n"
    ".LOh_143:\n"
    "s_add_u32 s41, s24, s37\n"
    "s_add_u32 s41, s41, 1\n"
    "s_min_u32 s41, s41, 63\n"
    "v_cmp_gt_u32 vcc, s41, v8\n"
    "s_cbranch_vccz .LOw_149\n"
    "s_nop 3\n"
    ".Lpl_150:\n"
    "buffer_load_dword v8, v9, s[8:11], 0 offen sc1\n"
    "s_waitcnt vmcnt(0)\n"
    "v_cmp_gt_u32 vcc, s41, v8\n"
    "s_nop 1\n"
    "s_cbranch_vccz .Lpd_151\n"
    "s_sleep 1\n"
    "s_sub_u32 s25, s25, 1\n"
    "s_cbranch_scc0 .Lpl_150\n"
    "s_branch .Lfail\n"
    ".Lpd_151:\n"
    ".LOw_149:\n"
    "buffer_load_dword v10, v6, s[8:11], 0 offen sc1\n"
    "s_add_u32 s40, s24, s37\n"
    "s_cmp_lt_u32 s40, s38\n"
    "s_cbranch_scc1 .LOu_148\n"
    "s_mov_b32 s35, 0\n"
    "s_waitcnt vmcnt(0)\n"
    "s_branch .LOl_146\n"
    ".LOu_148:\n"
    "s_add_u32 s40, s29, s55\n"
    "s_add_u32 s40, s40, 0\n"
    "buffer_load_dwordx4 a[0:3], v2, s[8:11], s40 offen offset:0 sc1\n"
    "buffer_load_dwordx4 a[4:7], v2, s[8:11], s40 offen offset:1024 sc1\n"
    "buffer_load_dwordx4 a[8:11], v2, s[8:11], s40 offen offset:2048 sc1\n"
    "buffer_load_dwordx4 a[12:15], v2, s[8:11], s40 offen offset:3072 sc1\n"
    "s_add_u32 s40, s29, s56\n"
    "s_add_u32 s40, s40, 0\n"
    "buffer_load_dwordx4 a[16:19], v2, s[8:11], s40 offen offset:0 sc1\n"
    "buffer_load_dwordx4 a[20:23], v2, s[8:11], s40 offen offset:1024 sc1\n"
    "buffer_load_dwordx4 a[24:27], v2, s[8:11], s40 offen offset:2048 sc1\n"
    "buffer_load_dwordx4 a[28:31], v2, s[8:11], s40 offen offset:3072 sc1\n"
    "s_add_u32 s40, s29, s55\n"
    "s_add_u32 s40, s40, 32768\n"
    "buffer_load_dwordx4 a[32:35], v2, s[8:11], s40 offen offset:0 sc1\n"
    "buffer_load_dwordx4 a[36:39], v2, s[8:11], s40 offen offset:1024 sc1\n"
    "buffer_load_dwordx4 a[40:43], v2, s[8:11], s40 offen offset:2048 sc1\n"
    "buffer_load_dwordx4 a[44:47], v2, s[8:11], s40 offen offset:3072 sc1\n"
    "s_add_u32 s40, s29, s56\n"
    "s_add_u32 s40, s40, 32768\n"
    "buffer_load_dwordx4 a[48:51], v2, s[8:11], s40 offen offset:0 sc1\n"
    "buffer_load_dwordx4 a[52:55], v2, s[8:11], s40 offen offset:1024 sc1\n"
    "buffer_load_dwordx4 a[56:59], v2, s[8:11], s40 offen offset:2048 sc1\n"
    "buffer_load_dwordx4 a[60:63], v2, s[8:11], s40 offen offset:3072 sc1\n"
    "s_add_u32 s29, s29, s52\n"
    "s_waitcnt vmcnt(16)\n"
    "v_cmp_gt_u32 vcc, s54, v10\n"
    "s_mov_b32 s35, 0\n"
    "s_cbranch_vccnz .LOl_146\n"
    "s_mov_b32 s35, 1\n"
    "s_sub_u32 s40, s54, 1\n"
    "s_mul_i32 s42, s40, 43\n"
    "s_lshr_b32 s42, s42, 7\n"
    "s_mul_i32 s36, s40, 655360\n"
    "s_lshl_b32 s43, s42, 17\n"
    "s_add_u32 s36, s36, s43\n"
    "s_lshr_b32 s42, s23, s42\n"
    "s_and_b32 s42, s42, 1\n"
    "s_lshl_b32 s42, s42, 13\n"
    "s_add_u32 s36, s36, s42\n"
    "s_add_u32 s36, s36, 161480704\n"
    "s_add_u32 s36, s36, s28\n"
    "s_add_u32 s40, s26, s55\n"
    "s_add_u32 s40, s40, 0\n"
    "buffer_load_dwordx4 v[52:55], v2, s[8:11], s40 offen offset:0 sc1\n"
    "buffer_load_dwordx4 v[60:63], v2, s[8:11], s40 offen offset:1024 sc1\n"
    "buffer_load_dwordx4 v[68:71], v2, s[8:11], s40 offen offset:2048 sc1\n"
    "buffer_load_dwordx4 v[76:79], v2, s[8:11], s40 offen offset:3072 sc1\n"
    "s_add_u32 s40, s26, s55\n"
    "s_add_u32 s40, s40, 32768\n"
    "buffer_load_dwordx4 v[56:59], v2, s[8:11], s40 offen offset:0 sc1\n"
    "buffer_load_dwordx4 v[64:67], v2, s[8:11], s40 offen offset:1024 sc1\n"
    "buffer_load_dwordx4 v[72:75], v2, s[8:11], s40 offen offset:2048 sc1\n"
    "buffer_load_dwordx4 v[80:83], v2, s[8:11], s40 offen offset:3072 sc1\n"
    "s_add_u32 s40, s27, 0\n"
    "buffer_load_dwordx4 v[116:119], v2, s[8:11], s40 offen offset:0 sc1\n"
    "s_add_u32 s40, s36, 0\n"
    "buffer_load_dwordx4 v[132:135], v2, s[8:11], s40 offen offset:0 sc1\n"
    "s_add_u32 s40, s27, 8192\n"
    "buffer_load_dwordx4 v[120:123], v2, s[8:11], s40 offen offset:0 sc1\n"
    "s_add_u32 s40, s36, 16384\n"
    "buffer_load_dwordx4 v[136:139], v2, s[8:11], s40 offen offset:0 sc1\n"
    "s_add_u32 s40, s27, 0\n"
    "buffer_load_dwordx4 v[124:127], v2, s[8:11], s40 offen offset:1024 sc1\n"
    "s_add_u32 s40, s36, 0\n"
    "buffer_load_dwordx4 v[140:143], v2, s[8:11], s40 offen offset:1024 sc1\n"
    "s_add_u32 s40, s27, 8192\n"
    "buffer_load_dwordx4 v[128:131], v2, s[8:11], s40 offen offset:1024 sc1\n"
    "s_add_u32 s40, s36, 16384\n"
    "buffer_load_dwordx4 v[144:147], v2, s[8:11], s40 offen offset:1024 sc1\n"
    "buffer_load_dwordx2 v[28:29], v11, s[8:11], s30 offen sc1\n"
    "s_add_u32 s40, s30, 40960\n"
    "buffer_load_dwordx2 v[30:31], v11, s[8:11], s40 offen sc1\n"
    "buffer_load_dwordx2 v[244:245], v3, s[16:19], s39 offen\n"
    "s_add_u32 s40, s26, s56\n"
    "s_add_u32 s40, s40, 0\n"
    "buffer_load_dwordx4 v[84:87], v2, s[8:11], s40 offen offset:0 sc1\n"
    "buffer_load_dwordx4 v[92:95], v2, s[8:11], s40 offen offset:1024 sc1\n"
    "buffer_load_dwordx4 v[100:103], v2, s[8:11], s40 offen offset:2048 sc1\n"
    "buffer_load_dwordx4 v[108:111], v2, s[8:11], s40 offen offset:3072 sc1\n"
    "s_add_u32 s40, s26, s56\n"
    "s_add_u32 s40, s40, 32768\n"
    "buffer_load_dwordx4 v[88:91], v2, s[8:11], s40 offen offset:0 sc1\n"
    "buffer_load_dwordx4 v[96:99], v2, s[8:11], s40 offen offset:1024 sc1\n"
    "buffer_load_dwordx4 v[104:107], v2, s[8:11], s40 offen offset:2048 sc1\n"
    "buffer_load_dwordx4 v[112:115], v2, s[8:11], s40 offen offset:3072 sc1\n"
    "s_add_u32 s39, s39, s53\n"
    "s_add_u32 s26, s26, s48\n"
    "s_add_u32 s27, s27, s49\n"
    "s_add_u32 s28, s28, s50\n"
    "s_add_u32 s30, s30, s51\n"
    "s_add_u32 s54, s54, s37\n"
    ".LOl_146:\n"
    "v_mfma_f32_16x16x32_bf16 v[12:15], v[228:231], v[212:215], 0\n"
    "v_mfma_f32_16x16x32_bf16 v[16:19], v[228:231], v[216:219], 0\n"
    "v_mfma_f32_16x16x32_bf16 v[20:23], v[232:235], v[212:215], 0\n"
    "v_mfma_f32_16x16x32_bf16 v[24:27], v[232:235], v[216:219], 0\n"
    "v_mfma_f32_16x16x32_bf16 v[12:15], v[236:239], v[220:223], v[12:15]\n"
    "v_mfma_f32_16x16x32_bf16 v[16:19], v[236:239], v[224:227], v[16:19]\n"
    "v_mfma_f32_16x16x32_bf16 v[20:23], v[240:243], v[220:223], v[20:23]\n"
    "v_mfma_f32_16x16x32_bf16 v[24:27], v[240:243], v[224:227], v[24:27]\n"
    "v_mfma_f32_16x16x32_bf16 v[12:15], a[64:67], v[148:151], v[12:15]\n"
    "v_mfma_f32_16x16x32_bf16 v[16:19], a[64:67], v[152:155], v[16:19]\n"
    "v_mfma_f32_16x16x32_bf16 v[20:23], a[96:99], v[148:151], v[20:23]\n"
    "v_mfma_f32_16x16x32_bf16 v[24:27], a[96:99], v[152:155], v[24:27]\n"
    "v_mfma_f32_16x16x32_bf16 v[12:15], a[68:71], v[156:159], v[12:15]\n"
    "v_mfma_f32_16x16x32_bf16 v[16:19], a[68:71], v[160:163], v[16:19]\n"
    "v_mfma_f32_16x16x32_bf16 v[20:23], a[100:103], v[156:159], v[20:23]\n"
    "v_mfma_f32_16x16x32_bf16 v[24:27], a[100:103], v[160:163], v[24:27]\n"
    "v_mfma_f32_16x16x32_bf16 v[12:15], a[72:75], v[164:167], v[12:15]\n"
    "v_mfma_f32_16x16x32_bf16 v[16:19], a[72:75], v[168:171], v[16:19]\n"
    "v_mfma_f32_16x16x32_bf16 v[20:23], a[104:107], v[164:167], v[20:23]\n"
    "v_mfma_f32_16x16x32_bf16 v[24:27], a[104:107], v[168:171], v[24:27]\n"
    "v_mfma_f32_16x16x32_bf16 v[12:15], a[76:79], v[172:175], v[12:15]\n"
    "v_mfma_f32_16x16x32_bf16 v[16:19], a[76:79], v[176:179], v[16:19]\n"
    "v_mfma_f32_16x16x32_bf16 v[20:23], a[108:111], v[172:175], v[20:23]\n"
    "v_mfma_f32_16x16x32_bf16 v[24:27], a[108:111], v[176:179], v[24:27]\n"
    "v_mfma_f32_16x16x32_bf16 v[12:15], a[80:83], v[180:183], v[12:15]\n"
    "v_mfma_f32_16x16x32_bf16 v[16:19], a[80:83], v[184:187], v[16:19]\n"
    "v_mfma_f32_16x16x32_bf16 v[20:23], a[112:115], v[180:183], v[20:23]\n"
    "v_mfma_f32_16x16x32_bf16 v[24:27], a[112:115], v[184:187], v[24:27]\n"
    "v_mfma_f32_16x16x32_bf16 v[12:15], a[84:87], v[188:191], v[12:15]\n"
    "v_mfma_f32_16x16x32_bf16 v[16:19], a[84:87], v[192:195], v[16:19]\n"
    "v_mfma_f32_16x16x32_bf16 v[20:23], a[116:119], v[188:191], v[20:23]\n"
    "v_mfma_f32_16x16x32_bf16 v[24:27], a[116:119], v[192:195], v[24:27]\n"
    "v_mfma_f32_16x16x32_bf16 v[12:15], a[88:91], v[196:199], v[12:15]\n"
    "v_mfma_f32_16x16x32_bf16 v[16:19], a[88:91], v[200:203], v[16:19]\n"
    "v_mfma_f32_16x16x32_bf16 v[20:23], a[120:123], v[196:199], v[20:23]\n"
    "v_mfma_f32_16x16x32_bf16 v[24:27], a[120:123], v[200:203], v[24:27]\n"
    "v_mfma_f32_16x16x32_bf16 v[12:15], a[92:95], v[204:207], v[12:15]\n"
    "v_mfma_f32_16x16x32_bf16 v[16:19], a[92:95], v[208:211], v[16:19]\n"
    "v_mfma_f32_16x16x32_bf16 v[20:23], a[124:127], v[204:207], v[20:23]\n"
    "v_mfma_f32_16x16x32_bf16 v[24:27], a[124:127], v[208:211], v[24:27]\n"
    "s_nop 4\n"
    "ds_write_b128 v4, v[12:15] offset:0\n"
    "ds_write_b128 v4, v[16:19] offset:1024\n"
    "ds_write_b128 v4, v[20:23] offset:2048\n"
    "ds_write_b128 v4, v[24:27] offset:3072\n"
    "s_waitcnt lgkmcnt(0)\n"
    "s_barrier\n"
    "ds_read_b128 v[36:39], v5 offset:0\n"
    "ds_read_b128 v[40:43], v5 offset:4096\n"
    "ds_read_b128 v[44:47], v5 offset:8192\n"
    "ds_read_b128 v[48:51], v5 offset:12288\n"
    "v_add_u32 v4, s34, v4\n"
    "v_add_u32 v5, s34, v5\n"
    "s_sub_u32 s34, 0, s34\n"
    "v_lshlrev_b32 v12, 16, v246\n"
    "v_and_b32 v13, 0xffff0000, v246\n"
    "v_lshlrev_b32 v14, 16, v247\n"
    "v_and_b32 v15, 0xffff0000, v247\n"
    "v_lshlrev_b32 v16, 16, v32\n"
    "v_and_b32 v17, 0xffff0000, v32\n"
    "v_pk_add_f32 v[12:13], v[12:13], v[16:17]\n"
    "v_lshlrev_b32 v18, 16, v33\n"
    "v_and_b32 v19, 0xffff0000, v33\n"
    "v_pk_add_f32 v[14:15], v[14:15], v[18:19]\n"
    "v_lshlrev_b32 v20, 16, v34\n"
    "v_and_b32 v21, 0xffff0000, v34\n"
    "v_pk_add_f32 v[12:13], v[12:13], v[20:21]\n"
    "v_lshlrev_b32 v22, 16, v35\n"
    "v_and_b32 v23, 0xffff0000, v35\n"
    "v_pk_add_f32 v[14:15], v[14:15], v[22:23]\n"
    "s_waitcnt lgkmcnt(3)\n"
    "v_pk_add_f32 v[12:13], v[12:13], v[36:37]\n"
    "v_pk_add_f32 v[14:15], v[14:15], v[38:39]\n"
    "s_waitcnt lgkmcnt(2)\n"
    "v_pk_add_f32 v[12:13], v[12:13], v[40:41]\n"
    "v_pk_add_f32 v[14:15], v[14:15], v[42:43]\n"
    "s_waitcnt lgkmcnt(1)\n"
    "v_pk_add_f32 v[12:13], v[12:13], v[44:45]\n"
    "v_pk_add_f32 v[14:15], v[14:15], v[46:47]\n"
    "s_waitcnt lgkmcnt(0)\n"
    "v_pk_add_f32 v[12:13], v[12:13], v[48:49]\n"
    "v_pk_add_f32 v[14:15], v[14:15], v[50:51]\n"
    "v_cvt_pk_bf16_f32 v36, v12, v13\n"
    "v_cvt_pk_bf16_f32 v37, v14, v15\n"
    "buffer_store_dwordx2 v[36:37], v3, s[12:15], s31 offen\n"
    "s_add_u32 s31, s31, s53\n"
    "s_add_u32 s24, s24, s37\n"
    "s_mul_i32 s40, s35, s37\n"
    "s_cmp_lt_u32 s40, 2\n"
    "s_cbranch_scc1 .Lop_152\n"
    "v_readfirstlane_b32 s40, v10\n"
    "s_nop 0\n"
    "s_sub_u32 s40, 61, s40\n"
    "s_mul_i32 s40, s40, 16\n"
    "s_sub_u32 s41, 64, s24\n"
    "s_mul_i32 s41, s41, 7\n"
    "s_cmp_lt_i32 s40, s41\n"
    "s_cbranch_scc1 .Lop_152\n"
    "s_sleep 60\n"
    ".Lop_152:\n"
    "s_cmp_lt_u32 s24, s38\n"
    "s_cbranch_scc1 .LO_loop\n"
    ".LO_done:\n"
    "s_cmp_eq_u32 s37, 1\n"
    "s_cbranch_scc1 .LO_more\n"
    "s_endpgm\n"
    ".LO_more:\n"
    "s_waitcnt lgkmcnt(0)\n"
    "s_barrier\n"
    "s_add_u32 s20, s20, 60\n"
    "s_branch .LZ_enter\n"
    ".LZ_enter:\n"
    "s_mov_b32 s12, s80\n"
    "s_and_b32 s13, s81, 0xffff\n"
    "s_mov_b32 s14, 2621440\n"
    "s_mov_b32 s15, 0x20000\n"
    "s_mov_b32 s16, s64\n"
    "s_and_b32 s17, s65, 0xffff\n"
    "s_mov_b32 s18, 2621440\n"
    "s_mov_b32 s19, 0x20000\n"
    "v_min_u32 v10, 54, v1\n"
    "v_lshlrev_b32 v10, 7, v10\n"
    "v_add_u32 v10, 4182016, v10\n"
    "buffer_load_dwordx2 v[8:9], v10, s[8:11], 0 offen sc1\n"
    "s_waitcnt vmcnt(0)\n"
    "v_cmp_ne_u32 vcc, s4, v8\n"
    "s_nop 1\n"
    "s_cmp_eq_u32 vcc_lo, 0\n"
    "s_cbranch_scc1 .Lrx_155\n"
    ".Lrp_153:\n"
    "v_min_u32 v10, 54, v1\n"
    "v_lshlrev_b32 v10, 7, v10\n"
    "v_add_u32 v10, 4182016, v10\n"
    "buffer_load_dwordx2 v[8:9], v10, s[8:11], 0 offen sc1\n"
    "s_waitcnt vmcnt(0)\n"
    "v_cmp_ne_u32 vcc, s4, v8\n"
    "s_nop 1\n"
    "s_cmp_eq_u32 vcc_lo, 0\n"
    "s_cbranch_scc1 .Lro_154\n"
    "s_sleep 2\n"
    "s_sub_u32 s25, s25, 1\n"
    "s_cbranch_scc0 .Lrp_153\n"
    "s_branch .Lfail\n"
    ".Lro_154:\n"
    ".Lrx_155:\n"
    "v_cmp_ne_u32 vcc, s4, v8\n"
    "s_nop 1\n"
    "s_cmp_eq_u32 vcc_hi, 0\n"
    "s_cbranch_scc1 .Lrx_158\n"
    ".Lrp_156:\n"
    "v_min_u32 v10, 54, v1\n"
    "v_lshlrev_b32 v10, 7, v10\n"
    "v_add_u32 v10, 4182016, v10\n"
    "buffer_load_dwordx2 v[8:9], v10, s[8:11], 0 offen sc1\n"
    "s_waitcnt vmcnt(0)\n"
    "v_cmp_ne_u32 vcc, s4, v8\n"
    "s_nop 1\n"
    "s_cmp_eq_u32 vcc_hi, 0\n"
    "s_cbranch_scc1 .Lro_157\n"
    "s_sleep 2\n"
    "s_sub_u32 s25, s25, 1\n"
    "s_cbranch_scc0 .Lrp_156\n"
    "s_branch .Lfail\n"
    ".Lro_157:\n"
    ".Lrx_158:\n"
    "v_cmp_ne_u32 vcc, 0, v9\n"
    "s_nop 1\n"
    "s_and_b32 s22, vcc_hi, 1\n"
    "s_lshl_b32 s22, s22, 13\n"
    "s_lshr_b32 s23, vcc_hi, 1\n"
    "s_cmp_ge_u32 s20, 40\n"
    "s_cselect_b32 s36, 1, 0\n"
    "s_cselect_b32 s40, 40, 0\n"
    "s_sub_u32 s20, s20, s40\n"
    "s_sub_u32 s37, 63, s36\n"
    "s_lshr_b32 s26, s20, 1\n"
    "s_and_b32 s27, s20, 1\n"
    "v_lshlrev_b32 v2, 4, v1\n"
    "v_and_b32 v0, 15, v1\n"
    "v_mul_u32_u24 v3, 1280, v0\n"
    "v_bfe_u32 v0, v1, 4, 2\n"
    "v_lshl_add_u32 v3, v0, 3, v3\n"
    "s_and_b32 s44, s21, 1\n"
    "s_lshl_b32 s45, s26, 1\n"
    "s_add_u32 s45, s45, s44\n"
    "s_mul_i32 s28, s27, 20480\n"
    "s_lshl_b32 s40, s45, 5\n"
    "s_add_u32 s28, s28, s40\n"
    "s_mul_i32 s38, s37, 40960\n"
    "s_add_u32 s38, s38, 0\n"
    "s_add_u32 s28, s28, s38\n"
    "s_lshl_b32 s40, s21, 11\n"
    "v_add_u32 v6, s40, v2\n"
    "s_lshl_b32 s40, s44, 10\n"
    "v_add_u32 v7, s40, v2\n"
    "s_mul_i32 s40, s45, 52\n"
    "s_lshr_b32 s40, s40, 8\n"
    "s_and_b32 s41, s40, 6\n"
    "s_lshl_b32 s42, s41, 11\n"
    "s_lshl_b32 s41, s41, 2\n"
    "s_and_b32 s40, s40, 1\n"
    "s_add_u32 s40, s40, s41\n"
    "s_lshl_b32 s41, s27, 1\n"
    "s_add_u32 s40, s40, s41\n"
    "s_lshl_b32 s40, s40, 7\n"
    "s_add_u32 s40, s40, 4284416\n"
    "v_bfe_u32 v4, v1, 0, 1\n"
    "v_lshlrev_b32 v4, 9, v4\n"
    "v_add_u32 v4, s40, v4\n"
    "s_lshl_b32 s43, s21, 10\n"
    "s_add_u32 s40, s42, s43\n"
    "s_mul_i32 s38, s37, 16384\n"
    "s_add_u32 s38, s38, 3211264\n"
    "s_add_u32 s40, s40, s38\n"
    "v_bfe_u32 v5, v1, 0, 3\n"
    "v_lshlrev_b32 v5, 7, v5\n"
    "v_add_u32 v0, s40, v5\n"
    "v_cmp_gt_u32 vcc, 8, v1\n"
    "s_nop 1\n"
    "v_cndmask_b32 v4, v4, v0, vcc\n"
    "s_lshl_b32 s40, s27, 12\n"
    "s_add_u32 s40, s40, s43\n"
    "s_add_u32 s40, s40, 4259840\n"
    "v_add_u32 v5, s40, v5\n"
    "s_add_u32 s41, s37, 1\n"
    "s_nop 0\n"
    ".Lpl_159:\n"
    "buffer_load_dword v32, v4, s[8:11], 0 offen sc1\n"
    "s_waitcnt vmcnt(0)\n"
    "v_cmp_gt_u32 vcc, s41, v32\n"
    "s_nop 1\n"
    "s_cbranch_vccz .Lpd_160\n"
    "s_sleep 32\n"
    "s_sub_u32 s25, s25, 1\n"
    "s_cbranch_scc0 .Lpl_159\n"
    "s_branch .Lfail\n"
    ".Lpd_160:\n"
    "s_lshl_b32 s42, s26, 16\n"
    "s_lshl_b32 s43, s21, 13\n"
    "s_add_u32 s42, s42, s43\n"
    "s_mul_i32 s38, s37, 1310720\n"
    "s_add_u32 s38, s38, 59768832\n"
    "s_add_u32 s42, s42, s38\n"
    "s_add_u32 s40, s42, 0\n"
    "buffer_load_dwordx4 a[0:3], v2, s[8:11], s40 offen offset:0 sc1\n"
    "buffer_load_dwordx4 a[4:7], v2, s[8:11], s40 offen offset:1024 sc1\n"
    "buffer_load_dwordx4 a[8:11], v2, s[8:11], s40 offen offset:2048 sc1\n"
    "buffer_load_dwordx4 a[12:15], v2, s[8:11], s40 offen offset:3072 sc1\n"
    "s_add_u32 s40, s42, 4096\n"
    "buffer_load_dwordx4 a[16:19], v2, s[8:11], s40 offen offset:0 sc1\n"
    "buffer_load_dwordx4 a[20:23], v2, s[8:11], s40 offen offset:1024 sc1\n"
    "buffer_load_dwordx4 a[24:27], v2, s[8:11], s40 offen offset:2048 sc1\n"
    "buffer_load_dwordx4 a[28:31], v2, s[8:11], s40 offen offset:3072 sc1\n"
    "s_add_u32 s40, s42, 32768\n"
    "buffer_load_dwordx4 a[32:35], v2, s[8:11], s40 offen offset:0 sc1\n"
    "buffer_load_dwordx4 a[36:39], v2, s[8:11], s40 offen offset:1024 sc1\n"
    "buffer_load_dwordx4 a[40:43], v2, s[8:11], s40 offen offset:2048 sc1\n"
    "buffer_load_dwordx4 a[44:47], v2, s[8:11], s40 offen offset:3072 sc1\n"
    "s_add_u32 s40, s42, 36864\n"
    "buffer_load_dwordx4 a[48:51], v2, s[8:11], s40 offen offset:0 sc1\n"
    "buffer_load_dwordx4 a[52:55], v2, s[8:11], s40 offen offset:1024 sc1\n"
    "buffer_load_dwordx4 a[56:59], v2, s[8:11], s40 offen offset:2048 sc1\n"
    "buffer_load_dwordx4 a[60:63], v2, s[8:11], s40 offen offset:3072 sc1\n"
    "s_lshl_b32 s40, s45, 1\n"
    "s_add_u32 s40, s40, s27\n"
    "s_lshl_b32 s40, s40, 9\n"
    "v_lshl_add_u32 v0, v1, 3, s40\n"
    "buffer_load_dwordx2 v[32:33], v3, s[16:19], s28 offen\n"
    "s_mul_i32 s38, s37, 81920\n"
    "s_add_u32 s38, s38, 155189248\n"
    "s_add_u32 s40, 0, s38\n"
    "buffer_load_dwordx2 v[34:35], v0, s[8:11], s40 offen sc1\n"
    "s_mul_i32 s38, s37, 81920\n"
    "s_add_u32 s38, s38, 155230208\n"
    "s_add_u32 s40, 0, s38\n"
    "buffer_load_dwordx2 v[36:37], v0, s[8:11], s40 offen sc1\n"
    "s_waitcnt vmcnt(2)\n"
    "v_lshlrev_b32 v16, 16, v32\n"
    "v_and_b32 v17, 0xffff0000, v32\n"
    "v_lshlrev_b32 v18, 16, v33\n"
    "v_and_b32 v19, 0xffff0000, v33\n"
    "s_waitcnt vmcnt(1)\n"
    "v_lshlrev_b32 v20, 16, v34\n"
    "v_and_b32 v21, 0xffff0000, v34\n"
    "v_pk_add_f32 v[16:17], v[16:17], v[20:21]\n"
    "v_lshlrev_b32 v22, 16, v35\n"
    "v_and_b32 v23, 0xffff0000, v35\n"
    "v_pk_add_f32 v[18:19], v[18:19], v[22:23]\n"
    "s_waitcnt vmcnt(0)\n"
    "v_lshlrev_b32 v24, 16, v36\n"
    "v_and_b32 v25, 0xffff0000, v36\n"
    "v_pk_add_f32 v[16:17], v[16:17], v[24:25]\n"
    "v_lshlrev_b32 v26, 16, v37\n"
    "v_and_b32 v27, 0xffff0000, v37\n"
    "v_pk_add_f32 v[18:19], v[18:19], v[26:27]\n"
    "s_mul_i32 s46, s37, 43\n"
    "s_lshr_b32 s46, s46, 7\n"
    "s_mul_i32 s30, s37, 655360\n"
    "s_lshl_b32 s47, s46, 17\n"
    "s_add_u32 s30, s30, s47\n"
    "s_lshr_b32 s46, s23, s46\n"
    "s_and_b32 s46, s46, 1\n"
    "s_lshl_b32 s46, s46, 13\n"
    "s_add_u32 s30, s30, s46\n"
    "s_add_u32 s30, s30, 161480704\n"
    "s_lshl_b32 s40, s26, 15\n"
    "s_add_u32 s30, s30, s40\n"
    "s_lshr_b32 s40, s43, 2\n"
    "s_add_u32 s30, s30, s40\n"
    "s_add_u32 s31, s30, 16384\n"
    "s_lshl_b32 s40, s27, 15\n"
    "s_add_u32 s40, s40, s43\n"
    "s_mul_i32 s38, s37, 65536\n"
    "s_add_u32 s38, s38, 143654912\n"
    "s_add_u32 s29, s40, s38\n"
    "s_add_u32 s34, s29, 4096\n"
    "s_lshr_b32 s40, s40, 2\n"
    "s_mul_i32 s38, s37, 16384\n"
    "s_add_u32 s38, s38, 147849216\n"
    "s_add_u32 s35, s40, s38\n"
    "s_mov_b64 exec, 0xff\n"
    "s_mov_b32 s41, 1\n"
    "v_mov_b32 v4, 4268032\n"
    "s_nop 1\n"
    ".Lpl_161:\n"
    "buffer_load_dword v32, v4, s[8:11], 0 offen sc1\n"
    "s_waitcnt vmcnt(0)\n"
    "v_cmp_gt_u32 vcc, s41, v32\n"
    "s_nop 1\n"
    "s_cbranch_vccz .Lpd_162\n"
    "s_sleep 4\n"
    "s_sub_u32 s25, s25, 1\n"
    "s_cbranch_scc0 .Lpl_161\n"
    "s_branch .Lfail\n"
    ".Lpd_162:\n"
    "s_sleep 20\n"
    "s_lshl_b32 s40, s36, 1\n"
    "s_sub_u32 s41, 64, s40\n"
    ".Lpl_163:\n"
    "buffer_load_dword v32, v5, s[8:11], 0 offen sc1\n"
    "s_waitcnt vmcnt(0)\n"
    "v_cmp_gt_u32 vcc, s41, v32\n"
    "s_nop 1\n"
    "s_cbranch_vccz .Lpd_164\n"
    "s_sleep 1\n"
    "s_sub_u32 s25, s25, 1\n"
    "s_cbranch_scc0 .Lpl_163\n"
    "s_branch .Lfail\n"
    ".Lpd_164:\n"
    "s_mov_b64 exec, -1\n"
    "s_nop 15\n"
    ".LZ_try:\n"
    "buffer_load_dwordx4 v[88:91], v2, s[8:11], s30 offen offset:0 sc1\n"
    "buffer_load_dwordx4 v[92:95], v2, s[8:11], s31 offen offset:0 sc1\n"
    "buffer_load_dwordx4 v[96:99], v2, s[8:11], s30 offen offset:1024 sc1\n"
    "buffer_load_dwordx4 v[100:103], v2, s[8:11], s31 offen offset:1024 sc1\n"
    "buffer_load_dwordx4 v[48:51], v2, s[8:11], s29 offen offset:0 sc1\n"
    "buffer_load_dwordx4 v[52:55], v2, s[8:11], s29 offen offset:1024 sc1\n"
    "buffer_load_dwordx4 v[56:59], v2, s[8:11], s29 offen offset:2048 sc1\n"
    "buffer_load_dwordx4 v[60:63], v2, s[8:11], s29 offen offset:3072 sc1\n"
    "buffer_load_dwordx4 v[64:67], v2, s[8:11], s34 offen offset:0 sc1\n"
    "buffer_load_dwordx4 v[68:71], v2, s[8:11], s34 offen offset:1024 sc1\n"
    "buffer_load_dwordx4 v[72:75], v2, s[8:11], s34 offen offset:2048 sc1\n"
    "buffer_load_dwordx4 v[76:79], v2, s[8:11], s34 offen offset:3072 sc1\n"
    "buffer_load_dwordx4 v[80:83], v2, s[8:11], s35 offen offset:0 sc1\n"
    "buffer_load_dwordx4 v[84:87], v2, s[8:11], s35 offen offset:1024 sc1\n"
    "v_mov_b32 v32, 0\n"
    "s_waitcnt vmcnt(13)\n"
    "v_max3_u32 v32, v32, v88, v89\n"
    "v_max3_u32 v32, v32, v90, v91\n"
    "s_waitcnt vmcnt(12)\n"
    "v_max3_u32 v32, v32, v92, v93\n"
    "v_max3_u32 v32, v32, v94, v95\n"
    "s_waitcnt vmcnt(11)\n"
    "v_max3_u32 v32, v32, v96, v97\n"
    "v_max3_u32 v32, v32, v98, v99\n"
    "s_waitcnt vmcnt(10)\n"
    "v_max3_u32 v32, v32, v100, v101\n"
    "v_max3_u32 v32, v32, v102, v103\n"
    "s_waitcnt vmcnt(9)\n"
    "v_mfma_f32_16x16x32_bf16 v[8:11], a[0:3], v[48:51], 0\n"
    "v_mfma_f32_16x16x32_bf16 v[12:15], a[32:35], v[48:51], 0\n"
    "v_max3_u32 v32, v32, v48, v49\n"
    "v_max3_u32 v32, v32, v50, v51\n"
    "s_waitcnt vmcnt(8)\n"
    "v_mfma_f32_16x16x32_bf16 v[8:11], a[4:7], v[52:55], v[8:11]\n"
    "v_mfma_f32_16x16x32_bf16 v[12:15], a[36:39], v[52:55], v[12:15]\n"
    "v_max3_u32 v32, v32, v52, v53\n"
    "v_max3_u32 v32, v32, v54, v55\n"
    "s_waitcnt vmcnt(7)\n"
    "v_mfma_f32_16x16x32_bf16 v[8:11], a[8:11], v[56:59], v[8:11]\n"
    "v_mfma_f32_16x16x32_bf16 v[12:15], a[40:43], v[56:59], v[12:15]\n"
    "v_max3_u32 v32, v32, v56, v57\n"
    "v_max3_u32 v32, v32, v58, v59\n"
    "s_waitcnt vmcnt(6)\n"
    "v_mfma_f32_16x16x32_bf16 v[8:11], a[12:15], v[60:63], v[8:11]\n"
    "v_mfma_f32_16x16x32_bf16 v[12:15], a[44:47], v[60:63], v[12:15]\n"
    "v_max3_u32 v32, v32, v60, v61\n"
    "v_max3_u32 v32, v32, v62, v63\n"
    "s_waitcnt vmcnt(5)\n"
    "v_mfma_f32_16x16x32_bf16 v[8:11], a[16:19], v[64:67], v[8:11]\n"
    "v_mfma_f32_16x16x32_bf16 v[12:15], a[48:51], v[64:67], v[12:15]\n"
    "v_max3_u32 v32, v32, v64, v65\n"
    "v_max3_u32 v32, v32, v66, v67\n"
    "s_waitcnt vmcnt(4)\n"
    "v_mfma_f32_16x16x32_bf16 v[8:11], a[20:23], v[68:71], v[8:11]\n"
    "v_mfma_f32_16x16x32_bf16 v[12:15], a[52:55], v[68:71], v[12:15]\n"
    "v_max3_u32 v32, v32, v68, v69\n"
    "v_max3_u32 v32, v32, v70, v71\n"
    "s_waitcnt vmcnt(3)\n"
    "v_mfma_f32_16x16x32_bf16 v[8:11], a[24:27], v[72:75], v[8:11]\n"
    "v_mfma_f32_16x16x32_bf16 v[12:15], a[56:59], v[72:75], v[12:15]\n"
    "v_max3_u32 v32, v32, v72, v73\n"
    "v_max3_u32 v32, v32, v74, v75\n"
    "s_waitcnt vmcnt(2)\n"
    "v_mfma_f32_16x16x32_bf16 v[8:11], a[28:31], v[76:79], v[8:11]\n"
    "v_mfma_f32_16x16x32_bf16 v[12:15], a[60:63], v[76:79], v[12:15]\n"
    "v_max3_u32 v32, v32, v76, v77\n"
    "v_max3_u32 v32, v32, v78, v79\n"
    "s_waitcnt vmcnt(1)\n"
    "v_mfma_f32_16x16x32_bf16 v[8:11], v[88:91], v[80:83], v[8:11]\n"
    "v_mfma_f32_16x16x32_bf16 v[12:15], v[92:95], v[80:83], v[12:15]\n"
    "v_max3_u32 v32, v32, v80, v81\n"
    "v_max3_u32 v32, v32, v82, v83\n"
    "s_waitcnt vmcnt(0)\n"
    "v_mfma_f32_16x16x32_bf16 v[8:11], v[96:99], v[84:87], v[8:11]\n"
    "v_mfma_f32_16x16x32_bf16 v[12:15], v[100:103], v[84:87], v[12:15]\n"
    "v_max3_u32 v32, v32, v84, v85\n"
    "v_max3_u32 v32, v32, v86, v87\n"
    "v_cmp_eq_u32 vcc, -1, v32\n"
    "s_cbranch_vccz .LZ_got\n"
    "s_nop 7\n"
    "s_sub_u32 s25, s25, 1\n"
    "s_cbranch_scc0 .LZ_try\n"
    "s_branch .Lfail\n"
    ".LZ_got:\n"
    "s_nop 3\n"
    "ds_write_b128 v6, v[8:11] offset:0\n"
    "ds_write_b128 v6, v[12:15] offset:1024\n"
    "s_cmp_ge_u32 s21, 2\n"
    "s_waitcnt lgkmcnt(0)\n"
    "s_barrier\n"
    "s_cbranch_scc1 .Lend\n"
    "ds_read_b128 v[32:35], v7 offset:0\n"
    "ds_read_b128 v[36:39], v7 offset:2048\n"
    "ds_read_b128 v[40:43], v7 offset:4096\n"
    "ds_read_b128 v[44:47], v7 offset:6144\n"
    "s_waitcnt lgkmcnt(3)\n"
    "v_pk_add_f32 v[16:17], v[16:17], v[32:33]\n"
    "v_pk_add_f32 v[18:19], v[18:19], v[34:35]\n"
    "s_waitcnt lgkmcnt(2)\n"
    "v_pk_add_f32 v[16:17], v[16:17], v[36:37]\n"
    "v_pk_add_f32 v[18:19], v[18:19], v[38:39]\n"
    "s_waitcnt lgkmcnt(1)\n"
    "v_pk_add_f32 v[16:17], v[16:17], v[40:41]\n"
    "v_pk_add_f32 v[18:19], v[18:19], v[42:43]\n"
    "s_waitcnt lgkmcnt(0)\n"
    "v_pk_add_f32 v[16:17], v[16:17], v[44:45]\n"
    "v_pk_add_f32 v[18:19], v[18:19], v[46:47]\n"
    "v_cvt_pk_bf16_f32 v32, v16, v17\n"
    "v_cvt_pk_bf16_f32 v33, v18, v19\n"
    "buffer_store_dwordx2 v[32:33], v3, s[12:15], s28 offen\n"
    "s_endpgm\n"
    ".Lrole_P:\n"
    "s_lshl_b32 s27, s20, 2\n"
    "s_add_u32 s27, s27, s21\n"
    "s_mul_i32 s40, s27, 171\n"
    "s_lshr_b32 s40, s40, 9\n"
    "s_mul_i32 s41, s40, 3\n"
    "s_sub_u32 s41, s27, s41\n"
    "s_lshr_b32 s42, s40, 1\n"
    "s_mul_i32 s42, s42, 384\n"
    "s_lshl_b32 s41, s41, 7\n"
    "s_add_u32 s48, s41, s42\n"
    "s_bitcmp0_b32 s40, 0\n"
    "s_cselect_b64 s[44:45], s[66:67], s[68:69]\n"
    "s_mov_b32 s12, s44\n"
    "s_and_b32 s13, s45, 0xffff\n"
    "s_mov_b32 s14, 7864320\n"
    "s_mov_b32 s15, 0x20000\n"
    "s_mul_i32 s26, s21, 4096\n"
    "s_add_u32 s26, s26, 40960\n"
    "s_and_b32 s40, s21, 0\n"
    "s_mul_i32 s41, s40, 256\n"
    "v_add_u32 v8, s41, v1\n"
    "s_add_u32 s41, s41, 64\n"
    "v_mul_u32_u24 v0, 8192, v8\n"
    "v_lshrrev_b32 v0, 16, v0\n"
    "v_mul_u32_u24 v12, 8, v0\n"
    "v_sub_u32 v8, v8, v12\n"
    "v_mul_u32_u24 v0, 12288, v0\n"
    "v_lshl_add_u32 v8, v8, 4, v0\n"
    "v_add_u32 v9, s41, v1\n"
    "s_add_u32 s41, s41, 64\n"
    "v_mul_u32_u24 v0, 8192, v9\n"
    "v_lshrrev_b32 v0, 16, v0\n"
    "v_mul_u32_u24 v12, 8, v0\n"
    "v_sub_u32 v9, v9, v12\n"
    "v_mul_u32_u24 v0, 12288, v0\n"
    "v_lshl_add_u32 v9, v9, 4, v0\n"
    "v_add_u32 v10, s41, v1\n"
    "s_add_u32 s41, s41, 64\n"
    "v_mul_u32_u24 v0, 8192, v10\n"
    "v_lshrrev_b32 v0, 16, v0\n"
    "v_mul_u32_u24 v12, 8, v0\n"
    "v_sub_u32 v10, v10, v12\n"
    "v_mul_u32_u24 v0, 12288, v0\n"
    "v_lshl_add_u32 v10, v10, 4, v0\n"
    "v_add_u32 v11, s41, v1\n"
    "s_add_u32 s41, s41, 64\n"
    "v_mul_u32_u24 v0, 8192, v11\n"
    "v_lshrrev_b32 v0, 16, v0\n"
    "v_mul_u32_u24 v12, 8, v0\n"
    "v_sub_u32 v11, v11, v12\n"
    "v_mul_u32_u24 v0, 12288, v0\n"
    "v_lshl_add_u32 v11, v11, 4, v0\n"
    "s_mul_i32 s40, s40, 3968\n"
    "s_sub_u32 s40, s26, s40\n"
    "v_bfe_u32 v12, v1, 4, 2\n"
    "v_lshlrev_b32 v12, 3, v12\n"
    "v_bfe_u32 v0, v1, 2, 2\n"
    "v_lshl_add_u32 v12, v0, 0, v12\n"
    "v_mul_u32_u24 v12, 128, v12\n"
    "v_and_b32 v0, 3, v1\n"
    "v_mul_u32_u24 v0, 16, v0\n"
    "v_add3_u32 v12, v12, v0, s40\n"
    "v_add_u32 v13, 57344, v12\n"
    "v_add_u32 v14, 114688, v12\n"
    "s_lshl_b32 s31, s27, 12\n"
    "s_add_u32 s31, s31, 9437184\n"
    "v_lshlrev_b32 v2, 4, v1\n"
    "s_mul_i32 s29, s21, 10240\n"
    "s_add_u32 s28, s29, 6291456\n"
    "s_add_u32 s40, s48, 0\n"
    "s_add_u32 m0, s26, 0\n"
    "s_nop 0\n"
    "buffer_load_dwordx4 v8, s[12:15], s40 offen lds\n"
    "s_add_u32 m0, s26, 1024\n"
    "s_nop 0\n"
    "buffer_load_dwordx4 v9, s[12:15], s40 offen lds\n"
    "s_add_u32 m0, s26, 2048\n"
    "s_nop 0\n"
    "buffer_load_dwordx4 v10, s[12:15], s40 offen lds\n"
    "s_add_u32 m0, s26, 3072\n"
    "s_nop 0\n"
    "buffer_load_dwordx4 v11, s[12:15], s40 offen lds\n"
    "s_add_u32 s40, s48, 393216\n"
    "s_add_u32 m0, s26, 16384\n"
    "s_nop 0\n"
    "buffer_load_dwordx4 v8, s[12:15], s40 offen lds\n"
    "s_add_u32 m0, s26, 17408\n"
    "s_nop 0\n"
    "buffer_load_dwordx4 v9, s[12:15], s40 offen lds\n"
    "s_add_u32 m0, s26, 18432\n"
    "s_nop 0\n"
    "buffer_load_dwordx4 v10, s[12:15], s40 offen lds\n"
    "s_add_u32 m0, s26, 19456\n"
    "s_nop 0\n"
    "buffer_load_dwordx4 v11, s[12:15], s40 offen lds\n"
    "s_add_u32 s40, s48, 786432\n"
    "s_add_u32 m0, s26, 32768\n"
    "s_nop 0\n"
    "buffer_load_dwordx4 v8, s[12:15], s40 offen lds\n"
    "s_add_u32 m0, s26, 33792\n"
    "s_nop 0\n"
    "buffer_load_dwordx4 v9, s[12:15], s40 offen lds\n"
    "s_add_u32 m0, s26, 34816\n"
    "s_nop 0\n"
    "buffer_load_dwordx4 v10, s[12:15], s40 offen lds\n"
    "s_add_u32 m0, s26, 35840\n"
    "s_nop 0\n"
    "buffer_load_dwordx4 v11, s[12:15], s40 offen lds\n"
    "s_add_u32 s40, s48, 1179648\n"
    "s_add_u32 m0, s26, 49152\n"
    "s_nop 0\n"
    "buffer_load_dwordx4 v8, s[12:15], s40 offen lds\n"
    "s_add_u32 m0, s26, 50176\n"
    "s_nop 0\n"
    "buffer_load_dwordx4 v9, s[12:15], s40 offen lds\n"
    "s_add_u32 m0, s26, 51200\n"
    "s_nop 0\n"
    "buffer_load_dwordx4 v10, s[12:15], s40 offen lds\n"
    "s_add_u32 m0, s26, 52224\n"
    "s_nop 0\n"
    "buffer_load_dwordx4 v11, s[12:15], s40 offen lds\n"
    "s_add_u32 s40, s48, 1572864\n"
    "s_add_u32 m0, s26, 65536\n"
    "s_nop 0\n"
    "buffer_load_dwordx4 v8, s[12:15], s40 offen lds\n"
    "s_add_u32 m0, s26, 66560\n"
    "s_nop 0\n"
    "buffer_load_dwordx4 v9, s[12:15], s40 offen lds\n"
    "s_add_u32 m0, s26, 67584\n"
    "s_nop 0\n"
    "buffer_load_dwordx4 v10, s[12:15], s40 offen lds\n"
    "s_add_u32 m0, s26, 68608\n"
    "s_nop 0\n"
    "buffer_load_dwordx4 v11, s[12:15], s40 offen lds\n"
    "s_add_u32 s40, s48, 1966080\n"
    "s_add_u32 m0, s26, 81920\n"
    "s_nop 0\n"
    "buffer_load_dwordx4 v8, s[12:15], s40 offen lds\n"
    "s_add_u32 m0, s26, 82944\n"
    "s_nop 0\n"
    "buffer_load_dwordx4 v9, s[12:15], s40 offen lds\n"
    "s_add_u32 m0, s26, 83968\n"
    "s_nop 0\n"
    "buffer_load_dwordx4 v10, s[12:15], s40 offen lds\n"
    "s_add_u32 m0, s26, 84992\n"
    "s_nop 0\n"
    "buffer_load_dwordx4 v11, s[12:15], s40 offen lds\n"
    "s_add_u32 s40, s48, 2359296\n"
    "s_add_u32 m0, s26, 98304\n"
    "s_nop 0\n"
    "buffer_load_dwordx4 v8, s[12:15], s40 offen lds\n"
    "s_add_u32 m0, s26, 99328\n"
    "s_nop 0\n"
    "buffer_load_dwordx4 v9, s[12:15], s40 offen lds\n"
    "s_add_u32 m0, s26, 100352\n"
    "s_nop 0\n"
    "buffer_load_dwordx4 v10, s[12:15], s40 offen lds\n"
    "s_add_u32 m0, s26, 101376\n"
    "s_nop 0\n"
    "buffer_load_dwordx4 v11, s[12:15], s40 offen lds\n"
    "s_waitcnt vmcnt(24)\n"
    "s_barrier\n"
    "ds_read_b64_tr_b16 a[0:1], v12 offset:0\n"
    "ds_read_b64_tr_b16 a[2:3], v12 offset:512\n"
    "ds_read_b64_tr_b16 a[4:5], v12 offset:8\n"
    "ds_read_b64_tr_b16 a[6:7], v12 offset:520\n"
    "ds_read_b64_tr_b16 a[8:9], v12 offset:64\n"
    "ds_read_b64_tr_b16 a[10:11], v12 offset:576\n"
    "ds_read_b64_tr_b16 a[12:13], v12 offset:72\n"
    "ds_read_b64_tr_b16 a[14:15], v12 offset:584\n"
    "s_waitcnt lgkmcnt(0)\n"
    "s_waitcnt vmcnt(20)\n"
    "s_barrier\n"
    "ds_read_b64_tr_b16 a[16:17], v12 offset:16384\n"
    "ds_read_b64_tr_b16 a[18:19], v12 offset:16896\n"
    "ds_read_b64_tr_b16 a[20:21], v12 offset:16392\n"
    "ds_read_b64_tr_b16 a[22:23], v12 offset:16904\n"
    "ds_read_b64_tr_b16 a[24:25], v12 offset:16448\n"
    "ds_read_b64_tr_b16 a[26:27], v12 offset:16960\n"
    "ds_read_b64_tr_b16 a[28:29], v12 offset:16456\n"
    "ds_read_b64_tr_b16 a[30:31], v12 offset:16968\n"
    "s_add_u32 s40, s48, 2752512\n"
    "s_add_u32 m0, s26, 0\n"
    "s_nop 0\n"
    "buffer_load_dwordx4 v8, s[12:15], s40 offen lds\n"
    "s_add_u32 m0, s26, 1024\n"
    "s_nop 0\n"
    "buffer_load_dwordx4 v9, s[12:15], s40 offen lds\n"
    "s_add_u32 m0, s26, 2048\n"
    "s_nop 0\n"
    "buffer_load_dwordx4 v10, s[12:15], s40 offen lds\n"
    "s_add_u32 m0, s26, 3072\n"
    "s_nop 0\n"
    "buffer_load_dwordx4 v11, s[12:15], s40 offen lds\n"
    "s_waitcnt lgkmcnt(0)\n"
    "s_waitcnt vmcnt(20)\n"
    "s_barrier\n"
    "ds_read_b64_tr_b16 a[32:33], v12 offset:32768\n"
    "ds_read_b64_tr_b16 a[34:35], v12 offset:33280\n"
    "ds_read_b64_tr_b16 a[36:37], v12 offset:32776\n"
    "ds_read_b64_tr_b16 a[38:39], v12 offset:33288\n"
    "ds_read_b64_tr_b16 a[40:41], v12 offset:32832\n"
    "ds_read_b64_tr_b16 a[42:43], v12 offset:33344\n"
    "ds_read_b64_tr_b16 a[44:45], v12 offset:32840\n"
    "ds_read_b64_tr_b16 a[46:47], v12 offset:33352\n"
    "s_add_u32 s40, s48, 3145728\n"
    "s_add_u32 m0, s26, 16384\n"
    "s_nop 0\n"
    "buffer_load_dwordx4 v8, s[12:15], s40 offen lds\n"
    "s_add_u32 m0, s26, 17408\n"
    "s_nop 0\n"
    "buffer_load_dwordx4 v9, s[12:15], s40 offen lds\n"
    "s_add_u32 m0, s26, 18432\n"
    "s_nop 0\n"
    "buffer_load_dwordx4 v10, s[12:15], s40 offen lds\n"
    "s_add_u32 m0, s26, 19456\n"
    "s_nop 0\n"
    "buffer_load_dwordx4 v11, s[12:15], s40 offen lds\n"
    "s_waitcnt lgkmcnt(0)\n"
    "s_waitcnt vmcnt(20)\n"
    "s_barrier\n"
    "ds_read_b64_tr_b16 a[48:49], v12 offset:49152\n"
    "ds_read_b64_tr_b16 a[50:51], v12 offset:49664\n"
    "ds_read_b64_tr_b16 a[52:53], v12 offset:49160\n"
    "ds_read_b64_tr_b16 a[54:55], v12 offset:49672\n"
    "ds_read_b64_tr_b16 a[56:57], v12 offset:49216\n"
    "ds_read_b64_tr_b16 a[58:59], v12 offset:49728\n"
    "ds_read_b64_tr_b16 a[60:61], v12 offset:49224\n"
    "ds_read_b64_tr_b16 a[62:63], v12 offset:49736\n"
    "s_add_u32 s40, s48, 3538944\n"
    "s_add_u32 m0, s26, 32768\n"
    "s_nop 0\n"
    "buffer_load_dwordx4 v8, s[12:15], s40 offen lds\n"
    "s_add_u32 m0, s26, 33792\n"
    "s_nop 0\n"
    "buffer_load_dwordx4 v9, s[12:15], s40 offen lds\n"
    "s_add_u32 m0, s26, 34816\n"
    "s_nop 0\n"
    "buffer_load_dwordx4 v10, s[12:15], s40 offen lds\n"
    "s_add_u32 m0, s26, 35840\n"
    "s_nop 0\n"
    "buffer_load_dwordx4 v11, s[12:15], s40 offen lds\n"
    "s_waitcnt lgkmcnt(0)\n"
    "s_waitcnt vmcnt(20)\n"
    "s_barrier\n"
    "ds_read_b64_tr_b16 a[64:65], v13 offset:8192\n"
    "ds_read_b64_tr_b16 a[66:67], v13 offset:8704\n"
    "ds_read_b64_tr_b16 a[68:69], v13 offset:8200\n"
    "ds_read_b64_tr_b16 a[70:71], v13 offset:8712\n"
    "ds_read_b64_tr_b16 a[72:73], v13 offset:8256\n"
    "ds_read_b64_tr_b16 a[74:75], v13 offset:8768\n"
    "ds_read_b64_tr_b16 a[76:77], v13 offset:8264\n"
    "ds_read_b64_tr_b16 a[78:79], v13 offset:8776\n"
    "s_add_u32 s40, s48, 3932160\n"
    "s_add_u32 m0, s26, 49152\n"
    "s_nop 0\n"
    "buffer_load_dwordx4 v8, s[12:15], s40 offen lds\n"
    "s_add_u32 m0, s26, 50176\n"
    "s_nop 0\n"
    "buffer_load_dwordx4 v9, s[12:15], s40 offen lds\n"
    "s_add_u32 m0, s26, 51200\n"
    "s_nop 0\n"
    "buffer_load_dwordx4 v10, s[12:15], s40 offen lds\n"
    "s_add_u32 m0, s26, 52224\n"
    "s_nop 0\n"
    "buffer_load_dwordx4 v11, s[12:15], s40 offen lds\n"
    "s_waitcnt lgkmcnt(0)\n"
    "s_waitcnt vmcnt(20)\n"
    "s_barrier\n"
    "ds_read_b64_tr_b16 a[80:81], v13 offset:24576\n"
    "ds_read_b64_tr_b16 a[82:83], v13 offset:25088\n"
    "ds_read_b64_tr_b16 a[84:85], v13 offset:24584\n"
    "ds_read_b64_tr_b16 a[86:87], v13 offset:25096\n"
    "ds_read_b64_tr_b16 a[88:89], v13 offset:24640\n"
    "ds_read_b64_tr_b16 a[90:91], v13 offset:25152\n"
    "ds_read_b64_tr_b16 a[92:93], v13 offset:24648\n"
    "ds_read_b64_tr_b16 a[94:95], v13 offset:25160\n"
    "s_add_u32 s40, s48, 4325376\n"
    "s_add_u32 m0, s26, 65536\n"
    "s_nop 0\n"
    "buffer_load_dwordx4 v8, s[12:15], s40 offen lds\n"
    "s_add_u32 m0, s26, 66560\n"
    "s_nop 0\n"
    "buffer_load_dwordx4 v9, s[12:15], s40 offen lds\n"
    "s_add_u32 m0, s26, 67584\n"
    "s_nop 0\n"
    "buffer_load_dwordx4 v10, s[12:15], s40 offen lds\n"
    "s_add_u32 m0, s26, 68608\n"
    "s_nop 0\n"
    "buffer_load_dwordx4 v11, s[12:15], s40 offen lds\n"
    "s_waitcnt lgkmcnt(0)\n"
    "s_waitcnt vmcnt(20)\n"
    "s_barrier\n"
    "ds_read_b64_tr_b16 a[96:97], v13 offset:40960\n"
    "ds_read_b64_tr_b16 a[98:99], v13 offset:41472\n"
    "ds_read_b64_tr_b16 a[100:101], v13 offset:40968\n"
    "ds_read_b64_tr_b16 a[102:103], v13 offset:41480\n"
    "ds_read_b64_tr_b16 a[104:105], v13 offset:41024\n"
    "ds_read_b64_tr_b16 a[106:107], v13 offset:41536\n"
    "ds_read_b64_tr_b16 a[108:109], v13 offset:41032\n"
    "ds_read_b64_tr_b16 a[110:111], v13 offset:41544\n"
    "s_add_u32 s40, s48, 4718592\n"
    "s_add_u32 m0, s26, 81920\n"
    "s_nop 0\n"
    "buffer_load_dwordx4 v8, s[12:15], s40 offen lds\n"
    "s_add_u32 m0, s26, 82944\n"
    "s_nop 0\n"
    "buffer_load_dwordx4 v9, s[12:15], s40 offen lds\n"
    "s_add_u32 m0, s26, 83968\n"
    "s_nop 0\n"
    "buffer_load_dwordx4 v10, s[12:15], s40 offen lds\n"
    "s_add_u32 m0, s26, 84992\n"
    "s_nop 0\n"
    "buffer_load_dwordx4 v11, s[12:15], s40 offen lds\n"
    "s_waitcnt lgkmcnt(0)\n"
    "s_waitcnt vmcnt(20)\n"
    "s_barrier\n"
    "ds_read_b64_tr_b16 a[112:113], v12 offset:0\n"
    "ds_read_b64_tr_b16 a[114:115], v12 offset:512\n"
    "ds_read_b64_tr_b16 a[116:117], v12 offset:8\n"
    "ds_read_b64_tr_b16 a[118:119], v12 offset:520\n"
    "ds_read_b64_tr_b16 a[120:121], v12 offset:64\n"
    "ds_read_b64_tr_b16 a[122:123], v12 offset:576\n"
    "ds_read_b64_tr_b16 a[124:125], v12 offset:72\n"
    "ds_read_b64_tr_b16 a[126:127], v12 offset:584\n"
    "s_add_u32 s40, s48, 5111808\n"
    "s_add_u32 m0, s26, 98304\n"
    "s_nop 0\n"
    "buffer_load_dwordx4 v8, s[12:15], s40 offen lds\n"
    "s_add_u32 m0, s26, 99328\n"
    "s_nop 0\n"
    "buffer_load_dwordx4 v9, s[12:15], s40 offen lds\n"
    "s_add_u32 m0, s26, 100352\n"
    "s_nop 0\n"
    "buffer_load_dwordx4 v10, s[12:15], s40 offen lds\n"
    "s_add_u32 m0, s26, 101376\n"
    "s_nop 0\n"
    "buffer_load_dwordx4 v11, s[12:15], s40 offen lds\n"
    "s_waitcnt lgkmcnt(0)\n"
    "s_add_u32 s4, s3, 1\n"
    "v_mov_b32 v16, s3\n"
    "s_or_b32 s41, s21, s6\n"
    "s_cbranch_scc1 .Lcl_165\n"
    "s_lshl_b32 s41, s5, 2\n"
    "s_add_u32 s41, s41, 4178944\n"
    "v_mov_b32 v17, s41\n"
    "v_mov_b32 v16, s4\n"
    "s_mov_b64 exec, 1\n"
    "buffer_atomic_swap v16, v17, s[8:11], 0 offen sc0 sc1\n"
    "s_mov_b64 exec, -1\n"
    ".Lcl_165:\n"
    "v_min_u32 v20, 31, v1\n"
    "v_lshlrev_b32 v20, 7, v20\n"
    "v_add_u32 v20, 4182016, v20\n"
    "buffer_load_dwordx2 v[18:19], v20, s[8:11], 0 offen sc1\n"
    "s_waitcnt lgkmcnt(0)\n"
    "s_waitcnt vmcnt(21)\n"
    "s_barrier\n"
    "ds_read_b64_tr_b16 a[128:129], v12 offset:16384\n"
    "ds_read_b64_tr_b16 a[130:131], v12 offset:16896\n"
    "ds_read_b64_tr_b16 a[132:133], v12 offset:16392\n"
    "ds_read_b64_tr_b16 a[134:135], v12 offset:16904\n"
    "ds_read_b64_tr_b16 a[136:137], v12 offset:16448\n"
    "ds_read_b64_tr_b16 a[138:139], v12 offset:16960\n"
    "ds_read_b64_tr_b16 a[140:141], v12 offset:16456\n"
    "ds_read_b64_tr_b16 a[142:143], v12 offset:16968\n"
    "s_add_u32 s40, s48, 5505024\n"
    "s_add_u32 m0, s26, 0\n"
    "s_nop 0\n"
    "buffer_load_dwordx4 v8, s[12:15], s40 offen lds\n"
    "s_add_u32 m0, s26, 1024\n"
    "s_nop 0\n"
    "buffer_load_dwordx4 v9, s[12:15], s40 offen lds\n"
    "s_add_u32 m0, s26, 2048\n"
    "s_nop 0\n"
    "buffer_load_dwordx4 v10, s[12:15], s40 offen lds\n"
    "s_add_u32 m0, s26, 3072\n"
    "s_nop 0\n"
    "buffer_load_dwordx4 v11, s[12:15], s40 offen lds\n"
    "s_waitcnt lgkmcnt(0)\n"
    "s_waitcnt vmcnt(21)\n"
    "s_barrier\n"
    "ds_read_b64_tr_b16 a[144:145], v12 offset:32768\n"
    "ds_read_b64_tr_b16 a[146:147], v12 offset:33280\n"
    "ds_read_b64_tr_b16 a[148:149], v12 offset:32776\n"
    "ds_read_b64_tr_b16 a[150:151], v12 offset:33288\n"
    "ds_read_b64_tr_b16 a[152:153], v12 offset:32832\n"
    "ds_read_b64_tr_b16 a[154:155], v12 offset:33344\n"
    "ds_read_b64_tr_b16 a[156:157], v12 offset:32840\n"
    "ds_read_b64_tr_b16 a[158:159], v12 offset:33352\n"
    "s_add_u32 s40, s48, 5898240\n"
    "s_add_u32 m0, s26, 16384\n"
    "s_nop 0\n"
    "buffer_load_dwordx4 v8, s[12:15], s40 offen lds\n"
    "s_add_u32 m0, s26, 17408\n"
    "s_nop 0\n"
    "buffer_load_dwordx4 v9, s[12:15], s40 offen lds\n"
    "s_add_u32 m0, s26, 18432\n"
    "s_nop 0\n"
    "buffer_load_dwordx4 v10, s[12:15], s40 offen lds\n"
    "s_add_u32 m0, s26, 19456\n"
    "s_nop 0\n"
    "buffer_load_dwordx4 v11, s[12:15], s40 offen lds\n"
    "s_waitcnt lgkmcnt(0)\n"
    "s_waitcnt vmcnt(21)\n"
    "s_barrier\n"
    "ds_read_b64_tr_b16 a[160:161], v12 offset:49152\n"
    "ds_read_b64_tr_b16 a[162:163], v12 offset:49664\n"
    "ds_read_b64_tr_b16 a[164:165], v12 offset:49160\n"
    "ds_read_b64_tr_b16 a[166:167], v12 offset:49672\n"
    "ds_read_b64_tr_b16 a[168:169], v12 offset:49216\n"
    "ds_read_b64_tr_b16 a[170:171], v12 offset:49728\n"
    "ds_read_b64_tr_b16 a[172:173], v12 offset:49224\n"
    "ds_read_b64_tr_b16 a[174:175], v12 offset:49736\n"
    "s_add_u32 s40, s48, 6291456\n"
    "s_add_u32 m0, s26, 32768\n"
    "s_nop 0\n"
    "buffer_load_dwordx4 v8, s[12:15], s40 offen lds\n"
    "s_add_u32 m0, s26, 33792\n"
    "s_nop 0\n"
    "buffer_load_dwordx4 v9, s[12:15], s40 offen lds\n"
    "s_add_u32 m0, s26, 34816\n"
    "s_nop 0\n"
    "buffer_load_dwordx4 v10, s[12:15], s40 offen lds\n"
    "s_add_u32 m0, s26, 35840\n"
    "s_nop 0\n"
    "buffer_load_dwordx4 v11, s[12:15], s40 offen lds\n"
    "s_waitcnt lgkmcnt(0)\n"
    "s_waitcnt vmcnt(21)\n"
    "s_barrier\n"
    "ds_read_b64_tr_b16 a[176:177], v13 offset:8192\n"
    "ds_read_b64_tr_b16 a[178:179], v13 offset:8704\n"
    "ds_read_b64_tr_b16 a[180:181], v13 offset:8200\n"
    "ds_read_b64_tr_b16 a[182:183], v13 offset:8712\n"
    "ds_read_b64_tr_b16 a[184:185], v13 offset:8256\n"
    "ds_read_b64_tr_b16 a[186:187], v13 offset:8768\n"
    "ds_read_b64_tr_b16 a[188:189], v13 offset:8264\n"
    "ds_read_b64_tr_b16 a[190:191], v13 offset:8776\n"
    "s_add_u32 s40, s48, 6684672\n"
    "s_add_u32 m0, s26, 49152\n"
    "s_nop 0\n"
    "buffer_load_dwordx4 v8, s[12:15], s40 offen lds\n"
    "s_add_u32 m0, s26, 50176\n"
    "s_nop 0\n"
    "buffer_load_dwordx4 v9, s[12:15], s40 offen lds\n"
    "s_add_u32 m0, s26, 51200\n"
    "s_nop 0\n"
    "buffer_load_dwordx4 v10, s[12:15], s40 offen lds\n"
    "s_add_u32 m0, s26, 52224\n"
    "s_nop 0\n"
    "buffer_load_dwordx4 v11, s[12:15], s40 offen lds\n"
    "s_waitcnt lgkmcnt(0)\n"
    "s_waitcnt vmcnt(21)\n"
    "s_barrier\n"
    "ds_read_b64_tr_b16 a[192:193], v13 offset:24576\n"
    "ds_read_b64_tr_b16 a[194:195], v13 offset:25088\n"
    "ds_read_b64_tr_b16 a[196:197], v13 offset:24584\n"
    "ds_read_b64_tr_b16 a[198:199], v13 offset:25096\n"
    "ds_read_b64_tr_b16 a[200:201], v13 offset:24640\n"
    "ds_read_b64_tr_b16 a[202:203], v13 offset:25152\n"
    "ds_read_b64_tr_b16 a[204:205], v13 offset:24648\n"
    "ds_read_b64_tr_b16 a[206:207], v13 offset:25160\n"
    "s_add_u32 s40, s48, 7077888\n"
    "s_add_u32 m0, s26, 65536\n"
    "s_nop 0\n"
    "buffer_load_dwordx4 v8, s[12:15], s40 offen lds\n"
    "s_add_u32 m0, s26, 66560\n"
    "s_nop 0\n"
    "buffer_load_dwordx4 v9, s[12:15], s40 offen lds\n"
    "s_add_u32 m0, s26, 67584\n"
    "s_nop 0\n"
    "buffer_load_dwordx4 v10, s[12:15], s40 offen lds\n"
    "s_add_u32 m0, s26, 68608\n"
    "s_nop 0\n"
    "buffer_load_dwordx4 v11, s[12:15], s40 offen lds\n"
    "s_waitcnt lgkmcnt(0)\n"
    "s_waitcnt vmcnt(21)\n"
    "s_barrier\n"
    "ds_read_b64_tr_b16 a[208:209], v13 offset:40960\n"
    "ds_read_b64_tr_b16 a[210:211], v13 offset:41472\n"
    "ds_read_b64_tr_b16 a[212:213], v13 offset:40968\n"
    "ds_read_b64_tr_b16 a[214:215], v13 offset:41480\n"
    "ds_read_b64_tr_b16 a[216:217], v13 offset:41024\n"
    "ds_read_b64_tr_b16 a[218:219], v13 offset:41536\n"
    "ds_read_b64_tr_b16 a[220:221], v13 offset:41032\n"
    "ds_read_b64_tr_b16 a[222:223], v13 offset:41544\n"
    "s_add_u32 s40, s48, 7471104\n"
    "s_add_u32 m0, s26, 81920\n"
    "s_nop 0\n"
    "buffer_load_dwordx4 v8, s[12:15], s40 offen lds\n"
    "s_add_u32 m0, s26, 82944\n"
    "s_nop 0\n"
    "buffer_load_dwordx4 v9, s[12:15], s40 offen lds\n"
    "s_add_u32 m0, s26, 83968\n"
    "s_nop 0\n"
    "buffer_load_dwordx4 v10, s[12:15], s40 offen lds\n"
    "s_add_u32 m0, s26, 84992\n"
    "s_nop 0\n"
    "buffer_load_dwordx4 v11, s[12:15], s40 offen lds\n"
    "s_waitcnt lgkmcnt(0)\n"
    "s_waitcnt vmcnt(24)\n"
    "v_cmp_ne_u32 vcc, s4, v18\n"
    "s_nop 1\n"
    "s_cmp_eq_u32 vcc_lo, 0\n"
    "s_cbranch_scc0 .Lrn_166\n"
    "s_add_u32 m0, s29, 0\n"
    "s_add_u32 s40, s28, 0\n"
    "buffer_load_dwordx4 v2, s[8:11], s40 offen offset:0 sc1 lds\n"
    "buffer_load_dwordx4 v2, s[8:11], s40 offen offset:1024 sc1 lds\n"
    "buffer_load_dwordx4 v2, s[8:11], s40 offen offset:2048 sc1 lds\n"
    "buffer_load_dwordx4 v2, s[8:11], s40 offen offset:3072 sc1 lds\n"
    "s_add_u32 m0, s29, 4096\n"
    "s_add_u32 s40, s28, 4096\n"
    "buffer_load_dwordx4 v2, s[8:11], s40 offen offset:0 sc1 lds\n"
    "buffer_load_dwordx4 v2, s[8:11], s40 offen offset:1024 sc1 lds\n"
    "buffer_load_dwordx4 v2, s[8:11], s40 offen offset:2048 sc1 lds\n"
    "buffer_load_dwordx4 v2, s[8:11], s40 offen offset:3072 sc1 lds\n"
    "s_add_u32 m0, s29, 8192\n"
    "s_add_u32 s40, s28, 8192\n"
    "buffer_load_dwordx4 v2, s[8:11], s40 offen offset:0 sc1 lds\n"
    "buffer_load_dwordx4 v2, s[8:11], s40 offen offset:1024 sc1 lds\n"
    "s_add_u32 s29, s29, 40960\n"
    "s_add_u32 s28, s28, 40960\n"
    ".Lrn_166:\n"
    "s_waitcnt vmcnt(20)\n"
    "s_barrier\n"
    "ds_read_b64_tr_b16 a[224:225], v12 offset:0\n"
    "ds_read_b64_tr_b16 a[226:227], v12 offset:512\n"
    "ds_read_b64_tr_b16 a[228:229], v12 offset:8\n"
    "ds_read_b64_tr_b16 a[230:231], v12 offset:520\n"
    "ds_read_b64_tr_b16 a[232:233], v12 offset:64\n"
    "ds_read_b64_tr_b16 a[234:235], v12 offset:576\n"
    "ds_read_b64_tr_b16 a[236:237], v12 offset:72\n"
    "ds_read_b64_tr_b16 a[238:239], v12 offset:584\n"
    "s_waitcnt lgkmcnt(0)\n"
    "s_waitcnt vmcnt(16)\n"
    "s_barrier\n"
    "ds_read_b64_tr_b16 a[240:241], v12 offset:16384\n"
    "ds_read_b64_tr_b16 a[242:243], v12 offset:16896\n"
    "ds_read_b64_tr_b16 a[244:245], v12 offset:16392\n"
    "ds_read_b64_tr_b16 a[246:247], v12 offset:16904\n"
    "ds_read_b64_tr_b16 a[248:249], v12 offset:16448\n"
    "ds_read_b64_tr_b16 a[250:251], v12 offset:16960\n"
    "ds_read_b64_tr_b16 a[252:253], v12 offset:16456\n"
    "ds_read_b64_tr_b16 a[254:255], v12 offset:16968\n"
    "s_waitcnt lgkmcnt(0)\n"
    "s_waitcnt vmcnt(12)\n"
    "s_barrier\n"
    "ds_read_b64_tr_b16 v[192:193], v12 offset:32768\n"
    "ds_read_b64_tr_b16 v[194:195], v12 offset:33280\n"
    "ds_read_b64_tr_b16 v[196:197], v12 offset:32776\n"
    "ds_read_b64_tr_b16 v[198:199], v12 offset:33288\n"
    "ds_read_b64_tr_b16 v[200:201], v12 offset:32832\n"
    "ds_read_b64_tr_b16 v[202:203], v12 offset:33344\n"
    "ds_read_b64_tr_b16 v[204:205], v12 offset:32840\n"
    "ds_read_b64_tr_b16 v[206:207], v12 offset:33352\n"
    "s_waitcnt lgkmcnt(0)\n"
    "s_waitcnt vmcnt(8)\n"
    "s_barrier\n"
    "ds_read_b64_tr_b16 v[208:209], v12 offset:49152\n"
    "ds_read_b64_tr_b16 v[210:211], v12 offset:49664\n"
    "ds_read_b64_tr_b16 v[212:213], v12 offset:49160\n"
    "ds_read_b64_tr_b16 v[214:215], v12 offset:49672\n"
    "ds_read_b64_tr_b16 v[216:217], v12 offset:49216\n"
    "ds_read_b64_tr_b16 v[218:219], v12 offset:49728\n"
    "ds_read_b64_tr_b16 v[220:221], v12 offset:49224\n"
    "ds_read_b64_tr_b16 v[222:223], v12 offset:49736\n"
    "s_waitcnt lgkmcnt(0)\n"
    "s_waitcnt vmcnt(4)\n"
    "s_barrier\n"
    "ds_read_b64_tr_b16 v[224:225], v13 offset:8192\n"
    "ds_read_b64_tr_b16 v[226:227], v13 offset:8704\n"
    "ds_read_b64_tr_b16 v[228:229], v13 offset:8200\n"
    "ds_read_b64_tr_b16 v[230:231], v13 offset:8712\n"
    "ds_read_b64_tr_b16 v[232:233], v13 offset:8256\n"
    "ds_read_b64_tr_b16 v[234:235], v13 offset:8768\n"
    "ds_read_b64_tr_b16 v[236:237], v13 offset:8264\n"
    "ds_read_b64_tr_b16 v[238:239], v13 offset:8776\n"
    "s_waitcnt lgkmcnt(0)\n"
    "s_waitcnt vmcnt(0)\n"
    "s_barrier\n"
    "ds_read_b64_tr_b16 v[240:241], v13 offset:24576\n"
    "ds_read_b64_tr_b16 v[242:243], v13 offset:25088\n"
    "ds_read_b64_tr_b16 v[244:245], v13 offset:24584\n"
    "ds_read_b64_tr_b16 v[246:247], v13 offset:25096\n"
    "ds_read_b64_tr_b16 v[248:249], v13 offset:24640\n"
    "ds_read_b64_tr_b16 v[250:251], v13 offset:25152\n"
    "ds_read_b64_tr_b16 v[252:253], v13 offset:24648\n"
    "ds_read_b64_tr_b16 v[254:255], v13 offset:25160\n"
    "s_waitcnt lgkmcnt(0)\n"
    "v_cmp_ne_u32 vcc, s4, v18\n"
    "s_nop 1\n"
    "s_cmp_eq_u32 vcc_lo, 0\n"
    "s_cbranch_scc1 .Lrx_169\n"
    ".Lrp_167:\n"
    "v_min_u32 v20, 31, v1\n"
    "v_lshlrev_b32 v20, 7, v20\n"
    "v_add_u32 v20, 4182016, v20\n"
    "buffer_load_dwordx2 v[18:19], v20, s[8:11], 0 offen sc1\n"
    "s_waitcnt vmcnt(0)\n"
    "v_cmp_ne_u32 vcc, s4, v18\n"
    "s_nop 1\n"
    "s_cmp_eq_u32 vcc_lo, 0\n"
    "s_cbranch_scc1 .Lro_168\n"
    "s_sleep 2\n"
    "s_sub_u32 s25, s25, 1\n"
    "s_cbranch_scc0 .Lrp_167\n"
    "s_branch .Lfail\n"
    ".Lro_168:\n"
    "s_add_u32 m0, s29, 0\n"
    "s_add_u32 s40, s28, 0\n"
    "buffer_load_dwordx4 v2, s[8:11], s40 offen offset:0 sc1 lds\n"
    "buffer_load_dwordx4 v2, s[8:11], s40 offen offset:1024 sc1 lds\n"
    "buffer_load_dwordx4 v2, s[8:11], s40 offen offset:2048 sc1 lds\n"
    "buffer_load_dwordx4 v2, s[8:11], s40 offen offset:3072 sc1 lds\n"
    "s_add_u32 m0, s29, 4096\n"
    "s_add_u32 s40, s28, 4096\n"
    "buffer_load_dwordx4 v2, s[8:11], s40 offen offset:0 sc1 lds\n"
    "buffer_load_dwordx4 v2, s[8:11], s40 offen offset:1024 sc1 lds\n"
    "buffer_load_dwordx4 v2, s[8:11], s40 offen offset:2048 sc1 lds\n"
    "buffer_load_dwordx4 v2, s[8:11], s40 offen offset:3072 sc1 lds\n"
    "s_add_u32 m0, s29, 8192\n"
    "s_add_u32 s40, s28, 8192\n"
    "buffer_load_dwordx4 v2, s[8:11], s40 offen offset:0 sc1 lds\n"
    "buffer_load_dwordx4 v2, s[8:11], s40 offen offset:1024 sc1 lds\n"
    "s_add_u32 s29, s29, 40960\n"
    "s_add_u32 s28, s28, 40960\n"
    ".Lrx_169:\n"
    "s_lshl_b32 s40, s27, 7\n"
    "s_add_u32 s40, s40, 4196352\n"
    "v_mov_b32 v4, s40\n"
    "v_cmp_ne_u32 vcc, 0, v1\n"
    "v_mov_b32 v0, 0x7ff00000\n"
    "s_nop 0\n"
    "v_cndmask_b32 v4, v4, v0, vcc\n"
    "v_bfe_u32 v6, v1, 3, 1\n"
    "v_lshlrev_b32 v6, 5, v6\n"
    "v_and_b32 v0, 48, v1\n"
    "v_xor_b32 v6, v6, v0\n"
    "v_bfe_u32 v0, v1, 0, 4\n"
    "v_lshl_add_u32 v6, v0, 6, v6\n"
    "v_xor_b32 v7, 16, v2\n"
    "v_mov_b32 v3, v6\n"
    "s_mov_b32 s30, 0\n"
    "s_mov_b32 s24, 0\n"
    "s_mov_b32 s34, s31\n"
    "s_mov_b32 s35, 0xbfb8aa3b\n"
    "s_waitcnt vmcnt(0)\n"
    "s_cmp_lg_u32 s21, 0\n"
    "s_cbranch_scc1 .Lgt_170\n"
    "v_mov_b32 v17, 162816\n"
    "v_readfirstlane_b32 s40, v16\n"
    "s_nop 1\n"
    "v_mov_b32 v16, s40\n"
    "ds_write_b32 v17, v16\n"
    "s_waitcnt lgkmcnt(0)\n"
    ".Lgt_170:\n"
    "s_barrier\n"
    "v_mov_b32 v17, 162816\n"
    "ds_read_b32 v16, v17\n"
    "s_waitcnt lgkmcnt(0)\n"
    "v_readfirstlane_b32 s40, v16\n"
    "s_barrier\n"
    "s_cmp_eq_u32 s40, s4\n"
    "s_cbranch_scc1 .Llost\n"
    "s_add_u32 m0, s29, 0\n"
    "s_add_u32 s40, s28, 0\n"
    "buffer_load_dwordx4 v2, s[8:11], s40 offen offset:0 sc1 lds\n"
    "buffer_load_dwordx4 v2, s[8:11], s40 offen offset:1024 sc1 lds\n"
    "buffer_load_dwordx4 v2, s[8:11], s40 offen offset:2048 sc1 lds\n"
    "buffer_load_dwordx4 v2, s[8:11], s40 offen offset:3072 sc1 lds\n"
    "s_add_u32 m0, s29, 4096\n"
    "s_add_u32 s40, s28, 4096\n"
    "buffer_load_dwordx4 v2, s[8:11], s40 offen offset:0 sc1 lds\n"
    "buffer_load_dwordx4 v2, s[8:11], s40 offen offset:1024 sc1 lds\n"
    "buffer_load_dwordx4 v2, s[8:11], s40 offen offset:2048 sc1 lds\n"
    "buffer_load_dwordx4 v2, s[8:11], s40 offen offset:3072 sc1 lds\n"
    "s_add_u32 m0, s29, 8192\n"
    "s_add_u32 s40, s28, 8192\n"
    "buffer_load_dwordx4 v2, s[8:11], s40 offen offset:0 sc1 lds\n"
    "buffer_load_dwordx4 v2, s[8:11], s40 offen offset:1024 sc1 lds\n"
    "s_add_u32 s29, s29, 40960\n"
    "s_add_u32 s28, s28, 40960\n"
    "ds_read_b128 v[48:51], v3 offset:0\n"
    "ds_read_b128 v[52:55], v3 offset:1024\n"
    "ds_read_b128 v[56:59], v3 offset:2048\n"
    ".p2align 4\n"
    ".LP_loop:\n"
    "s_waitcnt lgkmcnt(2)\n"
    "v_mfma_f32_16x16x32_bf16 v[16:19], a[0:3], v[48:51], 0\n"
    "ds_read_b128 v[60:63], v3 offset:3072\n"
    "v_mfma_f32_16x16x32_bf16 v[20:23], a[4:7], v[48:51], 0\n"
    "v_mfma_f32_16x16x32_bf16 v[24:27], a[8:11], v[48:51], 0\n"
    "v_mfma_f32_16x16x32_bf16 v[28:31], a[12:15], v[48:51], 0\n"
    "s_waitcnt lgkmcnt(2)\n"
    "v_mfma_f32_16x16x32_bf16 v[16:19], a[16:19], v[52:55], v[16:19]\n"
    "ds_read_b128 v[48:51], v3 offset:4096\n"
    "v_mfma_f32_16x16x32_bf16 v[20:23], a[20:23], v[52:55], v[20:23]\n"
    "v_mfma_f32_16x16x32_bf16 v[24:27], a[24:27], v[52:55], v[24:27]\n"
    "v_mfma_f32_16x16x32_bf16 v[28:31], a[28:31], v[52:55], v[28:31]\n"
    "v_cvt_pk_bf16_f32 v64, v32, v33\n"
    "v_cvt_pk_bf16_f32 v65, v34, v35\n"
    "s_waitcnt lgkmcnt(2)\n"
    "v_mfma_f32_16x16x32_bf16 v[16:19], a[32:35], v[56:59], v[16:19]\n"
    "ds_read_b128 v[52:55], v3 offset:5120\n"
    "v_cvt_pk_bf16_f32 v66, v36, v37\n"
    "v_cvt_pk_bf16_f32 v67, v38, v39\n"
    "v_mfma_f32_16x16x32_bf16 v[20:23], a[36:39], v[56:59], v[20:23]\n"
    "v_cvt_pk_bf16_f32 v68, v40, v41\n"
    "v_cvt_pk_bf16_f32 v69, v42, v43\n"
    "v_mfma_f32_16x16x32_bf16 v[24:27], a[40:43], v[56:59], v[24:27]\n"
    "v_cvt_pk_bf16_f32 v70, v44, v45\n"
    "v_cvt_pk_bf16_f32 v71, v46, v47\n"
    "v_mfma_f32_16x16x32_bf16 v[28:31], a[44:47], v[56:59], v[28:31]\n"
    "buffer_store_dwordx4 v[64:67], v7, s[8:11], s34 offen offset:1024 sc1\n"
    "s_waitcnt lgkmcnt(2)\n"
    "v_mfma_f32_16x16x32_bf16 v[16:19], a[48:51], v[60:63], v[16:19]\n"
    "ds_read_b128 v[56:59], v3 offset:6144\n"
    "v_mfma_f32_16x16x32_bf16 v[20:23], a[52:55], v[60:63], v[20:23]\n"
    "buffer_store_dwordx4 v[68:71], v7, s[8:11], s34 offen offset:3072 sc1\n"
    "v_mfma_f32_16x16x32_bf16 v[24:27], a[56:59], v[60:63], v[24:27]\n"
    "v_mfma_f32_16x16x32_bf16 v[28:31], a[60:63], v[60:63], v[28:31]\n"
    "s_waitcnt lgkmcnt(2)\n"
    "v_mfma_f32_16x16x32_bf16 v[16:19], a[64:67], v[48:51], v[16:19]\n"
    "ds_read_b128 v[60:63], v3 offset:7168\n"
    "v_mfma_f32_16x16x32_bf16 v[20:23], a[68:71], v[48:51], v[20:23]\n"
    "v_mfma_f32_16x16x32_bf16 v[24:27], a[72:75], v[48:51], v[24:27]\n"
    "v_mfma_f32_16x16x32_bf16 v[28:31], a[76:79], v[48:51], v[28:31]\n"
    "s_waitcnt lgkmcnt(2)\n"
    "v_mfma_f32_16x16x32_bf16 v[16:19], a[80:83], v[52:55], v[16:19]\n"
    "ds_read_b128 v[48:51], v3 offset:8192\n"
    "v_mfma_f32_16x16x32_bf16 v[20:23], a[84:87], v[52:55], v[20:23]\n"
    "v_mfma_f32_16x16x32_bf16 v[24:27], a[88:91], v[52:55], v[24:27]\n"
    "v_mfma_f32_16x16x32_bf16 v[28:31], a[92:95], v[52:55], v[28:31]\n"
    "s_waitcnt lgkmcnt(2)\n"
    "v_mfma_f32_16x16x32_bf16 v[16:19], a[96:99], v[56:59], v[16:19]\n"
    "ds_read_b128 v[52:55], v3 offset:9216\n"
    "v_mfma_f32_16x16x32_bf16 v[20:23], a[100:103], v[56:59], v[20:23]\n"
    "v_mfma_f32_16x16x32_bf16 v[24:27], a[104:107], v[56:59], v[24:27]\n"
    "v_mfma_f32_16x16x32_bf16 v[28:31], a[108:111], v[56:59], v[28:31]\n"
    "s_waitcnt lgkmcnt(2)\n"
    "v_mfma_f32_16x16x32_bf16 v[16:19], a[112:115], v[60:63], v[16:19]\n"
    "ds_read_b128 v[56:59], v3 offset:10240\n"
    "v_mfma_f32_16x16x32_bf16 v[20:23], a[116:119], v[60:63], v[20:23]\n"
    "v_mfma_f32_16x16x32_bf16 v[24:27], a[120:123], v[60:63], v[24:27]\n"
    "v_mfma_f32_16x16x32_bf16 v[28:31], a[124:127], v[60:63], v[28:31]\n"
    "s_waitcnt lgkmcnt(2)\n"
    "v_mfma_f32_16x16x32_bf16 v[16:19], a[128:131], v[48:51], v[16:19]\n"
    "ds_read_b128 v[60:63], v3 offset:11264\n"
    "v_mfma_f32_16x16x32_bf16 v[20:23], a[132:135], v[48:51], v[20:23]\n"
    "v_mfma_f32_16x16x32_bf16 v[24:27], a[136:139], v[48:51], v[24:27]\n"
    "v_mfma_f32_16x16x32_bf16 v[28:31], a[140:143], v[48:51], v[28:31]\n"
    "s_waitcnt lgkmcnt(2)\n"
    "v_mfma_f32_16x16x32_bf16 v[16:19], a[144:147], v[52:55], v[16:19]\n"
    "ds_read_b128 v[48:51], v3 offset:12288\n"
    "v_mfma_f32_16x16x32_bf16 v[20:23], a[148:151], v[52:55], v[20:23]\n"
    "v_mfma_f32_16x16x32_bf16 v[24:27], a[152:155], v[52:55], v[24:27]\n"
    "v_mfma_f32_16x16x32_bf16 v[28:31], a[156:159], v[52:55], v[28:31]\n"
    "s_waitcnt lgkmcnt(2)\n"
    "v_mfma_f32_16x16x32_bf16 v[16:19], a[160:163], v[56:59], v[16:19]\n"
    "ds_read_b128 v[52:55], v3 offset:13312\n"
    "v_mfma_f32_16x16x32_bf16 v[20:23], a[164:167], v[56:59], v[20:23]\n"
    "v_mfma_f32_16x16x32_bf16 v[24:27], a[168:171], v[56:59], v[24:27]\n"
    "v_mfma_f32_16x16x32_bf16 v[28:31], a[172:175], v[56:59], v[28:31]\n"
    "s_waitcnt lgkmcnt(2)\n"
    "v_mfma_f32_16x16x32_bf16 v[16:19], a[176:179], v[60:63], v[16:19]\n"
    "ds_read_b128 v[56:59], v3 offset:14336\n"
    "v_mfma_f32_16x16x32_bf16 v[20:23], a[180:183], v[60:63], v[20:23]\n"
    "v_mfma_f32_16x16x32_bf16 v[24:27], a[184:187], v[60:63], v[24:27]\n"
    "s_cmp_gt_u32 s24, 1\n"
    "s_cbranch_scc1 .LPe_171\n"
    "v_mov_b32 v5, s24\n"
    "s_waitcnt vmcnt(0)\n"
    "buffer_store_dword v5, v4, s[8:11], 0 offen sc1\n"
    ".LPe_171:\n"
    "v_mfma_f32_16x16x32_bf16 v[28:31], a[188:191], v[60:63], v[28:31]\n"
    "s_waitcnt lgkmcnt(2)\n"
    "v_mfma_f32_16x16x32_bf16 v[16:19], a[192:195], v[48:51], v[16:19]\n"
    "ds_read_b128 v[60:63], v3 offset:15360\n"
    "s_add_u32 m0, s29, 0\n"
    "s_add_u32 s40, s28, 0\n"
    "buffer_load_dwordx4 v2, s[8:11], s40 offen offset:0 sc1 lds\n"
    "v_mfma_f32_16x16x32_bf16 v[20:23], a[196:199], v[48:51], v[20:23]\n"
    "v_mfma_f32_16x16x32_bf16 v[24:27], a[200:203], v[48:51], v[24:27]\n"
    "v_mfma_f32_16x16x32_bf16 v[28:31], a[204:207], v[48:51], v[28:31]\n"
    "buffer_load_dwordx4 v2, s[8:11], s40 offen offset:1024 sc1 lds\n"
    "s_waitcnt lgkmcnt(2)\n"
    "v_mfma_f32_16x16x32_bf16 v[16:19], a[208:211], v[52:55], v[16:19]\n"
    "ds_read_b128 v[48:51], v3 offset:16384\n"
    "v_mfma_f32_16x16x32_bf16 v[20:23], a[212:215], v[52:55], v[20:23]\n"
    "v_mfma_f32_16x16x32_bf16 v[24:27], a[216:219], v[52:55], v[24:27]\n"
    "buffer_load_dwordx4 v2, s[8:11], s40 offen offset:2048 sc1 lds\n"
    "v_mfma_f32_16x16x32_bf16 v[28:31], a[220:223], v[52:55], v[28:31]\n"
    "s_waitcnt lgkmcnt(2)\n"
    "v_mfma_f32_16x16x32_bf16 v[16:19], a[224:227], v[56:59], v[16:19]\n"
    "ds_read_b128 v[52:55], v3 offset:17408\n"
    "v_mfma_f32_16x16x32_bf16 v[20:23], a[228:231], v[56:59], v[20:23]\n"
    "buffer_load_dwordx4 v2, s[8:11], s40 offen offset:3072 sc1 lds\n"
    "v_mfma_f32_16x16x32_bf16 v[24:27], a[232:235], v[56:59], v[24:27]\n"
    "v_mfma_f32_16x16x32_bf16 v[28:31], a[236:239], v[56:59], v[28:31]\n"
    "s_waitcnt lgkmcnt(2)\n"
    "v_mfma_f32_16x16x32_bf16 v[16:19], a[240:243], v[60:63], v[16:19]\n"
    "ds_read_b128 v[56:59], v3 offset:18432\n"
    "s_add_u32 m0, s29, 4096\n"
    "s_add_u32 s40, s28, 4096\n"
    "buffer_load_dwordx4 v2, s[8:11], s40 offen offset:0 sc1 lds\n"
    "v_mfma_f32_16x16x32_bf16 v[20:23], a[244:247], v[60:63], v[20:23]\n"
    "v_mfma_f32_16x16x32_bf16 v[24:27], a[248:251], v[60:63], v[24:27]\n"
    "v_mfma_f32_16x16x32_bf16 v[28:31], a[252:255], v[60:63], v[28:31]\n"
    "buffer_load_dwordx4 v2, s[8:11], s40 offen offset:1024 sc1 lds\n"
    "s_waitcnt lgkmcnt(2)\n"
    "v_mfma_f32_16x16x32_bf16 v[16:19], v[192:195], v[48:51], v[16:19]\n"
    "ds_read_b128 v[60:63], v3 offset:19456\n"
    "v_mfma_f32_16x16x32_bf16 v[20:23], v[196:199], v[48:51], v[20:23]\n"
    "v_mfma_f32_16x16x32_bf16 v[24:27], v[200:203], v[48:51], v[24:27]\n"
    "buffer_load_dwordx4 v2, s[8:11], s40 offen offset:2048 sc1 lds\n"
    "v_mfma_f32_16x16x32_bf16 v[28:31], v[204:207], v[48:51], v[28:31]\n"
    "s_waitcnt lgkmcnt(2)\n"
    "v_mfma_f32_16x16x32_bf16 v[16:19], v[208:211], v[52:55], v[16:19]\n"
    "ds_read_b128 v[48:51], v3 offset:20480\n"
    "v_mfma_f32_16x16x32_bf16 v[20:23], v[212:215], v[52:55], v[20:23]\n"
    "buffer_load_dwordx4 v2, s[8:11], s40 offen offset:3072 sc1 lds\n"
    "v_mfma_f32_16x16x32_bf16 v[24:27], v[216:219], v[52:55], v[24:27]\n"
    "v_mfma_f32_16x16x32_bf16 v[28:31], v[220:223], v[52:55], v[28:31]\n"
    "s_waitcnt lgkmcnt(2)\n"
    "v_mfma_f32_16x16x32_bf16 v[16:19], v[224:227], v[56:59], v[16:19]\n"
    "ds_read_b128 v[52:55], v3 offset:21504\n"
    "s_add_u32 m0, s29, 8192\n"
    "s_add_u32 s40, s28, 8192\n"
    "buffer_load_dwordx4 v2, s[8:11], s40 offen offset:0 sc1 lds\n"
    "v_mfma_f32_16x16x32_bf16 v[20:23], v[228:231], v[56:59], v[20:23]\n"
    "v_mfma_f32_16x16x32_bf16 v[24:27], v[232:235], v[56:59], v[24:27]\n"
    "v_mfma_f32_16x16x32_bf16 v[28:31], v[236:239], v[56:59], v[28:31]\n"
    "buffer_load_dwordx4 v2, s[8:11], s40 offen offset:1024 sc1 lds\n"
    "s_waitcnt lgkmcnt(2)\n"
    "v_mfma_f32_16x16x32_bf16 v[16:19], v[240:243], v[60:63], v[16:19]\n"
    "ds_read_b128 v[56:59], v3 offset:22528\n"
    "v_mfma_f32_16x16x32_bf16 v[20:23], v[244:247], v[60:63], v[20:23]\n"
    "v_mfma_f32_16x16x32_bf16 v[24:27], v[248:251], v[60:63], v[24:27]\n"
    "v_mfma_f32_16x16x32_bf16 v[28:31], v[252:255], v[60:63], v[28:31]\n"
    "s_waitcnt lgkmcnt(2)\n"
    "v_mfma_f32_16x16x32_bf16 v[32:35], a[0:3], v[48:51], 0\n"
    "ds_read_b128 v[60:63], v3 offset:23552\n"
    "v_mfma_f32_16x16x32_bf16 v[36:39], a[4:7], v[48:51], 0\n"
    "v_mfma_f32_16x16x32_bf16 v[40:43], a[8:11], v[48:51], 0\n"
    "v_mov_b32 v5, s24\n"
    "v_mfma_f32_16x16x32_bf16 v[44:47], a[12:15], v[48:51], 0\n"
    "s_waitcnt lgkmcnt(2)\n"
    "v_mfma_f32_16x16x32_bf16 v[32:35], a[16:19], v[52:55], v[32:35]\n"
    "ds_read_b128 v[48:51], v3 offset:24576\n"
    "v_mfma_f32_16x16x32_bf16 v[36:39], a[20:23], v[52:55], v[36:39]\n"
    "v_mfma_f32_16x16x32_bf16 v[40:43], a[24:27], v[52:55], v[40:43]\n"
    "v_mfma_f32_16x16x32_bf16 v[44:47], a[28:31], v[52:55], v[44:47]\n"
    "v_cvt_pk_bf16_f32 v64, v16, v17\n"
    "v_cvt_pk_bf16_f32 v65, v18, v19\n"
    "s_waitcnt lgkmcnt(2)\n"
    "v_mfma_f32_16x16x32_bf16 v[32:35], a[32:35], v[56:59], v[32:35]\n"
    "ds_read_b128 v[52:55], v3 offset:25600\n"
    "v_cvt_pk_bf16_f32 v66, v20, v21\n"
    "v_cvt_pk_bf16_f32 v67, v22, v23\n"
    "v_mfma_f32_16x16x32_bf16 v[36:39], a[36:39], v[56:59], v[36:39]\n"
    "v_cvt_pk_bf16_f32 v68, v24, v25\n"
    "v_cvt_pk_bf16_f32 v69, v26, v27\n"
    "v_mfma_f32_16x16x32_bf16 v[40:43], a[40:43], v[56:59], v[40:43]\n"
    "v_cvt_pk_bf16_f32 v70, v28, v29\n"
    "v_cvt_pk_bf16_f32 v71, v30, v31\n"
    "v_mfma_f32_16x16x32_bf16 v[44:47], a[44:47], v[56:59], v[44:47]\n"
    "buffer_store_dwordx4 v[64:67], v2, s[8:11], s31 offen offset:0 sc1\n"
    "s_waitcnt lgkmcnt(2)\n"
    "v_mfma_f32_16x16x32_bf16 v[32:35], a[48:51], v[60:63], v[32:35]\n"
    "ds_read_b128 v[56:59], v3 offset:26624\n"
    "v_mfma_f32_16x16x32_bf16 v[36:39], a[52:55], v[60:63], v[36:39]\n"
    "buffer_store_dwordx4 v[68:71], v2, s[8:11], s31 offen offset:2048 sc1\n"
    "v_mfma_f32_16x16x32_bf16 v[40:43], a[56:59], v[60:63], v[40:43]\n"
    "v_mfma_f32_16x16x32_bf16 v[44:47], a[60:63], v[60:63], v[44:47]\n"
    "s_waitcnt lgkmcnt(2)\n"
    "v_mfma_f32_16x16x32_bf16 v[32:35], a[64:67], v[48:51], v[32:35]\n"
    "ds_read_b128 v[60:63], v3 offset:27648\n"
    "v_mfma_f32_16x16x32_bf16 v[36:39], a[68:71], v[48:51], v[36:39]\n"
    "v_mfma_f32_16x16x32_bf16 v[40:43], a[72:75], v[48:51], v[40:43]\n"
    "v_mfma_f32_16x16x32_bf16 v[44:47], a[76:79], v[48:51], v[44:47]\n"
    "s_waitcnt lgkmcnt(2)\n"
    "v_mfma_f32_16x16x32_bf16 v[32:35], a[80:83], v[52:55], v[32:35]\n"
    "ds_read_b128 v[48:51], v3 offset:28672\n"
    "v_mfma_f32_16x16x32_bf16 v[36:39], a[84:87], v[52:55], v[36:39]\n"
    "v_mfma_f32_16x16x32_bf16 v[40:43], a[88:91], v[52:55], v[40:43]\n"
    "v_mfma_f32_16x16x32_bf16 v[44:47], a[92:95], v[52:55], v[44:47]\n"
    "s_waitcnt lgkmcnt(2)\n"
    "v_mfma_f32_16x16x32_bf16 v[32:35], a[96:99], v[56:59], v[32:35]\n"
    "ds_read_b128 v[52:55], v3 offset:29696\n"
    "v_mfma_f32_16x16x32_bf16 v[36:39], a[100:103], v[56:59], v[36:39]\n"
    "v_mfma_f32_16x16x32_bf16 v[40:43], a[104:107], v[56:59], v[40:43]\n"
    "v_mfma_f32_16x16x32_bf16 v[44:47], a[108:111], v[56:59], v[44:47]\n"
    "s_waitcnt lgkmcnt(2)\n"
    "v_mfma_f32_16x16x32_bf16 v[32:35], a[112:115], v[60:63], v[32:35]\n"
    "ds_read_b128 v[56:59], v3 offset:30720\n"
    "v_mfma_f32_16x16x32_bf16 v[36:39], a[116:119], v[60:63], v[36:39]\n"
    "v_mfma_f32_16x16x32_bf16 v[40:43], a[120:123], v[60:63], v[40:43]\n"
    "v_mfma_f32_16x16x32_bf16 v[44:47], a[124:127], v[60:63], v[44:47]\n"
    "s_waitcnt lgkmcnt(2)\n"
    "v_mfma_f32_16x16x32_bf16 v[32:35], a[128:131], v[48:51], v[32:35]\n"
    "ds_read_b128 v[60:63], v3 offset:31744\n"
    "v_mfma_f32_16x16x32_bf16 v[36:39], a[132:135], v[48:51], v[36:39]\n"
    "v_mfma_f32_16x16x32_bf16 v[40:43], a[136:139], v[48:51], v[40:43]\n"
    "v_mfma_f32_16x16x32_bf16 v[44:47], a[140:143], v[48:51], v[44:47]\n"
    "s_waitcnt lgkmcnt(2)\n"
    "v_mfma_f32_16x16x32_bf16 v[32:35], a[144:147], v[52:55], v[32:35]\n"
    "ds_read_b128 v[48:51], v3 offset:32768\n"
    "v_mfma_f32_16x16x32_bf16 v[36:39], a[148:151], v[52:55], v[36:39]\n"
    "v_mfma_f32_16x16x32_bf16 v[40:43], a[152:155], v[52:55], v[40:43]\n"
    "v_mfma_f32_16x16x32_bf16 v[44:47], a[156:159], v[52:55], v[44:47]\n"
    "s_waitcnt lgkmcnt(2)\n"
    "v_mfma_f32_16x16x32_bf16 v[32:35], a[160:163], v[56:59], v[32:35]\n"
    "ds_read_b128 v[52:55], v3 offset:33792\n"
    "v_mfma_f32_16x16x32_bf16 v[36:39], a[164:167], v[56:59], v[36:39]\n"
    "v_mfma_f32_16x16x32_bf16 v[40:43], a[168:171], v[56:59], v[40:43]\n"
    "v_mfma_f32_16x16x32_bf16 v[44:47], a[172:175], v[56:59], v[44:47]\n"
    "s_waitcnt lgkmcnt(2)\n"
    "v_mfma_f32_16x16x32_bf16 v[32:35], a[176:179], v[60:63], v[32:35]\n"
    "ds_read_b128 v[56:59], v3 offset:34816\n"
    "v_mfma_f32_16x16x32_bf16 v[36:39], a[180:183], v[60:63], v[36:39]\n"
    "v_mfma_f32_16x16x32_bf16 v[40:43], a[184:187], v[60:63], v[40:43]\n"
    "v_mfma_f32_16x16x32_bf16 v[44:47], a[188:191], v[60:63], v[44:47]\n"
    "s_waitcnt lgkmcnt(2)\n"
    "v_mfma_f32_16x16x32_bf16 v[32:35], a[192:195], v[48:51], v[32:35]\n"
    "ds_read_b128 v[60:63], v3 offset:35840\n"
    "v_mfma_f32_16x16x32_bf16 v[36:39], a[196:199], v[48:51], v[36:39]\n"
    "v_mfma_f32_16x16x32_bf16 v[40:43], a[200:203], v[48:51], v[40:43]\n"
    "v_mfma_f32_16x16x32_bf16 v[44:47], a[204:207], v[48:51], v[44:47]\n"
    "s_waitcnt lgkmcnt(2)\n"
    "v_mfma_f32_16x16x32_bf16 v[32:35], a[208:211], v[52:55], v[32:35]\n"
    "ds_read_b128 v[48:51], v3 offset:36864\n"
    "s_waitcnt vmcnt(12)\n"
    "buffer_store_dword v5, v4, s[8:11], 0 offen sc1\n"
    "v_mfma_f32_16x16x32_bf16 v[36:39], a[212:215], v[52:55], v[36:39]\n"
    "v_mfma_f32_16x16x32_bf16 v[40:43], a[216:219], v[52:55], v[40:43]\n"
    "s_mov_b32 s34, s31\n"
    "s_add_u32 s31, s31, 786432\n"
    "s_add_u32 s28, s28, 40960\n"
    "v_mfma_f32_16x16x32_bf16 v[44:47], a[220:223], v[52:55], v[44:47]\n"
    "s_add_u32 s29, s29, 40960\n"
    "s_cmp_ge_u32 s29, 122880\n"
    "s_cselect_b32 s40, 122880, 0\n"
    "s_waitcnt lgkmcnt(2)\n"
    "v_mfma_f32_16x16x32_bf16 v[32:35], a[224:227], v[56:59], v[32:35]\n"
    "ds_read_b128 v[52:55], v3 offset:37888\n"
    "s_sub_u32 s29, s29, s40\n"
    "s_add_u32 s30, s30, 40960\n"
    "v_mfma_f32_16x16x32_bf16 v[36:39], a[228:231], v[56:59], v[36:39]\n"
    "s_cmp_eq_u32 s30, 122880\n"
    "s_cselect_b32 s30, 0, s30\n"
    "v_mfma_f32_16x16x32_bf16 v[40:43], a[232:235], v[56:59], v[40:43]\n"
    "s_add_u32 s24, s24, 1\n"
    "s_cmp_lt_u32 s24, 64\n"
    "v_mfma_f32_16x16x32_bf16 v[44:47], a[236:239], v[56:59], v[44:47]\n"
    "s_waitcnt lgkmcnt(2)\n"
    "v_mfma_f32_16x16x32_bf16 v[32:35], a[240:243], v[60:63], v[32:35]\n"
    "ds_read_b128 v[56:59], v3 offset:38912\n"
    "v_mfma_f32_16x16x32_bf16 v[36:39], a[244:247], v[60:63], v[36:39]\n"
    "v_mfma_f32_16x16x32_bf16 v[40:43], a[248:251], v[60:63], v[40:43]\n"
    "v_mfma_f32_16x16x32_bf16 v[44:47], a[252:255], v[60:63], v[44:47]\n"
    "s_waitcnt lgkmcnt(2)\n"
    "v_mfma_f32_16x16x32_bf16 v[32:35], v[192:195], v[48:51], v[32:35]\n"
    "ds_read_b128 v[60:63], v3 offset:39936\n"
    "v_mfma_f32_16x16x32_bf16 v[36:39], v[196:199], v[48:51], v[36:39]\n"
    "v_mfma_f32_16x16x32_bf16 v[40:43], v[200:203], v[48:51], v[40:43]\n"
    "v_add_u32 v3, s30, v6\n"
    "v_mfma_f32_16x16x32_bf16 v[44:47], v[204:207], v[48:51], v[44:47]\n"
    "s_waitcnt lgkmcnt(2)\n"
    "v_mfma_f32_16x16x32_bf16 v[32:35], v[208:211], v[52:55], v[32:35]\n"
    "v_mfma_f32_16x16x32_bf16 v[36:39], v[212:215], v[52:55], v[36:39]\n"
    "s_waitcnt lgkmcnt(0)\n"
    "s_barrier\n"
    "ds_read_b128 v[48:51], v3 offset:0\n"
    "v_mfma_f32_16x16x32_bf16 v[40:43], v[216:219], v[52:55], v[40:43]\n"
    "v_mfma_f32_16x16x32_bf16 v[44:47], v[220:223], v[52:55], v[44:47]\n"
    "v_mfma_f32_16x16x32_bf16 v[32:35], v[224:227], v[56:59], v[32:35]\n"
    "ds_read_b128 v[52:55], v3 offset:1024\n"
    "v_mfma_f32_16x16x32_bf16 v[36:39], v[228:231], v[56:59], v[36:39]\n"
    "v_mfma_f32_16x16x32_bf16 v[40:43], v[232:235], v[56:59], v[40:43]\n"
    "v_mfma_f32_16x16x32_bf16 v[44:47], v[236:239], v[56:59], v[44:47]\n"
    "v_mfma_f32_16x16x32_bf16 v[32:35], v[240:243], v[60:63], v[32:35]\n"
    "ds_read_b128 v[56:59], v3 offset:2048\n"
    "v_mfma_f32_16x16x32_bf16 v[36:39], v[244:247], v[60:63], v[36:39]\n"
    "v_mfma_f32_16x16x32_bf16 v[40:43], v[248:251], v[60:63], v[40:43]\n"
    "v_mfma_f32_16x16x32_bf16 v[44:47], v[252:255], v[60:63], v[44:47]\n"
    "s_cbranch_scc1 .LP_loop\n"
    "s_nop 2\n"
    "v_cvt_pk_bf16_f32 v64, v32, v33\n"
    "v_cvt_pk_bf16_f32 v65, v34, v35\n"
    "v_cvt_pk_bf16_f32 v66, v36, v37\n"
    "v_cvt_pk_bf16_f32 v67, v38, v39\n"
    "v_cvt_pk_bf16_f32 v68, v40, v41\n"
    "v_cvt_pk_bf16_f32 v69, v42, v43\n"
    "v_cvt_pk_bf16_f32 v70, v44, v45\n"
    "v_cvt_pk_bf16_f32 v71, v46, v47\n"
    "buffer_store_dwordx4 v[64:67], v7, s[8:11], s34 offen offset:1024 sc1\n"
    "buffer_store_dwordx4 v[68:71], v7, s[8:11], s34 offen offset:3072 sc1\n"
    "v_mov_b32 v5, s24\n"
    "s_waitcnt vmcnt(0) lgkmcnt(0)\n"
    "buffer_store_dword v5, v4, s[8:11], 0 offen sc1\n"
    "s_barrier\n"
    "s_add_u32 s20, s20, 0\n"
    "s_branch .LZ_enter\n"
    ".Lrole_F:\n"
    "s_lshl_b32 s27, s20, 2\n"
    "s_add_u32 s27, s27, s21\n"
    "s_mov_b32 s12, s74\n"
    "s_and_b32 s13, s75, 0xffff\n"
    "s_mov_b32 s14, 1966080\n"
    "s_mov_b32 s15, 0x20000\n"
    "s_mov_b32 s16, s76\n"
    "s_and_b32 s17, s77, 0xffff\n"
    "s_mov_b32 s18, 1966080\n"
    "s_mov_b32 s19, 0x20000\n"
    "s_lshl_b32 s48, s20, 8\n"
    "s_mul_i32 s26, s21, 2048\n"
    "s_add_u32 s26, s26, 40960\n"
    "s_and_b32 s40, s21, 3\n"
    "s_mul_i32 s41, s40, 128\n"
    "v_add_u32 v8, s41, v1\n"
    "s_add_u32 s41, s41, 64\n"
    "v_mul_u32_u24 v0, 4096, v8\n"
    "v_lshrrev_b32 v0, 16, v0\n"
    "v_mul_u32_u24 v10, 16, v0\n"
    "v_sub_u32 v8, v8, v10\n"
    "v_mul_u32_u24 v0, 3072, v0\n"
    "v_lshl_add_u32 v8, v8, 4, v0\n"
    "v_add_u32 v9, s41, v1\n"
    "s_add_u32 s41, s41, 64\n"
    "v_mul_u32_u24 v0, 4096, v9\n"
    "v_lshrrev_b32 v0, 16, v0\n"
    "v_mul_u32_u24 v10, 16, v0\n"
    "v_sub_u32 v9, v9, v10\n"
    "v_mul_u32_u24 v0, 3072, v0\n"
    "v_lshl_add_u32 v9, v9, 4, v0\n"
    "s_mul_i32 s40, s40, 1984\n"
    "s_sub_u32 s40, s26, s40\n"
    "v_bfe_u32 v10, v1, 4, 2\n"
    "v_lshlrev_b32 v10, 3, v10\n"
    "v_bfe_u32 v0, v1, 2, 2\n"
    "v_lshl_add_u32 v10, v0, 0, v10\n"
    "v_mul_u32_u24 v10, 256, v10\n"
    "v_and_b32 v0, 3, v1\n"
    "v_mul_u32_u24 v0, 16, v0\n"
    "v_add3_u32 v10, v10, v0, s40\n"
    "v_add_u32 v11, 57344, v10\n"
    "v_add_u32 v12, 114688, v10\n"
    "s_lshl_b32 s31, s27, 11\n"
    "s_add_u32 s31, s31, 148897792\n"
    "s_add_u32 s27, s27, 192\n"
    "v_lshlrev_b32 v2, 4, v1\n"
    "s_mul_i32 s29, s21, 10240\n"
    "s_add_u32 s28, s29, 6291456\n"
    "s_sleep 37\n"
    "s_sleep 37\n"
    "s_sleep 37\n"
    "s_add_u32 s40, s48, 0\n"
    "s_add_u32 m0, s26, 0\n"
    "s_nop 0\n"
    "buffer_load_dwordx4 v8, s[12:15], s40 offen nt lds\n"
    "s_add_u32 m0, s26, 1024\n"
    "s_nop 0\n"
    "buffer_load_dwordx4 v9, s[12:15], s40 offen nt lds\n"
    "s_add_u32 s40, s48, 0\n"
    "s_add_u32 m0, s26, 8192\n"
    "s_nop 0\n"
    "buffer_load_dwordx4 v8, s[16:19], s40 offen nt lds\n"
    "s_add_u32 m0, s26, 9216\n"
    "s_nop 0\n"
    "buffer_load_dwordx4 v9, s[16:19], s40 offen nt lds\n"
    "s_add_u32 s40, s48, 98304\n"
    "s_add_u32 m0, s26, 16384\n"
    "s_nop 0\n"
    "buffer_load_dwordx4 v8, s[12:15], s40 offen nt lds\n"
    "s_add_u32 m0, s26, 17408\n"
    "s_nop 0\n"
    "buffer_load_dwordx4 v9, s[12:15], s40 offen nt lds\n"
    "s_add_u32 s40, s48, 98304\n"
    "s_add_u32 m0, s26, 24576\n"
    "s_nop 0\n"
    "buffer_load_dwordx4 v8, s[16:19], s40 offen nt lds\n"
    "s_add_u32 m0, s26, 25600\n"
    "s_nop 0\n"
    "buffer_load_dwordx4 v9, s[16:19], s40 offen nt lds\n"
    "s_add_u32 s40, s48, 196608\n"
    "s_add_u32 m0, s26, 32768\n"
    "s_nop 0\n"
    "buffer_load_dwordx4 v8, s[12:15], s40 offen nt lds\n"
    "s_add_u32 m0, s26, 33792\n"
    "s_nop 0\n"
    "buffer_load_dwordx4 v9, s[12:15], s40 offen nt lds\n"
    "s_add_u32 s40, s48, 196608\n"
    "s_add_u32 m0, s26, 40960\n"
    "s_nop 0\n"
    "buffer_load_dwordx4 v8, s[16:19], s40 offen nt lds\n"
    "s_add_u32 m0, s26, 41984\n"
    "s_nop 0\n"
    "buffer_load_dwordx4 v9, s[16:19], s40 offen nt lds\n"
    "s_add_u32 s40, s48, 294912\n"
    "s_add_u32 m0, s26, 49152\n"
    "s_nop 0\n"
    "buffer_load_dwordx4 v8, s[12:15], s40 offen nt lds\n"
    "s_add_u32 m0, s26, 50176\n"
    "s_nop 0\n"
    "buffer_load_dwordx4 v9, s[12:15], s40 offen nt lds\n"
    "s_add_u32 s40, s48, 294912\n"
    "s_add_u32 m0, s26, 57344\n"
    "s_nop 0\n"
    "buffer_load_dwordx4 v8, s[16:19], s40 offen nt lds\n"
    "s_add_u32 m0, s26, 58368\n"
    "s_nop 0\n"
    "buffer_load_dwordx4 v9, s[16:19], s40 offen nt lds\n"
    "s_add_u32 s40, s48, 393216\n"
    "s_add_u32 m0, s26, 65536\n"
    "s_nop 0\n"
    "buffer_load_dwordx4 v8, s[12:15], s40 offen nt lds\n"
    "s_add_u32 m0, s26, 66560\n"
    "s_nop 0\n"
    "buffer_load_dwordx4 v9, s[12:15], s40 offen nt lds\n"
    "s_add_u32 s40, s48, 393216\n"
    "s_add_u32 m0, s26, 73728\n"
    "s_nop 0\n"
    "buffer_load_dwordx4 v8, s[16:19], s40 offen nt lds\n"
    "s_add_u32 m0, s26, 74752\n"
    "s_nop 0\n"
    "buffer_load_dwordx4 v9, s[16:19], s40 offen nt lds\n"
    "s_add_u32 s40, s48, 491520\n"
    "s_add_u32 m0, s26, 81920\n"
    "s_nop 0\n"
    "buffer_load_dwordx4 v8, s[12:15], s40 offen nt lds\n"
    "s_add_u32 m0, s26, 82944\n"
    "s_nop 0\n"
    "buffer_load_dwordx4 v9, s[12:15], s40 offen nt lds\n"
    "s_add_u32 s40, s48, 491520\n"
    "s_add_u32 m0, s26, 90112\n"
    "s_nop 0\n"
    "buffer_load_dwordx4 v8, s[16:19], s40 offen nt lds\n"
    "s_add_u32 m0, s26, 91136\n"
    "s_nop 0\n"
    "buffer_load_dwordx4 v9, s[16:19], s40 offen nt lds\n"
    "s_add_u32 s40, s48, 589824\n"
    "s_add_u32 m0, s26, 98304\n"
    "s_nop 0\n"
    "buffer_load_dwordx4 v8, s[12:15], s40 offen nt lds\n"
    "s_add_u32 m0, s26, 99328\n"
    "s_nop 0\n"
    "buffer_load_dwordx4 v9, s[12:15], s40 offen nt lds\n"
    "s_add_u32 s40, s48, 589824\n"
    "s_add_u32 m0, s26, 106496\n"
    "s_nop 0\n"
    "buffer_load_dwordx4 v8, s[16:19], s40 offen nt lds\n"
    "s_add_u32 m0, s26, 107520\n"
    "s_nop 0\n"
    "buffer_load_dwordx4 v9, s[16:19], s40 offen nt lds\n"
    "s_waitcnt vmcnt(26)\n"
    "s_barrier\n"
    "ds_read_b64_tr_b16 a[0:1], v10 offset:0\n"
    "ds_read_b64_tr_b16 a[2:3], v10 offset:1024\n"
    "ds_read_b64_tr_b16 a[4:5], v10 offset:8\n"
    "ds_read_b64_tr_b16 a[6:7], v10 offset:1032\n"
    "s_waitcnt lgkmcnt(0)\n"
    "s_waitcnt vmcnt(24)\n"
    "s_barrier\n"
    "ds_read_b64_tr_b16 a[8:9], v10 offset:8192\n"
    "ds_read_b64_tr_b16 a[10:11], v10 offset:9216\n"
    "ds_read_b64_tr_b16 a[12:13], v10 offset:8200\n"
    "ds_read_b64_tr_b16 a[14:15], v10 offset:9224\n"
    "s_add_u32 s40, s48, 688128\n"
    "s_add_u32 m0, s26, 0\n"
    "s_nop 0\n"
    "buffer_load_dwordx4 v8, s[12:15], s40 offen nt lds\n"
    "s_add_u32 m0, s26, 1024\n"
    "s_nop 0\n"
    "buffer_load_dwordx4 v9, s[12:15], s40 offen nt lds\n"
    "s_waitcnt lgkmcnt(0)\n"
    "s_waitcnt vmcnt(24)\n"
    "s_barrier\n"
    "ds_read_b64_tr_b16 a[16:17], v10 offset:16384\n"
    "ds_read_b64_tr_b16 a[18:19], v10 offset:17408\n"
    "ds_read_b64_tr_b16 a[20:21], v10 offset:16392\n"
    "ds_read_b64_tr_b16 a[22:23], v10 offset:17416\n"
    "s_add_u32 s40, s48, 688128\n"
    "s_add_u32 m0, s26, 8192\n"
    "s_nop 0\n"
    "buffer_load_dwordx4 v8, s[16:19], s40 offen nt lds\n"
    "s_add_u32 m0, s26, 9216\n"
    "s_nop 0\n"
    "buffer_load_dwordx4 v9, s[16:19], s40 offen nt lds\n"
    "s_waitcnt lgkmcnt(0)\n"
    "s_waitcnt vmcnt(24)\n"
    "s_barrier\n"
    "ds_read_b64_tr_b16 a[24:25], v10 offset:24576\n"
    "ds_read_b64_tr_b16 a[26:27], v10 offset:25600\n"
    "ds_read_b64_tr_b16 a[28:29], v10 offset:24584\n"
    "ds_read_b64_tr_b16 a[30:31], v10 offset:25608\n"
    "s_add_u32 s40, s48, 786432\n"
    "s_add_u32 m0, s26, 16384\n"
    "s_nop 0\n"
    "buffer_load_dwordx4 v8, s[12:15], s40 offen nt lds\n"
    "s_add_u32 m0, s26, 17408\n"
    "s_nop 0\n"
    "buffer_load_dwordx4 v9, s[12:15], s40 offen nt lds\n"
    "s_waitcnt lgkmcnt(0)\n"
    "s_waitcnt vmcnt(24)\n"
    "s_barrier\n"
    "ds_read_b64_tr_b16 a[32:33], v10 offset:32768\n"
    "ds_read_b64_tr_b16 a[34:35], v10 offset:33792\n"
    "ds_read_b64_tr_b16 a[36:37], v10 offset:32776\n"
    "ds_read_b64_tr_b16 a[38:39], v10 offset:33800\n"
    "s_add_u32 s40, s48, 786432\n"
    "s_add_u32 m0, s26, 24576\n"
    "s_nop 0\n"
    "buffer_load_dwordx4 v8, s[16:19], s40 offen nt lds\n"
    "s_add_u32 m0, s26, 25600\n"
    "s_nop 0\n"
    "buffer_load_dwordx4 v9, s[16:19], s40 offen nt lds\n"
    "s_waitcnt lgkmcnt(0)\n"
    "s_waitcnt vmcnt(24)\n"
    "s_barrier\n"
    "ds_read_b64_tr_b16 a[40:41], v10 offset:40960\n"
    "ds_read_b64_tr_b16 a[42:43], v10 offset:41984\n"
    "ds_read_b64_tr_b16 a[44:45], v10 offset:40968\n"
    "ds_read_b64_tr_b16 a[46:47], v10 offset:41992\n"
    "s_add_u32 s40, s48, 884736\n"
    "s_add_u32 m0, s26, 32768\n"
    "s_nop 0\n"
    "buffer_load_dwordx4 v8, s[12:15], s40 offen nt lds\n"
    "s_add_u32 m0, s26, 33792\n"
    "s_nop 0\n"
    "buffer_load_dwordx4 v9, s[12:15], s40 offen nt lds\n"
    "s_waitcnt lgkmcnt(0)\n"
    "s_waitcnt vmcnt(24)\n"
    "s_barrier\n"
    "ds_read_b64_tr_b16 a[48:49], v10 offset:49152\n"
    "ds_read_b64_tr_b16 a[50:51], v10 offset:50176\n"
    "ds_read_b64_tr_b16 a[52:53], v10 offset:49160\n"
    "ds_read_b64_tr_b16 a[54:55], v10 offset:50184\n"
    "s_add_u32 s40, s48, 884736\n"
    "s_add_u32 m0, s26, 40960\n"
    "s_nop 0\n"
    "buffer_load_dwordx4 v8, s[16:19], s40 offen nt lds\n"
    "s_add_u32 m0, s26, 41984\n"
    "s_nop 0\n"
    "buffer_load_dwordx4 v9, s[16:19], s40 offen nt lds\n"
    "s_waitcnt lgkmcnt(0)\n"
    "s_waitcnt vmcnt(24)\n"
    "s_barrier\n"
    "ds_read_b64_tr_b16 a[56:57], v11 offset:0\n"
    "ds_read_b64_tr_b16 a[58:59], v11 offset:1024\n"
    "ds_read_b64_tr_b16 a[60:61], v11 offset:8\n"
    "ds_read_b64_tr_b16 a[62:63], v11 offset:1032\n"
    "s_add_u32 s40, s48, 983040\n"
    "s_add_u32 m0, s26, 49152\n"
    "s_nop 0\n"
    "buffer_load_dwordx4 v8, s[12:15], s40 offen nt lds\n"
    "s_add_u32 m0, s26, 50176\n"
    "s_nop 0\n"
    "buffer_load_dwordx4 v9, s[12:15], s40 offen nt lds\n"
    "s_waitcnt lgkmcnt(0)\n"
    "s_waitcnt vmcnt(24)\n"
    "s_barrier\n"
    "ds_read_b64_tr_b16 a[64:65], v11 offset:8192\n"
    "ds_read_b64_tr_b16 a[66:67], v11 offset:9216\n"
    "ds_read_b64_tr_b16 a[68:69], v11 offset:8200\n"
    "ds_read_b64_tr_b16 a[70:71], v11 offset:9224\n"
    "s_add_u32 s40, s48, 983040\n"
    "s_add_u32 m0, s26, 57344\n"
    "s_nop 0\n"
    "buffer_load_dwordx4 v8, s[16:19], s40 offen nt lds\n"
    "s_add_u32 m0, s26, 58368\n"
    "s_nop 0\n"
    "buffer_load_dwordx4 v9, s[16:19], s40 offen nt lds\n"
    "s_waitcnt lgkmcnt(0)\n"
    "s_waitcnt vmcnt(24)\n"
    "s_barrier\n"
    "ds_read_b64_tr_b16 a[72:73], v11 offset:16384\n"
    "ds_read_b64_tr_b16 a[74:75], v11 offset:17408\n"
    "ds_read_b64_tr_b16 a[76:77], v11 offset:16392\n"
    "ds_read_b64_tr_b16 a[78:79], v11 offset:17416\n"
    "s_add_u32 s40, s48, 1081344\n"
    "s_add_u32 m0, s26, 65536\n"
    "s_nop 0\n"
    "buffer_load_dwordx4 v8, s[12:15], s40 offen nt lds\n"
    "s_add_u32 m0, s26, 66560\n"
    "s_nop 0\n"
    "buffer_load_dwordx4 v9, s[12:15], s40 offen nt lds\n"
    "s_waitcnt lgkmcnt(0)\n"
    "s_waitcnt vmcnt(24)\n"
    "s_barrier\n"
    "ds_read_b64_tr_b16 a[80:81], v11 offset:24576\n"
    "ds_read_b64_tr_b16 a[82:83], v11 offset:25600\n"
    "ds_read_b64_tr_b16 a[84:85], v11 offset:24584\n"
    "ds_read_b64_tr_b16 a[86:87], v11 offset:25608\n"
    "s_add_u32 s40, s48, 1081344\n"
    "s_add_u32 m0, s26, 73728\n"
    "s_nop 0\n"
    "buffer_load_dwordx4 v8, s[16:19], s40 offen nt lds\n"
    "s_add_u32 m0, s26, 74752\n"
    "s_nop 0\n"
    "buffer_load_dwordx4 v9, s[16:19], s40 offen nt lds\n"
    "s_waitcnt lgkmcnt(0)\n"
    "s_waitcnt vmcnt(24)\n"
    "s_barrier\n"
    "ds_read_b64_tr_b16 a[88:89], v11 offset:32768\n"
    "ds_read_b64_tr_b16 a[90:91], v11 offset:33792\n"
    "ds_read_b64_tr_b16 a[92:93], v11 offset:32776\n"
    "ds_read_b64_tr_b16 a[94:95], v11 offset:33800\n"
    "s_add_u32 s40, s48, 1179648\n"
    "s_add_u32 m0, s26, 81920\n"
    "s_nop 0\n"
    "buffer_load_dwordx4 v8, s[12:15], s40 offen nt lds\n"
    "s_add_u32 m0, s26, 82944\n"
    "s_nop 0\n"
    "buffer_load_dwordx4 v9, s[12:15], s40 offen nt lds\n"
    "s_waitcnt lgkmcnt(0)\n"
    "s_waitcnt vmcnt(24)\n"
    "s_barrier\n"
    "ds_read_b64_tr_b16 a[96:97], v11 offset:40960\n"
    "ds_read_b64_tr_b16 a[98:99], v11 offset:41984\n"
    "ds_read_b64_tr_b16 a[100:101], v11 offset:40968\n"
    "ds_read_b64_tr_b16 a[102:103], v11 offset:41992\n"
    "s_add_u32 s40, s48, 1179648\n"
    "s_add_u32 m0, s26, 90112\n"
    "s_nop 0\n"
    "buffer_load_dwordx4 v8, s[16:19], s40 offen nt lds\n"
    "s_add_u32 m0, s26, 91136\n"
    "s_nop 0\n"
    "buffer_load_dwordx4 v9, s[16:19], s40 offen nt lds\n"
    "s_waitcnt lgkmcnt(0)\n"
    "s_waitcnt vmcnt(24)\n"
    "s_barrier\n"
    "ds_read_b64_tr_b16 a[104:105], v11 offset:49152\n"
    "ds_read_b64_tr_b16 a[106:107], v11 offset:50176\n"
    "ds_read_b64_tr_b16 a[108:109], v11 offset:49160\n"
    "ds_read_b64_tr_b16 a[110:111], v11 offset:50184\n"
    "s_add_u32 s40, s48, 1277952\n"
    "s_add_u32 m0, s26, 98304\n"
    "s_nop 0\n"
    "buffer_load_dwordx4 v8, s[12:15], s40 offen nt lds\n"
    "s_add_u32 m0, s26, 99328\n"
    "s_nop 0\n"
    "buffer_load_dwordx4 v9, s[12:15], s40 offen nt lds\n"
    "s_waitcnt lgkmcnt(0)\n"
    "s_waitcnt vmcnt(24)\n"
    "s_barrier\n"
    "ds_read_b64_tr_b16 a[112:113], v10 offset:0\n"
    "ds_read_b64_tr_b16 a[114:115], v10 offset:1024\n"
    "ds_read_b64_tr_b16 a[116:117], v10 offset:8\n"
    "ds_read_b64_tr_b16 a[118:119], v10 offset:1032\n"
    "s_add_u32 s40, s48, 1277952\n"
    "s_add_u32 m0, s26, 106496\n"
    "s_nop 0\n"
    "buffer_load_dwordx4 v8, s[16:19], s40 offen nt lds\n"
    "s_add_u32 m0, s26, 107520\n"
    "s_nop 0\n"
    "buffer_load_dwordx4 v9, s[16:19], s40 offen nt lds\n"
    "s_waitcnt lgkmcnt(0)\n"
    "s_add_u32 s4, s3, 1\n"
    "v_mov_b32 v16, s3\n"
    "s_or_b32 s41, s21, s6\n"
    "s_cbranch_scc1 .Lcl_172\n"
    "s_lshl_b32 s41, s5, 2\n"
    "s_add_u32 s41, s41, 4178944\n"
    "v_mov_b32 v17, s41\n"
    "v_mov_b32 v16, s4\n"
    "s_mov_b64 exec, 1\n"
    "buffer_atomic_swap v16, v17, s[8:11], 0 offen sc0 sc1\n"
    "s_mov_b64 exec, -1\n"
    ".Lcl_172:\n"
    "v_min_u32 v20, 31, v1\n"
    "v_lshlrev_b32 v20, 7, v20\n"
    "v_add_u32 v20, 4182016, v20\n"
    "buffer_load_dwordx2 v[18:19], v20, s[8:11], 0 offen sc1\n"
    "s_waitcnt lgkmcnt(0)\n"
    "s_waitcnt vmcnt(25)\n"
    "s_barrier\n"
    "ds_read_b64_tr_b16 a[120:121], v10 offset:8192\n"
    "ds_read_b64_tr_b16 a[122:123], v10 offset:9216\n"
    "ds_read_b64_tr_b16 a[124:125], v10 offset:8200\n"
    "ds_read_b64_tr_b16 a[126:127], v10 offset:9224\n"
    "s_add_u32 s40, s48, 1376256\n"
    "s_add_u32 m0, s26, 0\n"
    "s_nop 0\n"
    "buffer_load_dwordx4 v8, s[12:15], s40 offen nt lds\n"
    "s_add_u32 m0, s26, 1024\n"
    "s_nop 0\n"
    "buffer_load_dwordx4 v9, s[12:15], s40 offen nt lds\n"
    "s_waitcnt lgkmcnt(0)\n"
    "s_waitcnt vmcnt(25)\n"
    "s_barrier\n"
    "ds_read_b64_tr_b16 a[128:129], v10 offset:16384\n"
    "ds_read_b64_tr_b16 a[130:131], v10 offset:17408\n"
    "ds_read_b64_tr_b16 a[132:133], v10 offset:16392\n"
    "ds_read_b64_tr_b16 a[134:135], v10 offset:17416\n"
    "s_add_u32 s40, s48, 1376256\n"
    "s_add_u32 m0, s26, 8192\n"
    "s_nop 0\n"
    "buffer_load_dwordx4 v8, s[16:19], s40 offen nt lds\n"
    "s_add_u32 m0, s26, 9216\n"
    "s_nop 0\n"
    "buffer_load_dwordx4 v9, s[16:19], s40 offen nt lds\n"
    "s_waitcnt lgkmcnt(0)\n"
    "s_waitcnt vmcnt(25)\n"
    "s_barrier\n"
    "ds_read_b64_tr_b16 a[136:137], v10 offset:24576\n"
    "ds_read_b64_tr_b16 a[138:139], v10 offset:25600\n"
    "ds_read_b64_tr_b16 a[140:141], v10 offset:24584\n"
    "ds_read_b64_tr_b16 a[142:143], v10 offset:25608\n"
    "s_add_u32 s40, s48, 1474560\n"
    "s_add_u32 m0, s26, 16384\n"
    "s_nop 0\n"
    "buffer_load_dwordx4 v8, s[12:15], s40 offen nt lds\n"
    "s_add_u32 m0, s26, 17408\n"
    "s_nop 0\n"
    "buffer_load_dwordx4 v9, s[12:15], s40 offen nt lds\n"
    "s_waitcnt lgkmcnt(0)\n"
    "s_waitcnt vmcnt(25)\n"
    "s_barrier\n"
    "ds_read_b64_tr_b16 a[144:145], v10 offset:32768\n"
    "ds_read_b64_tr_b16 a[146:147], v10 offset:33792\n"
    "ds_read_b64_tr_b16 a[148:149], v10 offset:32776\n"
    "ds_read_b64_tr_b16 a[150:151], v10 offset:33800\n"
    "s_add_u32 s40, s48, 1474560\n"
    "s_add_u32 m0, s26, 24576\n"
    "s_nop 0\n"
    "buffer_load_dwordx4 v8, s[16:19], s40 offen nt lds\n"
    "s_add_u32 m0, s26, 25600\n"
    "s_nop 0\n"
    "buffer_load_dwordx4 v9, s[16:19], s40 offen nt lds\n"
    "s_waitcnt lgkmcnt(0)\n"
    "s_waitcnt vmcnt(25)\n"
    "s_barrier\n"
    "ds_read_b64_tr_b16 a[152:153], v10 offset:40960\n"
    "ds_read_b64_tr_b16 a[154:155], v10 offset:41984\n"
    "ds_read_b64_tr_b16 a[156:157], v10 offset:40968\n"
    "ds_read_b64_tr_b16 a[158:159], v10 offset:41992\n"
    "s_add_u32 s40, s48, 1572864\n"
    "s_add_u32 m0, s26, 32768\n"
    "s_nop 0\n"
    "buffer_load_dwordx4 v8, s[12:15], s40 offen nt lds\n"
    "s_add_u32 m0, s26, 33792\n"
    "s_nop 0\n"
    "buffer_load_dwordx4 v9, s[12:15], s40 offen nt lds\n"
    "s_waitcnt lgkmcnt(0)\n"
    "s_waitcnt vmcnt(25)\n"
    "s_barrier\n"
    "ds_read_b64_tr_b16 a[160:161], v10 offset:49152\n"
    "ds_read_b64_tr_b16 a[162:163], v10 offset:50176\n"
    "ds_read_b64_tr_b16 a[164:165], v10 offset:49160\n"
    "ds_read_b64_tr_b16 a[166:167], v10 offset:50184\n"
    "s_add_u32 s40, s48, 1572864\n"
    "s_add_u32 m0, s26, 40960\n"
    "s_nop 0\n"
    "buffer_load_dwordx4 v8, s[16:19], s40 offen nt lds\n"
    "s_add_u32 m0, s26, 41984\n"
    "s_nop 0\n"
    "buffer_load_dwordx4 v9, s[16:19], s40 offen nt lds\n"
    "s_waitcnt lgkmcnt(0)\n"
    "s_waitcnt vmcnt(25)\n"
    "s_barrier\n"
    "ds_read_b64_tr_b16 a[168:169], v11 offset:0\n"
    "ds_read_b64_tr_b16 a[170:171], v11 offset:1024\n"
    "ds_read_b64_tr_b16 a[172:173], v11 offset:8\n"
    "ds_read_b64_tr_b16 a[174:175], v11 offset:1032\n"
    "s_add_u32 s40, s48, 1671168\n"
    "s_add_u32 m0, s26, 49152\n"
    "s_nop 0\n"
    "buffer_load_dwordx4 v8, s[12:15], s40 offen nt lds\n"
    "s_add_u32 m0, s26, 50176\n"
    "s_nop 0\n"
    "buffer_load_dwordx4 v9, s[12:15], s40 offen nt lds\n"
    "s_waitcnt lgkmcnt(0)\n"
    "s_waitcnt vmcnt(25)\n"
    "s_barrier\n"
    "ds_read_b64_tr_b16 a[176:177], v11 offset:8192\n"
    "ds_read_b64_tr_b16 a[178:179], v11 offset:9216\n"
    "ds_read_b64_tr_b16 a[180:181], v11 offset:8200\n"
    "ds_read_b64_tr_b16 a[182:183], v11 offset:9224\n"
    "s_add_u32 s40, s48, 1671168\n"
    "s_add_u32 m0, s26, 57344\n"
    "s_nop 0\n"
    "buffer_load_dwordx4 v8, s[16:19], s40 offen nt lds\n"
    "s_add_u32 m0, s26, 58368\n"
    "s_nop 0\n"
    "buffer_load_dwordx4 v9, s[16:19], s40 offen nt lds\n"
    "s_waitcnt lgkmcnt(0)\n"
    "s_waitcnt vmcnt(25)\n"
    "s_barrier\n"
    "ds_read_b64_tr_b16 a[184:185], v11 offset:16384\n"
    "ds_read_b64_tr_b16 a[186:187], v11 offset:17408\n"
    "ds_read_b64_tr_b16 a[188:189], v11 offset:16392\n"
    "ds_read_b64_tr_b16 a[190:191], v11 offset:17416\n"
    "s_add_u32 s40, s48, 1769472\n"
    "s_add_u32 m0, s26, 65536\n"
    "s_nop 0\n"
    "buffer_load_dwordx4 v8, s[12:15], s40 offen nt lds\n"
    "s_add_u32 m0, s26, 66560\n"
    "s_nop 0\n"
    "buffer_load_dwordx4 v9, s[12:15], s40 offen nt lds\n"
    "s_waitcnt lgkmcnt(0)\n"
    "s_waitcnt vmcnt(25)\n"
    "s_barrier\n"
    "ds_read_b64_tr_b16 a[192:193], v11 offset:24576\n"
    "ds_read_b64_tr_b16 a[194:195], v11 offset:25600\n"
    "ds_read_b64_tr_b16 a[196:197], v11 offset:24584\n"
    "ds_read_b64_tr_b16 a[198:199], v11 offset:25608\n"
    "s_add_u32 s40, s48, 1769472\n"
    "s_add_u32 m0, s26, 73728\n"
    "s_nop 0\n"
    "buffer_load_dwordx4 v8, s[16:19], s40 offen nt lds\n"
    "s_add_u32 m0, s26, 74752\n"
    "s_nop 0\n"
    "buffer_load_dwordx4 v9, s[16:19], s40 offen nt lds\n"
    "s_waitcnt lgkmcnt(0)\n"
    "s_waitcnt vmcnt(25)\n"
    "s_barrier\n"
    "ds_read_b64_tr_b16 a[200:201], v11 offset:32768\n"
    "ds_read_b64_tr_b16 a[202:203], v11 offset:33792\n"
    "ds_read_b64_tr_b16 a[204:205], v11 offset:32776\n"
    "ds_read_b64_tr_b16 a[206:207], v11 offset:33800\n"
    "s_add_u32 s40, s48, 1867776\n"
    "s_add_u32 m0, s26, 81920\n"
    "s_nop 0\n"
    "buffer_load_dwordx4 v8, s[12:15], s40 offen nt lds\n"
    "s_add_u32 m0, s26, 82944\n"
    "s_nop 0\n"
    "buffer_load_dwordx4 v9, s[12:15], s40 offen nt lds\n"
    "s_waitcnt lgkmcnt(0)\n"
    "s_waitcnt vmcnt(25)\n"
    "s_barrier\n"
    "ds_read_b64_tr_b16 a[208:209], v11 offset:40960\n"
    "ds_read_b64_tr_b16 a[210:211], v11 offset:41984\n"
    "ds_read_b64_tr_b16 a[212:213], v11 offset:40968\n"
    "ds_read_b64_tr_b16 a[214:215], v11 offset:41992\n"
    "s_add_u32 s40, s48, 1867776\n"
    "s_add_u32 m0, s26, 90112\n"
    "s_nop 0\n"
    "buffer_load_dwordx4 v8, s[16:19], s40 offen nt lds\n"
    "s_add_u32 m0, s26, 91136\n"
    "s_nop 0\n"
    "buffer_load_dwordx4 v9, s[16:19], s40 offen nt lds\n"
    "s_waitcnt lgkmcnt(0)\n"
    "s_waitcnt vmcnt(25)\n"
    "s_barrier\n"
    "ds_read_b64_tr_b16 a[216:217], v11 offset:49152\n"
    "ds_read_b64_tr_b16 a[218:219], v11 offset:50176\n"
    "ds_read_b64_tr_b16 a[220:221], v11 offset:49160\n"
    "ds_read_b64_tr_b16 a[222:223], v11 offset:50184\n"
    "s_waitcnt lgkmcnt(0)\n"
    "s_waitcnt vmcnt(24)\n"
    "v_cmp_ne_u32 vcc, s4, v18\n"
    "s_nop 1\n"
    "s_cmp_eq_u32 vcc_lo, 0\n"
    "s_cbranch_scc0 .Lrn_173\n"
    "s_add_u32 m0, s29, 0\n"
    "s_add_u32 s40, s28, 0\n"
    "buffer_load_dwordx4 v2, s[8:11], s40 offen offset:0 sc1 lds\n"
    "buffer_load_dwordx4 v2, s[8:11], s40 offen offset:1024 sc1 lds\n"
    "buffer_load_dwordx4 v2, s[8:11], s40 offen offset:2048 sc1 lds\n"
    "buffer_load_dwordx4 v2, s[8:11], s40 offen offset:3072 sc1 lds\n"
    "s_add_u32 m0, s29, 4096\n"
    "s_add_u32 s40, s28, 4096\n"
    "buffer_load_dwordx4 v2, s[8:11], s40 offen offset:0 sc1 lds\n"
    "buffer_load_dwordx4 v2, s[8:11], s40 offen offset:1024 sc1 lds\n"
    "buffer_load_dwordx4 v2, s[8:11], s40 offen offset:2048 sc1 lds\n"
    "buffer_load_dwordx4 v2, s[8:11], s40 offen offset:3072 sc1 lds\n"
    "s_add_u32 m0, s29, 8192\n"
    "s_add_u32 s40, s28, 8192\n"
    "buffer_load_dwordx4 v2, s[8:11], s40 offen offset:0 sc1 lds\n"
    "buffer_load_dwordx4 v2, s[8:11], s40 offen offset:1024 sc1 lds\n"
    "s_add_u32 s29, s29, 40960\n"
    "s_add_u32 s28, s28, 40960\n"
    ".Lrn_173:\n"
    "s_waitcnt vmcnt(22)\n"
    "s_barrier\n"
    "ds_read_b64_tr_b16 a[224:225], v10 offset:0\n"
    "ds_read_b64_tr_b16 a[226:227], v10 offset:1024\n"
    "ds_read_b64_tr_b16 a[228:229], v10 offset:8\n"
    "ds_read_b64_tr_b16 a[230:231], v10 offset:1032\n"
    "s_waitcnt lgkmcnt(0)\n"
    "s_waitcnt vmcnt(20)\n"
    "s_barrier\n"
    "ds_read_b64_tr_b16 a[232:233], v10 offset:8192\n"
    "ds_read_b64_tr_b16 a[234:235], v10 offset:9216\n"
    "ds_read_b64_tr_b16 a[236:237], v10 offset:8200\n"
    "ds_read_b64_tr_b16 a[238:239], v10 offset:9224\n"
    "s_waitcnt lgkmcnt(0)\n"
    "s_waitcnt vmcnt(18)\n"
    "s_barrier\n"
    "ds_read_b64_tr_b16 a[240:241], v10 offset:16384\n"
    "ds_read_b64_tr_b16 a[242:243], v10 offset:17408\n"
    "ds_read_b64_tr_b16 a[244:245], v10 offset:16392\n"
    "ds_read_b64_tr_b16 a[246:247], v10 offset:17416\n"
    "s_waitcnt lgkmcnt(0)\n"
    "s_waitcnt vmcnt(16)\n"
    "s_barrier\n"
    "ds_read_b64_tr_b16 a[248:249], v10 offset:24576\n"
    "ds_read_b64_tr_b16 a[250:251], v10 offset:25600\n"
    "ds_read_b64_tr_b16 a[252:253], v10 offset:24584\n"
    "ds_read_b64_tr_b16 a[254:255], v10 offset:25608\n"
    "s_waitcnt lgkmcnt(0)\n"
    "s_waitcnt vmcnt(14)\n"
    "s_barrier\n"
    "ds_read_b64_tr_b16 v[192:193], v10 offset:32768\n"
    "ds_read_b64_tr_b16 v[194:195], v10 offset:33792\n"
    "ds_read_b64_tr_b16 v[196:197], v10 offset:32776\n"
    "ds_read_b64_tr_b16 v[198:199], v10 offset:33800\n"
    "s_waitcnt lgkmcnt(0)\n"
    "s_waitcnt vmcnt(12)\n"
    "s_barrier\n"
    "ds_read_b64_tr_b16 v[200:201], v10 offset:40960\n"
    "ds_read_b64_tr_b16 v[202:203], v10 offset:41984\n"
    "ds_read_b64_tr_b16 v[204:205], v10 offset:40968\n"
    "ds_read_b64_tr_b16 v[206:207], v10 offset:41992\n"
    "s_waitcnt lgkmcnt(0)\n"
    "s_waitcnt vmcnt(10)\n"
    "s_barrier\n"
    "ds_read_b64_tr_b16 v[208:209], v10 offset:49152\n"
    "ds_read_b64_tr_b16 v[210:211], v10 offset:50176\n"
    "ds_read_b64_tr_b16 v[212:213], v10 offset:49160\n"
    "ds_read_b64_tr_b16 v[214:215], v10 offset:50184\n"
    "s_waitcnt lgkmcnt(0)\n"
    "s_waitcnt vmcnt(8)\n"
    "s_barrier\n"
    "ds_read_b64_tr_b16 v[216:217], v11 offset:0\n"
    "ds_read_b64_tr_b16 v[218:219], v11 offset:1024\n"
    "ds_read_b64_tr_b16 v[220:221], v11 offset:8\n"
    "ds_read_b64_tr_b16 v[222:223], v11 offset:1032\n"
    "s_waitcnt lgkmcnt(0)\n"
    "s_waitcnt vmcnt(6)\n"
    "s_barrier\n"
    "ds_read_b64_tr_b16 v[224:225], v11 offset:8192\n"
    "ds_read_b64_tr_b16 v[226:227], v11 offset:9216\n"
    "ds_read_b64_tr_b16 v[228:229], v11 offset:8200\n"
    "ds_read_b64_tr_b16 v[230:231], v11 offset:9224\n"
    "s_waitcnt lgkmcnt(0)\n"
    "s_waitcnt vmcnt(4)\n"
    "s_barrier\n"
    "ds_read_b64_tr_b16 v[232:233], v11 offset:16384\n"
    "ds_read_b64_tr_b16 v[234:235], v11 offset:17408\n"
    "ds_read_b64_tr_b16 v[236:237], v11 offset:16392\n"
    "ds_read_b64_tr_b16 v[238:239], v11 offset:17416\n"
    "s_waitcnt lgkmcnt(0)\n"
    "s_waitcnt vmcnt(2)\n"
    "s_barrier\n"
    "ds_read_b64_tr_b16 v[240:241], v11 offset:24576\n"
    "ds_read_b64_tr_b16 v[242:243], v11 offset:25600\n"
    "ds_read_b64_tr_b16 v[244:245], v11 offset:24584\n"
    "ds_read_b64_tr_b16 v[246:247], v11 offset:25608\n"
    "s_waitcnt lgkmcnt(0)\n"
    "s_waitcnt vmcnt(0)\n"
    "s_barrier\n"
    "ds_read_b64_tr_b16 v[248:249], v11 offset:32768\n"
    "ds_read_b64_tr_b16 v[250:251], v11 offset:33792\n"
    "ds_read_b64_tr_b16 v[252:253], v11 offset:32776\n"
    "ds_read_b64_tr_b16 v[254:255], v11 offset:33800\n"
    "s_waitcnt lgkmcnt(0)\n"
    "v_cmp_ne_u32 vcc, s4, v18\n"
    "s_nop 1\n"
    "s_cmp_eq_u32 vcc_lo, 0\n"
    "s_cbranch_scc1 .Lrx_176\n"
    ".Lrp_174:\n"
    "v_min_u32 v20, 31, v1\n"
    "v_lshlrev_b32 v20, 7, v20\n"
    "v_add_u32 v20, 4182016, v20\n"
    "buffer_load_dwordx2 v[18:19], v20, s[8:11], 0 offen sc1\n"
    "s_waitcnt vmcnt(0)\n"
    "v_cmp_ne_u32 vcc, s4, v18\n"
    "s_nop 1\n"
    "s_cmp_eq_u32 vcc_lo, 0\n"
    "s_cbranch_scc1 .Lro_175\n"
    "s_sleep 2\n"
    "s_sub_u32 s25, s25, 1\n"
    "s_cbranch_scc0 .Lrp_174\n"
    "s_branch .Lfail\n"
    ".Lro_175:\n"
    "s_add_u32 m0, s29, 0\n"
    "s_add_u32 s40, s28, 0\n"
    "buffer_load_dwordx4 v2, s[8:11], s40 offen offset:0 sc1 lds\n"
    "buffer_load_dwordx4 v2, s[8:11], s40 offen offset:1024 sc1 lds\n"
    "buffer_load_dwordx4 v2, s[8:11], s40 offen offset:2048 sc1 lds\n"
    "buffer_load_dwordx4 v2, s[8:11], s40 offen offset:3072 sc1 lds\n"
    "s_add_u32 m0, s29, 4096\n"
    "s_add_u32 s40, s28, 4096\n"
    "buffer_load_dwordx4 v2, s[8:11], s40 offen offset:0 sc1 lds\n"
    "buffer_load_dwordx4 v2, s[8:11], s40 offen offset:1024 sc1 lds\n"
    "buffer_load_dwordx4 v2, s[8:11], s40 offen offset:2048 sc1 lds\n"
    "buffer_load_dwordx4 v2, s[8:11], s40 offen offset:3072 sc1 lds\n"
    "s_add_u32 m0, s29, 8192\n"
    "s_add_u32 s40, s28, 8192\n"
    "buffer_load_dwordx4 v2, s[8:11], s40 offen offset:0 sc1 lds\n"
    "buffer_load_dwordx4 v2, s[8:11], s40 offen offset:1024 sc1 lds\n"
    "s_add_u32 s29, s29, 40960\n"
    "s_add_u32 s28, s28, 40960\n"
    ".Lrx_176:\n"
    "s_lshl_b32 s40, s27, 7\n"
    "s_add_u32 s40, s40, 4196352\n"
    "v_mov_b32 v4, s40\n"
    "v_cmp_ne_u32 vcc, 0, v1\n"
    "v_mov_b32 v0, 0x7ff00000\n"
    "s_nop 0\n"
    "v_cndmask_b32 v4, v4, v0, vcc\n"
    "v_bfe_u32 v6, v1, 3, 1\n"
    "v_lshlrev_b32 v6, 5, v6\n"
    "v_and_b32 v0, 48, v1\n"
    "v_xor_b32 v6, v6, v0\n"
    "v_bfe_u32 v0, v1, 0, 4\n"
    "v_lshl_add_u32 v6, v0, 6, v6\n"
    "v_xor_b32 v7, 16, v2\n"
    "v_mov_b32 v3, v6\n"
    "s_mov_b32 s30, 0\n"
    "s_mov_b32 s24, 0\n"
    "s_mov_b32 s34, s31\n"
    "s_mov_b32 s35, 0xbfb8aa3b\n"
    "s_waitcnt vmcnt(0)\n"
    "s_cmp_lg_u32 s21, 0\n"
    "s_cbranch_scc1 .Lgt_177\n"
    "v_mov_b32 v17, 162816\n"
    "v_readfirstlane_b32 s40, v16\n"
    "s_nop 1\n"
    "v_mov_b32 v16, s40\n"
    "ds_write_b32 v17, v16\n"
    "s_waitcnt lgkmcnt(0)\n"
    ".Lgt_177:\n"
    "s_barrier\n"
    "v_mov_b32 v17, 162816\n"
    "ds_read_b32 v16, v17\n"
    "s_waitcnt lgkmcnt(0)\n"
    "v_readfirstlane_b32 s40, v16\n"
    "s_barrier\n"
    "s_cmp_eq_u32 s40, s4\n"
    "s_cbranch_scc1 .Llost\n"
    "s_add_u32 m0, s29, 0\n"
    "s_add_u32 s40, s28, 0\n"
    "buffer_load_dwordx4 v2, s[8:11], s40 offen offset:0 sc1 lds\n"
    "buffer_load_dwordx4 v2, s[8:11], s40 offen offset:1024 sc1 lds\n"
    "buffer_load_dwordx4 v2, s[8:11], s40 offen offset:2048 sc1 lds\n"
    "buffer_load_dwordx4 v2, s[8:11], s40 offen offset:3072 sc1 lds\n"
    "s_add_u32 m0, s29, 4096\n"
    "s_add_u32 s40, s28, 4096\n"
    "buffer_load_dwordx4 v2, s[8:11], s40 offen offset:0 sc1 lds\n"
    "buffer_load_dwordx4 v2, s[8:11], s40 offen offset:1024 sc1 lds\n"
    "buffer_load_dwordx4 v2, s[8:11], s40 offen offset:2048 sc1 lds\n"
    "buffer_load_dwordx4 v2, s[8:11], s40 offen offset:3072 sc1 lds\n"
    "s_add_u32 m0, s29, 8192\n"
    "s_add_u32 s40, s28, 8192\n"
    "buffer_load_dwordx4 v2, s[8:11], s40 offen offset:0 sc1 lds\n"
    "buffer_load_dwordx4 v2, s[8:11], s40 offen offset:1024 sc1 lds\n"
    "s_add_u32 s29, s29, 40960\n"
    "s_add_u32 s28, s28, 40960\n"
    "ds_read_b128 v[48:51], v3 offset:0\n"
    "ds_read_b128 v[52:55], v3 offset:1024\n"
    "ds_read_b128 v[56:59], v3 offset:2048\n"
    ".p2align 4\n"
    ".LF_loop:\n"
    "s_waitcnt lgkmcnt(2)\n"
    "v_mfma_f32_16x16x32_bf16 v[16:19], a[0:3], v[48:51], 0\n"
    "ds_read_b128 v[60:63], v3 offset:3072\n"
    "v_mfma_f32_16x16x32_bf16 v[20:23], a[4:7], v[48:51], 0\n"
    "v_mfma_f32_16x16x32_bf16 v[24:27], a[8:11], v[48:51], 0\n"
    "v_mfma_f32_16x16x32_bf16 v[28:31], a[12:15], v[48:51], 0\n"
    "s_waitcnt lgkmcnt(2)\n"
    "v_mfma_f32_16x16x32_bf16 v[16:19], a[16:19], v[52:55], v[16:19]\n"
    "ds_read_b128 v[48:51], v3 offset:4096\n"
    "v_mfma_f32_16x16x32_bf16 v[20:23], a[20:23], v[52:55], v[20:23]\n"
    "v_mfma_f32_16x16x32_bf16 v[24:27], a[24:27], v[52:55], v[24:27]\n"
    "v_mfma_f32_16x16x32_bf16 v[28:31], a[28:31], v[52:55], v[28:31]\n"
    "v_mul_f32 v72, s35, v32\n"
    "v_mul_f32 v73, s35, v33\n"
    "s_waitcnt lgkmcnt(2)\n"
    "v_mfma_f32_16x16x32_bf16 v[16:19], a[32:35], v[56:59], v[16:19]\n"
    "ds_read_b128 v[52:55], v3 offset:5120\n"
    "v_mul_f32 v74, s35, v34\n"
    "v_mul_f32 v75, s35, v35\n"
    "v_mfma_f32_16x16x32_bf16 v[20:23], a[36:39], v[56:59], v[20:23]\n"
    "v_mul_f32 v76, s35, v36\n"
    "v_mul_f32 v77, s35, v37\n"
    "v_mfma_f32_16x16x32_bf16 v[24:27], a[40:43], v[56:59], v[24:27]\n"
    "v_mul_f32 v78, s35, v38\n"
    "v_mul_f32 v79, s35, v39\n"
    "v_mfma_f32_16x16x32_bf16 v[28:31], a[44:47], v[56:59], v[28:31]\n"
    "v_exp_f32 v72, v72\n"
    "s_waitcnt lgkmcnt(2)\n"
    "v_mfma_f32_16x16x32_bf16 v[16:19], a[48:51], v[60:63], v[16:19]\n"
    "ds_read_b128 v[56:59], v3 offset:6144\n"
    "v_exp_f32 v73, v73\n"
    "v_mfma_f32_16x16x32_bf16 v[20:23], a[52:55], v[60:63], v[20:23]\n"
    "v_exp_f32 v74, v74\n"
    "v_mfma_f32_16x16x32_bf16 v[24:27], a[56:59], v[60:63], v[24:27]\n"
    "v_exp_f32 v75, v75\n"
    "v_mfma_f32_16x16x32_bf16 v[28:31], a[60:63], v[60:63], v[28:31]\n"
    "v_exp_f32 v76, v76\n"
    "s_waitcnt lgkmcnt(2)\n"
    "v_mfma_f32_16x16x32_bf16 v[16:19], a[64:67], v[48:51], v[16:19]\n"
    "ds_read_b128 v[60:63], v3 offset:7168\n"
    "v_exp_f32 v77, v77\n"
    "v_mfma_f32_16x16x32_bf16 v[20:23], a[68:71], v[48:51], v[20:23]\n"
    "v_exp_f32 v78, v78\n"
    "v_mfma_f32_16x16x32_bf16 v[24:27], a[72:75], v[48:51], v[24:27]\n"
    "v_exp_f32 v79, v79\n"
    "v_mfma_f32_16x16x32_bf16 v[28:31], a[76:79], v[48:51], v[28:31]\n"
    "v_mul_f32 v32, v32, v40\n"
    "v_mul_f32 v33, v33, v41\n"
    "s_waitcnt lgkmcnt(2)\n"
    "v_mfma_f32_16x16x32_bf16 v[16:19], a[80:83], v[52:55], v[16:19]\n"
    "ds_read_b128 v[48:51], v3 offset:8192\n"
    "v_mul_f32 v34, v34, v42\n"
    "v_mul_f32 v35, v35, v43\n"
    "v_mfma_f32_16x16x32_bf16 v[20:23], a[84:87], v[52:55], v[20:23]\n"
    "v_mul_f32 v36, v36, v44\n"
    "v_mul_f32 v37, v37, v45\n"
    "v_mfma_f32_16x16x32_bf16 v[24:27], a[88:91], v[52:55], v[24:27]\n"
    "v_mul_f32 v38, v38, v46\n"
    "v_mul_f32 v39, v39, v47\n"
    "v_mfma_f32_16x16x32_bf16 v[28:31], a[92:95], v[52:55], v[28:31]\n"
    "v_add_f32 v72, 1.0, v72\n"
    "v_add_f32 v73, 1.0, v73\n"
    "s_waitcnt lgkmcnt(2)\n"
    "v_mfma_f32_16x16x32_bf16 v[16:19], a[96:99], v[56:59], v[16:19]\n"
    "ds_read_b128 v[52:55], v3 offset:9216\n"
    "v_add_f32 v74, 1.0, v74\n"
    "v_add_f32 v75, 1.0, v75\n"
    "v_mfma_f32_16x16x32_bf16 v[20:23], a[100:103], v[56:59], v[20:23]\n"
    "v_add_f32 v76, 1.0, v76\n"
    "v_add_f32 v77, 1.0, v77\n"
    "v_mfma_f32_16x16x32_bf16 v[24:27], a[104:107], v[56:59], v[24:27]\n"
    "v_add_f32 v78, 1.0, v78\n"
    "v_add_f32 v79, 1.0, v79\n"
    "v_mfma_f32_16x16x32_bf16 v[28:31], a[108:111], v[56:59], v[28:31]\n"
    "v_rcp_f32 v72, v72\n"
    "s_waitcnt lgkmcnt(2)\n"
    "v_mfma_f32_16x16x32_bf16 v[16:19], a[112:115], v[60:63], v[16:19]\n"
    "ds_read_b128 v[56:59], v3 offset:10240\n"
    "v_rcp_f32 v73, v73\n"
    "v_mfma_f32_16x16x32_bf16 v[20:23], a[116:119], v[60:63], v[20:23]\n"
    "v_rcp_f32 v74, v74\n"
    "v_mfma_f32_16x16x32_bf16 v[24:27], a[120:123], v[60:63], v[24:27]\n"
    "v_rcp_f32 v75, v75\n"
    "v_mfma_f32_16x16x32_bf16 v[28:31], a[124:127], v[60:63], v[28:31]\n"
    "v_rcp_f32 v76, v76\n"
    "s_waitcnt lgkmcnt(2)\n"
    "v_mfma_f32_16x16x32_bf16 v[16:19], a[128:131], v[48:51], v[16:19]\n"
    "ds_read_b128 v[60:63], v3 offset:11264\n"
    "v_rcp_f32 v77, v77\n"
    "v_mfma_f32_16x16x32_bf16 v[20:23], a[132:135], v[48:51], v[20:23]\n"
    "v_rcp_f32 v78, v78\n"
    "v_mfma_f32_16x16x32_bf16 v[24:27], a[136:139], v[48:51], v[24:27]\n"
    "v_rcp_f32 v79, v79\n"
    "v_mfma_f32_16x16x32_bf16 v[28:31], a[140:143], v[48:51], v[28:31]\n"
    "v_mul_f32 v72, v72, v32\n"
    "v_mul_f32 v73, v73, v33\n"
    "s_waitcnt lgkmcnt(2)\n"
    "v_mfma_f32_16x16x32_bf16 v[16:19], a[144:147], v[52:55], v[16:19]\n"
    "ds_read_b128 v[48:51], v3 offset:12288\n"
    "v_mul_f32 v74, v74, v34\n"
    "v_mul_f32 v75, v75, v35\n"
    "v_mfma_f32_16x16x32_bf16 v[20:23], a[148:151], v[52:55], v[20:23]\n"
    "v_mul_f32 v76, v76, v36\n"
    "v_mul_f32 v77, v77, v37\n"
    "v_mfma_f32_16x16x32_bf16 v[24:27], a[152:155], v[52:55], v[24:27]\n"
    "v_mul_f32 v78, v78, v38\n"
    "v_mul_f32 v79, v79, v39\n"
    "v_mfma_f32_16x16x32_bf16 v[28:31], a[156:159], v[52:55], v[28:31]\n"
    "v_cvt_pk_bf16_f32 v64, v72, v73\n"
    "v_cvt_pk_bf16_f32 v65, v74, v75\n"
    "s_waitcnt lgkmcnt(2)\n"
    "v_mfma_f32_16x16x32_bf16 v[16:19], a[160:163], v[56:59], v[16:19]\n"
    "ds_read_b128 v[52:55], v3 offset:13312\n"
    "v_cvt_pk_bf16_f32 v66, v76, v77\n"
    "v_cvt_pk_bf16_f32 v67, v78, v79\n"
    "v_mfma_f32_16x16x32_bf16 v[20:23], a[164:167], v[56:59], v[20:23]\n"
    "buffer_store_dwordx4 v[64:67], v2, s[8:11], s34 offen offset:1024 sc1\n"
    "v_mfma_f32_16x16x32_bf16 v[24:27], a[168:171], v[56:59], v[24:27]\n"
    "v_mfma_f32_16x16x32_bf16 v[28:31], a[172:175], v[56:59], v[28:31]\n"
    "s_waitcnt lgkmcnt(2)\n"
    "v_mfma_f32_16x16x32_bf16 v[16:19], a[176:179], v[60:63], v[16:19]\n"
    "ds_read_b128 v[56:59], v3 offset:14336\n"
    "v_mfma_f32_16x16x32_bf16 v[20:23], a[180:183], v[60:63], v[20:23]\n"
    "v_mfma_f32_16x16x32_bf16 v[24:27], a[184:187], v[60:63], v[24:27]\n"
    "s_cmp_gt_u32 s24, 1\n"
    "s_cbranch_scc1 .LPe_178\n"
    "v_mov_b32 v5, s24\n"
    "s_waitcnt vmcnt(0)\n"
    "buffer_store_dword v5, v4, s[8:11], 0 offen sc1\n"
    ".LPe_178:\n"
    "v_mfma_f32_16x16x32_bf16 v[28:31], a[188:191], v[60:63], v[28:31]\n"
    "s_waitcnt lgkmcnt(2)\n"
    "v_mfma_f32_16x16x32_bf16 v[16:19], a[192:195], v[48:51], v[16:19]\n"
    "ds_read_b128 v[60:63], v3 offset:15360\n"
    "s_add_u32 m0, s29, 0\n"
    "s_add_u32 s40, s28, 0\n"
    "buffer_load_dwordx4 v2, s[8:11], s40 offen offset:0 sc1 lds\n"
    "v_mfma_f32_16x16x32_bf16 v[20:23], a[196:199], v[48:51], v[20:23]\n"
    "v_mfma_f32_16x16x32_bf16 v[24:27], a[200:203], v[48:51], v[24:27]\n"
    "v_mfma_f32_16x16x32_bf16 v[28:31], a[204:207], v[48:51], v[28:31]\n"
    "buffer_load_dwordx4 v2, s[8:11], s40 offen offset:1024 sc1 lds\n"
    "s_waitcnt lgkmcnt(2)\n"
    "v_mfma_f32_16x16x32_bf16 v[16:19], a[208:211], v[52:55], v[16:19]\n"
    "ds_read_b128 v[48:51], v3 offset:16384\n"
    "v_mfma_f32_16x16x32_bf16 v[20:23], a[212:215], v[52:55], v[20:23]\n"
    "v_mfma_f32_16x16x32_bf16 v[24:27], a[216:219], v[52:55], v[24:27]\n"
    "buffer_load_dwordx4 v2, s[8:11], s40 offen offset:2048 sc1 lds\n"
    "v_mfma_f32_16x16x32_bf16 v[28:31], a[220:223], v[52:55], v[28:31]\n"
    "s_waitcnt lgkmcnt(2)\n"
    "v_mfma_f32_16x16x32_bf16 v[16:19], a[224:227], v[56:59], v[16:19]\n"
    "ds_read_b128 v[52:55], v3 offset:17408\n"
    "v_mfma_f32_16x16x32_bf16 v[20:23], a[228:231], v[56:59], v[20:23]\n"
    "buffer_load_dwordx4 v2, s[8:11], s40 offen offset:3072 sc1 lds\n"
    "v_mfma_f32_16x16x32_bf16 v[24:27], a[232:235], v[56:59], v[24:27]\n"
    "v_mfma_f32_16x16x32_bf16 v[28:31], a[236:239], v[56:59], v[28:31]\n"
    "s_waitcnt lgkmcnt(2)\n"
    "v_mfma_f32_16x16x32_bf16 v[16:19], a[240:243], v[60:63], v[16:19]\n"
    "ds_read_b128 v[56:59], v3 offset:18432\n"
    "s_add_u32 m0, s29, 4096\n"
    "s_add_u32 s40, s28, 4096\n"
    "buffer_load_dwordx4 v2, s[8:11], s40 offen offset:0 sc1 lds\n"
    "v_mfma_f32_16x16x32_bf16 v[20:23], a[244:247], v[60:63], v[20:23]\n"
    "v_mfma_f32_16x16x32_bf16 v[24:27], a[248:251], v[60:63], v[24:27]\n"
    "v_mfma_f32_16x16x32_bf16 v[28:31], a[252:255], v[60:63], v[28:31]\n"
    "buffer_load_dwordx4 v2, s[8:11], s40 offen offset:1024 sc1 lds\n"
    "s_waitcnt lgkmcnt(2)\n"
    "v_mfma_f32_16x16x32_bf16 v[16:19], v[192:195], v[48:51], v[16:19]\n"
    "ds_read_b128 v[60:63], v3 offset:19456\n"
    "v_mfma_f32_16x16x32_bf16 v[20:23], v[196:199], v[48:51], v[20:23]\n"
    "v_mfma_f32_16x16x32_bf16 v[24:27], v[200:203], v[48:51], v[24:27]\n"
    "buffer_load_dwordx4 v2, s[8:11], s40 offen offset:2048 sc1 lds\n"
    "v_mfma_f32_16x16x32_bf16 v[28:31], v[204:207], v[48:51], v[28:31]\n"
    "s_waitcnt lgkmcnt(2)\n"
    "v_mfma_f32_16x16x32_bf16 v[16:19], v[208:211], v[52:55], v[16:19]\n"
    "ds_read_b128 v[48:51], v3 offset:20480\n"
    "v_mfma_f32_16x16x32_bf16 v[20:23], v[212:215], v[52:55], v[20:23]\n"
    "buffer_load_dwordx4 v2, s[8:11], s40 offen offset:3072 sc1 lds\n"
    "v_mfma_f32_16x16x32_bf16 v[24:27], v[216:219], v[52:55], v[24:27]\n"
    "v_mfma_f32_16x16x32_bf16 v[28:31], v[220:223], v[52:55], v[28:31]\n"
    "s_waitcnt lgkmcnt(2)\n"
    "v_mfma_f32_16x16x32_bf16 v[16:19], v[224:227], v[56:59], v[16:19]\n"
    "ds_read_b128 v[52:55], v3 offset:21504\n"
    "s_add_u32 m0, s29, 8192\n"
    "s_add_u32 s40, s28, 8192\n"
    "buffer_load_dwordx4 v2, s[8:11], s40 offen offset:0 sc1 lds\n"
    "v_mfma_f32_16x16x32_bf16 v[20:23], v[228:231], v[56:59], v[20:23]\n"
    "v_mfma_f32_16x16x32_bf16 v[24:27], v[232:235], v[56:59], v[24:27]\n"
    "v_mfma_f32_16x16x32_bf16 v[28:31], v[236:239], v[56:59], v[28:31]\n"
    "buffer_load_dwordx4 v2, s[8:11], s40 offen offset:1024 sc1 lds\n"
    "s_waitcnt lgkmcnt(2)\n"
    "v_mfma_f32_16x16x32_bf16 v[16:19], v[240:243], v[60:63], v[16:19]\n"
    "ds_read_b128 v[56:59], v3 offset:22528\n"
    "v_mfma_f32_16x16x32_bf16 v[20:23], v[244:247], v[60:63], v[20:23]\n"
    "v_mfma_f32_16x16x32_bf16 v[24:27], v[248:251], v[60:63], v[24:27]\n"
    "v_mfma_f32_16x16x32_bf16 v[28:31], v[252:255], v[60:63], v[28:31]\n"
    "s_waitcnt lgkmcnt(2)\n"
    "v_mfma_f32_16x16x32_bf16 v[32:35], a[0:3], v[48:51], 0\n"
    "ds_read_b128 v[60:63], v3 offset:23552\n"
    "v_mfma_f32_16x16x32_bf16 v[36:39], a[4:7], v[48:51], 0\n"
    "v_mfma_f32_16x16x32_bf16 v[40:43], a[8:11], v[48:51], 0\n"
    "v_mov_b32 v5, s24\n"
    "v_mfma_f32_16x16x32_bf16 v[44:47], a[12:15], v[48:51], 0\n"
    "s_waitcnt lgkmcnt(2)\n"
    "v_mfma_f32_16x16x32_bf16 v[32:35], a[16:19], v[52:55], v[32:35]\n"
    "ds_read_b128 v[48:51], v3 offset:24576\n"
    "v_mfma_f32_16x16x32_bf16 v[36:39], a[20:23], v[52:55], v[36:39]\n"
    "v_mfma_f32_16x16x32_bf16 v[40:43], a[24:27], v[52:55], v[40:43]\n"
    "v_mfma_f32_16x16x32_bf16 v[44:47], a[28:31], v[52:55], v[44:47]\n"
    "v_mul_f32 v72, s35, v16\n"
    "v_mul_f32 v73, s35, v17\n"
    "s_waitcnt lgkmcnt(2)\n"
    "v_mfma_f32_16x16x32_bf16 v[32:35], a[32:35], v[56:59], v[32:35]\n"
    "ds_read_b128 v[52:55], v3 offset:25600\n"
    "v_mul_f32 v74, s35, v18\n"
    "v_mul_f32 v75, s35, v19\n"
    "v_mfma_f32_16x16x32_bf16 v[36:39], a[36:39], v[56:59], v[36:39]\n"
    "v_mul_f32 v76, s35, v20\n"
    "v_mul_f32 v77, s35, v21\n"
    "v_mfma_f32_16x16x32_bf16 v[40:43], a[40:43], v[56:59], v[40:43]\n"
    "v_mul_f32 v78, s35, v22\n"
    "v_mul_f32 v79, s35, v23\n"
    "v_mfma_f32_16x16x32_bf16 v[44:47], a[44:47], v[56:59], v[44:47]\n"
    "v_exp_f32 v72, v72\n"
    "s_waitcnt lgkmcnt(2)\n"
    "v_mfma_f32_16x16x32_bf16 v[32:35], a[48:51], v[60:63], v[32:35]\n"
    "ds_read_b128 v[56:59], v3 offset:26624\n"
    "v_exp_f32 v73, v73\n"
    "v_mfma_f32_16x16x32_bf16 v[36:39], a[52:55], v[60:63], v[36:39]\n"
    "v_exp_f32 v74, v74\n"
    "v_mfma_f32_16x16x32_bf16 v[40:43], a[56:59], v[60:63], v[40:43]\n"
    "v_exp_f32 v75, v75\n"
    "v_mfma_f32_16x16x32_bf16 v[44:47], a[60:63], v[60:63], v[44:47]\n"
    "v_exp_f32 v76, v76\n"
    "s_waitcnt lgkmcnt(2)\n"
    "v_mfma_f32_16x16x32_bf16 v[32:35], a[64:67], v[48:51], v[32:35]\n"
    "ds_read_b128 v[60:63], v3 offset:27648\n"
    "v_exp_f32 v77, v77\n"
    "v_mfma_f32_16x16x32_bf16 v[36:39], a[68:71], v[48:51], v[36:39]\n"
    "v_exp_f32 v78, v78\n"
    "v_mfma_f32_16x16x32_bf16 v[40:43], a[72:75], v[48:51], v[40:43]\n"
    "v_exp_f32 v79, v79\n"
    "v_mfma_f32_16x16x32_bf16 v[44:47], a[76:79], v[48:51], v[44:47]\n"
    "v_mul_f32 v16, v16, v24\n"
    "v_mul_f32 v17, v17, v25\n"
    "s_waitcnt lgkmcnt(2)\n"
    "v_mfma_f32_16x16x32_bf16 v[32:35], a[80:83], v[52:55], v[32:35]\n"
    "ds_read_b128 v[48:51], v3 offset:28672\n"
    "v_mul_f32 v18, v18, v26\n"
    "v_mul_f32 v19, v19, v27\n"
    "v_mfma_f32_16x16x32_bf16 v[36:39], a[84:87], v[52:55], v[36:39]\n"
    "v_mul_f32 v20, v20, v28\n"
    "v_mul_f32 v21, v21, v29\n"
    "v_mfma_f32_16x16x32_bf16 v[40:43], a[88:91], v[52:55], v[40:43]\n"
    "v_mul_f32 v22, v22, v30\n"
    "v_mul_f32 v23, v23, v31\n"
    "v_mfma_f32_16x16x32_bf16 v[44:47], a[92:95], v[52:55], v[44:47]\n"
    "v_add_f32 v72, 1.0, v72\n"
    "v_add_f32 v73, 1.0, v73\n"
    "s_waitcnt lgkmcnt(2)\n"
    "v_mfma_f32_16x16x32_bf16 v[32:35], a[96:99], v[56:59], v[32:35]\n"
    "ds_read_b128 v[52:55], v3 offset:29696\n"
    "v_add_f32 v74, 1.0, v74\n"
    "v_add_f32 v75, 1.0, v75\n"
    "v_mfma_f32_16x16x32_bf16 v[36:39], a[100:103], v[56:59], v[36:39]\n"
    "v_add_f32 v76, 1.0, v76\n"
    "v_add_f32 v77, 1.0, v77\n"
    "v_mfma_f32_16x16x32_bf16 v[40:43], a[104:107], v[56:59], v[40:43]\n"
    "v_add_f32 v78, 1.0, v78\n"
    "v_add_f32 v79, 1.0, v79\n"
    "v_mfma_f32_16x16x32_bf16 v[44:47], a[108:111], v[56:59], v[44:47]\n"
    "v_rcp_f32 v72, v72\n"
    "s_waitcnt lgkmcnt(2)\n"
    "v_mfma_f32_16x16x32_bf16 v[32:35], a[112:115], v[60:63], v[32:35]\n"
    "ds_read_b128 v[56:59], v3 offset:30720\n"
    "v_rcp_f32 v73, v73\n"
    "v_mfma_f32_16x16x32_bf16 v[36:39], a[116:119], v[60:63], v[36:39]\n"
    "v_rcp_f32 v74, v74\n"
    "v_mfma_f32_16x16x32_bf16 v[40:43], a[120:123], v[60:63], v[40:43]\n"
    "v_rcp_f32 v75, v75\n"
    "v_mfma_f32_16x16x32_bf16 v[44:47], a[124:127], v[60:63], v[44:47]\n"
    "v_rcp_f32 v76, v76\n"
    "s_waitcnt lgkmcnt(2)\n"
    "v_mfma_f32_16x16x32_bf16 v[32:35], a[128:131], v[48:51], v[32:35]\n"
    "ds_read_b128 v[60:63], v3 offset:31744\n"
    "v_rcp_f32 v77, v77\n"
    "v_mfma_f32_16x16x32_bf16 v[36:39], a[132:135], v[48:51], v[36:39]\n"
    "v_rcp_f32 v78, v78\n"
    "v_mfma_f32_16x16x32_bf16 v[40:43], a[136:139], v[48:51], v[40:43]\n"
    "v_rcp_f32 v79, v79\n"
    "v_mfma_f32_16x16x32_bf16 v[44:47], a[140:143], v[48:51], v[44:47]\n"
    "v_mul_f32 v72, v72, v16\n"
    "v_mul_f32 v73, v73, v17\n"
    "s_waitcnt lgkmcnt(2)\n"
    "v_mfma_f32_16x16x32_bf16 v[32:35], a[144:147], v[52:55], v[32:35]\n"
    "ds_read_b128 v[48:51], v3 offset:32768\n"
    "v_mul_f32 v74, v74, v18\n"
    "v_mul_f32 v75, v75, v19\n"
    "v_mfma_f32_16x16x32_bf16 v[36:39], a[148:151], v[52:55], v[36:39]\n"
    "v_mul_f32 v76, v76, v20\n"
    "v_mul_f32 v77, v77, v21\n"
    "v_mfma_f32_16x16x32_bf16 v[40:43], a[152:155], v[52:55], v[40:43]\n"
    "v_mul_f32 v78, v78, v22\n"
    "v_mul_f32 v79, v79, v23\n"
    "v_mfma_f32_16x16x32_bf16 v[44:47], a[156:159], v[52:55], v[44:47]\n"
    "v_cvt_pk_bf16_f32 v64, v72, v73\n"
    "v_cvt_pk_bf16_f32 v65, v74, v75\n"
    "s_waitcnt lgkmcnt(2)\n"
    "v_mfma_f32_16x16x32_bf16 v[32:35], a[160:163], v[56:59], v[32:35]\n"
    "ds_read_b128 v[52:55], v3 offset:33792\n"
    "v_cvt_pk_bf16_f32 v66, v76, v77\n"
    "v_cvt_pk_bf16_f32 v67, v78, v79\n"
    "v_mfma_f32_16x16x32_bf16 v[36:39], a[164:167], v[56:59], v[36:39]\n"
    "buffer_store_dwordx4 v[64:67], v2, s[8:11], s31 offen offset:0 sc1\n"
    "v_mfma_f32_16x16x32_bf16 v[40:43], a[168:171], v[56:59], v[40:43]\n"
    "v_mfma_f32_16x16x32_bf16 v[44:47], a[172:175], v[56:59], v[44:47]\n"
    "s_waitcnt lgkmcnt(2)\n"
    "v_mfma_f32_16x16x32_bf16 v[32:35], a[176:179], v[60:63], v[32:35]\n"
    "ds_read_b128 v[56:59], v3 offset:34816\n"
    "v_mfma_f32_16x16x32_bf16 v[36:39], a[180:183], v[60:63], v[36:39]\n"
    "v_mfma_f32_16x16x32_bf16 v[40:43], a[184:187], v[60:63], v[40:43]\n"
    "v_mfma_f32_16x16x32_bf16 v[44:47], a[188:191], v[60:63], v[44:47]\n"
    "s_waitcnt lgkmcnt(2)\n"
    "v_mfma_f32_16x16x32_bf16 v[32:35], a[192:195], v[48:51], v[32:35]\n"
    "ds_read_b128 v[60:63], v3 offset:35840\n"
    "v_mfma_f32_16x16x32_bf16 v[36:39], a[196:199], v[48:51], v[36:39]\n"
    "v_mfma_f32_16x16x32_bf16 v[40:43], a[200:203], v[48:51], v[40:43]\n"
    "v_mfma_f32_16x16x32_bf16 v[44:47], a[204:207], v[48:51], v[44:47]\n"
    "s_waitcnt lgkmcnt(2)\n"
    "v_mfma_f32_16x16x32_bf16 v[32:35], a[208:211], v[52:55], v[32:35]\n"
    "ds_read_b128 v[48:51], v3 offset:36864\n"
    "s_waitcnt vmcnt(11)\n"
    "buffer_store_dword v5, v4, s[8:11], 0 offen sc1\n"
    "v_mfma_f32_16x16x32_bf16 v[36:39], a[212:215], v[52:55], v[36:39]\n"
    "v_mfma_f32_16x16x32_bf16 v[40:43], a[216:219], v[52:55], v[40:43]\n"
    "s_mov_b32 s34, s31\n"
    "s_add_u32 s31, s31, 98304\n"
    "s_add_u32 s28, s28, 40960\n"
    "v_mfma_f32_16x16x32_bf16 v[44:47], a[220:223], v[52:55], v[44:47]\n"
    "s_add_u32 s29, s29, 40960\n"
    "s_cmp_ge_u32 s29, 122880\n"
    "s_cselect_b32 s40, 122880, 0\n"
    "s_waitcnt lgkmcnt(2)\n"
    "v_mfma_f32_16x16x32_bf16 v[32:35], a[224:227], v[56:59], v[32:35]\n"
    "ds_read_b128 v[52:55], v3 offset:37888\n"
    "s_sub_u32 s29, s29, s40\n"
    "s_add_u32 s30, s30, 40960\n"
    "v_mfma_f32_16x16x32_bf16 v[36:39], a[228:231], v[56:59], v[36:39]\n"
    "s_cmp_eq_u32 s30, 122880\n"
    "s_cselect_b32 s30, 0, s30\n"
    "v_mfma_f32_16x16x32_bf16 v[40:43], a[232:235], v[56:59], v[40:43]\n"
    "s_add_u32 s24, s24, 1\n"
    "s_cmp_lt_u32 s24, 64\n"
    "v_mfma_f32_16x16x32_bf16 v[44:47], a[236:239], v[56:59], v[44:47]\n"
    "s_waitcnt lgkmcnt(2)\n"
    "v_mfma_f32_16x16x32_bf16 v[32:35], a[240:243], v[60:63], v[32:35]\n"
    "ds_read_b128 v[56:59], v3 offset:38912\n"
    "v_mfma_f32_16x16x32_bf16 v[36:39], a[244:247], v[60:63], v[36:39]\n"
    "v_mfma_f32_16x16x32_bf16 v[40:43], a[248:251], v[60:63], v[40:43]\n"
    "v_mfma_f32_16x16x32_bf16 v[44:47], a[252:255], v[60:63], v[44:47]\n"
    "s_waitcnt lgkmcnt(2)\n"
    "v_mfma_f32_16x16x32_bf16 v[32:35], v[192:195], v[48:51], v[32:35]\n"
    "ds_read_b128 v[60:63], v3 offset:39936\n"
    "v_mfma_f32_16x16x32_bf16 v[36:39], v[196:199], v[48:51], v[36:39]\n"
    "v_mfma_f32_16x16x32_bf16 v[40:43], v[200:203], v[48:51], v[40:43]\n"
    "v_add_u32 v3, s30, v6\n"
    "v_mfma_f32_16x16x32_bf16 v[44:47], v[204:207], v[48:51], v[44:47]\n"
    "s_waitcnt lgkmcnt(2)\n"
    "v_mfma_f32_16x16x32_bf16 v[32:35], v[208:211], v[52:55], v[32:35]\n"
    "v_mfma_f32_16x16x32_bf16 v[36:39], v[212:215], v[52:55], v[36:39]\n"
    "s_waitcnt lgkmcnt(0)\n"
    "s_barrier\n"
    "ds_read_b128 v[48:51], v3 offset:0\n"
    "v_mfma_f32_16x16x32_bf16 v[40:43], v[216:219], v[52:55], v[40:43]\n"
    "v_mfma_f32_16x16x32_bf16 v[44:47], v[220:223], v[52:55], v[44:47]\n"
    "v_mfma_f32_16x16x32_bf16 v[32:35], v[224:227], v[56:59], v[32:35]\n"
    "ds_read_b128 v[52:55], v3 offset:1024\n"
    "v_mfma_f32_16x16x32_bf16 v[36:39], v[228:231], v[56:59], v[36:39]\n"
    "v_mfma_f32_16x16x32_bf16 v[40:43], v[232:235], v[56:59], v[40:43]\n"
    "v_mfma_f32_16x16x32_bf16 v[44:47], v[236:239], v[56:59], v[44:47]\n"
    "v_mfma_f32_16x16x32_bf16 v[32:35], v[240:243], v[60:63], v[32:35]\n"
    "ds_read_b128 v[56:59], v3 offset:2048\n"
    "v_mfma_f32_16x16x32_bf16 v[36:39], v[244:247], v[60:63], v[36:39]\n"
    "v_mfma_f32_16x16x32_bf16 v[40:43], v[248:251], v[60:63], v[40:43]\n"
    "v_mfma_f32_16x16x32_bf16 v[44:47], v[252:255], v[60:63], v[44:47]\n"
    "s_cbranch_scc1 .LF_loop\n"
    "s_nop 2\n"
    "v_mul_f32 v72, s35, v32\n"
    "v_mul_f32 v73, s35, v33\n"
    "v_mul_f32 v74, s35, v34\n"
    "v_mul_f32 v75, s35, v35\n"
    "v_mul_f32 v76, s35, v36\n"
    "v_mul_f32 v77, s35, v37\n"
    "v_mul_f32 v78, s35, v38\n"
    "v_mul_f32 v79, s35, v39\n"
    "v_exp_f32 v72, v72\n"
    "v_exp_f32 v73, v73\n"
    "v_exp_f32 v74, v74\n"
    "v_exp_f32 v75, v75\n"
    "v_exp_f32 v76, v76\n"
    "v_exp_f32 v77, v77\n"
    "v_exp_f32 v78, v78\n"
    "v_exp_f32 v79, v79\n"
    "v_mul_f32 v32, v32, v40\n"
    "v_mul_f32 v33, v33, v41\n"
    "v_mul_f32 v34, v34, v42\n"
    "v_mul_f32 v35, v35, v43\n"
    "v_mul_f32 v36, v36, v44\n"
    "v_mul_f32 v37, v37, v45\n"
    "v_mul_f32 v38, v38, v46\n"
    "v_mul_f32 v39, v39, v47\n"
    "v_add_f32 v72, 1.0, v72\n"
    "v_add_f32 v73, 1.0, v73\n"
    "v_add_f32 v74, 1.0, v74\n"
    "v_add_f32 v75, 1.0, v75\n"
    "v_add_f32 v76, 1.0, v76\n"
    "v_add_f32 v77, 1.0, v77\n"
    "v_add_f32 v78, 1.0, v78\n"
    "v_add_f32 v79, 1.0, v79\n"
    "v_rcp_f32 v72, v72\n"
    "v_rcp_f32 v73, v73\n"
    "v_rcp_f32 v74, v74\n"
    "v_rcp_f32 v75, v75\n"
    "v_rcp_f32 v76, v76\n"
    "v_rcp_f32 v77, v77\n"
    "v_rcp_f32 v78, v78\n"
    "v_rcp_f32 v79, v79\n"
    "v_mul_f32 v72, v72, v32\n"
    "v_mul_f32 v73, v73, v33\n"
    "v_mul_f32 v74, v74, v34\n"
    "v_mul_f32 v75, v75, v35\n"
    "v_mul_f32 v76, v76, v36\n"
    "v_mul_f32 v77, v77, v37\n"
    "v_mul_f32 v78, v78, v38\n"
    "v_mul_f32 v79, v79, v39\n"
    "v_cvt_pk_bf16_f32 v64, v72, v73\n"
    "v_cvt_pk_bf16_f32 v65, v74, v75\n"
    "v_cvt_pk_bf16_f32 v66, v76, v77\n"
    "v_cvt_pk_bf16_f32 v67, v78, v79\n"
    "buffer_store_dwordx4 v[64:67], v2, s[8:11], s34 offen offset:1024 sc1\n"
    "v_mov_b32 v5, s24\n"
    "s_waitcnt vmcnt(0) lgkmcnt(0)\n"
    "buffer_store_dword v5, v4, s[8:11], 0 offen sc1\n"
    "s_cmp_lg_u32 s27, 192\n"
    "s_cbranch_scc1 .Leo_180\n"
    "s_mov_b32 s40, 4178944\n"
    ".Lep_179:\n"
    "buffer_load_dwordx4 v[8:11], v2, s[8:11], s40 offen sc1\n"
    "s_waitcnt vmcnt(0)\n"
    "v_xor_b32 v8, s4, v8\n"
    "v_xor_b32 v9, s4, v9\n"
    "v_xor_b32 v10, s4, v10\n"
    "v_xor_b32 v11, s4, v11\n"
    "v_or3_b32 v8, v8, v9, v10\n"
    "v_or_b32 v8, v8, v11\n"
    "v_cmp_ne_u32 vcc, 0, v8\n"
    "s_cbranch_vccz .Lep_179w\n"
    "s_sleep 8\n"
    "s_sub_u32 s25, s25, 1\n"
    "s_nop 1\n"
    "s_cbranch_scc0 .Lep_179\n"
    "s_branch .Lfail\n"
    ".Lep_179w:\n"
    "s_add_u32 s41, s4, 0x100\n"
    "v_mov_b32 v8, s41\n"
    "s_mov_b32 s40, 4177920\n"
    "v_mov_b32 v9, s40\n"
    "v_cmp_ne_u32 vcc, 0, v1\n"
    "v_mov_b32 v0, 0x7ff00000\n"
    "s_nop 0\n"
    "v_cndmask_b32 v9, v9, v0, vcc\n"
    "buffer_store_dword v8, v9, s[8:11], 0 offen sc1\n"
    ".Leo_180:\n"
    "s_barrier\n"
    "s_add_u32 s20, s20, 48\n"
    "s_branch .LZ_enter\n"
    ".Lrole_U:\n"
    "s_mov_b32 s12, s70\n"
    "s_and_b32 s13, s71, 0xffff\n"
    "s_mov_b32 s14, 7864320\n"
    "s_mov_b32 s15, 0x20000\n"
    "s_mov_b32 s16, s72\n"
    "s_and_b32 s17, s73, 0xffff\n"
    "s_mov_b32 s18, 7864320\n"
    "s_mov_b32 s19, 0x20000\n"
    "s_lshr_b32 s27, s20, 1\n"
    "s_and_b32 s24, s20, 1\n"
    "s_mul_i32 s48, s27, 384\n"
    "s_mul_i32 s49, s27, 245760\n"
    "s_mul_i32 s40, s21, 320\n"
    "s_add_u32 s49, s49, s40\n"
    "s_mul_i32 s26, s21, 3072\n"
    "s_add_u32 s26, s26, 0\n"
    "s_and_b32 s40, s21, 3\n"
    "s_mul_i32 s41, s40, 192\n"
    "v_add_u32 v10, s41, v1\n"
    "s_add_u32 s41, s41, 64\n"
    "v_mul_u32_u24 v0, 2731, v10\n"
    "v_lshrrev_b32 v0, 16, v0\n"
    "v_mul_u32_u24 v13, 24, v0\n"
    "v_sub_u32 v10, v10, v13\n"
    "v_mul_u32_u24 v0, 12288, v0\n"
    "v_lshl_add_u32 v10, v10, 4, v0\n"
    "v_add_u32 v11, s41, v1\n"
    "s_add_u32 s41, s41, 64\n"
    "v_mul_u32_u24 v0, 2731, v11\n"
    "v_lshrrev_b32 v0, 16, v0\n"
    "v_mul_u32_u24 v13, 24, v0\n"
    "v_sub_u32 v11, v11, v13\n"
    "v_mul_u32_u24 v0, 12288, v0\n"
    "v_lshl_add_u32 v11, v11, 4, v0\n"
    "v_add_u32 v12, s41, v1\n"
    "s_add_u32 s41, s41, 64\n"
    "v_mul_u32_u24 v0, 2731, v12\n"
    "v_lshrrev_b32 v0, 16, v0\n"
    "v_mul_u32_u24 v13, 24, v0\n"
    "v_sub_u32 v12, v12, v13\n"
    "v_mul_u32_u24 v0, 12288, v0\n"
    "v_lshl_add_u32 v12, v12, 4, v0\n"
    "s_mul_i32 s40, s40, 2976\n"
    "s_sub_u32 s40, s26, s40\n"
    "v_bfe_u32 v13, v1, 4, 2\n"
    "v_lshlrev_b32 v13, 3, v13\n"
    "v_bfe_u32 v0, v1, 2, 2\n"
    "v_lshl_add_u32 v13, v0, 0, v13\n"
    "v_mul_u32_u24 v13, 384, v13\n"
    "v_and_b32 v0, 3, v1\n"
    "v_mul_u32_u24 v0, 8, v0\n"
    "v_add3_u32 v13, v13, v0, s40\n"
    "v_add_u32 v14, 57344, v13\n"
    "v_add_u32 v15, 114688, v13\n"
    "v_lshlrev_b32 v2, 4, v1\n"
    "s_mul_i32 s29, s21, 10240\n"
    "s_mul_i32 s28, s24, 40960\n"
    "s_add_u32 s28, s28, s29\n"
    "s_add_u32 s28, s28, 6291456\n"
    "s_add_u32 s29, s29, 122880\n"
    "s_add_u32 s40, s48, 0\n"
    "s_add_u32 m0, s26, 0\n"
    "s_nop 0\n"
    "buffer_load_dwordx4 v10, s[12:15], s40 offen lds\n"
    "s_add_u32 m0, s26, 1024\n"
    "s_nop 0\n"
    "buffer_load_dwordx4 v11, s[12:15], s40 offen lds\n"
    "s_add_u32 m0, s26, 2048\n"
    "s_nop 0\n"
    "buffer_load_dwordx4 v12, s[12:15], s40 offen lds\n"
    "s_add_u32 s40, s48, 393216\n"
    "s_add_u32 m0, s26, 12288\n"
    "s_nop 0\n"
    "buffer_load_dwordx4 v10, s[12:15], s40 offen lds\n"
    "s_add_u32 m0, s26, 13312\n"
    "s_nop 0\n"
    "buffer_load_dwordx4 v11, s[12:15], s40 offen lds\n"
    "s_add_u32 m0, s26, 14336\n"
    "s_nop 0\n"
    "buffer_load_dwordx4 v12, s[12:15], s40 offen lds\n"
    "s_add_u32 s40, s48, 786432\n"
    "s_add_u32 m0, s26, 24576\n"
    "s_nop 0\n"
    "buffer_load_dwordx4 v10, s[12:15], s40 offen lds\n"
    "s_add_u32 m0, s26, 25600\n"
    "s_nop 0\n"
    "buffer_load_dwordx4 v11, s[12:15], s40 offen lds\n"
    "s_add_u32 m0, s26, 26624\n"
    "s_nop 0\n"
    "buffer_load_dwordx4 v12, s[12:15], s40 offen lds\n"
    "s_add_u32 s40, s48, 1179648\n"
    "s_add_u32 m0, s26, 36864\n"
    "s_nop 0\n"
    "buffer_load_dwordx4 v10, s[12:15], s40 offen lds\n"
    "s_add_u32 m0, s26, 37888\n"
    "s_nop 0\n"
    "buffer_load_dwordx4 v11, s[12:15], s40 offen lds\n"
    "s_add_u32 m0, s26, 38912\n"
    "s_nop 0\n"
    "buffer_load_dwordx4 v12, s[12:15], s40 offen lds\n"
    "s_add_u32 s40, s48, 1572864\n"
    "s_add_u32 m0, s26, 49152\n"
    "s_nop 0\n"
    "buffer_load_dwordx4 v10, s[12:15], s40 offen lds\n"
    "s_add_u32 m0, s26, 50176\n"
    "s_nop 0\n"
    "buffer_load_dwordx4 v11, s[12:15], s40 offen lds\n"
    "s_add_u32 m0, s26, 51200\n"
    "s_nop 0\n"
    "buffer_load_dwordx4 v12, s[12:15], s40 offen lds\n"
    "s_add_u32 s40, s48, 1966080\n"
    "s_add_u32 m0, s26, 61440\n"
    "s_nop 0\n"
    "buffer_load_dwordx4 v10, s[12:15], s40 offen lds\n"
    "s_add_u32 m0, s26, 62464\n"
    "s_nop 0\n"
    "buffer_load_dwordx4 v11, s[12:15], s40 offen lds\n"
    "s_add_u32 m0, s26, 63488\n"
    "s_nop 0\n"
    "buffer_load_dwordx4 v12, s[12:15], s40 offen lds\n"
    "s_add_u32 s40, s48, 2359296\n"
    "s_add_u32 m0, s26, 73728\n"
    "s_nop 0\n"
    "buffer_load_dwordx4 v10, s[12:15], s40 offen lds\n"
    "s_add_u32 m0, s26, 74752\n"
    "s_nop 0\n"
    "buffer_load_dwordx4 v11, s[12:15], s40 offen lds\n"
    "s_add_u32 m0, s26, 75776\n"
    "s_nop 0\n"
    "buffer_load_dwordx4 v12, s[12:15], s40 offen lds\n"
    "s_add_u32 s40, s48, 2752512\n"
    "s_add_u32 m0, s26, 86016\n"
    "s_nop 0\n"
    "buffer_load_dwordx4 v10, s[12:15], s40 offen lds\n"
    "s_add_u32 m0, s26, 87040\n"
    "s_nop 0\n"
    "buffer_load_dwordx4 v11, s[12:15], s40 offen lds\n"
    "s_add_u32 m0, s26, 88064\n"
    "s_nop 0\n"
    "buffer_load_dwordx4 v12, s[12:15], s40 offen lds\n"
    "s_add_u32 s40, s48, 3145728\n"
    "s_add_u32 m0, s26, 98304\n"
    "s_nop 0\n"
    "buffer_load_dwordx4 v10, s[12:15], s40 offen lds\n"
    "s_add_u32 m0, s26, 99328\n"
    "s_nop 0\n"
    "buffer_load_dwordx4 v11, s[12:15], s40 offen lds\n"
    "s_add_u32 m0, s26, 100352\n"
    "s_nop 0\n"
    "buffer_load_dwordx4 v12, s[12:15], s40 offen lds\n"
    "s_add_u32 s40, s48, 3538944\n"
    "s_add_u32 m0, s26, 110592\n"
    "s_nop 0\n"
    "buffer_load_dwordx4 v10, s[12:15], s40 offen lds\n"
    "s_add_u32 m0, s26, 111616\n"
    "s_nop 0\n"
    "buffer_load_dwordx4 v11, s[12:15], s40 offen lds\n"
    "s_add_u32 m0, s26, 112640\n"
    "s_nop 0\n"
    "buffer_load_dwordx4 v12, s[12:15], s40 offen lds\n"
    "s_mul_i32 s50, s21, 10240\n"
    "s_add_u32 s50, s50, 0\n"
    "s_and_b32 s40, s21, 0\n"
    "s_mul_i32 s41, s40, 640\n"
    "v_add_u32 v108, s41, v1\n"
    "s_add_u32 s41, s41, 64\n"
    "v_mul_u32_u24 v0, 3277, v108\n"
    "v_lshrrev_b32 v0, 16, v0\n"
    "v_mul_u32_u24 v98, 20, v0\n"
    "v_sub_u32 v108, v108, v98\n"
    "v_mul_u32_u24 v0, 1280, v0\n"
    "v_lshl_add_u32 v108, v108, 4, v0\n"
    "v_add_u32 v109, s41, v1\n"
    "s_add_u32 s41, s41, 64\n"
    "v_mul_u32_u24 v0, 3277, v109\n"
    "v_lshrrev_b32 v0, 16, v0\n"
    "v_mul_u32_u24 v98, 20, v0\n"
    "v_sub_u32 v109, v109, v98\n"
    "v_mul_u32_u24 v0, 1280, v0\n"
    "v_lshl_add_u32 v109, v109, 4, v0\n"
    "v_add_u32 v110, s41, v1\n"
    "s_add_u32 s41, s41, 64\n"
    "v_mul_u32_u24 v0, 3277, v110\n"
    "v_lshrrev_b32 v0, 16, v0\n"
    "v_mul_u32_u24 v98, 20, v0\n"
    "v_sub_u32 v110, v110, v98\n"
    "v_mul_u32_u24 v0, 1280, v0\n"
    "v_lshl_add_u32 v110, v110, 4, v0\n"
    "v_add_u32 v111, s41, v1\n"
    "s_add_u32 s41, s41, 64\n"
    "v_mul_u32_u24 v0, 3277, v111\n"
    "v_lshrrev_b32 v0, 16, v0\n"
    "v_mul_u32_u24 v98, 20, v0\n"
    "v_sub_u32 v111, v111, v98\n"
    "v_mul_u32_u24 v0, 1280, v0\n"
    "v_lshl_add_u32 v111, v111, 4, v0\n"
    "v_add_u32 v112, s41, v1\n"
    "s_add_u32 s41, s41, 64\n"
    "v_mul_u32_u24 v0, 3277, v112\n"
    "v_lshrrev_b32 v0, 16, v0\n"
    "v_mul_u32_u24 v98, 20, v0\n"
    "v_sub_u32 v112, v112, v98\n"
    "v_mul_u32_u24 v0, 1280, v0\n"
    "v_lshl_add_u32 v112, v112, 4, v0\n"
    "v_add_u32 v113, s41, v1\n"
    "s_add_u32 s41, s41, 64\n"
    "v_mul_u32_u24 v0, 3277, v113\n"
    "v_lshrrev_b32 v0, 16, v0\n"
    "v_mul_u32_u24 v98, 20, v0\n"
    "v_sub_u32 v113, v113, v98\n"
    "v_mul_u32_u24 v0, 1280, v0\n"
    "v_lshl_add_u32 v113, v113, 4, v0\n"
    "v_add_u32 v114, s41, v1\n"
    "s_add_u32 s41, s41, 64\n"
    "v_mul_u32_u24 v0, 3277, v114\n"
    "v_lshrrev_b32 v0, 16, v0\n"
    "v_mul_u32_u24 v98, 20, v0\n"
    "v_sub_u32 v114, v114, v98\n"
    "v_mul_u32_u24 v0, 1280, v0\n"
    "v_lshl_add_u32 v114, v114, 4, v0\n"
    "v_add_u32 v115, s41, v1\n"
    "s_add_u32 s41, s41, 64\n"
    "v_mul_u32_u24 v0, 3277, v115\n"
    "v_lshrrev_b32 v0, 16, v0\n"
    "v_mul_u32_u24 v98, 20, v0\n"
    "v_sub_u32 v115, v115, v98\n"
    "v_mul_u32_u24 v0, 1280, v0\n"
    "v_lshl_add_u32 v115, v115, 4, v0\n"
    "v_add_u32 v116, s41, v1\n"
    "s_add_u32 s41, s41, 64\n"
    "v_mul_u32_u24 v0, 3277, v116\n"
    "v_lshrrev_b32 v0, 16, v0\n"
    "v_mul_u32_u24 v98, 20, v0\n"
    "v_sub_u32 v116, v116, v98\n"
    "v_mul_u32_u24 v0, 1280, v0\n"
    "v_lshl_add_u32 v116, v116, 4, v0\n"
    "v_add_u32 v117, s41, v1\n"
    "s_add_u32 s41, s41, 64\n"
    "v_mul_u32_u24 v0, 3277, v117\n"
    "v_lshrrev_b32 v0, 16, v0\n"
    "v_mul_u32_u24 v98, 20, v0\n"
    "v_sub_u32 v117, v117, v98\n"
    "v_mul_u32_u24 v0, 1280, v0\n"
    "v_lshl_add_u32 v117, v117, 4, v0\n"
    "s_mul_i32 s40, s40, 9920\n"
    "s_sub_u32 s40, s50, s40\n"
    "v_bfe_u32 v98, v1, 4, 2\n"
    "v_lshlrev_b32 v98, 3, v98\n"
    "v_bfe_u32 v0, v1, 2, 2\n"
    "v_lshl_add_u32 v98, v0, 0, v98\n"
    "v_mul_u32_u24 v98, 320, v98\n"
    "v_and_b32 v0, 3, v1\n"
    "v_mul_u32_u24 v0, 8, v0\n"
    "v_add3_u32 v98, v98, v0, s40\n"
    "v_add_u32 v99, 57344, v98\n"
    "v_add_u32 v100, 114688, v98\n"
    "s_lshl_b32 s40, s21, 10\n"
    "v_add_u32 v4, s40, v2\n"
    "v_add_u32 v126, 49152, v4\n"
    "v_bfe_u32 v3, v1, 3, 1\n"
    "v_lshlrev_b32 v3, 5, v3\n"
    "v_and_b32 v0, 48, v1\n"
    "v_xor_b32 v3, v3, v0\n"
    "v_bfe_u32 v0, v1, 0, 4\n"
    "v_lshl_add_u32 v3, v0, 6, v3\n"
    "v_add_u32 v3, 122880, v3\n"
    "v_add_u32 v6, 98304, v2\n"
    "v_bfe_u32 v5, v1, 5, 1\n"
    "v_lshlrev_b32 v5, 8, v5\n"
    "v_bfe_u32 v0, v1, 0, 4\n"
    "v_lshl_add_u32 v5, v0, 4, v5\n"
    "v_bfe_u32 v0, v1, 4, 1\n"
    "v_lshl_add_u32 v5, v0, 3, v5\n"
    "s_mul_i32 s40, s21, 1536\n"
    "s_add_u32 s40, s40, 98304\n"
    "v_add_u32 v5, s40, v5\n"
    "s_lshl_b32 s40, s24, 2\n"
    "s_add_u32 s40, s40, s21\n"
    "s_lshl_b32 s40, s40, 5\n"
    "s_add_u32 s40, s40, s27\n"
    "s_lshl_b32 s40, s40, 7\n"
    "s_add_u32 s40, s40, 4227072\n"
    "v_mov_b32 v7, s40\n"
    "v_cmp_ne_u32 vcc, 0, v1\n"
    "v_mov_b32 v0, 0x7ff00000\n"
    "s_nop 0\n"
    "v_cndmask_b32 v7, v7, v0, vcc\n"
    "v_mov_b32 v8, 0\n"
    "s_mul_i32 s30, s24, 1310720\n"
    "s_mul_i32 s40, s21, 327680\n"
    "s_add_u32 s30, s30, s40\n"
    "s_lshl_b32 s40, s27, 10\n"
    "s_add_u32 s30, s30, s40\n"
    "s_add_u32 s30, s30, 59768832\n"
    "v_bfe_u32 v9, v1, 4, 2\n"
    "v_lshlrev_b32 v9, 14, v9\n"
    "v_bfe_u32 v0, v1, 0, 4\n"
    "v_lshl_add_u32 v9, v0, 4, v9\n"
    "s_mul_i32 s40, s21, 163840\n"
    "s_lshl_b32 s41, s27, 8\n"
    "s_add_u32 s40, s40, s41\n"
    "v_add_u32 v9, s40, v9\n"
    "v_mov_b64 v[122:123], -1\n"
    "v_mov_b64 v[124:125], -1\n"
    "s_waitcnt vmcnt(27)\n"
    "s_barrier\n"
    "ds_read_b64_tr_b16 a[0:1], v13 offset:0\n"
    "ds_read_b64_tr_b16 a[2:3], v13 offset:1536\n"
    "ds_read_b64_tr_b16 a[4:5], v13 offset:32\n"
    "ds_read_b64_tr_b16 a[6:7], v13 offset:1568\n"
    "ds_read_b64_tr_b16 a[8:9], v13 offset:64\n"
    "ds_read_b64_tr_b16 a[10:11], v13 offset:1600\n"
    "s_waitcnt lgkmcnt(0)\n"
    "s_waitcnt vmcnt(24)\n"
    "s_barrier\n"
    "ds_read_b64_tr_b16 a[12:13], v13 offset:12288\n"
    "ds_read_b64_tr_b16 a[14:15], v13 offset:13824\n"
    "ds_read_b64_tr_b16 a[16:17], v13 offset:12320\n"
    "ds_read_b64_tr_b16 a[18:19], v13 offset:13856\n"
    "ds_read_b64_tr_b16 a[20:21], v13 offset:12352\n"
    "ds_read_b64_tr_b16 a[22:23], v13 offset:13888\n"
    "s_add_u32 s40, s48, 3932160\n"
    "s_add_u32 m0, s26, 0\n"
    "s_nop 0\n"
    "buffer_load_dwordx4 v10, s[12:15], s40 offen lds\n"
    "s_add_u32 m0, s26, 1024\n"
    "s_nop 0\n"
    "buffer_load_dwordx4 v11, s[12:15], s40 offen lds\n"
    "s_add_u32 m0, s26, 2048\n"
    "s_nop 0\n"
    "buffer_load_dwordx4 v12, s[12:15], s40 offen lds\n"
    "s_waitcnt lgkmcnt(0)\n"
    "s_waitcnt vmcnt(24)\n"
    "s_barrier\n"
    "ds_read_b64_tr_b16 a[24:25], v13 offset:24576\n"
    "ds_read_b64_tr_b16 a[26:27], v13 offset:26112\n"
    "ds_read_b64_tr_b16 a[28:29], v13 offset:24608\n"
    "ds_read_b64_tr_b16 a[30:31], v13 offset:26144\n"
    "ds_read_b64_tr_b16 a[32:33], v13 offset:24640\n"
    "ds_read_b64_tr_b16 a[34:35], v13 offset:26176\n"
    "s_add_u32 s40, s48, 4325376\n"
    "s_add_u32 m0, s26, 12288\n"
    "s_nop 0\n"
    "buffer_load_dwordx4 v10, s[12:15], s40 offen lds\n"
    "s_add_u32 m0, s26, 13312\n"
    "s_nop 0\n"
    "buffer_load_dwordx4 v11, s[12:15], s40 offen lds\n"
    "s_add_u32 m0, s26, 14336\n"
    "s_nop 0\n"
    "buffer_load_dwordx4 v12, s[12:15], s40 offen lds\n"
    "s_waitcnt lgkmcnt(0)\n"
    "s_waitcnt vmcnt(24)\n"
    "s_barrier\n"
    "ds_read_b64_tr_b16 a[36:37], v13 offset:36864\n"
    "ds_read_b64_tr_b16 a[38:39], v13 offset:38400\n"
    "ds_read_b64_tr_b16 a[40:41], v13 offset:36896\n"
    "ds_read_b64_tr_b16 a[42:43], v13 offset:38432\n"
    "ds_read_b64_tr_b16 a[44:45], v13 offset:36928\n"
    "ds_read_b64_tr_b16 a[46:47], v13 offset:38464\n"
    "s_add_u32 s40, s48, 4718592\n"
    "s_add_u32 m0, s26, 24576\n"
    "s_nop 0\n"
    "buffer_load_dwordx4 v10, s[12:15], s40 offen lds\n"
    "s_add_u32 m0, s26, 25600\n"
    "s_nop 0\n"
    "buffer_load_dwordx4 v11, s[12:15], s40 offen lds\n"
    "s_add_u32 m0, s26, 26624\n"
    "s_nop 0\n"
    "buffer_load_dwordx4 v12, s[12:15], s40 offen lds\n"
    "s_waitcnt lgkmcnt(0)\n"
    "s_waitcnt vmcnt(24)\n"
    "s_barrier\n"
    "ds_read_b64_tr_b16 a[48:49], v13 offset:49152\n"
    "ds_read_b64_tr_b16 a[50:51], v13 offset:50688\n"
    "ds_read_b64_tr_b16 a[52:53], v13 offset:49184\n"
    "ds_read_b64_tr_b16 a[54:55], v13 offset:50720\n"
    "ds_read_b64_tr_b16 a[56:57], v13 offset:49216\n"
    "ds_read_b64_tr_b16 a[58:59], v13 offset:50752\n"
    "s_add_u32 s40, s48, 5111808\n"
    "s_add_u32 m0, s26, 36864\n"
    "s_nop 0\n"
    "buffer_load_dwordx4 v10, s[12:15], s40 offen lds\n"
    "s_add_u32 m0, s26, 37888\n"
    "s_nop 0\n"
    "buffer_load_dwordx4 v11, s[12:15], s40 offen lds\n"
    "s_add_u32 m0, s26, 38912\n"
    "s_nop 0\n"
    "buffer_load_dwordx4 v12, s[12:15], s40 offen lds\n"
    "s_waitcnt lgkmcnt(0)\n"
    "s_waitcnt vmcnt(24)\n"
    "s_barrier\n"
    "ds_read_b64_tr_b16 a[60:61], v14 offset:4096\n"
    "ds_read_b64_tr_b16 a[62:63], v14 offset:5632\n"
    "ds_read_b64_tr_b16 a[64:65], v14 offset:4128\n"
    "ds_read_b64_tr_b16 a[66:67], v14 offset:5664\n"
    "ds_read_b64_tr_b16 a[68:69], v14 offset:4160\n"
    "ds_read_b64_tr_b16 a[70:71], v14 offset:5696\n"
    "s_add_u32 s40, s48, 5505024\n"
    "s_add_u32 m0, s26, 49152\n"
    "s_nop 0\n"
    "buffer_load_dwordx4 v10, s[12:15], s40 offen lds\n"
    "s_add_u32 m0, s26, 50176\n"
    "s_nop 0\n"
    "buffer_load_dwordx4 v11, s[12:15], s40 offen lds\n"
    "s_add_u32 m0, s26, 51200\n"
    "s_nop 0\n"
    "buffer_load_dwordx4 v12, s[12:15], s40 offen lds\n"
    "s_waitcnt lgkmcnt(0)\n"
    "s_waitcnt vmcnt(24)\n"
    "s_barrier\n"
    "ds_read_b64_tr_b16 a[72:73], v14 offset:16384\n"
    "ds_read_b64_tr_b16 a[74:75], v14 offset:17920\n"
    "ds_read_b64_tr_b16 a[76:77], v14 offset:16416\n"
    "ds_read_b64_tr_b16 a[78:79], v14 offset:17952\n"
    "ds_read_b64_tr_b16 a[80:81], v14 offset:16448\n"
    "ds_read_b64_tr_b16 a[82:83], v14 offset:17984\n"
    "s_add_u32 s40, s48, 5898240\n"
    "s_add_u32 m0, s26, 61440\n"
    "s_nop 0\n"
    "buffer_load_dwordx4 v10, s[12:15], s40 offen lds\n"
    "s_add_u32 m0, s26, 62464\n"
    "s_nop 0\n"
    "buffer_load_dwordx4 v11, s[12:15], s40 offen lds\n"
    "s_add_u32 m0, s26, 63488\n"
    "s_nop 0\n"
    "buffer_load_dwordx4 v12, s[12:15], s40 offen lds\n"
    "s_waitcnt lgkmcnt(0)\n"
    "s_waitcnt vmcnt(24)\n"
    "s_barrier\n"
    "ds_read_b64_tr_b16 a[84:85], v14 offset:28672\n"
    "ds_read_b64_tr_b16 a[86:87], v14 offset:30208\n"
    "ds_read_b64_tr_b16 a[88:89], v14 offset:28704\n"
    "ds_read_b64_tr_b16 a[90:91], v14 offset:30240\n"
    "ds_read_b64_tr_b16 a[92:93], v14 offset:28736\n"
    "ds_read_b64_tr_b16 a[94:95], v14 offset:30272\n"
    "s_add_u32 s40, s48, 6291456\n"
    "s_add_u32 m0, s26, 73728\n"
    "s_nop 0\n"
    "buffer_load_dwordx4 v10, s[12:15], s40 offen lds\n"
    "s_add_u32 m0, s26, 74752\n"
    "s_nop 0\n"
    "buffer_load_dwordx4 v11, s[12:15], s40 offen lds\n"
    "s_add_u32 m0, s26, 75776\n"
    "s_nop 0\n"
    "buffer_load_dwordx4 v12, s[12:15], s40 offen lds\n"
    "s_waitcnt lgkmcnt(0)\n"
    "s_waitcnt vmcnt(24)\n"
    "s_barrier\n"
    "ds_read_b64_tr_b16 a[96:97], v14 offset:40960\n"
    "ds_read_b64_tr_b16 a[98:99], v14 offset:42496\n"
    "ds_read_b64_tr_b16 a[100:101], v14 offset:40992\n"
    "ds_read_b64_tr_b16 a[102:103], v14 offset:42528\n"
    "ds_read_b64_tr_b16 a[104:105], v14 offset:41024\n"
    "ds_read_b64_tr_b16 a[106:107], v14 offset:42560\n"
    "s_add_u32 s40, s48, 6684672\n"
    "s_add_u32 m0, s26, 86016\n"
    "s_nop 0\n"
    "buffer_load_dwordx4 v10, s[12:15], s40 offen lds\n"
    "s_add_u32 m0, s26, 87040\n"
    "s_nop 0\n"
    "buffer_load_dwordx4 v11, s[12:15], s40 offen lds\n"
    "s_add_u32 m0, s26, 88064\n"
    "s_nop 0\n"
    "buffer_load_dwordx4 v12, s[12:15], s40 offen lds\n"
    "s_waitcnt lgkmcnt(0)\n"
    "s_waitcnt vmcnt(24)\n"
    "s_barrier\n"
    "ds_read_b64_tr_b16 a[108:109], v14 offset:53248\n"
    "ds_read_b64_tr_b16 a[110:111], v14 offset:54784\n"
    "ds_read_b64_tr_b16 a[112:113], v14 offset:53280\n"
    "ds_read_b64_tr_b16 a[114:115], v14 offset:54816\n"
    "ds_read_b64_tr_b16 a[116:117], v14 offset:53312\n"
    "ds_read_b64_tr_b16 a[118:119], v14 offset:54848\n"
    "s_add_u32 s40, s48, 7077888\n"
    "s_add_u32 m0, s26, 98304\n"
    "s_nop 0\n"
    "buffer_load_dwordx4 v10, s[12:15], s40 offen lds\n"
    "s_add_u32 m0, s26, 99328\n"
    "s_nop 0\n"
    "buffer_load_dwordx4 v11, s[12:15], s40 offen lds\n"
    "s_add_u32 m0, s26, 100352\n"
    "s_nop 0\n"
    "buffer_load_dwordx4 v12, s[12:15], s40 offen lds\n"
    "s_waitcnt lgkmcnt(0)\n"
    "s_waitcnt vmcnt(24)\n"
    "s_barrier\n"
    "ds_read_b64_tr_b16 a[120:121], v13 offset:0\n"
    "ds_read_b64_tr_b16 a[122:123], v13 offset:1536\n"
    "ds_read_b64_tr_b16 a[124:125], v13 offset:32\n"
    "ds_read_b64_tr_b16 a[126:127], v13 offset:1568\n"
    "ds_read_b64_tr_b16 a[128:129], v13 offset:64\n"
    "ds_read_b64_tr_b16 a[130:131], v13 offset:1600\n"
    "s_add_u32 s40, s48, 7471104\n"
    "s_add_u32 m0, s26, 110592\n"
    "s_nop 0\n"
    "buffer_load_dwordx4 v10, s[12:15], s40 offen lds\n"
    "s_add_u32 m0, s26, 111616\n"
    "s_nop 0\n"
    "buffer_load_dwordx4 v11, s[12:15], s40 offen lds\n"
    "s_add_u32 m0, s26, 112640\n"
    "s_nop 0\n"
    "buffer_load_dwordx4 v12, s[12:15], s40 offen lds\n"
    "s_waitcnt lgkmcnt(0)\n"
    "s_waitcnt vmcnt(24)\n"
    "s_barrier\n"
    "ds_read_b64_tr_b16 a[132:133], v13 offset:12288\n"
    "ds_read_b64_tr_b16 a[134:135], v13 offset:13824\n"
    "ds_read_b64_tr_b16 a[136:137], v13 offset:12320\n"
    "ds_read_b64_tr_b16 a[138:139], v13 offset:13856\n"
    "ds_read_b64_tr_b16 a[140:141], v13 offset:12352\n"
    "ds_read_b64_tr_b16 a[142:143], v13 offset:13888\n"
    "s_waitcnt lgkmcnt(0)\n"
    "s_waitcnt vmcnt(21)\n"
    "s_barrier\n"
    "ds_read_b64_tr_b16 a[144:145], v13 offset:24576\n"
    "ds_read_b64_tr_b16 a[146:147], v13 offset:26112\n"
    "ds_read_b64_tr_b16 a[148:149], v13 offset:24608\n"
    "ds_read_b64_tr_b16 a[150:151], v13 offset:26144\n"
    "ds_read_b64_tr_b16 a[152:153], v13 offset:24640\n"
    "ds_read_b64_tr_b16 a[154:155], v13 offset:26176\n"
    "s_waitcnt lgkmcnt(0)\n"
    "s_waitcnt vmcnt(18)\n"
    "s_barrier\n"
    "ds_read_b64_tr_b16 a[156:157], v13 offset:36864\n"
    "ds_read_b64_tr_b16 a[158:159], v13 offset:38400\n"
    "ds_read_b64_tr_b16 a[160:161], v13 offset:36896\n"
    "ds_read_b64_tr_b16 a[162:163], v13 offset:38432\n"
    "ds_read_b64_tr_b16 a[164:165], v13 offset:36928\n"
    "ds_read_b64_tr_b16 a[166:167], v13 offset:38464\n"
    "s_waitcnt lgkmcnt(0)\n"
    "s_waitcnt vmcnt(15)\n"
    "s_barrier\n"
    "ds_read_b64_tr_b16 a[168:169], v13 offset:49152\n"
    "ds_read_b64_tr_b16 a[170:171], v13 offset:50688\n"
    "ds_read_b64_tr_b16 a[172:173], v13 offset:49184\n"
    "ds_read_b64_tr_b16 a[174:175], v13 offset:50720\n"
    "ds_read_b64_tr_b16 a[176:177], v13 offset:49216\n"
    "ds_read_b64_tr_b16 a[178:179], v13 offset:50752\n"
    "s_add_u32 s40, s49, 0\n"
    "s_add_u32 m0, s50, 0\n"
    "s_nop 0\n"
    "buffer_load_dwordx4 v108, s[16:19], s40 offen lds\n"
    "s_add_u32 m0, s50, 1024\n"
    "s_nop 0\n"
    "buffer_load_dwordx4 v109, s[16:19], s40 offen lds\n"
    "s_add_u32 m0, s50, 2048\n"
    "s_nop 0\n"
    "buffer_load_dwordx4 v110, s[16:19], s40 offen lds\n"
    "s_add_u32 m0, s50, 3072\n"
    "s_nop 0\n"
    "buffer_load_dwordx4 v111, s[16:19], s40 offen lds\n"
    "s_add_u32 m0, s50, 4096\n"
    "s_nop 0\n"
    "buffer_load_dwordx4 v112, s[16:19], s40 offen lds\n"
    "s_add_u32 m0, s50, 5120\n"
    "s_nop 0\n"
    "buffer_load_dwordx4 v113, s[16:19], s40 offen lds\n"
    "s_add_u32 m0, s50, 6144\n"
    "s_nop 0\n"
    "buffer_load_dwordx4 v114, s[16:19], s40 offen lds\n"
    "s_add_u32 m0, s50, 7168\n"
    "s_nop 0\n"
    "buffer_load_dwordx4 v115, s[16:19], s40 offen lds\n"
    "s_add_u32 m0, s50, 8192\n"
    "s_nop 0\n"
    "buffer_load_dwordx4 v116, s[16:19], s40 offen lds\n"
    "s_add_u32 m0, s50, 9216\n"
    "s_nop 0\n"
    "buffer_load_dwordx4 v117, s[16:19], s40 offen lds\n"
    "s_waitcnt lgkmcnt(0)\n"
    "s_waitcnt vmcnt(22)\n"
    "s_barrier\n"
    "ds_read_b64_tr_b16 a[180:181], v14 offset:4096\n"
    "ds_read_b64_tr_b16 a[182:183], v14 offset:5632\n"
    "ds_read_b64_tr_b16 a[184:185], v14 offset:4128\n"
    "ds_read_b64_tr_b16 a[186:187], v14 offset:5664\n"
    "ds_read_b64_tr_b16 a[188:189], v14 offset:4160\n"
    "ds_read_b64_tr_b16 a[190:191], v14 offset:5696\n"
    "s_waitcnt lgkmcnt(0)\n"
    "s_waitcnt vmcnt(19)\n"
    "s_barrier\n"
    "ds_read_b64_tr_b16 a[192:193], v14 offset:16384\n"
    "ds_read_b64_tr_b16 a[194:195], v14 offset:17920\n"
    "ds_read_b64_tr_b16 a[196:197], v14 offset:16416\n"
    "ds_read_b64_tr_b16 a[198:199], v14 offset:17952\n"
    "ds_read_b64_tr_b16 a[200:201], v14 offset:16448\n"
    "ds_read_b64_tr_b16 a[202:203], v14 offset:17984\n"
    "s_waitcnt lgkmcnt(0)\n"
    "s_waitcnt vmcnt(16)\n"
    "s_barrier\n"
    "ds_read_b64_tr_b16 a[204:205], v14 offset:28672\n"
    "ds_read_b64_tr_b16 a[206:207], v14 offset:30208\n"
    "ds_read_b64_tr_b16 a[208:209], v14 offset:28704\n"
    "ds_read_b64_tr_b16 a[210:211], v14 offset:30240\n"
    "ds_read_b64_tr_b16 a[212:213], v14 offset:28736\n"
    "ds_read_b64_tr_b16 a[214:215], v14 offset:30272\n"
    "s_add_u32 s40, s49, 40960\n"
    "s_add_u32 m0, s50, 40960\n"
    "s_nop 0\n"
    "buffer_load_dwordx4 v108, s[16:19], s40 offen lds\n"
    "s_add_u32 m0, s50, 41984\n"
    "s_nop 0\n"
    "buffer_load_dwordx4 v109, s[16:19], s40 offen lds\n"
    "s_add_u32 m0, s50, 43008\n"
    "s_nop 0\n"
    "buffer_load_dwordx4 v110, s[16:19], s40 offen lds\n"
    "s_add_u32 m0, s50, 44032\n"
    "s_nop 0\n"
    "buffer_load_dwordx4 v111, s[16:19], s40 offen lds\n"
    "s_add_u32 m0, s50, 45056\n"
    "s_nop 0\n"
    "buffer_load_dwordx4 v112, s[16:19], s40 offen lds\n"
    "s_add_u32 m0, s50, 46080\n"
    "s_nop 0\n"
    "buffer_load_dwordx4 v113, s[16:19], s40 offen lds\n"
    "s_add_u32 m0, s50, 47104\n"
    "s_nop 0\n"
    "buffer_load_dwordx4 v114, s[16:19], s40 offen lds\n"
    "s_add_u32 m0, s50, 48128\n"
    "s_nop 0\n"
    "buffer_load_dwordx4 v115, s[16:19], s40 offen lds\n"
    "s_add_u32 m0, s50, 49152\n"
    "s_nop 0\n"
    "buffer_load_dwordx4 v116, s[16:19], s40 offen lds\n"
    "s_add_u32 m0, s50, 50176\n"
    "s_nop 0\n"
    "buffer_load_dwordx4 v117, s[16:19], s40 offen lds\n"
    "s_waitcnt lgkmcnt(0)\n"
    "s_add_u32 s4, s3, 1\n"
    "v_mov_b32 v102, s3\n"
    "s_or_b32 s41, s21, s6\n"
    "s_cbranch_scc1 .Lcl_181\n"
    "s_lshl_b32 s41, s5, 2\n"
    "s_add_u32 s41, s41, 4178944\n"
    "v_mov_b32 v103, s41\n"
    "v_mov_b32 v102, s4\n"
    "s_mov_b64 exec, 1\n"
    "buffer_atomic_swap v102, v103, s[8:11], 0 offen sc0 sc1\n"
    "s_mov_b64 exec, -1\n"
    ".Lcl_181:\n"
    "v_min_u32 v106, 54, v1\n"
    "v_lshlrev_b32 v106, 7, v106\n"
    "v_add_u32 v106, 4182016, v106\n"
    "buffer_load_dwordx2 v[104:105], v106, s[8:11], 0 offen sc1\n"
    "s_waitcnt lgkmcnt(0)\n"
    "s_waitcnt vmcnt(24)\n"
    "s_barrier\n"
    "ds_read_b64_tr_b16 a[216:217], v14 offset:40960\n"
    "ds_read_b64_tr_b16 a[218:219], v14 offset:42496\n"
    "ds_read_b64_tr_b16 a[220:221], v14 offset:40992\n"
    "ds_read_b64_tr_b16 a[222:223], v14 offset:42528\n"
    "ds_read_b64_tr_b16 a[224:225], v14 offset:41024\n"
    "ds_read_b64_tr_b16 a[226:227], v14 offset:42560\n"
    "s_waitcnt lgkmcnt(0)\n"
    "s_waitcnt vmcnt(21)\n"
    "s_barrier\n"
    "ds_read_b64_tr_b16 a[228:229], v14 offset:53248\n"
    "ds_read_b64_tr_b16 a[230:231], v14 offset:54784\n"
    "ds_read_b64_tr_b16 a[232:233], v14 offset:53280\n"
    "ds_read_b64_tr_b16 a[234:235], v14 offset:54816\n"
    "ds_read_b64_tr_b16 a[236:237], v14 offset:53312\n"
    "ds_read_b64_tr_b16 a[238:239], v14 offset:54848\n"
    "s_waitcnt lgkmcnt(0)\n"
    "s_waitcnt vmcnt(11)\n"
    "s_barrier\n"
    "ds_read_b64_tr_b16 a[240:241], v98 offset:0\n"
    "ds_read_b64_tr_b16 a[242:243], v98 offset:1280\n"
    "ds_read_b64_tr_b16 v[18:19], v98 offset:32\n"
    "ds_read_b64_tr_b16 v[20:21], v98 offset:1312\n"
    "ds_read_b64_tr_b16 v[136:137], v98 offset:64\n"
    "ds_read_b64_tr_b16 v[138:139], v98 offset:1344\n"
    "ds_read_b64_tr_b16 v[22:23], v98 offset:96\n"
    "ds_read_b64_tr_b16 v[24:25], v98 offset:1376\n"
    "ds_read_b64_tr_b16 v[160:161], v98 offset:128\n"
    "ds_read_b64_tr_b16 v[162:163], v98 offset:1408\n"
    "ds_read_b64_tr_b16 v[26:27], v98 offset:160\n"
    "ds_read_b64_tr_b16 v[28:29], v98 offset:1440\n"
    "ds_read_b64_tr_b16 v[184:185], v98 offset:192\n"
    "ds_read_b64_tr_b16 v[186:187], v98 offset:1472\n"
    "ds_read_b64_tr_b16 v[30:31], v98 offset:224\n"
    "ds_read_b64_tr_b16 v[32:33], v98 offset:1504\n"
    "ds_read_b64_tr_b16 v[208:209], v98 offset:256\n"
    "ds_read_b64_tr_b16 v[210:211], v98 offset:1536\n"
    "ds_read_b64_tr_b16 v[232:233], v98 offset:288\n"
    "ds_read_b64_tr_b16 v[234:235], v98 offset:1568\n"
    "s_add_u32 s40, s49, 81920\n"
    "s_add_u32 m0, s50, 81920\n"
    "s_nop 0\n"
    "buffer_load_dwordx4 v108, s[16:19], s40 offen lds\n"
    "s_add_u32 m0, s50, 82944\n"
    "s_nop 0\n"
    "buffer_load_dwordx4 v109, s[16:19], s40 offen lds\n"
    "s_add_u32 m0, s50, 83968\n"
    "s_nop 0\n"
    "buffer_load_dwordx4 v110, s[16:19], s40 offen lds\n"
    "s_add_u32 m0, s50, 84992\n"
    "s_nop 0\n"
    "buffer_load_dwordx4 v111, s[16:19], s40 offen lds\n"
    "s_add_u32 m0, s50, 86016\n"
    "s_nop 0\n"
    "buffer_load_dwordx4 v112, s[16:19], s40 offen lds\n"
    "s_add_u32 m0, s50, 87040\n"
    "s_nop 0\n"
    "buffer_load_dwordx4 v113, s[16:19], s40 offen lds\n"
    "s_add_u32 m0, s50, 88064\n"
    "s_nop 0\n"
    "buffer_load_dwordx4 v114, s[16:19], s40 offen lds\n"
    "s_add_u32 m0, s50, 89088\n"
    "s_nop 0\n"
    "buffer_load_dwordx4 v115, s[16:19], s40 offen lds\n"
    "s_add_u32 m0, s50, 90112\n"
    "s_nop 0\n"
    "buffer_load_dwordx4 v116, s[16:19], s40 offen lds\n"
    "s_add_u32 m0, s50, 91136\n"
    "s_nop 0\n"
    "buffer_load_dwordx4 v117, s[16:19], s40 offen lds\n"
    "s_waitcnt lgkmcnt(0)\n"
    "s_waitcnt vmcnt(11)\n"
    "s_barrier\n"
    "ds_read_b64_tr_b16 a[244:245], v98 offset:40960\n"
    "ds_read_b64_tr_b16 a[246:247], v98 offset:42240\n"
    "ds_read_b64_tr_b16 v[34:35], v98 offset:40992\n"
    "ds_read_b64_tr_b16 v[36:37], v98 offset:42272\n"
    "ds_read_b64_tr_b16 v[140:141], v98 offset:41024\n"
    "ds_read_b64_tr_b16 v[142:143], v98 offset:42304\n"
    "ds_read_b64_tr_b16 v[38:39], v98 offset:41056\n"
    "ds_read_b64_tr_b16 v[40:41], v98 offset:42336\n"
    "ds_read_b64_tr_b16 v[164:165], v98 offset:41088\n"
    "ds_read_b64_tr_b16 v[166:167], v98 offset:42368\n"
    "ds_read_b64_tr_b16 v[42:43], v98 offset:41120\n"
    "ds_read_b64_tr_b16 v[44:45], v98 offset:42400\n"
    "ds_read_b64_tr_b16 v[188:189], v98 offset:41152\n"
    "ds_read_b64_tr_b16 v[190:191], v98 offset:42432\n"
    "ds_read_b64_tr_b16 v[46:47], v98 offset:41184\n"
    "ds_read_b64_tr_b16 v[48:49], v98 offset:42464\n"
    "ds_read_b64_tr_b16 v[212:213], v98 offset:41216\n"
    "ds_read_b64_tr_b16 v[214:215], v98 offset:42496\n"
    "ds_read_b64_tr_b16 v[236:237], v98 offset:41248\n"
    "ds_read_b64_tr_b16 v[238:239], v98 offset:42528\n"
    "s_add_u32 s40, s49, 122880\n"
    "s_add_u32 m0, s50, 0\n"
    "s_nop 0\n"
    "buffer_load_dwordx4 v108, s[16:19], s40 offen lds\n"
    "s_add_u32 m0, s50, 1024\n"
    "s_nop 0\n"
    "buffer_load_dwordx4 v109, s[16:19], s40 offen lds\n"
    "s_add_u32 m0, s50, 2048\n"
    "s_nop 0\n"
    "buffer_load_dwordx4 v110, s[16:19], s40 offen lds\n"
    "s_add_u32 m0, s50, 3072\n"
    "s_nop 0\n"
    "buffer_load_dwordx4 v111, s[16:19], s40 offen lds\n"
    "s_add_u32 m0, s50, 4096\n"
    "s_nop 0\n"
    "buffer_load_dwordx4 v112, s[16:19], s40 offen lds\n"
    "s_add_u32 m0, s50, 5120\n"
    "s_nop 0\n"
    "buffer_load_dwordx4 v113, s[16:19], s40 offen lds\n"
    "s_add_u32 m0, s50, 6144\n"
    "s_nop 0\n"
    "buffer_load_dwordx4 v114, s[16:19], s40 offen lds\n"
    "s_add_u32 m0, s50, 7168\n"
    "s_nop 0\n"
    "buffer_load_dwordx4 v115, s[16:19], s40 offen lds\n"
    "s_add_u32 m0, s50, 8192\n"
    "s_nop 0\n"
    "buffer_load_dwordx4 v116, s[16:19], s40 offen lds\n"
    "s_add_u32 m0, s50, 9216\n"
    "s_nop 0\n"
    "buffer_load_dwordx4 v117, s[16:19], s40 offen lds\n"
    "s_waitcnt lgkmcnt(0)\n"
    "s_waitcnt vmcnt(20)\n"
    "v_cmp_ne_u32 vcc, s4, v104\n"
    "s_nop 1\n"
    "s_cmp_eq_u32 vcc_lo, 0\n"
    "s_cbranch_scc0 .Lrn_182\n"
    "s_add_u32 m0, s29, 0\n"
    "s_add_u32 s40, s28, 0\n"
    "buffer_load_dwordx4 v2, s[8:11], s40 offen offset:0 sc1 lds\n"
    "buffer_load_dwordx4 v2, s[8:11], s40 offen offset:1024 sc1 lds\n"
    "buffer_load_dwordx4 v2, s[8:11], s40 offen offset:2048 sc1 lds\n"
    "buffer_load_dwordx4 v2, s[8:11], s40 offen offset:3072 sc1 lds\n"
    "s_add_u32 m0, s29, 4096\n"
    "s_add_u32 s40, s28, 4096\n"
    "buffer_load_dwordx4 v2, s[8:11], s40 offen offset:0 sc1 lds\n"
    "buffer_load_dwordx4 v2, s[8:11], s40 offen offset:1024 sc1 lds\n"
    "buffer_load_dwordx4 v2, s[8:11], s40 offen offset:2048 sc1 lds\n"
    "buffer_load_dwordx4 v2, s[8:11], s40 offen offset:3072 sc1 lds\n"
    "s_add_u32 m0, s29, 8192\n"
    "s_add_u32 s40, s28, 8192\n"
    "buffer_load_dwordx4 v2, s[8:11], s40 offen offset:0 sc1 lds\n"
    "buffer_load_dwordx4 v2, s[8:11], s40 offen offset:1024 sc1 lds\n"
    ".Lrn_182:\n"
    "s_waitcnt vmcnt(10)\n"
    "s_barrier\n"
    "ds_read_b64_tr_b16 a[248:249], v99 offset:24576\n"
    "ds_read_b64_tr_b16 a[250:251], v99 offset:25856\n"
    "ds_read_b64_tr_b16 v[50:51], v99 offset:24608\n"
    "ds_read_b64_tr_b16 v[52:53], v99 offset:25888\n"
    "ds_read_b64_tr_b16 v[144:145], v99 offset:24640\n"
    "ds_read_b64_tr_b16 v[146:147], v99 offset:25920\n"
    "ds_read_b64_tr_b16 v[54:55], v99 offset:24672\n"
    "ds_read_b64_tr_b16 v[56:57], v99 offset:25952\n"
    "ds_read_b64_tr_b16 v[168:169], v99 offset:24704\n"
    "ds_read_b64_tr_b16 v[170:171], v99 offset:25984\n"
    "ds_read_b64_tr_b16 v[58:59], v99 offset:24736\n"
    "ds_read_b64_tr_b16 v[60:61], v99 offset:26016\n"
    "ds_read_b64_tr_b16 v[192:193], v99 offset:24768\n"
    "ds_read_b64_tr_b16 v[194:195], v99 offset:26048\n"
    "ds_read_b64_tr_b16 v[62:63], v99 offset:24800\n"
    "ds_read_b64_tr_b16 v[64:65], v99 offset:26080\n"
    "ds_read_b64_tr_b16 v[216:217], v99 offset:24832\n"
    "ds_read_b64_tr_b16 v[218:219], v99 offset:26112\n"
    "ds_read_b64_tr_b16 v[240:241], v99 offset:24864\n"
    "ds_read_b64_tr_b16 v[242:243], v99 offset:26144\n"
    "s_add_u32 s40, s49, 163840\n"
    "s_add_u32 m0, s50, 40960\n"
    "s_nop 0\n"
    "buffer_load_dwordx4 v108, s[16:19], s40 offen lds\n"
    "s_add_u32 m0, s50, 41984\n"
    "s_nop 0\n"
    "buffer_load_dwordx4 v109, s[16:19], s40 offen lds\n"
    "s_add_u32 m0, s50, 43008\n"
    "s_nop 0\n"
    "buffer_load_dwordx4 v110, s[16:19], s40 offen lds\n"
    "s_add_u32 m0, s50, 44032\n"
    "s_nop 0\n"
    "buffer_load_dwordx4 v111, s[16:19], s40 offen lds\n"
    "s_add_u32 m0, s50, 45056\n"
    "s_nop 0\n"
    "buffer_load_dwordx4 v112, s[16:19], s40 offen lds\n"
    "s_add_u32 m0, s50, 46080\n"
    "s_nop 0\n"
    "buffer_load_dwordx4 v113, s[16:19], s40 offen lds\n"
    "s_add_u32 m0, s50, 47104\n"
    "s_nop 0\n"
    "buffer_load_dwordx4 v114, s[16:19], s40 offen lds\n"
    "s_add_u32 m0, s50, 48128\n"
    "s_nop 0\n"
    "buffer_load_dwordx4 v115, s[16:19], s40 offen lds\n"
    "s_add_u32 m0, s50, 49152\n"
    "s_nop 0\n"
    "buffer_load_dwordx4 v116, s[16:19], s40 offen lds\n"
    "s_add_u32 m0, s50, 50176\n"
    "s_nop 0\n"
    "buffer_load_dwordx4 v117, s[16:19], s40 offen lds\n"
    "s_waitcnt lgkmcnt(0)\n"
    "s_waitcnt vmcnt(10)\n"
    "s_barrier\n"
    "ds_read_b64_tr_b16 a[252:253], v98 offset:0\n"
    "ds_read_b64_tr_b16 a[254:255], v98 offset:1280\n"
    "ds_read_b64_tr_b16 v[66:67], v98 offset:32\n"
    "ds_read_b64_tr_b16 v[68:69], v98 offset:1312\n"
    "ds_read_b64_tr_b16 v[148:149], v98 offset:64\n"
    "ds_read_b64_tr_b16 v[150:151], v98 offset:1344\n"
    "ds_read_b64_tr_b16 v[70:71], v98 offset:96\n"
    "ds_read_b64_tr_b16 v[72:73], v98 offset:1376\n"
    "ds_read_b64_tr_b16 v[172:173], v98 offset:128\n"
    "ds_read_b64_tr_b16 v[174:175], v98 offset:1408\n"
    "ds_read_b64_tr_b16 v[74:75], v98 offset:160\n"
    "ds_read_b64_tr_b16 v[76:77], v98 offset:1440\n"
    "ds_read_b64_tr_b16 v[196:197], v98 offset:192\n"
    "ds_read_b64_tr_b16 v[198:199], v98 offset:1472\n"
    "ds_read_b64_tr_b16 v[78:79], v98 offset:224\n"
    "ds_read_b64_tr_b16 v[80:81], v98 offset:1504\n"
    "ds_read_b64_tr_b16 v[220:221], v98 offset:256\n"
    "ds_read_b64_tr_b16 v[222:223], v98 offset:1536\n"
    "ds_read_b64_tr_b16 v[244:245], v98 offset:288\n"
    "ds_read_b64_tr_b16 v[246:247], v98 offset:1568\n"
    "s_add_u32 s40, s49, 204800\n"
    "s_add_u32 m0, s50, 81920\n"
    "s_nop 0\n"
    "buffer_load_dwordx4 v108, s[16:19], s40 offen lds\n"
    "s_add_u32 m0, s50, 82944\n"
    "s_nop 0\n"
    "buffer_load_dwordx4 v109, s[16:19], s40 offen lds\n"
    "s_add_u32 m0, s50, 83968\n"
    "s_nop 0\n"
    "buffer_load_dwordx4 v110, s[16:19], s40 offen lds\n"
    "s_add_u32 m0, s50, 84992\n"
    "s_nop 0\n"
    "buffer_load_dwordx4 v111, s[16:19], s40 offen lds\n"
    "s_add_u32 m0, s50, 86016\n"
    "s_nop 0\n"
    "buffer_load_dwordx4 v112, s[16:19], s40 offen lds\n"
    "s_add_u32 m0, s50, 87040\n"
    "s_nop 0\n"
    "buffer_load_dwordx4 v113, s[16:19], s40 offen lds\n"
    "s_add_u32 m0, s50, 88064\n"
    "s_nop 0\n"
    "buffer_load_dwordx4 v114, s[16:19], s40 offen lds\n"
    "s_add_u32 m0, s50, 89088\n"
    "s_nop 0\n"
    "buffer_load_dwordx4 v115, s[16:19], s40 offen lds\n"
    "s_add_u32 m0, s50, 90112\n"
    "s_nop 0\n"
    "buffer_load_dwordx4 v116, s[16:19], s40 offen lds\n"
    "s_add_u32 m0, s50, 91136\n"
    "s_nop 0\n"
    "buffer_load_dwordx4 v117, s[16:19], s40 offen lds\n"
    "s_waitcnt lgkmcnt(0)\n"
    "v_cmp_ne_u32 vcc, s4, v104\n"
    "s_nop 1\n"
    "s_cmp_eq_u32 vcc_lo, 0\n"
    "s_cbranch_scc1 .Lrx_185\n"
    ".Lrp_183:\n"
    "v_min_u32 v106, 54, v1\n"
    "v_lshlrev_b32 v106, 7, v106\n"
    "v_add_u32 v106, 4182016, v106\n"
    "buffer_load_dwordx2 v[104:105], v106, s[8:11], 0 offen sc1\n"
    "s_waitcnt vmcnt(0)\n"
    "v_cmp_ne_u32 vcc, s4, v104\n"
    "s_nop 1\n"
    "s_cmp_eq_u32 vcc_lo, 0\n"
    "s_cbranch_scc1 .Lro_184\n"
    "s_sleep 2\n"
    "s_sub_u32 s25, s25, 1\n"
    "s_cbranch_scc0 .Lrp_183\n"
    "s_branch .Lfail\n"
    ".Lro_184:\n"
    "s_add_u32 m0, s29, 0\n"
    "s_add_u32 s40, s28, 0\n"
    "buffer_load_dwordx4 v2, s[8:11], s40 offen offset:0 sc1 lds\n"
    "buffer_load_dwordx4 v2, s[8:11], s40 offen offset:1024 sc1 lds\n"
    "buffer_load_dwordx4 v2, s[8:11], s40 offen offset:2048 sc1 lds\n"
    "buffer_load_dwordx4 v2, s[8:11], s40 offen offset:3072 sc1 lds\n"
    "s_add_u32 m0, s29, 4096\n"
    "s_add_u32 s40, s28, 4096\n"
    "buffer_load_dwordx4 v2, s[8:11], s40 offen offset:0 sc1 lds\n"
    "buffer_load_dwordx4 v2, s[8:11], s40 offen offset:1024 sc1 lds\n"
    "buffer_load_dwordx4 v2, s[8:11], s40 offen offset:2048 sc1 lds\n"
    "buffer_load_dwordx4 v2, s[8:11], s40 offen offset:3072 sc1 lds\n"
    "s_add_u32 m0, s29, 8192\n"
    "s_add_u32 s40, s28, 8192\n"
    "buffer_load_dwordx4 v2, s[8:11], s40 offen offset:0 sc1 lds\n"
    "buffer_load_dwordx4 v2, s[8:11], s40 offen offset:1024 sc1 lds\n"
    "s_waitcnt vmcnt(0)\n"
    ".Lrx_185:\n"
    "s_waitcnt vmcnt(20)\n"
    "buffer_load_dwordx2 v[104:105], v106, s[8:11], 0 offen sc1\n"
    "s_barrier\n"
    "s_cmp_lg_u32 s21, 0\n"
    "s_cbranch_scc1 .Lgt_186\n"
    "v_mov_b32 v103, 0\n"
    "v_readfirstlane_b32 s40, v102\n"
    "s_nop 1\n"
    "v_mov_b32 v102, s40\n"
    "ds_write_b32 v103, v102\n"
    "s_waitcnt lgkmcnt(0)\n"
    ".Lgt_186:\n"
    "s_barrier\n"
    "v_mov_b32 v103, 0\n"
    "ds_read_b32 v102, v103\n"
    "s_waitcnt lgkmcnt(0)\n"
    "v_readfirstlane_b32 s40, v102\n"
    "s_barrier\n"
    "s_cmp_eq_u32 s40, s4\n"
    "s_cbranch_scc1 .Llost\n"
    "ds_write_b128 v4, v[18:21] offset:0\n"
    "ds_write_b128 v4, v[22:25] offset:4096\n"
    "ds_write_b128 v4, v[26:29] offset:8192\n"
    "ds_write_b128 v4, v[30:33] offset:12288\n"
    "ds_write_b128 v4, v[34:37] offset:16384\n"
    "ds_write_b128 v4, v[38:41] offset:20480\n"
    "ds_write_b128 v4, v[42:45] offset:24576\n"
    "ds_write_b128 v4, v[46:49] offset:28672\n"
    "ds_write_b128 v4, v[50:53] offset:32768\n"
    "ds_write_b128 v4, v[54:57] offset:36864\n"
    "s_waitcnt lgkmcnt(0)\n"
    "ds_read_b128 v[34:37], v3 offset:0\n"
    "ds_read_b128 v[38:41], v3 offset:20480\n"
    "ds_read_b128 v[42:45], v3 offset:1024\n"
    "ds_read_b128 v[46:49], v3 offset:21504\n"
    "s_branch .LU_loop\n"
    ".LU_resume:\n"
    "s_waitcnt lgkmcnt(0)\n"
    "s_waitcnt vmcnt(10)\n"
    "s_barrier\n"
    "ds_read_b64_tr_b16 v[128:129], v98 offset:40960\n"
    "ds_read_b64_tr_b16 v[130:131], v98 offset:42240\n"
    "ds_read_b64_tr_b16 v[82:83], v98 offset:40992\n"
    "ds_read_b64_tr_b16 v[84:85], v98 offset:42272\n"
    "ds_read_b64_tr_b16 v[152:153], v98 offset:41024\n"
    "ds_read_b64_tr_b16 v[154:155], v98 offset:42304\n"
    "ds_read_b64_tr_b16 v[86:87], v98 offset:41056\n"
    "ds_read_b64_tr_b16 v[88:89], v98 offset:42336\n"
    "ds_read_b64_tr_b16 v[176:177], v98 offset:41088\n"
    "ds_read_b64_tr_b16 v[178:179], v98 offset:42368\n"
    "ds_read_b64_tr_b16 v[90:91], v98 offset:41120\n"
    "ds_read_b64_tr_b16 v[92:93], v98 offset:42400\n"
    "ds_read_b64_tr_b16 v[200:201], v98 offset:41152\n"
    "ds_read_b64_tr_b16 v[202:203], v98 offset:42432\n"
    "ds_read_b64_tr_b16 v[94:95], v98 offset:41184\n"
    "ds_read_b64_tr_b16 v[96:97], v98 offset:42464\n"
    "ds_read_b64_tr_b16 v[224:225], v98 offset:41216\n"
    "ds_read_b64_tr_b16 v[226:227], v98 offset:42496\n"
    "ds_read_b64_tr_b16 v[248:249], v98 offset:41248\n"
    "ds_read_b64_tr_b16 v[250:251], v98 offset:42528\n"
    "s_waitcnt lgkmcnt(0)\n"
    "s_barrier\n"
    "ds_write_b128 v4, v[58:61] offset:40960\n"
    "ds_write_b128 v4, v[62:65] offset:45056\n"
    "ds_write_b128 v126, v[66:69] offset:0\n"
    "ds_write_b128 v126, v[70:73] offset:4096\n"
    "ds_write_b128 v126, v[74:77] offset:8192\n"
    "ds_write_b128 v126, v[78:81] offset:12288\n"
    "ds_write_b128 v126, v[82:85] offset:16384\n"
    "ds_write_b128 v126, v[86:89] offset:20480\n"
    "ds_write_b128 v126, v[90:93] offset:24576\n"
    "ds_write_b128 v126, v[94:97] offset:28672\n"
    "s_waitcnt lgkmcnt(0)\n"
    "s_waitcnt vmcnt(0)\n"
    "s_barrier\n"
    "ds_read_b64_tr_b16 v[132:133], v99 offset:24576\n"
    "ds_read_b64_tr_b16 v[134:135], v99 offset:25856\n"
    "ds_read_b64_tr_b16 v[58:59], v99 offset:24608\n"
    "ds_read_b64_tr_b16 v[60:61], v99 offset:25888\n"
    "ds_read_b64_tr_b16 v[156:157], v99 offset:24640\n"
    "ds_read_b64_tr_b16 v[158:159], v99 offset:25920\n"
    "ds_read_b64_tr_b16 v[62:63], v99 offset:24672\n"
    "ds_read_b64_tr_b16 v[64:65], v99 offset:25952\n"
    "ds_read_b64_tr_b16 v[180:181], v99 offset:24704\n"
    "ds_read_b64_tr_b16 v[182:183], v99 offset:25984\n"
    "ds_read_b64_tr_b16 v[66:67], v99 offset:24736\n"
    "ds_read_b64_tr_b16 v[68:69], v99 offset:26016\n"
    "ds_read_b64_tr_b16 v[204:205], v99 offset:24768\n"
    "ds_read_b64_tr_b16 v[206:207], v99 offset:26048\n"
    "ds_read_b64_tr_b16 v[70:71], v99 offset:24800\n"
    "ds_read_b64_tr_b16 v[72:73], v99 offset:26080\n"
    "ds_read_b64_tr_b16 v[228:229], v99 offset:24832\n"
    "ds_read_b64_tr_b16 v[230:231], v99 offset:26112\n"
    "ds_read_b64_tr_b16 v[252:253], v99 offset:24864\n"
    "ds_read_b64_tr_b16 v[254:255], v99 offset:26144\n"
    "s_waitcnt lgkmcnt(0)\n"
    "s_barrier\n"
    "ds_write_b128 v126, v[58:61] offset:32768\n"
    "ds_write_b128 v126, v[62:65] offset:36864\n"
    "ds_write_b128 v126, v[66:69] offset:40960\n"
    "ds_write_b128 v126, v[70:73] offset:45056\n"
    "s_waitcnt lgkmcnt(0)\n"
    "v_cmp_ne_u32 vcc, s4, v104\n"
    "s_nop 1\n"
    "s_cmp_eq_u32 vcc_hi, 0\n"
    "s_cbranch_scc1 .Lrx_189\n"
    ".Lrp_187:\n"
    "v_min_u32 v106, 54, v1\n"
    "v_lshlrev_b32 v106, 7, v106\n"
    "v_add_u32 v106, 4182016, v106\n"
    "buffer_load_dwordx2 v[104:105], v106, s[8:11], 0 offen sc1\n"
    "s_waitcnt vmcnt(0)\n"
    "v_cmp_ne_u32 vcc, s4, v104\n"
    "s_nop 1\n"
    "s_cmp_eq_u32 vcc_hi, 0\n"
    "s_cbranch_scc1 .Lro_188\n"
    "s_sleep 2\n"
    "s_sub_u32 s25, s25, 1\n"
    "s_cbranch_scc0 .Lrp_187\n"
    "s_branch .Lfail\n"
    ".Lro_188:\n"
    ".Lrx_189:\n"
    "v_cmp_ne_u32 vcc, 0, v105\n"
    "s_nop 1\n"
    "s_and_b32 s22, vcc_hi, 1\n"
    "s_lshl_b32 s22, s22, 13\n"
    "s_lshr_b32 s23, vcc_hi, 1\n"
    "ds_read_b128 v[106:109], v4 offset:0\n"
    "ds_read_b128 v[110:113], v4 offset:16384\n"
    "ds_read_b128 v[114:117], v4 offset:32768\n"
    "ds_read_b128 v[118:121], v126 offset:0\n"
    "s_branch .LU_exchange\n"
    ".p2align 4\n"
    ".LU_loop:\n"
    "s_waitcnt lgkmcnt(3)\n"
    "v_mfma_f32_16x16x32_bf16 v[10:13], a[0:3], v[34:37], 0\n"
    "ds_read_b128 v[50:53], v3 offset:2048\n"
    "v_mfma_f32_16x16x32_bf16 v[14:17], a[4:7], v[34:37], 0\n"
    "v_mfma_f32_16x16x32_bf16 v[18:21], a[8:11], v[34:37], 0\n"
    "s_waitcnt lgkmcnt(3)\n"
    "v_mfma_f32_16x16x32_bf16 v[22:25], a[0:3], v[38:41], 0\n"
    "ds_read_b128 v[54:57], v3 offset:22528\n"
    "v_mfma_f32_16x16x32_bf16 v[26:29], a[4:7], v[38:41], 0\n"
    "v_mfma_f32_16x16x32_bf16 v[30:33], a[8:11], v[38:41], 0\n"
    "s_waitcnt lgkmcnt(3)\n"
    "v_mfma_f32_16x16x32_bf16 v[10:13], a[12:15], v[42:45], v[10:13]\n"
    "ds_read_b128 v[34:37], v3 offset:3072\n"
    "v_mfma_f32_16x16x32_bf16 v[14:17], a[16:19], v[42:45], v[14:17]\n"
    "v_mfma_f32_16x16x32_bf16 v[18:21], a[20:23], v[42:45], v[18:21]\n"
    "s_waitcnt lgkmcnt(3)\n"
    "v_mfma_f32_16x16x32_bf16 v[22:25], a[12:15], v[46:49], v[22:25]\n"
    "ds_read_b128 v[38:41], v3 offset:23552\n"
    "v_mfma_f32_16x16x32_bf16 v[26:29], a[16:19], v[46:49], v[26:29]\n"
    "v_mfma_f32_16x16x32_bf16 v[30:33], a[20:23], v[46:49], v[30:33]\n"
    "s_waitcnt lgkmcnt(3)\n"
    "v_mfma_f32_16x16x32_bf16 v[10:13], a[24:27], v[50:53], v[10:13]\n"
    "ds_read_b128 v[42:45], v3 offset:4096\n"
    "v_mfma_f32_16x16x32_bf16 v[14:17], a[28:31], v[50:53], v[14:17]\n"
    "v_mfma_f32_16x16x32_bf16 v[18:21], a[32:35], v[50:53], v[18:21]\n"
    "s_waitcnt lgkmcnt(3)\n"
    "v_mfma_f32_16x16x32_bf16 v[22:25], a[24:27], v[54:57], v[22:25]\n"
    "ds_read_b128 v[46:49], v3 offset:24576\n"
    "v_mfma_f32_16x16x32_bf16 v[26:29], a[28:31], v[54:57], v[26:29]\n"
    "v_mfma_f32_16x16x32_bf16 v[30:33], a[32:35], v[54:57], v[30:33]\n"
    "s_waitcnt lgkmcnt(3)\n"
    "v_mfma_f32_16x16x32_bf16 v[10:13], a[36:39], v[34:37], v[10:13]\n"
    "ds_read_b128 v[50:53], v3 offset:5120\n"
    "v_mfma_f32_16x16x32_bf16 v[14:17], a[40:43], v[34:37], v[14:17]\n"
    "v_mfma_f32_16x16x32_bf16 v[18:21], a[44:47], v[34:37], v[18:21]\n"
    "s_waitcnt lgkmcnt(3)\n"
    "v_mfma_f32_16x16x32_bf16 v[22:25], a[36:39], v[38:41], v[22:25]\n"
    "ds_read_b128 v[54:57], v3 offset:25600\n"
    "v_mfma_f32_16x16x32_bf16 v[26:29], a[40:43], v[38:41], v[26:29]\n"
    "v_mfma_f32_16x16x32_bf16 v[30:33], a[44:47], v[38:41], v[30:33]\n"
    "s_waitcnt lgkmcnt(3)\n"
    "v_mfma_f32_16x16x32_bf16 v[10:13], a[48:51], v[42:45], v[10:13]\n"
    "ds_read_b128 v[34:37], v3 offset:6144\n"
    "v_mfma_f32_16x16x32_bf16 v[14:17], a[52:55], v[42:45], v[14:17]\n"
    "v_mfma_f32_16x16x32_bf16 v[18:21], a[56:59], v[42:45], v[18:21]\n"
    "s_waitcnt lgkmcnt(3)\n"
    "v_mfma_f32_16x16x32_bf16 v[22:25], a[48:51], v[46:49], v[22:25]\n"
    "ds_read_b128 v[38:41], v3 offset:26624\n"
    "v_mfma_f32_16x16x32_bf16 v[26:29], a[52:55], v[46:49], v[26:29]\n"
    "v_mfma_f32_16x16x32_bf16 v[30:33], a[56:59], v[46:49], v[30:33]\n"
    "s_waitcnt lgkmcnt(3)\n"
    "v_mfma_f32_16x16x32_bf16 v[10:13], a[60:63], v[50:53], v[10:13]\n"
    "ds_read_b128 v[42:45], v3 offset:7168\n"
    "v_mfma_f32_16x16x32_bf16 v[14:17], a[64:67], v[50:53], v[14:17]\n"
    "v_mfma_f32_16x16x32_bf16 v[18:21], a[68:71], v[50:53], v[18:21]\n"
    "s_waitcnt lgkmcnt(3)\n"
    "v_mfma_f32_16x16x32_bf16 v[22:25], a[60:63], v[54:57], v[22:25]\n"
    "ds_read_b128 v[46:49], v3 offset:27648\n"
    "v_mfma_f32_16x16x32_bf16 v[26:29], a[64:67], v[54:57], v[26:29]\n"
    "v_mfma_f32_16x16x32_bf16 v[30:33], a[68:71], v[54:57], v[30:33]\n"
    "s_waitcnt lgkmcnt(3)\n"
    "v_mfma_f32_16x16x32_bf16 v[10:13], a[72:75], v[34:37], v[10:13]\n"
    "ds_read_b128 v[50:53], v3 offset:8192\n"
    "v_mfma_f32_16x16x32_bf16 v[14:17], a[76:79], v[34:37], v[14:17]\n"
    "v_mfma_f32_16x16x32_bf16 v[18:21], a[80:83], v[34:37], v[18:21]\n"
    "s_waitcnt lgkmcnt(3)\n"
    "v_mfma_f32_16x16x32_bf16 v[22:25], a[72:75], v[38:41], v[22:25]\n"
    "ds_read_b128 v[54:57], v3 offset:28672\n"
    "v_mfma_f32_16x16x32_bf16 v[26:29], a[76:79], v[38:41], v[26:29]\n"
    "v_mfma_f32_16x16x32_bf16 v[30:33], a[80:83], v[38:41], v[30:33]\n"
    "s_waitcnt lgkmcnt(3)\n"
    "v_mfma_f32_16x16x32_bf16 v[10:13], a[84:87], v[42:45], v[10:13]\n"
    "ds_read_b128 v[34:37], v3 offset:9216\n"
    "v_mfma_f32_16x16x32_bf16 v[14:17], a[88:91], v[42:45], v[14:17]\n"
    "v_mfma_f32_16x16x32_bf16 v[18:21], a[92:95], v[42:45], v[18:21]\n"
    "s_waitcnt lgkmcnt(3)\n"
    "v_mfma_f32_16x16x32_bf16 v[22:25], a[84:87], v[46:49], v[22:25]\n"
    "ds_read_b128 v[38:41], v3 offset:29696\n"
    "v_mfma_f32_16x16x32_bf16 v[26:29], a[88:91], v[46:49], v[26:29]\n"
    "v_mfma_f32_16x16x32_bf16 v[30:33], a[92:95], v[46:49], v[30:33]\n"
    "s_waitcnt lgkmcnt(3)\n"
    "v_mfma_f32_16x16x32_bf16 v[10:13], a[96:99], v[50:53], v[10:13]\n"
    "ds_read_b128 v[42:45], v3 offset:10240\n"
    "v_mfma_f32_16x16x32_bf16 v[14:17], a[100:103], v[50:53], v[14:17]\n"
    "v_mfma_f32_16x16x32_bf16 v[18:21], a[104:107], v[50:53], v[18:21]\n"
    "s_waitcnt lgkmcnt(3)\n"
    "v_mfma_f32_16x16x32_bf16 v[22:25], a[96:99], v[54:57], v[22:25]\n"
    "ds_read_b128 v[46:49], v3 offset:30720\n"
    "v_mfma_f32_16x16x32_bf16 v[26:29], a[100:103], v[54:57], v[26:29]\n"
    "v_mfma_f32_16x16x32_bf16 v[30:33], a[104:107], v[54:57], v[30:33]\n"
    "s_waitcnt lgkmcnt(3)\n"
    "v_mfma_f32_16x16x32_bf16 v[10:13], a[108:111], v[34:37], v[10:13]\n"
    "ds_read_b128 v[50:53], v3 offset:11264\n"
    "v_mfma_f32_16x16x32_bf16 v[14:17], a[112:115], v[34:37], v[14:17]\n"
    "s_cmp_lt_u32 s24, 2\n"
    "v_mfma_f32_16x16x32_bf16 v[18:21], a[116:119], v[34:37], v[18:21]\n"
    "s_cbranch_scc1 .LU_noword\n"
    "s_waitcnt vmcnt(0)\n"
    "buffer_store_dword v8, v7, s[8:11], 0 offen sc1\n"
    ".LU_noword:\n"
    "s_waitcnt lgkmcnt(3)\n"
    "v_mfma_f32_16x16x32_bf16 v[22:25], a[108:111], v[38:41], v[22:25]\n"
    "ds_read_b128 v[54:57], v3 offset:31744\n"
    "v_mfma_f32_16x16x32_bf16 v[26:29], a[112:115], v[38:41], v[26:29]\n"
    "v_mfma_f32_16x16x32_bf16 v[30:33], a[116:119], v[38:41], v[30:33]\n"
    "s_waitcnt lgkmcnt(3)\n"
    "v_mfma_f32_16x16x32_bf16 v[10:13], a[120:123], v[42:45], v[10:13]\n"
    "ds_read_b128 v[34:37], v3 offset:12288\n"
    "v_mfma_f32_16x16x32_bf16 v[14:17], a[124:127], v[42:45], v[14:17]\n"
    "v_mfma_f32_16x16x32_bf16 v[18:21], a[128:131], v[42:45], v[18:21]\n"
    "s_waitcnt lgkmcnt(3)\n"
    "v_mfma_f32_16x16x32_bf16 v[22:25], a[120:123], v[46:49], v[22:25]\n"
    "ds_read_b128 v[38:41], v3 offset:32768\n"
    "v_mfma_f32_16x16x32_bf16 v[26:29], a[124:127], v[46:49], v[26:29]\n"
    "v_mfma_f32_16x16x32_bf16 v[30:33], a[128:131], v[46:49], v[30:33]\n"
    "s_waitcnt lgkmcnt(3)\n"
    "v_mfma_f32_16x16x32_bf16 v[10:13], a[132:135], v[50:53], v[10:13]\n"
    "ds_read_b128 v[42:45], v3 offset:13312\n"
    "v_mfma_f32_16x16x32_bf16 v[14:17], a[136:139], v[50:53], v[14:17]\n"
    "v_mfma_f32_16x16x32_bf16 v[18:21], a[140:143], v[50:53], v[18:21]\n"
    "s_waitcnt lgkmcnt(3)\n"
    "v_mfma_f32_16x16x32_bf16 v[22:25], a[132:135], v[54:57], v[22:25]\n"
    "ds_read_b128 v[46:49], v3 offset:33792\n"
    "v_mfma_f32_16x16x32_bf16 v[26:29], a[136:139], v[54:57], v[26:29]\n"
    "s_add_u32 s28, s28, 81920\n"
    "s_add_u32 s40, s24, 1\n"
    "v_mfma_f32_16x16x32_bf16 v[30:33], a[140:143], v[54:57], v[30:33]\n"
    "v_mov_b32 v8, s40\n"
    "s_waitcnt lgkmcnt(3)\n"
    "v_mfma_f32_16x16x32_bf16 v[10:13], a[144:147], v[34:37], v[10:13]\n"
    "ds_read_b128 v[50:53], v3 offset:14336\n"
    "v_mfma_f32_16x16x32_bf16 v[14:17], a[148:151], v[34:37], v[14:17]\n"
    "v_mfma_f32_16x16x32_bf16 v[18:21], a[152:155], v[34:37], v[18:21]\n"
    "s_waitcnt lgkmcnt(3)\n"
    "v_mfma_f32_16x16x32_bf16 v[22:25], a[144:147], v[38:41], v[22:25]\n"
    "ds_read_b128 v[54:57], v3 offset:34816\n"
    "v_mfma_f32_16x16x32_bf16 v[26:29], a[148:151], v[38:41], v[26:29]\n"
    "v_mfma_f32_16x16x32_bf16 v[30:33], a[152:155], v[38:41], v[30:33]\n"
    "s_waitcnt lgkmcnt(3)\n"
    "v_mfma_f32_16x16x32_bf16 v[10:13], a[156:159], v[42:45], v[10:13]\n"
    "ds_read_b128 v[34:37], v3 offset:15360\n"
    "v_mfma_f32_16x16x32_bf16 v[14:17], a[160:163], v[42:45], v[14:17]\n"
    "v_mfma_f32_16x16x32_bf16 v[18:21], a[164:167], v[42:45], v[18:21]\n"
    "s_waitcnt lgkmcnt(3)\n"
    "v_mfma_f32_16x16x32_bf16 v[22:25], a[156:159], v[46:49], v[22:25]\n"
    "ds_read_b128 v[38:41], v3 offset:35840\n"
    "v_mfma_f32_16x16x32_bf16 v[26:29], a[160:163], v[46:49], v[26:29]\n"
    "v_mfma_f32_16x16x32_bf16 v[30:33], a[164:167], v[46:49], v[30:33]\n"
    "s_waitcnt lgkmcnt(3)\n"
    "v_mfma_f32_16x16x32_bf16 v[10:13], a[168:171], v[50:53], v[10:13]\n"
    "ds_read_b128 v[42:45], v3 offset:16384\n"
    "v_mfma_f32_16x16x32_bf16 v[14:17], a[172:175], v[50:53], v[14:17]\n"
    "v_mfma_f32_16x16x32_bf16 v[18:21], a[176:179], v[50:53], v[18:21]\n"
    "s_waitcnt lgkmcnt(3)\n"
    "v_mfma_f32_16x16x32_bf16 v[22:25], a[168:171], v[54:57], v[22:25]\n"
    "ds_read_b128 v[46:49], v3 offset:36864\n"
    "v_mfma_f32_16x16x32_bf16 v[26:29], a[172:175], v[54:57], v[26:29]\n"
    "v_mfma_f32_16x16x32_bf16 v[30:33], a[176:179], v[54:57], v[30:33]\n"
    "s_waitcnt lgkmcnt(3)\n"
    "v_mfma_f32_16x16x32_bf16 v[10:13], a[180:183], v[34:37], v[10:13]\n"
    "ds_read_b128 v[50:53], v3 offset:17408\n"
    "v_mfma_f32_16x16x32_bf16 v[14:17], a[184:187], v[34:37], v[14:17]\n"
    "v_mfma_f32_16x16x32_bf16 v[18:21], a[188:191], v[34:37], v[18:21]\n"
    "s_waitcnt lgkmcnt(3)\n"
    "v_mfma_f32_16x16x32_bf16 v[22:25], a[180:183], v[38:41], v[22:25]\n"
    "ds_read_b128 v[54:57], v3 offset:37888\n"
    "v_mfma_f32_16x16x32_bf16 v[26:29], a[184:187], v[38:41], v[26:29]\n"
    "v_mfma_f32_16x16x32_bf16 v[30:33], a[188:191], v[38:41], v[30:33]\n"
    "s_waitcnt lgkmcnt(3)\n"
    "v_mfma_f32_16x16x32_bf16 v[10:13], a[192:195], v[42:45], v[10:13]\n"
    "ds_read_b128 v[34:37], v3 offset:18432\n"
    "v_mfma_f32_16x16x32_bf16 v[14:17], a[196:199], v[42:45], v[14:17]\n"
    "v_mfma_f32_16x16x32_bf16 v[18:21], a[200:203], v[42:45], v[18:21]\n"
    "s_waitcnt lgkmcnt(3)\n"
    "v_mfma_f32_16x16x32_bf16 v[22:25], a[192:195], v[46:49], v[22:25]\n"
    "ds_read_b128 v[38:41], v3 offset:38912\n"
    "v_mfma_f32_16x16x32_bf16 v[26:29], a[196:199], v[46:49], v[26:29]\n"
    "v_mfma_f32_16x16x32_bf16 v[30:33], a[200:203], v[46:49], v[30:33]\n"
    "s_waitcnt lgkmcnt(3)\n"
    "v_mfma_f32_16x16x32_bf16 v[10:13], a[204:207], v[50:53], v[10:13]\n"
    "ds_read_b128 v[42:45], v3 offset:19456\n"
    "v_mfma_f32_16x16x32_bf16 v[14:17], a[208:211], v[50:53], v[14:17]\n"
    "v_mfma_f32_16x16x32_bf16 v[18:21], a[212:215], v[50:53], v[18:21]\n"
    "s_waitcnt lgkmcnt(3)\n"
    "v_mfma_f32_16x16x32_bf16 v[22:25], a[204:207], v[54:57], v[22:25]\n"
    "ds_read_b128 v[46:49], v3 offset:39936\n"
    "v_mfma_f32_16x16x32_bf16 v[26:29], a[208:211], v[54:57], v[26:29]\n"
    "v_mfma_f32_16x16x32_bf16 v[30:33], a[212:215], v[54:57], v[30:33]\n"
    "s_waitcnt lgkmcnt(3)\n"
    "v_mfma_f32_16x16x32_bf16 v[10:13], a[216:219], v[34:37], v[10:13]\n"
    "v_mfma_f32_16x16x32_bf16 v[14:17], a[220:223], v[34:37], v[14:17]\n"
    "v_mfma_f32_16x16x32_bf16 v[18:21], a[224:227], v[34:37], v[18:21]\n"
    "ds_read_b128 v[106:109], v4 offset:0\n"
    "s_waitcnt lgkmcnt(3)\n"
    "v_mfma_f32_16x16x32_bf16 v[22:25], a[216:219], v[38:41], v[22:25]\n"
    "ds_read_b128 v[110:113], v4 offset:16384\n"
    "v_mfma_f32_16x16x32_bf16 v[26:29], a[220:223], v[38:41], v[26:29]\n"
    "ds_read_b128 v[114:117], v4 offset:32768\n"
    "v_mfma_f32_16x16x32_bf16 v[30:33], a[224:227], v[38:41], v[30:33]\n"
    "ds_read_b128 v[118:121], v126 offset:0\n"
    "s_waitcnt lgkmcnt(5)\n"
    "v_mfma_f32_16x16x32_bf16 v[10:13], a[228:231], v[42:45], v[10:13]\n"
    "v_mfma_f32_16x16x32_bf16 v[14:17], a[232:235], v[42:45], v[14:17]\n"
    "v_mfma_f32_16x16x32_bf16 v[18:21], a[236:239], v[42:45], v[18:21]\n"
    "s_waitcnt lgkmcnt(4)\n"
    "v_mfma_f32_16x16x32_bf16 v[22:25], a[228:231], v[46:49], v[22:25]\n"
    "v_mfma_f32_16x16x32_bf16 v[26:29], a[232:235], v[46:49], v[26:29]\n"
    "s_cmp_lt_u32 s24, 2\n"
    "v_mfma_f32_16x16x32_bf16 v[30:33], a[236:239], v[46:49], v[30:33]\n"
    "s_cbranch_scc1 .LU_resume\n"
    ".LU_exchange:\n"
    "v_cvt_pk_bf16_f32 v10, v10, v11\n"
    "v_cvt_pk_bf16_f32 v11, v12, v13\n"
    "ds_write_b64 v5, v[10:11] offset:0\n"
    "v_cvt_pk_bf16_f32 v14, v14, v15\n"
    "v_cvt_pk_bf16_f32 v15, v16, v17\n"
    "ds_write_b64 v5, v[14:15] offset:512\n"
    "v_cvt_pk_bf16_f32 v18, v18, v19\n"
    "v_cvt_pk_bf16_f32 v19, v20, v21\n"
    "ds_write_b64 v5, v[18:19] offset:1024\n"
    "v_cvt_pk_bf16_f32 v22, v22, v23\n"
    "v_cvt_pk_bf16_f32 v23, v24, v25\n"
    "ds_write_b64 v5, v[22:23] offset:6144\n"
    "v_cvt_pk_bf16_f32 v26, v26, v27\n"
    "v_cvt_pk_bf16_f32 v27, v28, v29\n"
    "ds_write_b64 v5, v[26:27] offset:6656\n"
    "v_cvt_pk_bf16_f32 v30, v30, v31\n"
    "v_cvt_pk_bf16_f32 v31, v32, v33\n"
    "ds_write_b64 v5, v[30:31] offset:7168\n"
    "s_waitcnt lgkmcnt(0)\n"
    "s_barrier\n"
    "ds_read_b128 v[58:61], v6 offset:0\n"
    "ds_read_b128 v[62:65], v6 offset:6144\n"
    "ds_read_b128 v[66:69], v6 offset:1024\n"
    "ds_read_b128 v[70:73], v6 offset:7168\n"
    "ds_read_b128 v[74:77], v6 offset:2048\n"
    "ds_read_b128 v[78:81], v6 offset:8192\n"
    "ds_read_b128 v[82:85], v6 offset:3072\n"
    "ds_read_b128 v[86:89], v6 offset:9216\n"
    "ds_read_b128 v[90:93], v6 offset:4096\n"
    "ds_read_b128 v[94:97], v6 offset:10240\n"
    "ds_read_b128 v[98:101], v6 offset:5120\n"
    "ds_read_b128 v[102:105], v6 offset:11264\n"
    "s_waitcnt lgkmcnt(11)\n"
    "v_mfma_f32_16x16x32_bf16 v[10:13], v[58:61], a[240:243], 0\n"
    "s_waitcnt lgkmcnt(10)\n"
    "v_mfma_f32_16x16x32_bf16 v[14:17], v[62:65], a[240:243], 0\n"
    "s_add_u32 m0, s29, 0\n"
    "s_add_u32 s41, s28, 0\n"
    "buffer_load_dwordx4 v2, s[8:11], s41 offen offset:0 sc1 lds\n"
    "s_waitcnt lgkmcnt(9)\n"
    "v_mfma_f32_16x16x32_bf16 v[10:13], v[66:69], a[244:247], v[10:13]\n"
    "s_waitcnt lgkmcnt(8)\n"
    "v_mfma_f32_16x16x32_bf16 v[14:17], v[70:73], a[244:247], v[14:17]\n"
    "buffer_load_dwordx4 v2, s[8:11], s41 offen offset:1024 sc1 lds\n"
    "s_waitcnt lgkmcnt(7)\n"
    "v_mfma_f32_16x16x32_bf16 v[10:13], v[74:77], a[248:251], v[10:13]\n"
    "s_waitcnt lgkmcnt(6)\n"
    "v_mfma_f32_16x16x32_bf16 v[14:17], v[78:81], a[248:251], v[14:17]\n"
    "buffer_load_dwordx4 v2, s[8:11], s41 offen offset:2048 sc1 lds\n"
    "s_waitcnt lgkmcnt(5)\n"
    "v_mfma_f32_16x16x32_bf16 v[10:13], v[82:85], a[252:255], v[10:13]\n"
    "s_waitcnt lgkmcnt(4)\n"
    "v_mfma_f32_16x16x32_bf16 v[14:17], v[86:89], a[252:255], v[14:17]\n"
    "buffer_load_dwordx4 v2, s[8:11], s41 offen offset:3072 sc1 lds\n"
    "s_waitcnt lgkmcnt(3)\n"
    "v_mfma_f32_16x16x32_bf16 v[10:13], v[90:93], v[128:131], v[10:13]\n"
    "s_waitcnt lgkmcnt(2)\n"
    "v_mfma_f32_16x16x32_bf16 v[14:17], v[94:97], v[128:131], v[14:17]\n"
    "s_add_u32 m0, s29, 4096\n"
    "s_add_u32 s41, s28, 4096\n"
    "buffer_load_dwordx4 v2, s[8:11], s41 offen offset:0 sc1 lds\n"
    "s_waitcnt lgkmcnt(1)\n"
    "v_mfma_f32_16x16x32_bf16 v[10:13], v[98:101], v[132:135], v[10:13]\n"
    "s_waitcnt lgkmcnt(0)\n"
    "v_mfma_f32_16x16x32_bf16 v[14:17], v[102:105], v[132:135], v[14:17]\n"
    "buffer_load_dwordx4 v2, s[8:11], s41 offen offset:1024 sc1 lds\n"
    "v_mfma_f32_16x16x32_bf16 v[18:21], v[58:61], v[106:109], 0\n"
    "v_mfma_f32_16x16x32_bf16 v[22:25], v[62:65], v[106:109], 0\n"
    "buffer_load_dwordx4 v2, s[8:11], s41 offen offset:2048 sc1 lds\n"
    "ds_read_b128 v[106:109], v126 offset:16384\n"
    "v_mfma_f32_16x16x32_bf16 v[18:21], v[66:69], v[110:113], v[18:21]\n"
    "v_mfma_f32_16x16x32_bf16 v[22:25], v[70:73], v[110:113], v[22:25]\n"
    "buffer_load_dwordx4 v2, s[8:11], s41 offen offset:3072 sc1 lds\n"
    "ds_read_b128 v[110:113], v126 offset:32768\n"
    "v_mfma_f32_16x16x32_bf16 v[18:21], v[74:77], v[114:117], v[18:21]\n"
    "v_mfma_f32_16x16x32_bf16 v[22:25], v[78:81], v[114:117], v[22:25]\n"
    "s_add_u32 m0, s29, 8192\n"
    "s_add_u32 s41, s28, 8192\n"
    "buffer_load_dwordx4 v2, s[8:11], s41 offen offset:0 sc1 lds\n"
    "ds_read_b128 v[114:117], v4 offset:4096\n"
    "v_mfma_f32_16x16x32_bf16 v[18:21], v[82:85], v[118:121], v[18:21]\n"
    "v_mfma_f32_16x16x32_bf16 v[22:25], v[86:89], v[118:121], v[22:25]\n"
    "buffer_load_dwordx4 v2, s[8:11], s41 offen offset:1024 sc1 lds\n"
    "ds_read_b128 v[118:121], v4 offset:20480\n"
    "v_cvt_pk_bf16_f32 v26, v10, v11\n"
    "v_cvt_pk_bf16_f32 v27, v12, v13\n"
    "s_waitcnt lgkmcnt(3)\n"
    "v_mfma_f32_16x16x32_bf16 v[18:21], v[90:93], v[106:109], v[18:21]\n"
    "v_cvt_pk_bf16_f32 v28, v14, v15\n"
    "v_cvt_pk_bf16_f32 v29, v16, v17\n"
    "s_add_u32 s40, s30, 0\n"
    "v_mfma_f32_16x16x32_bf16 v[22:25], v[94:97], v[106:109], v[22:25]\n"
    "ds_read_b128 v[106:109], v4 offset:36864\n"
    "buffer_store_dwordx4 v[26:29], v2, s[8:11], s40 offen sc1\n"
    "s_waitcnt lgkmcnt(3)\n"
    "v_mfma_f32_16x16x32_bf16 v[18:21], v[98:101], v[110:113], v[18:21]\n"
    "s_add_u32 s44, s24, 1\n"
    "v_mfma_f32_16x16x32_bf16 v[22:25], v[102:105], v[110:113], v[22:25]\n"
    "ds_read_b128 v[110:113], v126 offset:4096\n"
    "s_mul_i32 s42, s44, 43\n"
    "v_mfma_f32_16x16x32_bf16 v[10:13], v[58:61], v[136:139], 0\n"
    "s_lshr_b32 s42, s42, 7\n"
    "v_mfma_f32_16x16x32_bf16 v[14:17], v[62:65], v[136:139], 0\n"
    "s_mul_i32 s31, s44, 655360\n"
    "v_mfma_f32_16x16x32_bf16 v[10:13], v[66:69], v[140:143], v[10:13]\n"
    "s_lshl_b32 s43, s42, 17\n"
    "v_mfma_f32_16x16x32_bf16 v[14:17], v[70:73], v[140:143], v[14:17]\n"
    "s_add_u32 s31, s31, s43\n"
    "v_mfma_f32_16x16x32_bf16 v[10:13], v[74:77], v[144:147], v[10:13]\n"
    "s_lshr_b32 s42, s23, s42\n"
    "v_mfma_f32_16x16x32_bf16 v[14:17], v[78:81], v[144:147], v[14:17]\n"
    "s_and_b32 s42, s42, 1\n"
    "v_mfma_f32_16x16x32_bf16 v[10:13], v[82:85], v[148:151], v[10:13]\n"
    "s_lshl_b32 s42, s42, 13\n"
    "v_mfma_f32_16x16x32_bf16 v[14:17], v[86:89], v[148:151], v[14:17]\n"
    "v_cvt_pk_bf16_f32 v30, v18, v19\n"
    "v_cvt_pk_bf16_f32 v31, v20, v21\n"
    "s_add_u32 s31, s31, s42\n"
    "v_mfma_f32_16x16x32_bf16 v[10:13], v[90:93], v[152:155], v[10:13]\n"
    "v_cvt_pk_bf16_f32 v32, v22, v23\n"
    "v_cvt_pk_bf16_f32 v33, v24, v25\n"
    "s_add_u32 s40, s30, 32768\n"
    "s_add_u32 s31, s31, 161480704\n"
    "v_mfma_f32_16x16x32_bf16 v[14:17], v[94:97], v[152:155], v[14:17]\n"
    "buffer_store_dwordx4 v[30:33], v2, s[8:11], s40 offen sc1\n"
    "v_mfma_f32_16x16x32_bf16 v[10:13], v[98:101], v[156:159], v[10:13]\n"
    "v_mfma_f32_16x16x32_bf16 v[14:17], v[102:105], v[156:159], v[14:17]\n"
    "s_waitcnt lgkmcnt(3)\n"
    "v_mfma_f32_16x16x32_bf16 v[18:21], v[58:61], v[114:117], 0\n"
    "v_mfma_f32_16x16x32_bf16 v[22:25], v[62:65], v[114:117], 0\n"
    "ds_read_b128 v[114:117], v126 offset:20480\n"
    "s_waitcnt lgkmcnt(3)\n"
    "v_mfma_f32_16x16x32_bf16 v[18:21], v[66:69], v[118:121], v[18:21]\n"
    "v_mfma_f32_16x16x32_bf16 v[22:25], v[70:73], v[118:121], v[22:25]\n"
    "ds_read_b128 v[118:121], v126 offset:36864\n"
    "s_add_u32 s42, s31, 0\n"
    "buffer_store_dwordx4 v[122:125], v9, s[8:11], s42 offen sc1\n"
    "s_waitcnt lgkmcnt(3)\n"
    "v_mfma_f32_16x16x32_bf16 v[18:21], v[74:77], v[106:109], v[18:21]\n"
    "v_mfma_f32_16x16x32_bf16 v[22:25], v[78:81], v[106:109], v[22:25]\n"
    "ds_read_b128 v[106:109], v4 offset:8192\n"
    "s_waitcnt lgkmcnt(3)\n"
    "v_mfma_f32_16x16x32_bf16 v[18:21], v[82:85], v[110:113], v[18:21]\n"
    "v_mfma_f32_16x16x32_bf16 v[22:25], v[86:89], v[110:113], v[22:25]\n"
    "ds_read_b128 v[110:113], v4 offset:24576\n"
    "v_cvt_pk_bf16_f32 v26, v10, v11\n"
    "v_cvt_pk_bf16_f32 v27, v12, v13\n"
    "s_waitcnt lgkmcnt(3)\n"
    "v_mfma_f32_16x16x32_bf16 v[18:21], v[90:93], v[114:117], v[18:21]\n"
    "v_cvt_pk_bf16_f32 v28, v14, v15\n"
    "v_cvt_pk_bf16_f32 v29, v16, v17\n"
    "s_add_u32 s40, s30, 65536\n"
    "v_mfma_f32_16x16x32_bf16 v[22:25], v[94:97], v[114:117], v[22:25]\n"
    "ds_read_b128 v[114:117], v4 offset:40960\n"
    "buffer_store_dwordx4 v[26:29], v2, s[8:11], s40 offen sc1\n"
    "s_waitcnt lgkmcnt(3)\n"
    "v_mfma_f32_16x16x32_bf16 v[18:21], v[98:101], v[118:121], v[18:21]\n"
    "v_mfma_f32_16x16x32_bf16 v[22:25], v[102:105], v[118:121], v[22:25]\n"
    "ds_read_b128 v[118:121], v126 offset:8192\n"
    "v_mfma_f32_16x16x32_bf16 v[10:13], v[58:61], v[160:163], 0\n"
    "v_mfma_f32_16x16x32_bf16 v[14:17], v[62:65], v[160:163], 0\n"
    "v_mfma_f32_16x16x32_bf16 v[10:13], v[66:69], v[164:167], v[10:13]\n"
    "v_mfma_f32_16x16x32_bf16 v[14:17], v[70:73], v[164:167], v[14:17]\n"
    "s_add_u32 s42, s31, 65536\n"
    "buffer_store_dwordx4 v[122:125], v9, s[8:11], s42 offen sc1\n"
    "v_mfma_f32_16x16x32_bf16 v[10:13], v[74:77], v[168:171], v[10:13]\n"
    "v_mfma_f32_16x16x32_bf16 v[14:17], v[78:81], v[168:171], v[14:17]\n"
    "v_mfma_f32_16x16x32_bf16 v[10:13], v[82:85], v[172:175], v[10:13]\n"
    "v_mfma_f32_16x16x32_bf16 v[14:17], v[86:89], v[172:175], v[14:17]\n"
    "v_cvt_pk_bf16_f32 v30, v18, v19\n"
    "v_cvt_pk_bf16_f32 v31, v20, v21\n"
    "v_mfma_f32_16x16x32_bf16 v[10:13], v[90:93], v[176:179], v[10:13]\n"
    "v_cvt_pk_bf16_f32 v32, v22, v23\n"
    "v_cvt_pk_bf16_f32 v33, v24, v25\n"
    "s_add_u32 s40, s30, 98304\n"
    "v_mfma_f32_16x16x32_bf16 v[14:17], v[94:97], v[176:179], v[14:17]\n"
    "buffer_store_dwordx4 v[30:33], v2, s[8:11], s40 offen sc1\n"
    "v_mfma_f32_16x16x32_bf16 v[10:13], v[98:101], v[180:183], v[10:13]\n"
    "v_mfma_f32_16x16x32_bf16 v[14:17], v[102:105], v[180:183], v[14:17]\n"
    "s_waitcnt lgkmcnt(3)\n"
    "v_mfma_f32_16x16x32_bf16 v[18:21], v[58:61], v[106:109], 0\n"
    "v_mfma_f32_16x16x32_bf16 v[22:25], v[62:65], v[106:109], 0\n"
    "ds_read_b128 v[106:109], v126 offset:24576\n"
    "s_waitcnt lgkmcnt(3)\n"
    "v_mfma_f32_16x16x32_bf16 v[18:21], v[66:69], v[110:113], v[18:21]\n"
    "v_mfma_f32_16x16x32_bf16 v[22:25], v[70:73], v[110:113], v[22:25]\n"
    "ds_read_b128 v[110:113], v126 offset:40960\n"
    "s_add_u32 s42, s31, 98304\n"
    "buffer_store_dwordx4 v[122:125], v9, s[8:11], s42 offen sc1\n"
    "s_waitcnt lgkmcnt(3)\n"
    "v_mfma_f32_16x16x32_bf16 v[18:21], v[74:77], v[114:117], v[18:21]\n"
    "v_mfma_f32_16x16x32_bf16 v[22:25], v[78:81], v[114:117], v[22:25]\n"
    "ds_read_b128 v[114:117], v4 offset:12288\n"
    "s_waitcnt lgkmcnt(3)\n"
    "v_mfma_f32_16x16x32_bf16 v[18:21], v[82:85], v[118:121], v[18:21]\n"
    "v_mfma_f32_16x16x32_bf16 v[22:25], v[86:89], v[118:121], v[22:25]\n"
    "ds_read_b128 v[118:121], v4 offset:28672\n"
    "v_cvt_pk_bf16_f32 v26, v10, v11\n"
    "v_cvt_pk_bf16_f32 v27, v12, v13\n"
    "s_waitcnt lgkmcnt(3)\n"
    "v_mfma_f32_16x16x32_bf16 v[18:21], v[90:93], v[106:109], v[18:21]\n"
    "v_cvt_pk_bf16_f32 v28, v14, v15\n"
    "v_cvt_pk_bf16_f32 v29, v16, v17\n"
    "s_add_u32 s40, s30, 131072\n"
    "v_mfma_f32_16x16x32_bf16 v[22:25], v[94:97], v[106:109], v[22:25]\n"
    "ds_read_b128 v[106:109], v4 offset:45056\n"
    "buffer_store_dwordx4 v[26:29], v2, s[8:11], s40 offen sc1\n"
    "s_waitcnt lgkmcnt(3)\n"
    "v_mfma_f32_16x16x32_bf16 v[18:21], v[98:101], v[110:113], v[18:21]\n"
    "v_mfma_f32_16x16x32_bf16 v[22:25], v[102:105], v[110:113], v[22:25]\n"
    "ds_read_b128 v[110:113], v126 offset:12288\n"
    "v_mfma_f32_16x16x32_bf16 v[10:13], v[58:61], v[184:187], 0\n"
    "v_mfma_f32_16x16x32_bf16 v[14:17], v[62:65], v[184:187], 0\n"
    "v_mfma_f32_16x16x32_bf16 v[10:13], v[66:69], v[188:191], v[10:13]\n"
    "v_mfma_f32_16x16x32_bf16 v[14:17], v[70:73], v[188:191], v[14:17]\n"
    "v_mfma_f32_16x16x32_bf16 v[10:13], v[74:77], v[192:195], v[10:13]\n"
    "v_mfma_f32_16x16x32_bf16 v[14:17], v[78:81], v[192:195], v[14:17]\n"
    "v_mfma_f32_16x16x32_bf16 v[10:13], v[82:85], v[196:199], v[10:13]\n"
    "v_mfma_f32_16x16x32_bf16 v[14:17], v[86:89], v[196:199], v[14:17]\n"
    "v_cvt_pk_bf16_f32 v30, v18, v19\n"
    "v_cvt_pk_bf16_f32 v31, v20, v21\n"
    "v_mfma_f32_16x16x32_bf16 v[10:13], v[90:93], v[200:203], v[10:13]\n"
    "v_cvt_pk_bf16_f32 v32, v22, v23\n"
    "v_cvt_pk_bf16_f32 v33, v24, v25\n"
    "s_add_u32 s40, s30, 163840\n"
    "v_mfma_f32_16x16x32_bf16 v[14:17], v[94:97], v[200:203], v[14:17]\n"
    "buffer_store_dwordx4 v[30:33], v2, s[8:11], s40 offen sc1\n"
    "v_mfma_f32_16x16x32_bf16 v[10:13], v[98:101], v[204:207], v[10:13]\n"
    "v_mfma_f32_16x16x32_bf16 v[14:17], v[102:105], v[204:207], v[14:17]\n"
    "s_waitcnt lgkmcnt(3)\n"
    "v_mfma_f32_16x16x32_bf16 v[18:21], v[58:61], v[114:117], 0\n"
    "v_mfma_f32_16x16x32_bf16 v[22:25], v[62:65], v[114:117], 0\n"
    "ds_read_b128 v[114:117], v126 offset:28672\n"
    "s_waitcnt lgkmcnt(3)\n"
    "v_mfma_f32_16x16x32_bf16 v[18:21], v[66:69], v[118:121], v[18:21]\n"
    "v_mfma_f32_16x16x32_bf16 v[22:25], v[70:73], v[118:121], v[22:25]\n"
    "ds_read_b128 v[118:121], v126 offset:45056\n"
    "s_waitcnt lgkmcnt(3)\n"
    "v_mfma_f32_16x16x32_bf16 v[18:21], v[74:77], v[106:109], v[18:21]\n"
    "v_mfma_f32_16x16x32_bf16 v[22:25], v[78:81], v[106:109], v[22:25]\n"
    "s_waitcnt lgkmcnt(2)\n"
    "v_mfma_f32_16x16x32_bf16 v[18:21], v[82:85], v[110:113], v[18:21]\n"
    "v_mfma_f32_16x16x32_bf16 v[22:25], v[86:89], v[110:113], v[22:25]\n"
    "v_cvt_pk_bf16_f32 v26, v10, v11\n"
    "v_cvt_pk_bf16_f32 v27, v12, v13\n"
    "s_waitcnt lgkmcnt(1)\n"
    "v_mfma_f32_16x16x32_bf16 v[18:21], v[90:93], v[114:117], v[18:21]\n"
    "v_cvt_pk_bf16_f32 v28, v14, v15\n"
    "v_cvt_pk_bf16_f32 v29, v16, v17\n"
    "s_add_u32 s40, s30, 196608\n"
    "v_mfma_f32_16x16x32_bf16 v[22:25], v[94:97], v[114:117], v[22:25]\n"
    "buffer_store_dwordx4 v[26:29], v2, s[8:11], s40 offen sc1\n"
    "s_waitcnt lgkmcnt(0)\n"
    "v_mfma_f32_16x16x32_bf16 v[18:21], v[98:101], v[118:121], v[18:21]\n"
    "v_mfma_f32_16x16x32_bf16 v[22:25], v[102:105], v[118:121], v[22:25]\n"
    "v_mfma_f32_16x16x32_bf16 v[10:13], v[58:61], v[208:211], 0\n"
    "v_mfma_f32_16x16x32_bf16 v[14:17], v[62:65], v[208:211], 0\n"
    "v_mfma_f32_16x16x32_bf16 v[10:13], v[66:69], v[212:215], v[10:13]\n"
    "v_mfma_f32_16x16x32_bf16 v[14:17], v[70:73], v[212:215], v[14:17]\n"
    "v_mfma_f32_16x16x32_bf16 v[10:13], v[74:77], v[216:219], v[10:13]\n"
    "v_mfma_f32_16x16x32_bf16 v[14:17], v[78:81], v[216:219], v[14:17]\n"
    "v_mfma_f32_16x16x32_bf16 v[10:13], v[82:85], v[220:223], v[10:13]\n"
    "v_mfma_f32_16x16x32_bf16 v[14:17], v[86:89], v[220:223], v[14:17]\n"
    "v_cvt_pk_bf16_f32 v30, v18, v19\n"
    "v_cvt_pk_bf16_f32 v31, v20, v21\n"
    "v_mfma_f32_16x16x32_bf16 v[10:13], v[90:93], v[224:227], v[10:13]\n"
    "v_cvt_pk_bf16_f32 v32, v22, v23\n"
    "v_cvt_pk_bf16_f32 v33, v24, v25\n"
    "s_add_u32 s40, s30, 229376\n"
    "v_mfma_f32_16x16x32_bf16 v[14:17], v[94:97], v[224:227], v[14:17]\n"
    "buffer_store_dwordx4 v[30:33], v2, s[8:11], s40 offen sc1\n"
    "v_mfma_f32_16x16x32_bf16 v[10:13], v[98:101], v[228:231], v[10:13]\n"
    "v_mfma_f32_16x16x32_bf16 v[14:17], v[102:105], v[228:231], v[14:17]\n"
    "v_mfma_f32_16x16x32_bf16 v[18:21], v[58:61], v[232:235], 0\n"
    "v_mfma_f32_16x16x32_bf16 v[22:25], v[62:65], v[232:235], 0\n"
    "v_mfma_f32_16x16x32_bf16 v[18:21], v[66:69], v[236:239], v[18:21]\n"
    "s_waitcnt vmcnt(11)\n"
    "s_barrier\n"
    "ds_read_b128 v[34:37], v3 offset:0\n"
    "v_mfma_f32_16x16x32_bf16 v[22:25], v[70:73], v[236:239], v[22:25]\n"
    "ds_read_b128 v[38:41], v3 offset:20480\n"
    "v_mfma_f32_16x16x32_bf16 v[18:21], v[74:77], v[240:243], v[18:21]\n"
    "ds_read_b128 v[42:45], v3 offset:1024\n"
    "v_mfma_f32_16x16x32_bf16 v[22:25], v[78:81], v[240:243], v[22:25]\n"
    "ds_read_b128 v[46:49], v3 offset:21504\n"
    "v_mfma_f32_16x16x32_bf16 v[18:21], v[82:85], v[244:247], v[18:21]\n"
    "v_mfma_f32_16x16x32_bf16 v[22:25], v[86:89], v[244:247], v[22:25]\n"
    "v_cvt_pk_bf16_f32 v26, v10, v11\n"
    "v_cvt_pk_bf16_f32 v27, v12, v13\n"
    "v_mfma_f32_16x16x32_bf16 v[18:21], v[90:93], v[248:251], v[18:21]\n"
    "v_cvt_pk_bf16_f32 v28, v14, v15\n"
    "v_cvt_pk_bf16_f32 v29, v16, v17\n"
    "s_add_u32 s40, s30, 262144\n"
    "v_mfma_f32_16x16x32_bf16 v[22:25], v[94:97], v[248:251], v[22:25]\n"
    "buffer_store_dwordx4 v[26:29], v2, s[8:11], s40 offen sc1\n"
    "v_mfma_f32_16x16x32_bf16 v[18:21], v[98:101], v[252:255], v[18:21]\n"
    "v_mfma_f32_16x16x32_bf16 v[22:25], v[102:105], v[252:255], v[22:25]\n"
    "s_add_u32 s24, s24, 2\n"
    "s_nop 5\n"
    "v_cvt_pk_bf16_f32 v30, v18, v19\n"
    "v_cvt_pk_bf16_f32 v31, v20, v21\n"
    "v_cvt_pk_bf16_f32 v32, v22, v23\n"
    "v_cvt_pk_bf16_f32 v33, v24, v25\n"
    "s_add_u32 s40, s30, 294912\n"
    "buffer_store_dwordx4 v[30:33], v2, s[8:11], s40 offen sc1\n"
    "s_add_u32 s30, s30, 2621440\n"
    "s_cmp_gt_u32 s24, 5\n"
    "s_cbranch_scc1 .LU_more\n"
    "s_waitcnt vmcnt(0)\n"
    "buffer_store_dword v8, v7, s[8:11], 0 offen sc1\n"
    ".LU_more:\n"
    "s_cmp_lt_u32 s24, 64\n"
    "s_cbranch_scc1 .LU_loop\n"
    "s_waitcnt vmcnt(0) lgkmcnt(0)\n"
    "buffer_store_dword v8, v7, s[8:11], 0 offen sc1\n"
    "s_cmp_ge_u32 s20, 60\n"
    "s_cbranch_scc1 .Lend\n"
    "s_barrier\n"
    "s_mul_i32 s40, s20, 103\n"
    "s_lshr_b32 s40, s40, 11\n"
    "s_mul_i32 s24, s40, 1\n"
    "s_add_u32 s24, s24, 35\n"
    "s_mov_b32 s41, s40\n"
    ".Log_190:\n"
    "s_sleep 40\n"
    "s_sub_u32 s41, s41, 1\n"
    "s_cbranch_scc0 .Log_190\n"
    "s_mul_i32 s40, s40, 20\n"
    "s_sub_u32 s20, s20, s40\n"
    "s_mov_b32 s37, 3\n"
    "s_mov_b32 s38, 62\n"
    "s_branch .LO_enter\n"
    ".Lcode1:\n"

    :: "{s[0:1]}"(__builtin_amdgcn_kernarg_segment_ptr()), "{s2}"(blockIdx.x), "{v0}"(threadIdx.x), "{s3}"((unsigned)(size_t)lds)
    :
      "v1", "v2", "v3", "v4", "v5", "v6", "v7", "v8", "v9", "v10", "v11", "v12", "v13", "v14", "v15", "v16", "v17", "v18", "v19", "v20", "v21", "v22", "v23", "v24",
      "v25", "v26", "v27", "v28", "v29", "v30", "v31", "v32", "v33", "v34", "v35", "v36", "v37", "v38", "v39", "v40", "v41", "v42", "v43", "v44", "v45", "v46", "v47", "v48",
      "v49", "v50", "v51", "v52", "v53", "v54", "v55", "v56", "v57", "v58", "v59", "v60", "v61", "v62", "v63", "v64", "v65", "v66", "v67", "v68", "v69", "v70", "v71", "v72",
      "v73", "v74", "v75", "v76", "v77", "v78", "v79", "v80", "v81", "v82", "v83", "v84", "v85", "v86", "v87", "v88", "v89", "v90", "v91", "v92", "v93", "v94", "v95", "v96",
      "v97", "v98", "v99", "v100", "v101", "v102", "v103", "v104", "v105", "v106", "v107", "v108", "v109", "v110", "v111", "v112", "v113", "v114", "v115", "v116", "v117", "v118", "v119", "v120",
      "v121", "v122", "v123", "v124", "v125", "v126", "v127", "v128", "v129", "v130", "v131", "v132", "v133", "v134", "v135", "v136", "v137", "v138", "v139", "v140", "v141", "v142", "v143", "v144",
      "v145", "v146", "v147", "v148", "v149", "v150", "v151", "v152", "v153", "v154", "v155", "v156", "v157", "v158", "v159", "v160", "v161", "v162", "v163", "v164", "v165", "v166", "v167", "v168",
      "v169", "v170", "v171", "v172", "v173", "v174", "v175", "v176", "v177", "v178", "v179", "v180", "v181", "v182", "v183", "v184", "v185", "v186", "v187", "v188", "v189", "v190", "v191", "v192",
      "v193", "v194", "v195", "v196", "v197", "v198", "v199", "v200", "v201", "v202", "v203", "v204", "v205", "v206", "v207", "v208", "v209", "v210", "v211", "v212", "v213", "v214", "v215", "v216",
      "v217", "v218", "v219", "v220", "v221", "v222", "v223", "v224", "v225", "v226", "v227", "v228", "v229", "v230", "v231", "v232", "v233", "v234", "v235", "v236", "v237", "v238", "v239", "v240",
      "v241", "v242", "v243", "v244", "v245", "v246", "v247", "v248", "v249", "v250", "v251", "v252", "v253", "v254", "v255", "a0", "a1", "a2", "a3", "a4", "a5", "a6", "a7", "a8",
      "a9", "a10", "a11", "a12", "a13", "a14", "a15", "a16", "a17", "a18", "a19", "a20", "a21", "a22", "a23", "a24", "a25", "a26", "a27", "a28", "a29", "a30", "a31", "a32",
      "a33", "a34", "a35", "a36", "a37", "a38", "a39", "a40", "a41", "a42", "a43", "a44", "a45", "a46", "a47", "a48", "a49", "a50", "a51", "a52", "a53", "a54", "a55", "a56",
      "a57", "a58", "a59", "a60", "a61", "a62", "a63", "a64", "a65", "a66", "a67", "a68", "a69", "a70", "a71", "a72", "a73", "a74", "a75", "a76", "a77", "a78", "a79", "a80",
      "a81", "a82", "a83", "a84", "a85", "a86", "a87", "a88", "a89", "a90", "a91", "a92", "a93", "a94", "a95", "a96", "a97", "a98", "a99", "a100", "a101", "a102", "a103", "a104",
      "a105", "a106", "a107", "a108", "a109", "a110", "a111", "a112", "a113", "a114", "a115", "a116", "a117", "a118", "a119", "a120", "a121", "a122", "a123", "a124", "a125", "a126", "a127", "a128",
      "a129", "a130", "a131", "a132", "a133", "a134", "a135", "a136", "a137", "a138", "a139", "a140", "a141", "a142", "a143", "a144", "a145", "a146", "a147", "a148", "a149", "a150", "a151", "a152",
      "a153", "a154", "a155", "a156", "a157", "a158", "a159", "a160", "a161", "a162", "a163", "a164", "a165", "a166", "a167", "a168", "a169", "a170", "a171", "a172", "a173", "a174", "a175", "a176",
      "a177", "a178", "a179", "a180", "a181", "a182", "a183", "a184", "a185", "a186", "a187", "a188", "a189", "a190", "a191", "a192", "a193", "a194", "a195", "a196", "a197", "a198", "a199", "a200",
      "a201", "a202", "a203", "a204", "a205", "a206", "a207", "a208", "a209", "a210", "a211", "a212", "a213", "a214", "a215", "a216", "a217", "a218", "a219", "a220", "a221", "a222", "a223", "a224",
      "a225", "a226", "a227", "a228", "a229", "a230", "a231", "a232", "a233", "a234", "a235", "a236", "a237", "a238", "a239", "a240", "a241", "a242", "a243", "a244", "a245", "a246", "a247", "a248",
      "a249", "a250", "a251", "a252", "a253", "a254", "a255", "s4", "s5", "s6", "s7", "s8", "s9", "s10", "s11", "s12", "s13", "s14", "s15", "s16", "s17", "s18", "s19", "s20",
      "s21", "s22", "s23", "s24", "s25", "s26", "s27", "s28", "s29", "s30", "s31", "s33", "s34", "s35", "s36", "s37", "s38", "s39", "s40", "s41", "s42", "s43", "s44", "s45",
      "s46", "s47", "s48", "s49", "s50", "s51", "s52", "s53", "s54", "s55", "s56", "s57", "s58", "s59", "s60", "s61", "s62", "s63", "s64", "s65", "s66", "s67", "s68", "s69",
      "s70", "s71", "s72", "s73", "s74", "s75", "s76", "s77", "s78", "s79", "s80", "s81", "s82", "s83", "s84", "s85", "s86", "s87", "s88", "s89", "s90", "s91", "s92", "s93",
      "s94", "s95", "s96", "s97", "s98", "s99", "vcc", "scc", "memory"

  );
}

extern "C" void kernel_launch(void* const* d_in, const int* in_sizes, int n_in, void* d_out, int out_size, void* d_ws, size_t ws_size, hipStream_t stream) {
  k_main<<<256, 256, 0, stream>>>((const u16*)d_in[0], (const u16*)d_in[2], (const u16*)d_in[3], (const u16*)d_in[4], (const u16*)d_in[5], (const u16*)d_in[8],
                                  (const u16*)d_in[9], (const u16*)d_in[10], (u16*)d_out, (unsigned char*)d_ws, (const u16*)d_in[1]
                                  );
}
```
